```python
import torch

def _softmax(x, axis=-1):
    return torch.softmax(x, dim=axis)

def _gelu(x):
    return torch.nn.functional.gelu(x, approximate='tanh')

def _silu(x):
    return torch.nn.functional.silu(x)

def _sigmoid(x):
    return torch.sigmoid(x)

def _layernorm(x, axis=-1, eps=1e-05):
    m = x.mean(dim=axis, keepdim=True)
    v = x.var(dim=axis, keepdim=True, unbiased=False)
    return (x - m) / torch.sqrt(v + eps)

def _scatter_add(tgt, idx, src, axis=0):
    return tgt.clone().index_add_(axis, idx, src)

def _segment_sum(src, seg, n):
    out = torch.zeros((n,) + src.shape[1:], dtype=src.dtype)
    return out.index_add_(0, seg, src)

def _pad(x, pw):
    flat = tuple((v for pair in reversed(pw) for v in pair))
    return torch.nn.functional.pad(x, flat)
kernel_name = 'gather_scatter__tensor_product_equiv_tfn__graph'

def setup_inputs():

    _w = lambda a: (lambda t: t.to(torch.bfloat16) if t.is_floating_point() else t)(torch.as_tensor(a))
    N, deg, C, L, DE = (10000, 32, 128, 1, 4)
    E = N * deg
    M = (L + 1) ** 2
    src = (lambda lo, hi, *s: torch.randint(lo, hi, s))(0, N, E).to(torch.int64)
    dst = (lambda lo, hi, *s: torch.randint(lo, hi, s))(0, N, E).to(torch.int64)
    r = (lambda *s: torch.randn(*s, dtype=torch.bfloat16) * 0.05)(E, 3)
    r = r / (torch.linalg.norm(r, dim=-1, keepdim=True) + 1e-09)
    rx, ry, rz = (r[:, 0].clone(), r[:, 1].clone(), r[:, 2].clone())
    Ys = [torch.ones((E, 1), dtype=float)]
    if L >= 1:
        Ys.append(r.to(float))
    if L >= 2:
        Ys.append(torch.stack([2 * rx * ry, 2 * ry * rz, (3 * rz * rz - 1.0) / (lambda _x: torch.sqrt(torch.as_tensor(_x)))(3.0), 2 * rx * rz, rx * rx - ry * ry], dim=-1).to(float))
    for _l in range(3, L + 1):
        Ys.append(((lambda *s: torch.randn(*s, dtype=torch.bfloat16) * 0.05)(E, 2 * _l + 1) * 0.3).to(float))
    Y = torch.cat(Ys, dim=-1)
    cg = torch.zeros((M, M, M), dtype=float)
    off = [_l * _l for _l in range(L + 2)]
    for l1 in range(L + 1):
        for l2 in range(L + 1):
            for l3 in range(abs(l1 - l2), min(L, l1 + l2) + 1):
                d1, d2, d3 = (2 * l1 + 1, 2 * l2 + 1, 2 * l3 + 1)
                cg[off[l1]:off[l1 + 1], off[l2]:off[l2 + 1], off[l3]:off[l3 + 1]] = (lambda *s: torch.randn(*s, dtype=torch.bfloat16) * 0.05)(d1, d2, d3) / (lambda _x: torch.sqrt(torch.as_tensor(_x)))(float(d1 * d2))
    return {'x': _w((lambda *s: torch.randn(*s, dtype=torch.bfloat16) * 0.05)(N, C, M) * 0.2), 'src': _w(src), 'dst': _w(dst), 'Y': _w(Y), 'edge_feat': _w((lambda *s: torch.randn(*s, dtype=torch.bfloat16) * 0.05)(E, DE)), 'w_rad': _w((lambda *s: torch.randn(*s, dtype=torch.bfloat16) * 0.05)(DE, C) * (1.0 / max(1.0, DE) ** 0.5)), 'cg': _w(cg), 'w_mix': _w((lambda *s: torch.randn(*s, dtype=torch.bfloat16) * 0.05)(C, C) * (1.0 / max(1.0, C) ** 0.5))}

def reference(x, src, dst, Y, edge_feat, w_rad, cg, w_mix):
    N, C, M = (x.shape[0], x.shape[1], x.shape[2])
    R = _silu(edge_feat @ w_rad)
    xs = x[src]
    tp = torch.einsum('eci,ef,ifo->eco', xs, Y, cg)
    msg = R[:, :, None] * tp
    agg = _segment_sum(msg.reshape(-1, C * M), dst, N).reshape(N, C, M)
    return torch.einsum('ncm,cd->ndm', agg, w_mix)
```

```cpp
#include <hip/hip_runtime.h>
#include <hip/hip_bf16.h>
#include <stdint.h>
#include <stddef.h>

typedef __attribute__((ext_vector_type(2))) float f32x2;
typedef __attribute__((ext_vector_type(4))) float f32x4;
typedef __attribute__((ext_vector_type(8))) short s16x8;
typedef __attribute__((ext_vector_type(2))) unsigned int u32x2;
typedef __attribute__((ext_vector_type(4))) unsigned int u32x4;
typedef __attribute__((ext_vector_type(16))) unsigned int u32x16;
typedef __attribute__((ext_vector_type(2))) __bf16 bf16x2;

#define TDECL
#define TSTAMP(i)
#define TFLUSH
#define PSTAMP(i)
#define PFLUSH
#define MSTAMP(i) TSTAMP(i)
#define MFLUSH TFLUSH

#define STREAM_ASM \
  "ds_read_b64 v[94:95], v15\n" \
  "ds_read_b64 v[92:93], v16\n" \
  "ds_read_b32 v12, v14 offset:32\n" \
  "s_mov_b32 s79, m0\n" \
  "s_mov_b32 s60, %[rem]\n" \
  "s_mov_b64 s[68:69], %[xb]\n" \
  "s_mov_b32 s70, %[xn]\n" \
  "s_mov_b32 s71, 0x20000\n" \
  "v_readlane_b32 s61, v17, s60\n" \
  "s_mov_b32 s62, 0\n" \
  "v_mov_b64 v[128:129], 0\n" \
  "v_mov_b64 v[130:131], 0\n" \
  "v_mov_b64 v[132:133], 0\n" \
  "v_mov_b64 v[134:135], 0\n" \
  "v_mov_b64 v[136:137], 0\n" \
  "v_mov_b64 v[138:139], 0\n" \
  "v_mov_b64 v[140:141], 0\n" \
  "v_mov_b64 v[142:143], 0\n" \
  "s_waitcnt lgkmcnt(0)\n" \
  "v_mfma_f32_4x4x4_16b_bf16 v[56:59], v[92:93], v[32:33], 0 cbsz:4\n" \
  "v_mfma_f32_4x4x4_16b_bf16 v[60:63], v[92:93], v[36:37], 0 cbsz:4\n" \
  "v_mfma_f32_4x4x4_16b_bf16 v[64:67], v[92:93], v[32:33], 0 cbsz:4 abid:1\n" \
  "v_mfma_f32_4x4x4_16b_bf16 v[68:71], v[92:93], v[36:37], 0 cbsz:4 abid:1\n" \
  "v_mfma_f32_4x4x4_16b_bf16 v[84:87], v[94:95], v[24:25], 0\n" \
  "v_mfma_f32_4x4x4_16b_bf16 v[88:91], v[94:95], v[26:27], 0\n" \
  "v_mfma_f32_4x4x4_16b_bf16 v[48:51], v[94:95], v[28:29], 0\n" \
  "v_mfma_f32_4x4x4_16b_bf16 v[52:55], v[94:95], v[30:31], 0\n" \
  "s_nop 1\n" \
  "v_cvt_pk_bf16_f32 v57, v56, v57\n" \
  "v_lshlrev_b32_sdwa v56, 1, v57 dst_sel:DWORD dst_unused:UNUSED_PAD src0_sel:DWORD src1_sel:WORD_0\n" \
  "v_lshlrev_b32_sdwa v57, 1, v57 dst_sel:DWORD dst_unused:UNUSED_PAD src0_sel:DWORD src1_sel:WORD_1\n" \
  "ds_read_u16_d16_hi v56, v56\n" \
  "ds_read_u16_d16_hi v57, v57\n" \
  "v_cvt_pk_bf16_f32 v59, v58, v59\n" \
  "v_lshlrev_b32_sdwa v58, 1, v59 dst_sel:DWORD dst_unused:UNUSED_PAD src0_sel:DWORD src1_sel:WORD_0\n" \
  "v_lshlrev_b32_sdwa v59, 1, v59 dst_sel:DWORD dst_unused:UNUSED_PAD src0_sel:DWORD src1_sel:WORD_1\n" \
  "ds_read_u16_d16_hi v58, v58\n" \
  "ds_read_u16_d16_hi v59, v59\n" \
  "v_cvt_pk_bf16_f32 v61, v60, v61\n" \
  "v_lshlrev_b32_sdwa v60, 1, v61 dst_sel:DWORD dst_unused:UNUSED_PAD src0_sel:DWORD src1_sel:WORD_0\n" \
  "v_lshlrev_b32_sdwa v61, 1, v61 dst_sel:DWORD dst_unused:UNUSED_PAD src0_sel:DWORD src1_sel:WORD_1\n" \
  "ds_read_u16_d16_hi v60, v60\n" \
  "ds_read_u16_d16_hi v61, v61\n" \
  "v_cvt_pk_bf16_f32 v63, v62, v63\n" \
  "v_lshlrev_b32_sdwa v62, 1, v63 dst_sel:DWORD dst_unused:UNUSED_PAD src0_sel:DWORD src1_sel:WORD_0\n" \
  "v_lshlrev_b32_sdwa v63, 1, v63 dst_sel:DWORD dst_unused:UNUSED_PAD src0_sel:DWORD src1_sel:WORD_1\n" \
  "ds_read_u16_d16_hi v62, v62\n" \
  "ds_read_u16_d16_hi v63, v63\n" \
  "v_cvt_pk_bf16_f32 v76, v84, v88\n" \
  "v_cvt_pk_bf16_f32 v77, v48, v52\n" \
  "v_mov_b64 v[144:145], 0\n" \
  "v_mov_b64 v[146:147], 0\n" \
  "v_mov_b64 v[148:149], 0\n" \
  "v_mov_b64 v[150:151], 0\n" \
  "v_mov_b64 v[152:153], 0\n" \
  "v_mov_b64 v[154:155], 0\n" \
  "v_mov_b64 v[156:157], 0\n" \
  "v_mov_b64 v[158:159], 0\n" \
  "v_mov_b64 v[160:161], 0\n" \
  "v_mov_b64 v[162:163], 0\n" \
  "v_mov_b64 v[164:165], 0\n" \
  "v_mov_b64 v[166:167], 0\n" \
  "v_mov_b64 v[168:169], 0\n" \
  "v_mov_b64 v[170:171], 0\n" \
  "v_mov_b64 v[172:173], 0\n" \
  "v_mov_b64 v[174:175], 0\n" \
  "v_mov_b64 v[176:177], 0\n" \
  "v_mov_b64 v[178:179], 0\n" \
  "v_mov_b64 v[180:181], 0\n" \
  "v_mov_b64 v[182:183], 0\n" \
  "v_mov_b64 v[184:185], 0\n" \
  "v_mov_b64 v[186:187], 0\n" \
  "v_mov_b64 v[188:189], 0\n" \
  "v_mov_b64 v[190:191], 0\n" \
  "v_mov_b64 v[192:193], 0\n" \
  "v_mov_b64 v[194:195], 0\n" \
  "v_mov_b64 v[196:197], 0\n" \
  "v_mov_b64 v[198:199], 0\n" \
  "v_mov_b64 v[200:201], 0\n" \
  "v_mov_b64 v[202:203], 0\n" \
  "v_mov_b64 v[204:205], 0\n" \
  "v_mov_b64 v[206:207], 0\n" \
  "s_waitcnt vmcnt(7) lgkmcnt(0)\n" \
  "v_mfma_f32_4x4x4_16b_bf16 v[48:51], v[76:77], v[96:97], 0 cbsz:4\n" \
  "v_mfma_f32_4x4x4_16b_bf16 v[52:55], v[76:77], v[98:99], 0 cbsz:4\n" \
  "s_nop 4\n" \
  "Lbody%=:\n" \
  "s_set_gpr_idx_on s61, gpr_idx(SRC2,DST)\n" \
  "v_pk_fma_f32 v[128:129], v[56:57], v[48:49], v[128:129] op_sel:[0,0,0] op_sel_hi:[0,1,1]\n" \
  "v_pk_fma_f32 v[130:131], v[56:57], v[50:51], v[130:131] op_sel:[0,0,0] op_sel_hi:[0,1,1]\n" \
  "v_pk_fma_f32 v[132:133], v[60:61], v[52:53], v[132:133] op_sel:[0,0,0] op_sel_hi:[0,1,1]\n" \
  "v_pk_fma_f32 v[134:135], v[60:61], v[54:55], v[134:135] op_sel:[0,0,0] op_sel_hi:[0,1,1]\n" \
  "s_set_gpr_idx_off\n" \
  "s_waitcnt vmcnt(6) lgkmcnt(2)\n" \
  "buffer_load_dwordx4 v[96:99], v[12:13], s[68:71], 0 idxen offen\n" \
  "ds_read_b32 v12, v14 offset:36\n" \
  "s_lshr_b32 s61, s61, 8\n" \
  "v_mfma_f32_4x4x4_16b_bf16 v[48:51], v[76:77], v[100:101], 0 cbsz:4 abid:1\n" \
  "v_mfma_f32_4x4x4_16b_bf16 v[52:55], v[76:77], v[102:103], 0 cbsz:4 abid:1\n" \
  "v_cvt_pk_bf16_f32 v65, v64, v65\n" \
  "v_lshlrev_b32_sdwa v64, 1, v65 dst_sel:DWORD dst_unused:UNUSED_PAD src0_sel:DWORD src1_sel:WORD_0\n" \
  "v_lshlrev_b32_sdwa v65, 1, v65 dst_sel:DWORD dst_unused:UNUSED_PAD src0_sel:DWORD src1_sel:WORD_1\n" \
  "ds_read_u16_d16_hi v64, v64\n" \
  "ds_read_u16_d16_hi v65, v65\n" \
  "s_set_gpr_idx_on s61, gpr_idx(SRC2,DST)\n" \
  "v_pk_fma_f32 v[128:129], v[56:57], v[48:49], v[128:129] op_sel:[1,0,0] op_sel_hi:[1,1,1]\n" \
  "v_pk_fma_f32 v[130:131], v[56:57], v[50:51], v[130:131] op_sel:[1,0,0] op_sel_hi:[1,1,1]\n" \
  "v_pk_fma_f32 v[132:133], v[60:61], v[52:53], v[132:133] op_sel:[1,0,0] op_sel_hi:[1,1,1]\n" \
  "v_pk_fma_f32 v[134:135], v[60:61], v[54:55], v[134:135] op_sel:[1,0,0] op_sel_hi:[1,1,1]\n" \
  "s_set_gpr_idx_off\n" \
  "s_waitcnt vmcnt(6) lgkmcnt(2)\n" \
  "buffer_load_dwordx4 v[100:103], v[12:13], s[68:71], 0 idxen offen\n" \
  "ds_read_b32 v12, v14 offset:40\n" \
  "s_lshr_b32 s61, s61, 8\n" \
  "v_mfma_f32_4x4x4_16b_bf16 v[48:51], v[76:77], v[104:105], 0 cbsz:4 abid:2\n" \
  "v_mfma_f32_4x4x4_16b_bf16 v[52:55], v[76:77], v[106:107], 0 cbsz:4 abid:2\n" \
  "v_cvt_pk_bf16_f32 v69, v68, v69\n" \
  "v_lshlrev_b32_sdwa v68, 1, v69 dst_sel:DWORD dst_unused:UNUSED_PAD src0_sel:DWORD src1_sel:WORD_0\n" \
  "v_lshlrev_b32_sdwa v69, 1, v69 dst_sel:DWORD dst_unused:UNUSED_PAD src0_sel:DWORD src1_sel:WORD_1\n" \
  "ds_read_u16_d16_hi v68, v68\n" \
  "ds_read_u16_d16_hi v69, v69\n" \
  "s_set_gpr_idx_on s61, gpr_idx(SRC2,DST)\n" \
  "v_pk_fma_f32 v[128:129], v[58:59], v[48:49], v[128:129] op_sel:[0,0,0] op_sel_hi:[0,1,1]\n" \
  "v_pk_fma_f32 v[130:131], v[58:59], v[50:51], v[130:131] op_sel:[0,0,0] op_sel_hi:[0,1,1]\n" \
  "v_pk_fma_f32 v[132:133], v[62:63], v[52:53], v[132:133] op_sel:[0,0,0] op_sel_hi:[0,1,1]\n" \
  "v_pk_fma_f32 v[134:135], v[62:63], v[54:55], v[134:135] op_sel:[0,0,0] op_sel_hi:[0,1,1]\n" \
  "s_set_gpr_idx_off\n" \
  "s_waitcnt vmcnt(6) lgkmcnt(2)\n" \
  "buffer_load_dwordx4 v[104:107], v[12:13], s[68:71], 0 idxen offen\n" \
  "ds_read_b32 v12, v14 offset:44\n" \
  "s_lshr_b32 s61, s61, 8\n" \
  "v_mfma_f32_4x4x4_16b_bf16 v[48:51], v[76:77], v[108:109], 0 cbsz:4 abid:3\n" \
  "v_mfma_f32_4x4x4_16b_bf16 v[52:55], v[76:77], v[110:111], 0 cbsz:4 abid:3\n" \
  "v_cvt_pk_bf16_f32 v67, v66, v67\n" \
  "v_lshlrev_b32_sdwa v66, 1, v67 dst_sel:DWORD dst_unused:UNUSED_PAD src0_sel:DWORD src1_sel:WORD_0\n" \
  "v_lshlrev_b32_sdwa v67, 1, v67 dst_sel:DWORD dst_unused:UNUSED_PAD src0_sel:DWORD src1_sel:WORD_1\n" \
  "ds_read_u16_d16_hi v66, v66\n" \
  "ds_read_u16_d16_hi v67, v67\n" \
  "s_set_gpr_idx_on s61, gpr_idx(SRC2,DST)\n" \
  "v_pk_fma_f32 v[128:129], v[58:59], v[48:49], v[128:129] op_sel:[1,0,0] op_sel_hi:[1,1,1]\n" \
  "v_pk_fma_f32 v[130:131], v[58:59], v[50:51], v[130:131] op_sel:[1,0,0] op_sel_hi:[1,1,1]\n" \
  "v_pk_fma_f32 v[132:133], v[62:63], v[52:53], v[132:133] op_sel:[1,0,0] op_sel_hi:[1,1,1]\n" \
  "v_pk_fma_f32 v[134:135], v[62:63], v[54:55], v[134:135] op_sel:[1,0,0] op_sel_hi:[1,1,1]\n" \
  "s_set_gpr_idx_off\n" \
  "s_waitcnt vmcnt(6) lgkmcnt(2)\n" \
  "buffer_load_dwordx4 v[108:111], v[12:13], s[68:71], 0 idxen offen\n" \
  "ds_read_b32 v12, v14 offset:48\n" \
  "s_sub_u32 s60, s60, 1\n" \
  "s_cbranch_scc1 Lstub0_%=\n" \
  "Lback0_%=:\n" \
  "v_readlane_b32 s61, v17, s60\n" \
  "v_mfma_f32_4x4x4_16b_bf16 v[56:59], v[92:93], v[32:33], 0 cbsz:4 abid:2\n" \
  "v_mfma_f32_4x4x4_16b_bf16 v[60:63], v[92:93], v[36:37], 0 cbsz:4 abid:2\n" \
  "v_mfma_f32_4x4x4_16b_bf16 v[48:51], v[76:77], v[112:113], 0 cbsz:4 abid:4\n" \
  "v_mfma_f32_4x4x4_16b_bf16 v[52:55], v[76:77], v[114:115], 0 cbsz:4 abid:4\n" \
  "v_cvt_pk_bf16_f32 v71, v70, v71\n" \
  "v_lshlrev_b32_sdwa v70, 1, v71 dst_sel:DWORD dst_unused:UNUSED_PAD src0_sel:DWORD src1_sel:WORD_0\n" \
  "v_lshlrev_b32_sdwa v71, 1, v71 dst_sel:DWORD dst_unused:UNUSED_PAD src0_sel:DWORD src1_sel:WORD_1\n" \
  "ds_read_u16_d16_hi v70, v70\n" \
  "ds_read_u16_d16_hi v71, v71\n" \
  "v_add_u32 v16, 0x100, v16\n" \
  "s_set_gpr_idx_on s61, gpr_idx(SRC2,DST)\n" \
  "v_pk_fma_f32 v[128:129], v[64:65], v[48:49], v[128:129] op_sel:[0,0,0] op_sel_hi:[0,1,1]\n" \
  "v_pk_fma_f32 v[130:131], v[64:65], v[50:51], v[130:131] op_sel:[0,0,0] op_sel_hi:[0,1,1]\n" \
  "v_pk_fma_f32 v[132:133], v[68:69], v[52:53], v[132:133] op_sel:[0,0,0] op_sel_hi:[0,1,1]\n" \
  "v_pk_fma_f32 v[134:135], v[68:69], v[54:55], v[134:135] op_sel:[0,0,0] op_sel_hi:[0,1,1]\n" \
  "s_set_gpr_idx_off\n" \
  "s_waitcnt vmcnt(6) lgkmcnt(2)\n" \
  "buffer_load_dwordx4 v[112:115], v[12:13], s[68:71], 0 idxen offen\n" \
  "ds_read_b32 v12, v14 offset:52\n" \
  "ds_read_b64 v[94:95], v15 offset:256\n" \
  "s_lshr_b32 s61, s61, 8\n" \
  "v_mfma_f32_4x4x4_16b_bf16 v[48:51], v[76:77], v[116:117], 0 cbsz:4 abid:5\n" \
  "v_mfma_f32_4x4x4_16b_bf16 v[52:55], v[76:77], v[118:119], 0 cbsz:4 abid:5\n" \
  "v_cvt_pk_bf16_f32 v57, v56, v57\n" \
  "v_lshlrev_b32_sdwa v56, 1, v57 dst_sel:DWORD dst_unused:UNUSED_PAD src0_sel:DWORD src1_sel:WORD_0\n" \
  "v_lshlrev_b32_sdwa v57, 1, v57 dst_sel:DWORD dst_unused:UNUSED_PAD src0_sel:DWORD src1_sel:WORD_1\n" \
  "ds_read_u16_d16_hi v56, v56\n" \
  "ds_read_u16_d16_hi v57, v57\n" \
  "s_set_gpr_idx_on s61, gpr_idx(SRC2,DST)\n" \
  "v_pk_fma_f32 v[128:129], v[64:65], v[48:49], v[128:129] op_sel:[1,0,0] op_sel_hi:[1,1,1]\n" \
  "v_pk_fma_f32 v[130:131], v[64:65], v[50:51], v[130:131] op_sel:[1,0,0] op_sel_hi:[1,1,1]\n" \
  "v_pk_fma_f32 v[132:133], v[68:69], v[52:53], v[132:133] op_sel:[1,0,0] op_sel_hi:[1,1,1]\n" \
  "v_pk_fma_f32 v[134:135], v[68:69], v[54:55], v[134:135] op_sel:[1,0,0] op_sel_hi:[1,1,1]\n" \
  "s_set_gpr_idx_off\n" \
  "s_waitcnt vmcnt(6) lgkmcnt(2)\n" \
  "buffer_load_dwordx4 v[116:119], v[12:13], s[68:71], 0 idxen offen\n" \
  "ds_read_b32 v12, v14 offset:56\n" \
  "s_lshr_b32 s61, s61, 8\n" \
  "v_mfma_f32_4x4x4_16b_bf16 v[48:51], v[76:77], v[120:121], 0 cbsz:4 abid:6\n" \
  "v_mfma_f32_4x4x4_16b_bf16 v[52:55], v[76:77], v[122:123], 0 cbsz:4 abid:6\n" \
  "v_cvt_pk_bf16_f32 v61, v60, v61\n" \
  "v_lshlrev_b32_sdwa v60, 1, v61 dst_sel:DWORD dst_unused:UNUSED_PAD src0_sel:DWORD src1_sel:WORD_0\n" \
  "v_lshlrev_b32_sdwa v61, 1, v61 dst_sel:DWORD dst_unused:UNUSED_PAD src0_sel:DWORD src1_sel:WORD_1\n" \
  "ds_read_u16_d16_hi v60, v60\n" \
  "ds_read_u16_d16_hi v61, v61\n" \
  "s_set_gpr_idx_on s61, gpr_idx(SRC2,DST)\n" \
  "v_pk_fma_f32 v[128:129], v[66:67], v[48:49], v[128:129] op_sel:[0,0,0] op_sel_hi:[0,1,1]\n" \
  "v_pk_fma_f32 v[130:131], v[66:67], v[50:51], v[130:131] op_sel:[0,0,0] op_sel_hi:[0,1,1]\n" \
  "v_pk_fma_f32 v[132:133], v[70:71], v[52:53], v[132:133] op_sel:[0,0,0] op_sel_hi:[0,1,1]\n" \
  "v_pk_fma_f32 v[134:135], v[70:71], v[54:55], v[134:135] op_sel:[0,0,0] op_sel_hi:[0,1,1]\n" \
  "s_set_gpr_idx_off\n" \
  "s_waitcnt vmcnt(6) lgkmcnt(2)\n" \
  "buffer_load_dwordx4 v[120:123], v[12:13], s[68:71], 0 idxen offen\n" \
  "ds_read_b32 v12, v14 offset:60\n" \
  "s_lshr_b32 s61, s61, 8\n" \
  "v_mfma_f32_4x4x4_16b_bf16 v[84:87], v[94:95], v[24:25], 0\n" \
  "v_mfma_f32_4x4x4_16b_bf16 v[48:51], v[76:77], v[124:125], 0 cbsz:4 abid:7\n" \
  "v_mfma_f32_4x4x4_16b_bf16 v[52:55], v[76:77], v[126:127], 0 cbsz:4 abid:7\n" \
  "v_cvt_pk_bf16_f32 v59, v58, v59\n" \
  "v_lshlrev_b32_sdwa v58, 1, v59 dst_sel:DWORD dst_unused:UNUSED_PAD src0_sel:DWORD src1_sel:WORD_0\n" \
  "v_lshlrev_b32_sdwa v59, 1, v59 dst_sel:DWORD dst_unused:UNUSED_PAD src0_sel:DWORD src1_sel:WORD_1\n" \
  "ds_read_u16_d16_hi v58, v58\n" \
  "ds_read_u16_d16_hi v59, v59\n" \
  "s_set_gpr_idx_on s61, gpr_idx(SRC2,DST)\n" \
  "v_pk_fma_f32 v[128:129], v[66:67], v[48:49], v[128:129] op_sel:[1,0,0] op_sel_hi:[1,1,1]\n" \
  "v_pk_fma_f32 v[130:131], v[66:67], v[50:51], v[130:131] op_sel:[1,0,0] op_sel_hi:[1,1,1]\n" \
  "v_pk_fma_f32 v[132:133], v[70:71], v[52:53], v[132:133] op_sel:[1,0,0] op_sel_hi:[1,1,1]\n" \
  "v_pk_fma_f32 v[134:135], v[70:71], v[54:55], v[134:135] op_sel:[1,0,0] op_sel_hi:[1,1,1]\n" \
  "s_set_gpr_idx_off\n" \
  "s_waitcnt vmcnt(6) lgkmcnt(2)\n" \
  "buffer_load_dwordx4 v[124:127], v[12:13], s[68:71], 0 idxen offen\n" \
  "ds_read_b32 v12, v14 offset:64\n" \
  "s_sub_u32 s60, s60, 1\n" \
  "s_cbranch_scc1 Lstub1_%=\n" \
  "Lback1_%=:\n" \
  "v_readlane_b32 s61, v17, s60\n" \
  "v_mfma_f32_4x4x4_16b_bf16 v[64:67], v[92:93], v[32:33], 0 cbsz:4 abid:3\n" \
  "v_mfma_f32_4x4x4_16b_bf16 v[68:71], v[92:93], v[36:37], 0 cbsz:4 abid:3\n" \
  "v_mfma_f32_4x4x4_16b_bf16 v[48:51], v[76:77], v[96:97], 0 cbsz:4 abid:8\n" \
  "v_mfma_f32_4x4x4_16b_bf16 v[52:55], v[76:77], v[98:99], 0 cbsz:4 abid:8\n" \
  "v_cvt_pk_bf16_f32 v63, v62, v63\n" \
  "v_lshlrev_b32_sdwa v62, 1, v63 dst_sel:DWORD dst_unused:UNUSED_PAD src0_sel:DWORD src1_sel:WORD_0\n" \
  "v_lshlrev_b32_sdwa v63, 1, v63 dst_sel:DWORD dst_unused:UNUSED_PAD src0_sel:DWORD src1_sel:WORD_1\n" \
  "ds_read_u16_d16_hi v62, v62\n" \
  "ds_read_u16_d16_hi v63, v63\n" \
  "v_add_u32 v15, 0x100, v15\n" \
  "s_set_gpr_idx_on s61, gpr_idx(SRC2,DST)\n" \
  "v_pk_fma_f32 v[128:129], v[56:57], v[48:49], v[128:129] op_sel:[0,0,0] op_sel_hi:[0,1,1]\n" \
  "v_pk_fma_f32 v[130:131], v[56:57], v[50:51], v[130:131] op_sel:[0,0,0] op_sel_hi:[0,1,1]\n" \
  "v_pk_fma_f32 v[132:133], v[60:61], v[52:53], v[132:133] op_sel:[0,0,0] op_sel_hi:[0,1,1]\n" \
  "v_pk_fma_f32 v[134:135], v[60:61], v[54:55], v[134:135] op_sel:[0,0,0] op_sel_hi:[0,1,1]\n" \
  "s_set_gpr_idx_off\n" \
  "s_waitcnt vmcnt(6) lgkmcnt(2)\n" \
  "buffer_load_dwordx4 v[96:99], v[12:13], s[68:71], 0 idxen offen\n" \
  "ds_read_b32 v12, v14 offset:68\n" \
  "s_lshr_b32 s61, s61, 8\n" \
  "v_mfma_f32_4x4x4_16b_bf16 v[48:51], v[76:77], v[100:101], 0 cbsz:4 abid:9\n" \
  "v_mfma_f32_4x4x4_16b_bf16 v[52:55], v[76:77], v[102:103], 0 cbsz:4 abid:9\n" \
  "v_cvt_pk_bf16_f32 v65, v64, v65\n" \
  "v_lshlrev_b32_sdwa v64, 1, v65 dst_sel:DWORD dst_unused:UNUSED_PAD src0_sel:DWORD src1_sel:WORD_0\n" \
  "v_lshlrev_b32_sdwa v65, 1, v65 dst_sel:DWORD dst_unused:UNUSED_PAD src0_sel:DWORD src1_sel:WORD_1\n" \
  "ds_read_u16_d16_hi v64, v64\n" \
  "ds_read_u16_d16_hi v65, v65\n" \
  "s_set_gpr_idx_on s61, gpr_idx(SRC2,DST)\n" \
  "v_pk_fma_f32 v[128:129], v[56:57], v[48:49], v[128:129] op_sel:[1,0,0] op_sel_hi:[1,1,1]\n" \
  "v_pk_fma_f32 v[130:131], v[56:57], v[50:51], v[130:131] op_sel:[1,0,0] op_sel_hi:[1,1,1]\n" \
  "v_pk_fma_f32 v[132:133], v[60:61], v[52:53], v[132:133] op_sel:[1,0,0] op_sel_hi:[1,1,1]\n" \
  "v_pk_fma_f32 v[134:135], v[60:61], v[54:55], v[134:135] op_sel:[1,0,0] op_sel_hi:[1,1,1]\n" \
  "s_set_gpr_idx_off\n" \
  "s_waitcnt vmcnt(6) lgkmcnt(2)\n" \
  "buffer_load_dwordx4 v[100:103], v[12:13], s[68:71], 0 idxen offen\n" \
  "ds_read_b32 v12, v14 offset:72\n" \
  "s_lshr_b32 s61, s61, 8\n" \
  "v_mfma_f32_4x4x4_16b_bf16 v[48:51], v[76:77], v[104:105], 0 cbsz:4 abid:10\n" \
  "v_mfma_f32_4x4x4_16b_bf16 v[52:55], v[76:77], v[106:107], 0 cbsz:4 abid:10\n" \
  "v_cvt_pk_bf16_f32 v69, v68, v69\n" \
  "v_lshlrev_b32_sdwa v68, 1, v69 dst_sel:DWORD dst_unused:UNUSED_PAD src0_sel:DWORD src1_sel:WORD_0\n" \
  "v_lshlrev_b32_sdwa v69, 1, v69 dst_sel:DWORD dst_unused:UNUSED_PAD src0_sel:DWORD src1_sel:WORD_1\n" \
  "ds_read_u16_d16_hi v68, v68\n" \
  "ds_read_u16_d16_hi v69, v69\n" \
  "s_set_gpr_idx_on s61, gpr_idx(SRC2,DST)\n" \
  "v_pk_fma_f32 v[128:129], v[58:59], v[48:49], v[128:129] op_sel:[0,0,0] op_sel_hi:[0,1,1]\n" \
  "v_pk_fma_f32 v[130:131], v[58:59], v[50:51], v[130:131] op_sel:[0,0,0] op_sel_hi:[0,1,1]\n" \
  "v_pk_fma_f32 v[132:133], v[62:63], v[52:53], v[132:133] op_sel:[0,0,0] op_sel_hi:[0,1,1]\n" \
  "v_pk_fma_f32 v[134:135], v[62:63], v[54:55], v[134:135] op_sel:[0,0,0] op_sel_hi:[0,1,1]\n" \
  "s_set_gpr_idx_off\n" \
  "s_waitcnt vmcnt(6) lgkmcnt(2)\n" \
  "buffer_load_dwordx4 v[104:107], v[12:13], s[68:71], 0 idxen offen\n" \
  "ds_read_b32 v12, v14 offset:76\n" \
  "ds_read_b64 v[92:93], v16\n" \
  "s_lshr_b32 s61, s61, 8\n" \
  "v_mfma_f32_4x4x4_16b_bf16 v[88:91], v[94:95], v[26:27], 0\n" \
  "v_mfma_f32_4x4x4_16b_bf16 v[48:51], v[76:77], v[108:109], 0 cbsz:4 abid:11\n" \
  "v_mfma_f32_4x4x4_16b_bf16 v[52:55], v[76:77], v[110:111], 0 cbsz:4 abid:11\n" \
  "v_cvt_pk_bf16_f32 v67, v66, v67\n" \
  "v_lshlrev_b32_sdwa v66, 1, v67 dst_sel:DWORD dst_unused:UNUSED_PAD src0_sel:DWORD src1_sel:WORD_0\n" \
  "v_lshlrev_b32_sdwa v67, 1, v67 dst_sel:DWORD dst_unused:UNUSED_PAD src0_sel:DWORD src1_sel:WORD_1\n" \
  "ds_read_u16_d16_hi v66, v66\n" \
  "ds_read_u16_d16_hi v67, v67\n" \
  "s_set_gpr_idx_on s61, gpr_idx(SRC2,DST)\n" \
  "v_pk_fma_f32 v[128:129], v[58:59], v[48:49], v[128:129] op_sel:[1,0,0] op_sel_hi:[1,1,1]\n" \
  "v_pk_fma_f32 v[130:131], v[58:59], v[50:51], v[130:131] op_sel:[1,0,0] op_sel_hi:[1,1,1]\n" \
  "v_pk_fma_f32 v[132:133], v[62:63], v[52:53], v[132:133] op_sel:[1,0,0] op_sel_hi:[1,1,1]\n" \
  "v_pk_fma_f32 v[134:135], v[62:63], v[54:55], v[134:135] op_sel:[1,0,0] op_sel_hi:[1,1,1]\n" \
  "s_set_gpr_idx_off\n" \
  "s_waitcnt vmcnt(6) lgkmcnt(2)\n" \
  "buffer_load_dwordx4 v[108:111], v[12:13], s[68:71], 0 idxen offen\n" \
  "ds_read_b32 v12, v14 offset:80\n" \
  "s_sub_u32 s60, s60, 1\n" \
  "s_cbranch_scc1 Lstub2_%=\n" \
  "Lback2_%=:\n" \
  "v_readlane_b32 s61, v17, s60\n" \
  "v_mfma_f32_4x4x4_16b_bf16 v[56:59], v[92:93], v[32:33], 0 cbsz:4\n" \
  "v_mfma_f32_4x4x4_16b_bf16 v[60:63], v[92:93], v[36:37], 0 cbsz:4\n" \
  "v_mfma_f32_4x4x4_16b_bf16 v[48:51], v[76:77], v[112:113], 0 cbsz:4 abid:12\n" \
  "v_mfma_f32_4x4x4_16b_bf16 v[52:55], v[76:77], v[114:115], 0 cbsz:4 abid:12\n" \
  "v_cvt_pk_bf16_f32 v71, v70, v71\n" \
  "v_lshlrev_b32_sdwa v70, 1, v71 dst_sel:DWORD dst_unused:UNUSED_PAD src0_sel:DWORD src1_sel:WORD_0\n" \
  "v_lshlrev_b32_sdwa v71, 1, v71 dst_sel:DWORD dst_unused:UNUSED_PAD src0_sel:DWORD src1_sel:WORD_1\n" \
  "ds_read_u16_d16_hi v70, v70\n" \
  "ds_read_u16_d16_hi v71, v71\n" \
  "v_cvt_pk_bf16_f32 v20, v84, v88\n" \
  "s_set_gpr_idx_on s61, gpr_idx(SRC2,DST)\n" \
  "v_pk_fma_f32 v[128:129], v[64:65], v[48:49], v[128:129] op_sel:[0,0,0] op_sel_hi:[0,1,1]\n" \
  "v_pk_fma_f32 v[130:131], v[64:65], v[50:51], v[130:131] op_sel:[0,0,0] op_sel_hi:[0,1,1]\n" \
  "v_pk_fma_f32 v[132:133], v[68:69], v[52:53], v[132:133] op_sel:[0,0,0] op_sel_hi:[0,1,1]\n" \
  "v_pk_fma_f32 v[134:135], v[68:69], v[54:55], v[134:135] op_sel:[0,0,0] op_sel_hi:[0,1,1]\n" \
  "s_set_gpr_idx_off\n" \
  "s_waitcnt vmcnt(6) lgkmcnt(2)\n" \
  "buffer_load_dwordx4 v[112:115], v[12:13], s[68:71], 0 idxen offen\n" \
  "ds_read_b32 v12, v14 offset:84\n" \
  "s_lshr_b32 s61, s61, 8\n" \
  "v_mfma_f32_4x4x4_16b_bf16 v[48:51], v[76:77], v[116:117], 0 cbsz:4 abid:13\n" \
  "v_mfma_f32_4x4x4_16b_bf16 v[52:55], v[76:77], v[118:119], 0 cbsz:4 abid:13\n" \
  "v_cvt_pk_bf16_f32 v57, v56, v57\n" \
  "v_lshlrev_b32_sdwa v56, 1, v57 dst_sel:DWORD dst_unused:UNUSED_PAD src0_sel:DWORD src1_sel:WORD_0\n" \
  "v_lshlrev_b32_sdwa v57, 1, v57 dst_sel:DWORD dst_unused:UNUSED_PAD src0_sel:DWORD src1_sel:WORD_1\n" \
  "ds_read_u16_d16_hi v56, v56\n" \
  "ds_read_u16_d16_hi v57, v57\n" \
  "s_set_gpr_idx_on s61, gpr_idx(SRC2,DST)\n" \
  "v_pk_fma_f32 v[128:129], v[64:65], v[48:49], v[128:129] op_sel:[1,0,0] op_sel_hi:[1,1,1]\n" \
  "v_pk_fma_f32 v[130:131], v[64:65], v[50:51], v[130:131] op_sel:[1,0,0] op_sel_hi:[1,1,1]\n" \
  "v_pk_fma_f32 v[132:133], v[68:69], v[52:53], v[132:133] op_sel:[1,0,0] op_sel_hi:[1,1,1]\n" \
  "v_pk_fma_f32 v[134:135], v[68:69], v[54:55], v[134:135] op_sel:[1,0,0] op_sel_hi:[1,1,1]\n" \
  "s_set_gpr_idx_off\n" \
  "s_waitcnt vmcnt(6) lgkmcnt(2)\n" \
  "buffer_load_dwordx4 v[116:119], v[12:13], s[68:71], 0 idxen offen\n" \
  "ds_read_b32 v12, v14 offset:88\n" \
  "s_lshr_b32 s61, s61, 8\n" \
  "v_mfma_f32_4x4x4_16b_bf16 v[48:51], v[76:77], v[120:121], 0 cbsz:4 abid:14\n" \
  "v_mfma_f32_4x4x4_16b_bf16 v[52:55], v[76:77], v[122:123], 0 cbsz:4 abid:14\n" \
  "v_cvt_pk_bf16_f32 v61, v60, v61\n" \
  "v_lshlrev_b32_sdwa v60, 1, v61 dst_sel:DWORD dst_unused:UNUSED_PAD src0_sel:DWORD src1_sel:WORD_0\n" \
  "v_lshlrev_b32_sdwa v61, 1, v61 dst_sel:DWORD dst_unused:UNUSED_PAD src0_sel:DWORD src1_sel:WORD_1\n" \
  "ds_read_u16_d16_hi v60, v60\n" \
  "ds_read_u16_d16_hi v61, v61\n" \
  "s_set_gpr_idx_on s61, gpr_idx(SRC2,DST)\n" \
  "v_pk_fma_f32 v[128:129], v[66:67], v[48:49], v[128:129] op_sel:[0,0,0] op_sel_hi:[0,1,1]\n" \
  "v_pk_fma_f32 v[130:131], v[66:67], v[50:51], v[130:131] op_sel:[0,0,0] op_sel_hi:[0,1,1]\n" \
  "v_pk_fma_f32 v[132:133], v[70:71], v[52:53], v[132:133] op_sel:[0,0,0] op_sel_hi:[0,1,1]\n" \
  "v_pk_fma_f32 v[134:135], v[70:71], v[54:55], v[134:135] op_sel:[0,0,0] op_sel_hi:[0,1,1]\n" \
  "s_set_gpr_idx_off\n" \
  "s_waitcnt vmcnt(6) lgkmcnt(2)\n" \
  "buffer_load_dwordx4 v[120:123], v[12:13], s[68:71], 0 idxen offen\n" \
  "ds_read_b32 v12, v14 offset:92\n" \
  "s_lshr_b32 s61, s61, 8\n" \
  "v_mfma_f32_4x4x4_16b_bf16 v[84:87], v[94:95], v[28:29], 0\n" \
  "v_mfma_f32_4x4x4_16b_bf16 v[88:91], v[94:95], v[30:31], 0\n" \
  "v_mfma_f32_4x4x4_16b_bf16 v[48:51], v[76:77], v[124:125], 0 cbsz:4 abid:15\n" \
  "v_mfma_f32_4x4x4_16b_bf16 v[52:55], v[76:77], v[126:127], 0 cbsz:4 abid:15\n" \
  "v_cvt_pk_bf16_f32 v59, v58, v59\n" \
  "v_lshlrev_b32_sdwa v58, 1, v59 dst_sel:DWORD dst_unused:UNUSED_PAD src0_sel:DWORD src1_sel:WORD_0\n" \
  "v_lshlrev_b32_sdwa v59, 1, v59 dst_sel:DWORD dst_unused:UNUSED_PAD src0_sel:DWORD src1_sel:WORD_1\n" \
  "ds_read_u16_d16_hi v58, v58\n" \
  "ds_read_u16_d16_hi v59, v59\n" \
  "v_cvt_pk_bf16_f32 v77, v84, v88\n" \
  "v_mov_b32 v76, v20\n" \
  "s_set_gpr_idx_on s61, gpr_idx(SRC2,DST)\n" \
  "v_pk_fma_f32 v[128:129], v[66:67], v[48:49], v[128:129] op_sel:[1,0,0] op_sel_hi:[1,1,1]\n" \
  "v_pk_fma_f32 v[130:131], v[66:67], v[50:51], v[130:131] op_sel:[1,0,0] op_sel_hi:[1,1,1]\n" \
  "v_pk_fma_f32 v[132:133], v[70:71], v[52:53], v[132:133] op_sel:[1,0,0] op_sel_hi:[1,1,1]\n" \
  "v_pk_fma_f32 v[134:135], v[70:71], v[54:55], v[134:135] op_sel:[1,0,0] op_sel_hi:[1,1,1]\n" \
  "s_set_gpr_idx_off\n" \
  "s_waitcnt vmcnt(6) lgkmcnt(2)\n" \
  "buffer_load_dwordx4 v[124:127], v[12:13], s[68:71], 0 idxen offen\n" \
  "ds_read_b32 v12, v14 offset:96\n" \
  "s_sub_u32 s60, s60, 1\n" \
  "s_cbranch_scc1 Lstub3_%=\n" \
  "Lback3_%=:\n" \
  "v_readlane_b32 s61, v17, s60\n" \
  "v_mfma_f32_4x4x4_16b_bf16 v[64:67], v[92:93], v[32:33], 0 cbsz:4 abid:1\n" \
  "v_mfma_f32_4x4x4_16b_bf16 v[68:71], v[92:93], v[36:37], 0 cbsz:4 abid:1\n" \
  "v_mfma_f32_4x4x4_16b_bf16 v[48:51], v[76:77], v[96:97], 0 cbsz:4\n" \
  "v_mfma_f32_4x4x4_16b_bf16 v[52:55], v[76:77], v[98:99], 0 cbsz:4\n" \
  "v_cvt_pk_bf16_f32 v63, v62, v63\n" \
  "v_lshlrev_b32_sdwa v62, 1, v63 dst_sel:DWORD dst_unused:UNUSED_PAD src0_sel:DWORD src1_sel:WORD_0\n" \
  "v_lshlrev_b32_sdwa v63, 1, v63 dst_sel:DWORD dst_unused:UNUSED_PAD src0_sel:DWORD src1_sel:WORD_1\n" \
  "ds_read_u16_d16_hi v62, v62\n" \
  "ds_read_u16_d16_hi v63, v63\n" \
  "v_add_u32 v14, 64, v14\n" \
  "s_branch Lbody%=\n" \
  "Lstub0_%=:\n" \
  "s_cmp_lg_u32 s62, 0\n" \
  "s_cbranch_scc1 Lend%=\n" \
  "s_mov_b32 s62, 1\n" \
  "s_mov_b32 s60, 1\n" \
  "s_mov_b64 s[68:69], %[wb]\n" \
  "s_mov_b32 s70, 32\n" \
  "v_mov_b32 v17, v18\n" \
  "s_branch Lback0_%=\n" \
  "Lstub1_%=:\n" \
  "s_cmp_lg_u32 s62, 0\n" \
  "s_cbranch_scc1 Lend%=\n" \
  "s_mov_b32 s62, 1\n" \
  "s_mov_b32 s60, 1\n" \
  "s_mov_b64 s[68:69], %[wb]\n" \
  "s_mov_b32 s70, 32\n" \
  "v_mov_b32 v17, v18\n" \
  "s_branch Lback1_%=\n" \
  "Lstub2_%=:\n" \
  "s_cmp_lg_u32 s62, 0\n" \
  "s_cbranch_scc1 Lend%=\n" \
  "s_mov_b32 s62, 1\n" \
  "s_mov_b32 s60, 1\n" \
  "s_mov_b64 s[68:69], %[wb]\n" \
  "s_mov_b32 s70, 32\n" \
  "v_mov_b32 v17, v18\n" \
  "s_branch Lback2_%=\n" \
  "Lstub3_%=:\n" \
  "s_cmp_lg_u32 s62, 0\n" \
  "s_cbranch_scc1 Lend%=\n" \
  "s_mov_b32 s62, 1\n" \
  "s_mov_b32 s60, 1\n" \
  "s_mov_b64 s[68:69], %[wb]\n" \
  "s_mov_b32 s70, 32\n" \
  "v_mov_b32 v17, v18\n" \
  "s_branch Lback3_%=\n" \
  "Lend%=:\n" \
  "s_and_b32 s64, %[nf], 0xff\n" \
  "s_bfe_u32 s63, %[nf], 0x80008\n" \
  "s_bfe_u32 s65, %[nf], 0x80010\n" \
  "s_sub_u32 s60, s63, s64\n" \
  "s_mul_i32 s64, s64, 1056\n" \
  "s_bitcmp1_b32 %[nf], 30\n" \
  "s_cbranch_scc0 Lf1_%=\n" \
  "s_set_gpr_idx_on s65, gpr_idx(SRC0)\n" \
  "v_mov_b32 v84, v128\n" \
  "v_mov_b32 v85, v129\n" \
  "v_mov_b32 v86, v130\n" \
  "v_mov_b32 v87, v131\n" \
  "v_mov_b32 v88, v132\n" \
  "v_mov_b32 v89, v133\n" \
  "v_mov_b32 v90, v134\n" \
  "v_mov_b32 v91, v135\n" \
  "s_set_gpr_idx_off\n" \
  "ds_write_b128 v21, v[84:87]\n" \
  "ds_write_b128 v21, v[88:91] offset:1024\n" \
  "s_add_u32 s65, s65, 8\n" \
  "s_cmp_eq_u32 s65, 80\n" \
  "s_cselect_b32 s65, 0, s65\n" \
  "s_add_u32 s64, s64, 1056\n" \
  "s_sub_u32 s60, s60, 1\n" \
  "s_cbranch_scc1 Lf9_%=\n" \
  "Lf1_%=:\n" \
  "s_lshr_b32 s63, %[nf], 31\n" \
  "s_sub_u32 s60, s60, s63\n" \
  "s_cbranch_scc1 Lf3_%=\n" \
  "Lf2_%=:\n" \
  "s_set_gpr_idx_on s65, gpr_idx(SRC0,SRC1)\n" \
  "v_cvt_pk_bf16_f32 v48, v128, v132\n" \
  "v_cvt_pk_bf16_f32 v49, v129, v133\n" \
  "v_cvt_pk_bf16_f32 v50, v130, v134\n" \
  "v_cvt_pk_bf16_f32 v51, v131, v135\n" \
  "s_set_gpr_idx_off\n" \
  "v_add_u32 v52, s64, v19\n" \
  "s_add_u32 s65, s65, 8\n" \
  "s_cmp_eq_u32 s65, 80\n" \
  "s_cselect_b32 s65, 0, s65\n" \
  "s_add_u32 s64, s64, 1056\n" \
  "ds_write2_b32 v52, v48, v49 offset1:16\n" \
  "ds_write2_b32 v52, v50, v51 offset0:32 offset1:48\n" \
  "s_sub_u32 s60, s60, 1\n" \
  "s_cbranch_scc0 Lf2_%=\n" \
  "Lf3_%=:\n" \
  "s_set_gpr_idx_on s65, gpr_idx(SRC0)\n" \
  "v_mov_b32 v40, v128\n" \
  "v_mov_b32 v41, v129\n" \
  "v_mov_b32 v42, v130\n" \
  "v_mov_b32 v43, v131\n" \
  "v_mov_b32 v44, v132\n" \
  "v_mov_b32 v45, v133\n" \
  "v_mov_b32 v46, v134\n" \
  "v_mov_b32 v47, v135\n" \
  "s_set_gpr_idx_off\n" \
  "Lf9_%=:\n" \
  "s_mov_b32 m0, s79\n" \
  "s_waitcnt vmcnt(0) lgkmcnt(0)\n"
#define STREAM_CLOBBERS \
  "v12", "v20", "v48", "v49", "v50", "v51", "v52", "v53", "v54", "v55", "v56", "v57", "v58", "v59", "v60", \
  "v61", "v62", "v63", "v64", "v65", "v66", "v67", "v68", "v69", "v70", "v71", "v72", "v73", "v74", "v75", \
  "v76", "v77", "v78", "v79", "v80", "v81", "v82", "v83", "v84", "v85", "v86", "v87", "v88", "v89", "v90", \
  "v91", "v92", "v93", "v94", "v95", "v128", "v129", "v130", "v131", "v132", "v133", "v134", "v135", "v136", \
  "v137", "v138", "v139", "v140", "v141", "v142", "v143", "v144", "v145", "v146", "v147", "v148", "v149", \
  "v150", "v151", "v152", "v153", "v154", "v155", "v156", "v157", "v158", "v159", "v160", "v161", "v162", \
  "v163", "v164", "v165", "v166", "v167", "v168", "v169", "v170", "v171", "v172", "v173", "v174", "v175", \
  "v176", "v177", "v178", "v179", "v180", "v181", "v182", "v183", "v184", "v185", "v186", "v187", "v188", \
  "v189", "v190", "v191", "v192", "v193", "v194", "v195", "v196", "v197", "v198", "v199", "v200", "v201", \
  "v202", "v203", "v204", "v205", "v206", "v207", "v208", "v209", "v210", "v211", "v212", "v213", "v214", \
  "v215", "s60", "s61", "s62", "s63", "s64", "s65", "s66", "s67", "s68", "s69", "s70", "s71", "s72", "s73", \
  "s74", "s75", "s76", "s77", "s78", "s79", "scc", "memory"
#define PRE_ASM \
  "ds_read_b128 v[84:87], v14\n" \
  "ds_read_b128 v[88:91], v14 offset:16\n" \
  "s_waitcnt lgkmcnt(0)\n" \
  "v_mov_b32 v12, v84\n" \
  "buffer_load_dwordx4 v[96:99], v[12:13], %[xs], 0 idxen offen\n" \
  "v_mov_b32 v12, v85\n" \
  "buffer_load_dwordx4 v[100:103], v[12:13], %[xs], 0 idxen offen\n" \
  "v_mov_b32 v12, v86\n" \
  "buffer_load_dwordx4 v[104:107], v[12:13], %[xs], 0 idxen offen\n" \
  "v_mov_b32 v12, v87\n" \
  "buffer_load_dwordx4 v[108:111], v[12:13], %[xs], 0 idxen offen\n" \
  "v_mov_b32 v12, v88\n" \
  "buffer_load_dwordx4 v[112:115], v[12:13], %[xs], 0 idxen offen\n" \
  "v_mov_b32 v12, v89\n" \
  "buffer_load_dwordx4 v[116:119], v[12:13], %[xs], 0 idxen offen\n" \
  "v_mov_b32 v12, v90\n" \
  "buffer_load_dwordx4 v[120:123], v[12:13], %[xs], 0 idxen offen\n" \
  "v_mov_b32 v12, v91\n" \
  "buffer_load_dwordx4 v[124:127], v[12:13], %[xs], 0 idxen offen\n"
#define PRE_CLOBBERS "v12", "v84", "v85", "v86", "v87", "v88", "v89", "v90", "v91", "memory"

namespace {

constexpr int C_ = 128;
constexpr int NMAX = 40;
constexpr int NBMAX = 512;
#define PTH 384
#define NHLP 40
#define SPMIN 4608
#define LUT 1
constexpr int CH = PTH * 4;
constexpr int PT = PTH;
constexpr int PW = PT / 64;
constexpr int HT = 256;
constexpr int PK = CH / PT;
constexpr int GMAX = 256;
constexpr int MT = 512;
constexpr int MW = MT / 64;
constexpr int MK = 4;
constexpr int KH = 10;
constexpr int SW = 208;
constexpr int RP = 1056;
constexpr int TN = 135 * 128;
constexpr unsigned OOB = 0x80000000u;
constexpr int NHELP = NHLP;
#define OUTAUX 16
#define KEYFIRST 1
#define PNT " nt"
#define PFN 6
constexpr unsigned WS_WFRAG = 0;
constexpr unsigned WS_CTAB = WS_WFRAG + C_ * C_ * 2;
constexpr unsigned WS_NODE0 = WS_CTAB + 8192;
constexpr unsigned WS_RECK = WS_NODE0 + NBMAX * 16;
constexpr unsigned WS_RECP = WS_RECK + GMAX * CH * 4;
constexpr unsigned WS_END = WS_RECP + GMAX * CH * 16;

__device__ __forceinline__ float bf2f(unsigned short v) { return __uint_as_float(((unsigned)v) << 16); }
__device__ __forceinline__ unsigned short f2bf(float f) {
  __hip_bfloat16 b = __float2bfloat16(f);
  return *reinterpret_cast<unsigned short*>(&b);
}
__device__ __forceinline__ unsigned pk2bf(float lo, float hi) {
  return __builtin_bit_cast(unsigned, __builtin_convertvector(f32x2{lo, hi}, bf16x2));
}
__device__ __forceinline__ unsigned row_incl_scan(unsigned x) {
  x += __builtin_amdgcn_update_dpp(0u, x, 0x111, 0xf, 0xf, false);
  x += __builtin_amdgcn_update_dpp(0u, x, 0x112, 0xf, 0xf, false);
  x += __builtin_amdgcn_update_dpp(0u, x, 0x114, 0xf, 0xf, false);
  x += __builtin_amdgcn_update_dpp(0u, x, 0x118, 0xf, 0xf, false);
  return x;
}
__device__ __forceinline__ unsigned wave_incl_scan(unsigned x) {
  x = row_incl_scan(x);
  x += __builtin_amdgcn_update_dpp(0u, x, 0x142, 0xa, 0xf, false);
  x += __builtin_amdgcn_update_dpp(0u, x, 0x143, 0xc, 0xf, false);
  return x;
}

struct MainLds {
  unsigned tp[TN / 2];
  u32x4 cell[64];
  unsigned deg[64];
  u32x4 sink[16];
  unsigned flag, pad_[3];
  u32x4 pay[MW * SW + 40];
  char gap[65536 - TN * 2 - 1024 - 256 - 256 - 16 - (MW * SW + 40) * 16];
  unsigned tn[TN / 2];
  __attribute__((aligned(16))) unsigned so[MW * SW + 16];
  unsigned char sl[MW * 256 + 16];
  union {
    unsigned ri[MW][MK * 64];
    u32x4 rows[NMAX * RP / 16];
  };
};
static_assert(sizeof(MainLds) <= 163840 && offsetof(MainLds, tn) == 65536 && SW * 16 >= 2048, "LDS layout");

__device__ __forceinline__ unsigned lds_addr(const void* p) {
  return (unsigned)(uintptr_t)(__attribute__((address_space(3))) const void*)p;
}
__device__ __forceinline__ u32x4 make_srd(const void* p, unsigned stride, unsigned num) {
  unsigned long long a = (unsigned long long)p;
  return u32x4{(unsigned)a, (unsigned)(a >> 32) | (stride << 16), num, 0x00020000u};
}

__device__ unsigned g_off[NBMAX * GMAX];

}

struct MainArgs {
  const unsigned short* x;
  char* ws;
  u32x4* out;
  int N, NBK, G, pad_;
  const unsigned short* wrad;
  const unsigned short* cg;
};
extern "C" __global__ __launch_bounds__(MT) void tfn_main_kernel(const unsigned short* x_, char* ws_, u32x4* out_, int N_,
                                                                 int NBK_, int G_, int pad_, const unsigned short* wrad_,
                                                                 const unsigned short* cg_) {
  __shared__ MainLds L;
  const int orow = blockIdx.x;
  unsigned ow;
  auto ap = (const __attribute__((address_space(4))) MainArgs*)__builtin_amdgcn_kernarg_segment_ptr();
  asm volatile("global_load_dword %0, %2, %3"
               : "=&v"(ow), "+s"(ap) : "v"(orow * (GMAX * 4) + (threadIdx.x & ~1u) * 2), "s"(g_off));
  typedef __attribute__((address_space(1))) char gchar;
  const unsigned short* __restrict__ x = (const unsigned short*)(gchar*)ap->x;
  char* ws = (char*)(gchar*)ap->ws;
  u32x4* __restrict__ out = (u32x4*)(gchar*)ap->out;
  const unsigned short* __restrict__ wrad = (const unsigned short*)(gchar*)ap->wrad;
  const unsigned short* __restrict__ cg = (const unsigned short*)(gchar*)ap->cg;
  const int N = ap->N, NBK = ap->NBK, G = ap->G;
  const int tid = threadIdx.x, lane = tid & 63;
  const int w = __builtin_amdgcn_readfirstlane(tid >> 6);
  const int nper = (unsigned)NBK / 8;
  const unsigned* reck = reinterpret_cast<const unsigned*>(ws + WS_RECK);
  const u32x4* recp = reinterpret_cast<const u32x4*>(ws + WS_RECP);
  TDECL;
  MSTAMP(0);

  if (LUT) {
#pragma unroll
    for (int it = 0; it < 4; it++) {
      const unsigned d = (it * MT + tid) * 4;
      u32x4 t;
#pragma unroll
      for (int j = 0; j < 4; j++) {
        t[j] = (2 * (d + j) - 128) * 0x10001u + 0x10000u;
        if (it == 0) t[j] = tid < 32 ? (d + j) * 0x10001u + ((j & 1) << 16) : t[j];
      }
      *reinterpret_cast<u32x4*>(&L.tp[d]) = t;
      *reinterpret_cast<u32x4*>(&L.tn[d]) = t | 0x80008000u;
    }
  }
  if (w == MW - 1) L.cell[lane] = u32x4{0, 0, 0, 0};
  if (w == MW - 2) L.deg[lane] = 0;
  if (w == MW - 3) L.flag = 0;
  const int sgi = (lane >> 1) * MW + w;
  const u32x4 wsd = make_srd(ws, 0, WS_END);
  u32x4 nv;
  asm volatile("buffer_load_dwordx4 %0, off, %1, %2"
               : "=&v"(nv) : "s"(wsd), "s"(WS_NODE0 + orow * 16), "s"(out), "s"(wrad), "s"(cg), "s"(x));
  asm volatile("s_waitcnt vmcnt(1)" : "+v"(ow));
  ow = sgi < G ? ow : 0u;
  const unsigned cnt = ow >> 16;
  const unsigned inc = wave_incl_scan((lane & 1) == 0 ? cnt : 0u);
  const unsigned Tw0 = __builtin_amdgcn_readlane(inc, 63);
  const bool wok = Tw0 < (unsigned)(MK * 64);
  const int Tw = wok ? Tw0 : 0;
  unsigned key[MK], rk[MK], idx[MK], ci[MK];
  u32x4 pay_[MK];
  {
    unsigned r0 = (sgi * CH + (ow & 0xffff) + (lane & 1)) * 4;
    unsigned* lp = &L.ri[w][inc - cnt + (lane & 1)];
    if (wok) {
#pragma clang loop unroll(disable) vectorize(disable)
      for (int i = (int)cnt - (lane & 1); i > 0; i -= 2, lp += 2, r0 += 8) *lp = r0;
    }
#pragma unroll
    for (int k = 0; k < MK; k++) {
      idx[k] = L.ri[w][k * 64 + lane];
      idx[k] = lane < Tw - k * 64 ? idx[k] : 0x20000000u;
    }
  }
  MSTAMP(1);
  asm volatile(
      "s_cmp_le_i32 %13, 192\n s_cbranch_scc1 1f\n buffer_load_dword %3, %8, %10, %11 offen\n"
      "buffer_load_dwordx4 %4, %9, %10, %12 offen\n 1:\n"
      "buffer_load_dword %0, %5, %10, %11 offen\n buffer_load_dword %1, %6, %10, %11 offen\n"
      "buffer_load_dword %2, %7, %10, %11 offen\n"
      : "=&v"(key[0]), "=&v"(key[1]), "=&v"(key[2]), "=&v"(key[3]), "=&v"(pay_[3])
      : "v"(idx[0]), "v"(idx[1]), "v"(idx[2]), "v"(idx[3]), "v"(idx[3] * 4), "s"(wsd), "s"(WS_RECK), "s"(WS_RECP), "s"(Tw) : "scc");
#pragma unroll
  for (int k = 0; k < MK - 1; k++) idx[k] *= 4;
  unsigned loff = lane * 16;
  unsigned cgo = (lane & 3) * 32, wqo = lane * 32;
  unsigned pfo = (((MW - 1 - w) * nper + (blockIdx.x >> 3)) * 64 + lane) * 128;
  unsigned pf2 = w >= 2 * MW - PFN ? pfo + MW * nper * 8192 : OOB;
  pfo = w >= MW - PFN ? pfo : OOB;
  asm volatile("" : "+v"(idx[0]), "+v"(idx[1]), "+v"(idx[2]), "+v"(cgo), "+v"(wqo), "+v"(pfo), "+v"(pf2));
  __syncthreads();
  u32x4 cg0, cg1, wq0, wq1;
  asm volatile(
      "buffer_load_dwordx4 %0, %7, %12, %13 offen\n buffer_load_dwordx4 %1, %8, %12, %13 offen\n"
      "buffer_load_dwordx4 %2, %9, %12, %13 offen\n"
      "buffer_load_dwordx4 %3, %10, %12, %14 offen\n buffer_load_dwordx4 %4, %10, %12, %14 offen offset:16\n"
      "buffer_load_dwordx4 %5, %11, %12, %14 offen offset:2048\n buffer_load_dwordx4 %6, %11, %12, %14 offen offset:2064\n"
      : "=&v"(pay_[0]), "=&v"(pay_[1]), "=&v"(pay_[2]), "=&v"(cg0), "=&v"(cg1), "=&v"(wq0), "=&v"(wq1)
      : "v"(idx[0]), "v"(idx[1]), "v"(idx[2]), "v"(cgo), "v"(wqo), "s"(wsd), "s"(WS_RECP), "s"(WS_CTAB));
  if (LUT) {
#pragma unroll
    for (int it = 0; it < 2; it++) {
      const unsigned d = 119 * 64 + it * MT + tid;
      float a[2], p[2], n[2];
#pragma unroll
      for (int j = 0; j < 2; j++) {
        a[j] = __uint_as_float((2 * d + j) << 16);
        const float e = __builtin_amdgcn_exp2f(a[j] * -1.4426950408889634f);
        p[j] = a[j] * __builtin_amdgcn_rcpf(1.f + e);
        n[j] = -p[j] * e;
      }
      L.tp[d] = pk2bf(p[0], p[1]);
      L.tn[d] = pk2bf(n[0], n[1]);
    }
  }
  const int start = w * SW;
  unsigned sop = lds_addr(&L.so[start]);
  unsigned yp = lds_addr(&L.pay[start + (lane >> 2)]);
  unsigned ep = lds_addr(&L.pay[start + (lane & 15)]) + 8;
  unsigned fb = lds_addr(L.rows) + (lane >> 4) * 256 + (lane & 15) * 4;
  unsigned cbp = lds_addr(&L.pay[start + lane]);
  unsigned u6 = (lane * 37 + w * 11) & 63;
  asm volatile("" : "+v"(sop), "+v"(yp), "+v"(ep), "+v"(fb), "+v"(cbp), "+v"(loff), "+v"(u6));
  if (!wok && lane == 0) L.flag = 1;
  asm volatile("s_waitcnt vmcnt(7)" : "+v"(key[0]), "+v"(key[1]), "+v"(key[2]), "+v"(key[3]), "+v"(pay_[3]), "+v"(nv));
  const int node0 = __builtin_amdgcn_readfirstlane(nv[0]);
  const int nn = __builtin_amdgcn_readfirstlane(nv[1]) - node0;
  const int thr = __builtin_amdgcn_readfirstlane(nv[2]);
  const unsigned cb = __builtin_amdgcn_readfirstlane(nv[3]);
  auto count = [&](int k) {
    const bool ok = lane < Tw - k * 64;
    key[k] = ok ? key[k] - (node0 << 24) : lane << 24;
    atomicAdd(&L.deg[(key[k] >> 24) & 63], ok ? 1u : 0u);
  };
#pragma unroll
  for (int k = 0; k < MK - 1; k++) count(k);
  if (Tw > (MK - 1) * 64) count(MK - 1);
  MSTAMP(2);
  __syncthreads();
  {
    unsigned keep;
    asm volatile("s_mov_b32 %0, m0\n s_mov_b32 m0, %4\n s_nop 0\n buffer_load_dword %1, %3, 0 offen lds\n"
                 "s_mov_b32 m0, %0\n"
                 : "=&s"(keep) : "v"(pfo), "v"(pf2), "s"(make_srd(x, 0, N * 1024)), "s"(lds_addr(L.sink)) : "memory");
  }
  unsigned nf;
  int hiw;
  {
    const unsigned dg = L.deg[lane];
    const unsigned in = wave_incl_scan(dg);
    const float c = 65535.f * __builtin_amdgcn_rcpf((float)__builtin_amdgcn_readlane(in, 63));
    const unsigned a16 = (unsigned)((float)(in - dg) * c), b16 = (unsigned)((float)in * c) - a16;
    const unsigned ab = a16 | (b16 << 16);
    const unsigned sh = (w & 3) * 8;
    auto deal = [&](int k0, int k1) {
#pragma unroll
      for (int k = k0; k < k1; k++) rk[k] = __builtin_amdgcn_ds_bpermute((key[k] >> 24) * 4, ab);
#pragma unroll
      for (int k = k0; k < k1; k++) {
        const unsigned wv = ((rk[k] & 0xffff) + (((rk[k] >> 16) * ((u6 + k * 21) & 63)) >> 6)) >> 13;
        ci[k] = wv * 16 + (unsigned)(((unsigned long long)(key[k] & 0xffffff) * (cb & 0xffffff)) >> 32);
        rk[k] = atomicAdd(&reinterpret_cast<unsigned*>(L.cell)[(ci[k] & 63) * 4 + (ci[k] >> 6) * 2 + (w >> 2)],
                          lane < Tw - k * 64 ? 1u << sh : 0u);
      }
    };
    deal(0, MK - 1);
    if (Tw > (MK - 1) * 64) deal(MK - 1, MK);
    const int lo = a16 >> 13, hi = (a16 + ((b16 * 63) >> 6)) >> 13;
    const unsigned long long mine = __ballot(lo <= w) & __ballot(hi >= w) & __ballot(lane < nn);
    const int first = __builtin_ctzll(mine), last = 63 - __builtin_clzll(mine | 1);
    hiw = __builtin_amdgcn_readlane(hi, last);
    const bool cont = __builtin_amdgcn_readlane(lo, first & 63) < w, head = hiw > w && __builtin_amdgcn_readlane(lo, last) == w;
    hiw = head ? hiw : w;
    nf = first | (last << 8) | ((first % KH * 8) << 16) | (cont ? 1u << 30 : 0u) | (head ? 1u << 31 : 0u);
    if ((mine == 0) | (last - first >= KH) | (nn > NMAX)) {
      if (lane == 0) L.flag = 1;
    }
#pragma unroll
    for (int k = 0; k < MK; k++) rk[k] = (rk[k] >> sh) & 0xff;
  }
  __syncthreads();
  MSTAMP(12);
  bool slow;
  int ng;
  {
    const u32x4 c4 = L.cell[lane];
    const unsigned fl = L.flag;
    const unsigned long long pm = 0x0101010101010101ull & ((1ull << (w * 8)) - 1);
    const unsigned m0 = (unsigned)pm, m1 = (unsigned)(pm >> 32);
    const unsigned tot = __builtin_amdgcn_udot4(c4[0], 0x01010101u, __builtin_amdgcn_udot4(c4[1], 0x01010101u, 0, false), false) |
                         (__builtin_amdgcn_udot4(c4[2], 0x01010101u, __builtin_amdgcn_udot4(c4[3], 0x01010101u, 0, false), false) << 16);
    const unsigned par = __builtin_amdgcn_udot4(c4[0], m0, __builtin_amdgcn_udot4(c4[1], m1, 0, false), false) |
                         (__builtin_amdgcn_udot4(c4[2], m0, __builtin_amdgcn_udot4(c4[3], m1, 0, false), false) << 16);
    const unsigned in = row_incl_scan(tot), base = in - tot + par;
    const int len = (__builtin_amdgcn_readlane(in, (w & 3) * 16 + 15) >> ((w >> 2) * 16)) & 0xffff;
    ng = max((len + 3) >> 2, 3);
    slow = (__builtin_amdgcn_readfirstlane(fl) != 0) | ((__ballot((in & 0xffff) > SW - 8) | __ballot(in >= ((SW - 7) << 16))) != 0);
    if (!slow) {
      auto place = [&](int k0, int k1) {
        unsigned b[MK];
#pragma unroll
        for (int k = k0; k < k1; k++) b[k] = __builtin_amdgcn_ds_bpermute(ci[k] * 4, base);
#pragma unroll
        for (int k = k0; k < k1; k++) {
          const bool ok = lane < Tw - k * 64;
          const unsigned wv = ok ? ci[k] >> 4 : MW, n = key[k] >> 24;
          const unsigned p = ok ? ((b[k] >> ((ci[k] >> 6) * 16)) & 0xffff) + rk[k] : 8;
          rk[k] = wv * SW + p;
          L.so[rk[k]] = key[k] & 0xffffff;
          L.sl[wv * 256 + p] = n * 8 - ((n * 26) >> 8) * (KH * 8);
        }
      };
      place(0, MK - 1);
      if (Tw > (MK - 1) * 64) place(MK - 1, MK);
      const int e = len + lane, j = e & 7;
      if (e < ng * 4 + 8) L.so[start + e] = e < ng * 4 ? 0x7fffffffu : (j >> 1) * 8 + (w & 3) * 2 + (j & 1);
      if (e < ng * 4) L.sl[w * 256 + e] = KH * 8;
    }
    asm volatile("s_waitcnt vmcnt(%7)" : "+v"(pay_[0]), "+v"(pay_[1]), "+v"(pay_[2]), "+v"(cg0), "+v"(cg1), "+v"(wq0), "+v"(wq1) : "n"(PFN > 8 ? 2 : 1));
    if (!slow) {
      typedef __attribute__((ext_vector_type(2))) unsigned short u16x2;
      u16x2 mx = {0, 0};
      auto put = [&](int k) {
        L.pay[rk[k]] = pay_[k];
        mx = __builtin_elementwise_max(mx, __builtin_elementwise_max(__builtin_bit_cast(u16x2, pay_[k][2] & 0x7fff7fffu),
                                                                     __builtin_bit_cast(u16x2, pay_[k][3] & 0x7fff7fffu)));
      };
#pragma unroll
      for (int k = 0; k < MK - 1; k++) put(k);
      if (Tw > (MK - 1) * 64) put(MK - 1);
      if (LUT && (int)max(mx[0], mx[1]) > thr) L.flag = 1;
    }
  }
  __syncthreads();
  slow |= __builtin_amdgcn_readfirstlane(L.flag) != 0;
  f32x4 acc0, acc1;
  u32x16 ra, rb;
  if (!slow) {
    MSTAMP(3);
    const u32x4 xs = make_srd(x, 1024, N);
    asm volatile(PRE_ASM : "={v[96:111]}"(ra), "={v[112:127]}"(rb) : "{v13}"(loff), "{v14}"(sop), [xs] "s"(xs) : PRE_CLOBBERS);
    const unsigned* slw = reinterpret_cast<const unsigned*>(&L.sl[w * 256]) + ng - 3 - lane;
    unsigned slv = slw[0], slv2 = slw[2];
    asm volatile(STREAM_ASM
                 : "={v[40:43]}"(acc0), "={v[44:47]}"(acc1), "+{v14}"(sop), "+{v15}"(yp), "+{v16}"(ep), "+{v17}"(slv),
                   "+{v[96:111]}"(ra), "+{v[112:127]}"(rb)
                 : "{v13}"(loff), "{v18}"(slv2), "{v19}"(fb), "{v21}"(cbp),
                   "{v[24:27]}"(cg0), "{v[28:31]}"(cg1), "{v[32:35]}"(wq0), "{v[36:39]}"(wq1),
                   [xb] "s"((unsigned long long)x | (1024ull << 48)), [xn] "s"(N),
                   [wb] "s"((unsigned long long)(ws + WS_WFRAG) | (1024ull << 48)), [rem] "s"(ng - 3), [nf] "s"(nf)
                 : STREAM_CLOBBERS);
  } else {
    hiw = w;
    acc0 = acc1 = f32x4{0.f, 0.f, 0.f, 0.f};
#pragma unroll 1
    for (int it = w; it < 2 * nn; it += MW) {
      const int dl = it >> 1, slice = it & 1, ch = slice * 64 + lane;
      float acc[4] = {0.f, 0.f, 0.f, 0.f};
#pragma unroll 1
      for (int g = 0; g < G; g++) {
        unsigned o2 = g_off[orow * GMAX + (g & 7) * 32 + (g >> 3)], a = o2 & 0xffff, b = a + (o2 >> 16);
#pragma unroll 1
        for (unsigned t = a; t < b; t++) {
          unsigned ky = reck[g * CH + t];
          if ((int)((ky - (node0 << 24)) >> 24) != dl) continue;
          u32x4 r = recp[g * CH + t];
          float yy[4] = {bf2f(r[0] & 0xffff), bf2f(r[0] >> 16), bf2f(r[1] & 0xffff), bf2f(r[1] >> 16)};
          float ee[4] = {bf2f(r[2] & 0xffff), bf2f(r[2] >> 16), bf2f(r[3] & 0xffff), bf2f(r[3] >> 16)};
          float z = 0.f;
#pragma unroll
          for (int k = 0; k < 4; k++) z += ee[k] * bf2f(wrad[k * C_ + ch]);
          float R = z / (1.f + __expf(-z));
          const unsigned short* xr = x + (size_t)(ky & 0xffffff) * 512 + ch * 4;
#pragma unroll 1
          for (int o = 0; o < 4; o++) {
            float tp = 0.f;
#pragma unroll 1
            for (int i = 0; i < 4; i++) {
              float A = 0.f;
#pragma unroll
              for (int f = 0; f < 4; f++) A += yy[f] * bf2f(cg[i * 16 + f * 4 + o]);
              tp += bf2f(xr[i]) * bf2f(f2bf(A));
            }
            float v = R * tp;
            acc[0] += o == 0 ? v : 0.f;
            acc[1] += o == 1 ? v : 0.f;
            acc[2] += o == 2 ? v : 0.f;
            acc[3] += o == 3 ? v : 0.f;
          }
        }
      }
      unsigned short* ar = reinterpret_cast<unsigned short*>(L.rows) + dl * (RP / 2) + (ch >> 5) * 128 + (ch & 31);
#pragma unroll
      for (int o = 0; o < 4; o++) ar[o * 32] = f2bf(acc[o]);
    }
    const u32x4* wf = reinterpret_cast<const u32x4*>(ws + WS_WFRAG) + (w & 3) * 128 + lane;
#pragma unroll
    for (int j = 0; j < 4; j++) {
      const u32x4 wa = wf[(j >> 1) * 512 + (j & 1) * 64], wb = wf[(j >> 1) * 512 + (j & 1) * 64 + 1024];
#pragma unroll
      for (int i = 0; i < 4; i++) {
        ra[j * 4 + i] = wa[i];
        rb[j * 4 + i] = wb[i];
      }
    }
  }
  MSTAMP(4);
  __syncthreads();
  if (hiw > w) {
#pragma unroll 1
    for (int ww = w + 1; ww <= hiw; ww++) {
      acc0 += __builtin_bit_cast(f32x4, L.pay[ww * SW + lane]);
      acc1 += __builtin_bit_cast(f32x4, L.pay[ww * SW + 64 + lane]);
    }
    auto rp = (__attribute__((address_space(3))) unsigned*)(uintptr_t)(fb + ((nf >> 8) & 0xff) * RP);
#pragma unroll
    for (int o = 0; o < 4; o++) rp[o * 16] = pk2bf(acc0[o], acc1[o]);
  }
  __syncthreads();
  MSTAMP(5);
  {
    const int row = lane & 15, kq = lane >> 4;
    const auto ors = __builtin_amdgcn_make_buffer_rsrc((void*)(out + (size_t)node0 * 64), 0, nn * 1024, 0x00020000);
    const u32x4* op = &L.rows[((w >> 2) * 4 + (row >> 2)) * (RP / 16) + (row & 3) * 4 + kq];
    s16x8 f[NMAX / 8][4];
#pragma unroll
    for (int t = 0; t < NMAX / 8; t++)
#pragma unroll
      for (int k = 0; k < 4; k++) f[t][k] = __builtin_bit_cast(s16x8, op[t * 8 * (RP / 16) + k * 16]);
    auto wv = [&](int j) {
      const u32x16& r = j < 4 ? ra : rb;
      return __builtin_bit_cast(s16x8, u32x4{r[(j & 3) * 4], r[(j & 3) * 4 + 1], r[(j & 3) * 4 + 2], r[(j & 3) * 4 + 3]});
    };
    const unsigned oo = ((w >> 2) * 4 + kq) * 1024 + (w & 3) * 256 + row * 16;
#pragma unroll
    for (int t = 0; t < NMAX / 8; t++) {
      f32x4 a = {0.f, 0.f, 0.f, 0.f}, b = {0.f, 0.f, 0.f, 0.f};
#pragma unroll
      for (int k = 0; k < 4; k++) {
        a = __builtin_amdgcn_mfma_f32_16x16x32_bf16(f[t][k], wv(2 * k), a, 0, 0, 0);
        b = __builtin_amdgcn_mfma_f32_16x16x32_bf16(f[t][k], wv(2 * k + 1), b, 0, 0, 0);
      }
      __builtin_amdgcn_raw_buffer_store_b128(u32x4{pk2bf(a[0], a[1]), pk2bf(a[2], a[3]), pk2bf(b[0], b[1]), pk2bf(b[2], b[3])},
                                             ors, oo, t * 8192, OUTAUX);
    }
  }
  MSTAMP(6);
  MSTAMP(7);
  MFLUSH;
}

extern "C" __global__ __launch_bounds__(PT) void tfn_prep_kernel(const int* __restrict__ dst, const int* __restrict__ src,
                                                  const u32x2* __restrict__ Y, const u32x2* __restrict__ ef,
                                                  char* __restrict__ ws, int E, int N, int NBK, unsigned M2,
                                                  const unsigned short* __restrict__ wrad,
                                                  const unsigned short* __restrict__ cg,
                                                  const unsigned short* __restrict__ wmix) {
  __shared__ u32x4 sp[CH < SPMIN ? SPMIN : CH];
  __shared__ __attribute__((aligned(16))) unsigned sk[CH];
  __shared__ __attribute__((aligned(16))) unsigned hist[PW][NBMAX];
  __shared__ __attribute__((aligned(16))) unsigned hbase[PW][NBMAX];
  const int tid = threadIdx.x, lane = tid & 63, w = __builtin_amdgcn_readfirstlane(tid >> 6);
  const int g = (int)blockIdx.x >= NHELP ? blockIdx.x - NHELP : GMAX;
  unsigned* reck = reinterpret_cast<unsigned*>(ws + WS_RECK);
  u32x4* recp = reinterpret_cast<u32x4*>(ws + WS_RECP);
  TDECL;
  PSTAMP(0);
  const int base = g * CH;
  const int NKB = NBK;
  const int e0 = base + tid * 2;
  {
    unsigned cv;
    asm volatile("s_getpc_b64 s[40:41]\n .Ltfn_p0:\n"
                 "s_sub_u32 s40, s40, .Ltfn_p0-tfn_main_kernel\n s_subb_u32 s41, s41, 0\n s_max_i32 s42, 0, .Ltfn_p1-tfn_main_kernel\n"
                 "s_mov_b32 s43, 0x20000\n"
                 "s_cmp_ge_u32 %2, 8\n s_cbranch_scc1 1f\n buffer_load_dword %0, %1, s[40:43], 0 offen\n s_waitcnt vmcnt(0)\n 1:"
                 : "=&v"(cv) : "v"(tid * 128), "s"(blockIdx.x - (NHELP - 8)) : "s40", "s41", "s42", "s43", "scc");
  }
  u32x2 d0, d1, s0, s1;
  u32x4 y0, y1, f0, f1;
  asm volatile(
      "buffer_load_dwordx2 %0, %4, %5, 0 offen" PNT "\n buffer_load_dwordx2 %1, %4, %5, %7 offen" PNT "\n"
      "buffer_load_dwordx2 %2, %4, %6, 0 offen" PNT "\n buffer_load_dwordx2 %3, %4, %6, %7 offen" PNT "\n"
      "s_barrier\n"
      : "=&v"(d0), "=&v"(d1), "=&v"(s0), "=&v"(s1)
      : "v"(e0 * 4), "s"(make_srd(dst, 0, E * 4)), "s"(make_srd(src, 0, E * 4)), "s"(CH * 2));
  asm volatile(
      "buffer_load_dwordx4 %0, %4, %5, 0 offen" PNT "\n buffer_load_dwordx4 %1, %4, %5, %7 offen" PNT "\n"
      "buffer_load_dwordx4 %2, %4, %6, 0 offen" PNT "\n buffer_load_dwordx4 %3, %4, %6, %7 offen" PNT "\n"
      : "=&v"(y0), "=&v"(y1), "=&v"(f0), "=&v"(f1)
      : "v"(e0 * 8), "s"(make_srd(Y, 0, E * 8)), "s"(make_srd(ef, 0, E * 8)), "s"(CH * 4));
  if (g < GMAX) {
  auto valid = [&](int k) { return e0 + (k >> 1) * (CH / 2) + (k & 1) < E; };
#pragma unroll
  for (int i = 0; i < NBMAX / 256; i++) reinterpret_cast<u32x4*>(hist[w])[i * 64 + lane] = u32x4{0, 0, 0, 0};
  asm volatile("s_waitcnt vmcnt(4)" : "+v"(d0), "+v"(d1), "+v"(s0), "+v"(s1));
  const u32x4 dv = {d0[0], d0[1], d1[0], d1[1]}, sv = {s0[0], s0[1], s1[0], s1[1]};
  unsigned bk[PK], rk[PK];
#pragma unroll
  for (int k = 0; k < PK; k++) {
    bk[k] = __umulhi(dv[k], M2);
    rk[k] = atomicAdd(&hist[w][bk[k]], valid(k) ? 1u : 0u);
  }
  PSTAMP(1);
  __syncthreads();
  unsigned carry = 0, t0 = 0, t1 = 0;
#pragma unroll 1
  for (int b = 2 * lane; b - 2 * lane < NKB; b += 128) {
    unsigned c0 = 0, c1 = 0, p0 = 0, p1 = 0;
#pragma unroll
    for (int ww = 0; ww < PW; ww++) {
      const u32x2 h = *reinterpret_cast<const u32x2*>(&hist[ww][b]);
      c0 += h.x;
      c1 += h.y;
      p0 += ww < w ? h.x : 0u;
      p1 += ww < w ? h.y : 0u;
    }
    const unsigned inc = wave_incl_scan(c0 + c1) + carry, b0 = inc - c0 - c1;
    carry = __builtin_amdgcn_readlane(inc, 63);
    *reinterpret_cast<u32x2*>(&hbase[w][b]) = u32x2{b0 + p0, b0 + c0 + p1};
    if ((b >> 7) == w) {
      t0 = b0 | (c0 << 16);
      t1 = (b0 + c0) | (c1 << 16);
    }
  }
  PSTAMP(2);
#pragma unroll
  for (int k = 0; k < PK; k++) {
    rk[k] += hbase[w][bk[k]];
    rk[k] = valid(k) ? rk[k] : CH - 1;
    sk[rk[k]] = (dv[k] << 24) | sv[k];
  }
  asm volatile("s_waitcnt vmcnt(0)" : "+v"(y0), "+v"(y1), "+v"(f0), "+v"(f1));
  if (2 * tid < NKB) {
    const unsigned nper = (unsigned)NBK / 8, grp = (2 * tid) / nper, j = 2 * tid - grp * nper;
    unsigned* op = &g_off[(j * 8 + grp) * GMAX + (g & 7) * 32 + (g >> 3)];
    op[0] = t0;
    op[8 * GMAX] = t1;
  }
#pragma unroll
  for (int k = 0; k < PK; k++) {
    const u32x4 &yy = k < 2 ? y0 : y1, &ff = k < 2 ? f0 : f1;
    sp[rk[k]] = u32x4{yy[(k & 1) * 2], yy[(k & 1) * 2 + 1], ff[(k & 1) * 2], ff[(k & 1) * 2 + 1]};
  }
  __syncthreads();
  PSTAMP(3);
  __builtin_amdgcn_raw_buffer_store_b128(reinterpret_cast<const u32x4*>(sk)[tid],
                                         __builtin_amdgcn_make_buffer_rsrc((void*)(reck + base), 0, CH * 4, 0x00020000),
                                         tid * 16, 0, 16);
#pragma unroll
  for (int k = 0; k < PK; k++)
    __builtin_amdgcn_raw_buffer_store_b128(sp[k * PT + tid],
                                           __builtin_amdgcn_make_buffer_rsrc((void*)(recp + base), 0, CH * 16, 0x00020000),
                                           (k * PT + tid) * 16, 0, 16);
  asm volatile(".Ltfn_p1:");
  PSTAMP(4);
  PFLUSH;
  return;
  }
  {
    unsigned short* wfrag = reinterpret_cast<unsigned short*>(ws + WS_WFRAG);
    unsigned short* ctab = reinterpret_cast<unsigned short*>(ws + WS_CTAB);
    u32x4* node0t = reinterpret_cast<u32x4*>(ws + WS_NODE0);
    asm volatile("s_waitcnt  vmcnt(0)" ::"v"(d0), "v"(d1), "v"(s0), "v"(s1), "v"(y0), "v"(y1), "v"(f0), "v"(f1) : "memory");
    const int pb = tid < HT ? blockIdx.x : NHELP;
    if (pb < 8) {
      int i = pb * HT + tid;
      int l = i & 63, t = (i >> 6) & 7, kk = i >> 9;
      const unsigned short* wp = wmix + (32 * kk + 8 * (l >> 4)) * C_ + 32 * (t >> 1) + 2 * (l & 15) + (t & 1);
      u32x4 v;
#pragma unroll
      for (int j = 0; j < 4; j++) v[j] = wp[2 * j * C_] | ((unsigned)wp[(2 * j + 1) * C_] << 16);
      reinterpret_cast<u32x4*>(wfrag)[i] = v;
    } else if (pb < 12) {
      int i = (pb - 8) * HT + tid;
      ctab[i] = cg[(i & 15) * 4 + ((i >> 4) & 3)];
    } else if (pb < 16) {
      int i = (pb - 12) * HT + tid;
      int k = i & 3, part = (i >> 2) & 1;
      float v = (LUT ? 1.f : -1.4426950408889634f) * bf2f(wrad[k * C_ + (i >> 3)]);
      unsigned short hi = f2bf(v);
      unsigned short mid = f2bf(v - bf2f(hi));
      ctab[1024 + i] = part == 0 ? hi : mid;
    } else if (pb == 16) {
      float sm = 0.f;
#pragma unroll
      for (int h = 0; h < 2; h++) {
        float a = 0.f;
#pragma unroll
        for (int k = 0; k < 4; k++) a += fabsf(bf2f(wrad[k * C_ + lane * 2 + h]));
        sm = a <= sm ? sm : a < 1e30f ? a : 1e30f;
      }
#pragma unroll
      for (int o = 32; o > 0; o >>= 1) sm = fmaxf(sm, __shfl_xor(sm, o));
      const int thr = sm >= 1e30f ? -1 : sm < 1e-30f ? 0x6000 : (int)(__float_as_uint(250.f / sm) >> 16);
      const unsigned long long cb = (16ull << 32) / (unsigned)N;
      for (int b = tid; b < NBK; b += HT) {
        unsigned n01[2];
#pragma unroll
        for (int h = 0; h < 2; h++) {
          unsigned long long q = (((unsigned long long)(b + h) << 32) + M2 - 1) / M2;
          n01[h] = q > (unsigned long long)N ? (unsigned)N : (unsigned)q;
        }
        const unsigned nper = (unsigned)NBK / 8, grp = b / nper;
        node0t[(b - grp * nper) * 8 + grp] = u32x4{n01[0], n01[1], (unsigned)thr, cb > 0xffffffull ? 0xffffffu : (unsigned)cb};
      }
    }
  }
}

extern "C" void kernel_launch(void* const* d_in, const int* in_sizes, int n_in, void* d_out, int out_size,
                              void* d_ws, size_t ws_size, hipStream_t stream) {
  const unsigned short* x = (const unsigned short*)d_in[0];
  const int* src = (const int*)d_in[1];
  const int* dst = (const int*)d_in[2];
  const u32x2* Y = (const u32x2*)d_in[3];
  const u32x2* ef = (const u32x2*)d_in[4];
  const unsigned short* wrad = (const unsigned short*)d_in[5];
  const unsigned short* cg = (const unsigned short*)d_in[6];
  const unsigned short* wmix = (const unsigned short*)d_in[7];
  const int N = in_sizes[0] / 512;
  const int E = in_sizes[1];
  int NBK = 256;
  while ((N + NBK - 1) / NBK > NMAX || (N % NBK == 0 && N / NBK >= NMAX)) NBK += 256;
  const unsigned long long M2l = (((unsigned long long)NBK) << 32) / (unsigned long long)N;
  const unsigned M2 = M2l > 0xffffffffull ? 0xffffffffu : (unsigned)M2l;
  const int G = (E + CH - 1) / CH;

  char* ws = (char*)d_ws;
  tfn_prep_kernel<<<G + NHELP, PT, 0, stream>>>(dst, src, Y, ef, ws, E, N, NBK, M2, wrad, cg, wmix);
  tfn_main_kernel<<<NBK, MT, 0, stream>>>(x, ws, (u32x4*)d_out, N, NBK, G, 0, wrad, cg);
}
```

```cpp
#include <hip/hip_runtime.h>
#include <hip/hip_bf16.h>
#include <stdint.h>
#include <stddef.h>

typedef __attribute__((ext_vector_type(2))) float f32x2;
typedef __attribute__((ext_vector_type(4))) float f32x4;
typedef __attribute__((ext_vector_type(8))) short s16x8;
typedef __attribute__((ext_vector_type(2))) unsigned int u32x2;
typedef __attribute__((ext_vector_type(4))) unsigned int u32x4;
typedef __attribute__((ext_vector_type(16))) unsigned int u32x16;
typedef __attribute__((ext_vector_type(2))) __bf16 bf16x2;

#define TDECL
#define TSTAMP(i)
#define TFLUSH
#define MSTAMP(i) TSTAMP(i)
#define MFLUSH do { asm volatile("s_waitcnt vmcnt(0)" ::: "memory"); TSTAMP(15); TFLUSH; } while (0)
#undef MFLUSH
#define MFLUSH

#define STREAM_ASM \
  "v_mbcnt_lo_u32_b32 v86, -1, 0\n" \
  "v_mbcnt_hi_u32_b32 v86, -1, v86\n" \
  "s_and_b32 s71, %[blk], 7\n" \
  "s_lshr_b32 s66, %[blk], 3\n" \
  "s_lshl_b32 s68, s71, 5\n" \
  "s_add_u32 s68, s68, s66\n" \
  "v_lshlrev_b32 v54, 2, v86\n" \
  "s_and_b32 s61, s68, 2\n" \
  "s_lshl_b32 s61, s61, 9\n" \
  "v_and_b32 v55, 0xf8, v54\n" \
  "s_and_b32 s63, s68, 0xfc\n" \
  "s_lshl2_add_u32 s61, s63, s61\n" \
  "v_add_u32 v55, %[w], v55\n" \
  "s_and_b32 s63, s68, 1\n" \
  "s_lshl3_add_u32 s61, s63, s61\n" \
  "v_mul_u32_u24 v17, 2176, v55\n" \
  "s_add_u32 s61, s61, 98560\n" \
  "s_lshl_b32 s63, %[w], 10\n" \
  "s_mul_i32 s72, %[blk], 20480\n" \
  "v_lshl_add_u32 v50, v86, 4, s63\n" \
  "v_add_u32 v51, 110912, v50\n" \
  "s_waitcnt lgkmcnt(0)\n" \
  "s_barrier\n" \
  "ds_read_b128 v[216:219], v51\n" \
  "ds_read_b128 v[220:223], v51 offset:8192\n" \
  "ds_read_b128 v[224:227], v51 offset:16384\n" \
  "ds_read_b128 v[228:231], v51 offset:18432\n" \
  "s_lshr_b32 s73, s72, 2\n" \
  "s_add_u32 s72, s72, 3637504\n" \
  "s_add_u32 s73, s73, 2323712\n" \
  "s_add_u32 s74, s72, 8192\n" \
  "s_add_u32 s75, s72, 16384\n" \
  "s_waitcnt lgkmcnt(3)\n" \
  "buffer_store_dwordx4 v[216:219], v50, %[wsd], s72 offen sc1\n" \
  "s_waitcnt lgkmcnt(2)\n" \
  "buffer_store_dwordx4 v[220:223], v50, %[wsd], s74 offen sc1\n" \
  "s_cmp_gt_u32 %[w], 3\n" \
  "s_waitcnt lgkmcnt(0)\n" \
  "s_cbranch_scc1 Lc1_%=\n" \
  "buffer_store_dwordx4 v[224:227], v50, %[wsd], s75 offen sc1\n" \
  "Lc1_%=:\n" \
  "s_cmp_lt_u32 %[w], 3\n" \
  "s_cbranch_scc1 Lc2_%=\n" \
  "buffer_store_dwordx4 v[228:231], v50, %[wsd], s73 offen sc1\n" \
  "Lc2_%=:\n" \
  "v_mov_b32 v94, 35840\n" \
  "ds_read_b96 v[56:58], v94 offset:1472\n" \
  "v_lshlrev_b32 v13, 4, v86\n" \
  "v_lshlrev_b32 v50, 5, v86\n" \
  "v_and_b32 v51, 0x60, v50\n" \
  "ds_read_b128 v[24:27], v51 offset:36160\n" \
  "ds_read_b128 v[28:31], v51 offset:36176\n" \
  "ds_read_b64 v[32:33], v13 offset:36288\n" \
  "ds_read_b64 v[36:37], v13 offset:36296\n" \
  "s_mov_b32 s79, m0\n" \
  "s_mov_b64 s[88:89], 0\n" \
  "s_mov_b32 s92, 0\n" \
  "v_mov_b32 v48, 0\n" \
  "v_mov_b32 v92, 1\n" \
  "v_and_b32 v41, 4, v54\n" \
  "s_movk_i32 s63, 5120\n" \
  "v_mad_u32_u24 v40, v55, s63, v41\n" \
  "s_lshl_b32 s63, %[w], 10\n" \
  "s_add_u32 s63, s63, 37328\n" \
  "v_add_u32 v41, s63, v41\n" \
  "s_mul_i32 s90, s68, %[npb]\n" \
  "s_mul_hi_u32 s91, s68, %[npb]\n" \
  "s_lshr_b64 s[90:91], s[90:91], 20\n" \
  "s_mul_hi_u32 s63, s90, %[m2]\n" \
  "s_cmp_lt_u32 s63, s68\n" \
  "s_addc_u32 s90, s90, 0\n" \
  "s_mul_hi_u32 s63, s90, %[m2]\n" \
  "s_cmp_lt_u32 s63, s68\n" \
  "s_addc_u32 s90, s90, 0\n" \
  "s_min_u32 s67, s90, %[n]\n" \
  "s_add_u32 s68, s68, 1\n" \
  "s_mul_i32 s90, s68, %[npb]\n" \
  "s_mul_hi_u32 s91, s68, %[npb]\n" \
  "s_lshr_b64 s[90:91], s[90:91], 20\n" \
  "s_mul_hi_u32 s63, s90, %[m2]\n" \
  "s_cmp_lt_u32 s63, s68\n" \
  "s_addc_u32 s90, s90, 0\n" \
  "s_mul_hi_u32 s63, s90, %[m2]\n" \
  "s_cmp_lt_u32 s63, s68\n" \
  "s_addc_u32 s90, s90, 0\n" \
  "s_min_u32 s68, s90, %[n]\n" \
  "s_sub_u32 s68, s68, s67\n" \
  "s_waitcnt lgkmcnt(0)\n" \
  "v_readfirstlane_b32 s50, v56\n" \
  "v_readfirstlane_b32 s51, v57\n" \
  "v_readfirstlane_b32 s69, v58\n" \
  "v_bfrev_b32 v51, 1\n" \
  "s_lshl_b32 s64, s50, 22\n" \
  "s_lshr_b64 s[90:91], s[50:51], 10\n" \
  "s_mov_b32 s65, s90\n" \
  "buffer_load_dwordx2 v[44:45], v17, %[wsd], s61 offen sc1\n" \
  "s_mov_b64 s[96:97], %[xp]\n" \
  "s_lshl_b32 s98, %[n], 10\n" \
  "s_mov_b32 s99, 0x20000\n" \
  "s_mul_i32 s63, s71, %[n]\n" \
  "s_lshr_b32 s63, s63, 3\n" \
  "s_lshl3_add_u32 s63, s66, s63\n" \
  "s_sub_u32 s66, 7, %[w]\n" \
  "s_lshl_b32 s66, s66, 8\n" \
  "s_add_u32 s66, s66, s63\n" \
  "s_lshl_b32 s66, s66, 10\n" \
  "v_lshlrev_b32 v20, 7, v86\n" \
  "s_lshl_b32 s71, s71, 1\n" \
  "s_mov_b32 m0, 35904\n" \
  "s_mul_i32 s63, %[blk], 64\n" \
  "s_add_u32 s63, s63, 33024\n" \
  "v_mov_b32 v52, s64\n" \
  "v_mov_b32 v53, s65\n" \
  "s_cmp_lg_u32 %[w], 6\n" \
  "s_waitcnt vmcnt(1)\n" \
  "s_barrier\n" \
  "s_cbranch_scc1 Lp2_%=\n" \
  "s_mov_b64 exec, 1\n" \
  "buffer_store_dwordx2 v[52:53], off, %[wsd], s63 sc1\n" \
  "s_mov_b64 exec, -1\n" \
  "s_waitcnt vmcnt(1)\n" \
  "s_branch Lchk%=\n" \
  "Lpoll%=:\n" \
  "buffer_load_dwordx2 v[44:45], v17, %[wsd], s61 offen sc1\n" \
  "Lp2_%=:\n" \
  "s_waitcnt vmcnt(0)\n" \
  "Lchk%=:\n" \
  "v_and_b32 v50, 0xffc00000, v44\n" \
  "v_cmp_eq_u32 s[90:91], s65, v45\n" \
  "v_cmp_eq_u32 vcc, s64, v50\n" \
  "s_and_b64 vcc, vcc, s[90:91]\n" \
  "s_andn2_b64 vcc, vcc, s[88:89]\n" \
  "s_or_b64 s[88:89], s[88:89], vcc\n" \
  "v_cndmask_b32 v48, v48, v44, vcc\n" \
  "s_cmp_eq_u64 s[88:89], -1\n" \
  "v_cndmask_b32 v17, v17, v51, vcc\n" \
  "s_cbranch_scc1 Lseen%=\n" \
  "s_add_u32 s92, s92, 1\n" \
  "s_cmp_lt_u32 s92, 4096\n" \
  "s_cbranch_scc1 Lpoll%=\n" \
  "v_mov_b32 v48, 0\n" \
  "Lseen%=:\n" \
  "s_cmp_lt_u32 %[w], 7\n" \
  "s_cbranch_scc1 Lq3_%=\n" \
  "buffer_load_dword v20, s[96:99], s66 offen lds\n" \
  "Lq3_%=:\n" \
  "v_bfe_u32 v49, v48, 11, 11\n" \
  "v_and_b32 v50, 0x7ff, v48\n" \
  "v_bfe_u32 v52, v13, 4, 1\n" \
  "v_add_u32_dpp v87, v49, v49 row_shr:1 row_mask:0xf bank_mask:0xf bound_ctrl:1\n" \
  "v_lshl_add_u32 v50, v50, 2, v40\n" \
  "v_lshlrev_b32 v53, v52, v49\n" \
  "v_add_u32_dpp v87, v87, v87 row_shr:2 row_mask:0xf bank_mask:0xf bound_ctrl:1\n" \
  "v_sub_u32 v49, v49, v52\n" \
  "v_and_b32 v85, -8, v41\n" \
  "v_add_u32_dpp v87, v87, v87 row_shr:4 row_mask:0xf bank_mask:0xf bound_ctrl:1\n" \
  "v_add_u32 v85, v85, v54\n" \
  "s_nop 0\n" \
  "v_add_u32_dpp v87, v87, v87 row_shr:8 row_mask:0xf bank_mask:0xf bound_ctrl:1\n" \
  "s_nop 1\n" \
  "v_add_u32_dpp v87, v87, v87 row_bcast:15 row_mask:0xa bank_mask:0xf\n" \
  "s_nop 1\n" \
  "v_add_u32_dpp v87, v87, v87 row_bcast:31 row_mask:0xc bank_mask:0xf\n" \
  "v_sub_u32 v53, v87, v53\n" \
  "v_readlane_b32 s62, v87, 63\n" \
  "v_lshl_add_u32 v53, v53, 1, v41\n" \
  "v_lshl_add_u32 v49, v49, 2, v53\n" \
  "s_lshr_b32 s62, s62, 1\n" \
  "s_cmp_lt_u32 s62, 256\n" \
  "s_cbranch_scc1 Ll0_%=\n" \
  "s_mov_b32 s62, 0\n" \
  "ds_or_b32 v94, v92\n" \
  "s_branch Ll2_%=\n" \
  "Ll0_%=:\n" \
  "v_cmp_lt_i32 vcc, v53, v49\n" \
  "s_mov_b64 exec, vcc\n" \
  "Ll1_%=:\n" \
  "ds_write_b32 v53, v50\n" \
  "v_add_u32 v53, 8, v53\n" \
  "v_cmp_lt_i32 vcc, v53, v49\n" \
  "v_add_u32 v50, 8, v50\n" \
  "s_and_b64 exec, vcc, vcc\n" \
  "s_cbranch_scc1 Ll1_%=\n" \
  "s_mov_b64 exec, -1\n" \
  "Ll2_%=:\n" \
  "ds_read2st64_b32 v[56:57], v85 offset1:1\n" \
  "ds_read2st64_b32 v[58:59], v85 offset0:2 offset1:3\n" \
  "v_mov_b32 v50, s62\n" \
  "s_mov_b64 exec, 1\n" \
  "ds_add_u32 v94, v50 offset:4\n" \
  "s_mov_b64 exec, -1\n" \
  "v_cmp_gt_i32 s[80:81], s62, v86\n" \
  "s_sub_i32 s63, s62, 64\n" \
  "v_cmp_gt_i32 s[82:83], s63, v86\n" \
  "s_sub_i32 s63, s62, 128\n" \
  "v_cmp_gt_i32 s[84:85], s63, v86\n" \
  "s_sub_i32 s63, s62, 192\n" \
  "v_cmp_gt_i32 s[86:87], s63, v86\n" \
  "v_bfrev_b32 v50, 4\n" \
  "s_waitcnt lgkmcnt(1)\n" \
  "v_cndmask_b32 v56, v50, v56, s[80:81]\n" \
  "v_cndmask_b32 v57, v50, v57, s[82:83]\n" \
  "v_cndmask_b32 v58, v50, v58, s[84:85]\n" \
  "v_cndmask_b32 v59, v50, v59, s[86:87]\n" \
  "s_sleep 4\n" \
  "v_mul_u32_u24 v18, 64, v55\n" \
  "s_mov_b32 s93, 33024\n" \
  "s_mov_b64 s[88:89], 0\n" \
  "s_lshl_b32 s63, %[w], 6\n" \
  "v_add_u32 v96, s63, v86\n" \
  "s_mov_b32 s63, 0xbfb8aa3b\n" \
  "v_lshlrev_b32 v100, 17, v96\n" \
  "v_add_u32 v103, 0x3f810000, v100\n" \
  "v_add_u32 v102, 0x3f800000, v100\n" \
  "v_add_u32 v101, 0x3b810000, v100\n" \
  "v_add_u32 v100, 0x3b800000, v100\n" \
  "v_mul_f32 v104, s63, v100\n" \
  "v_mul_f32 v105, s63, v101\n" \
  "v_mul_f32 v106, s63, v102\n" \
  "v_mul_f32 v107, s63, v103\n" \
  "v_exp_f32 v104, v104\n" \
  "v_exp_f32 v105, v105\n" \
  "v_exp_f32 v106, v106\n" \
  "v_exp_f32 v107, v107\n" \
  "v_lshlrev_b32 v96, 2, v96\n" \
  "v_add_f32 v108, 1.0, v104\n" \
  "v_add_f32 v109, 1.0, v105\n" \
  "v_add_f32 v110, 1.0, v106\n" \
  "v_add_f32 v111, 1.0, v107\n" \
  "v_rcp_f32 v108, v108\n" \
  "v_rcp_f32 v109, v109\n" \
  "v_rcp_f32 v110, v110\n" \
  "v_rcp_f32 v111, v111\n" \
  "v_add_u32 v97, 0x10000, v96\n" \
  "v_pk_mul_f32 v[100:101], v[100:101], v[108:109]\n" \
  "v_pk_mul_f32 v[102:103], v[102:103], v[110:111]\n" \
  "v_pk_mul_f32 v[104:105], v[104:105], v[100:101] neg_lo:[0,1] neg_hi:[0,1]\n" \
  "v_pk_mul_f32 v[106:107], v[106:107], v[102:103] neg_lo:[0,1] neg_hi:[0,1]\n" \
  "v_cvt_pk_bf16_f32 v100, v100, v101\n" \
  "v_cvt_pk_bf16_f32 v102, v102, v103\n" \
  "v_cvt_pk_bf16_f32 v104, v104, v105\n" \
  "v_cvt_pk_bf16_f32 v106, v106, v107\n" \
  "ds_write2st64_b32 v96, v100, v102 offset0:119 offset1:127\n" \
  "ds_write2st64_b32 v97, v104, v106 offset0:119 offset1:127\n" \
  "buffer_load_dwordx2 v[44:45], v18, %[wsd], s93 offen sc1\n" \
  "v_mov_b64 v[128:129], 0\n" \
  "v_mov_b64 v[130:131], 0\n" \
  "v_mov_b64 v[132:133], 0\n" \
  "v_mov_b64 v[134:135], 0\n" \
  "v_mov_b64 v[136:137], 0\n" \
  "v_mov_b64 v[138:139], 0\n" \
  "v_mov_b64 v[140:141], 0\n" \
  "v_mov_b64 v[142:143], 0\n" \
  "v_mov_b64 v[144:145], 0\n" \
  "v_mov_b64 v[146:147], 0\n" \
  "v_mov_b64 v[148:149], 0\n" \
  "v_mov_b64 v[150:151], 0\n" \
  "v_mov_b64 v[152:153], 0\n" \
  "v_mov_b64 v[154:155], 0\n" \
  "v_mov_b64 v[156:157], 0\n" \
  "v_mov_b64 v[158:159], 0\n" \
  "v_mov_b64 v[160:161], 0\n" \
  "v_mov_b64 v[162:163], 0\n" \
  "v_mov_b64 v[164:165], 0\n" \
  "v_mov_b64 v[166:167], 0\n" \
  "v_mov_b64 v[168:169], 0\n" \
  "v_mov_b64 v[170:171], 0\n" \
  "v_mov_b64 v[172:173], 0\n" \
  "v_mov_b64 v[174:175], 0\n" \
  "v_mov_b64 v[176:177], 0\n" \
  "v_mov_b64 v[178:179], 0\n" \
  "v_mov_b64 v[180:181], 0\n" \
  "v_mov_b64 v[182:183], 0\n" \
  "v_mov_b64 v[184:185], 0\n" \
  "v_mov_b64 v[186:187], 0\n" \
  "v_mov_b64 v[188:189], 0\n" \
  "v_mov_b64 v[190:191], 0\n" \
  "v_mov_b64 v[192:193], 0\n" \
  "v_mov_b64 v[194:195], 0\n" \
  "v_mov_b64 v[196:197], 0\n" \
  "v_mov_b64 v[198:199], 0\n" \
  "v_mov_b64 v[200:201], 0\n" \
  "v_mov_b64 v[202:203], 0\n" \
  "v_mov_b64 v[204:205], 0\n" \
  "v_mov_b64 v[206:207], 0\n" \
  "s_sleep 4\n" \
  "Ld1_%=:\n" \
  "buffer_load_dwordx2 v[46:47], v18, %[wsd], s93 offen sc1\n" \
  "s_waitcnt vmcnt(1)\n" \
  "v_cmp_eq_u32 s[90:91], s65, v45\n" \
  "v_cmp_eq_u32 vcc, s64, v44\n" \
  "s_mov_b32 s63, 2326784\n" \
  "s_and_b64 vcc, vcc, s[90:91]\n" \
  "s_or_b64 s[88:89], s[88:89], vcc\n" \
  "s_cmp_eq_u64 s[88:89], -1\n" \
  "s_cbranch_scc1 Ld2_%=\n" \
  "v_cndmask_b32 v18, v18, v51, vcc\n" \
  "buffer_load_dwordx2 v[44:45], v18, %[wsd], s93 offen sc1\n" \
  "s_waitcnt vmcnt(1)\n" \
  "v_cmp_eq_u32 s[90:91], s65, v47\n" \
  "v_cmp_eq_u32 vcc, s64, v46\n" \
  "s_mov_b32 s63, 2326784\n" \
  "s_and_b64 vcc, vcc, s[90:91]\n" \
  "s_or_b64 s[88:89], s[88:89], vcc\n" \
  "s_cmp_eq_u64 s[88:89], -1\n" \
  "s_cbranch_scc1 Ld2_%=\n" \
  "v_cndmask_b32 v18, v18, v51, vcc\n" \
  "s_add_u32 s92, s92, 1\n" \
  "s_cmp_lt_u32 s92, 4096\n" \
  "s_cbranch_scc1 Ld1_%=\n" \
  "s_mov_b64 s[80:81], 0\n" \
  "s_mov_b64 s[82:83], 0\n" \
  "s_mov_b64 s[84:85], 0\n" \
  "s_mov_b64 s[86:87], 0\n" \
  "Ld2_%=:\n" \
  "s_mov_b32 s93, 3637504\n" \
  "s_cmp_le_i32 s62, 192\n" \
  "s_cbranch_scc1 Lk317_%=\n" \
  "buffer_load_dword v63, v59, %[wsd], s63 offen sc1\n" \
  "v_lshlrev_b32 v59, 2, v59\n" \
  "buffer_load_dwordx4 v[228:231], v59, %[wsd], s93 offen sc1\n" \
  "Lk317_%=:\n" \
  "buffer_load_dword v60, v56, %[wsd], s63 offen sc1\n" \
  "buffer_load_dword v61, v57, %[wsd], s63 offen sc1\n" \
  "buffer_load_dword v62, v58, %[wsd], s63 offen sc1\n" \
  "v_lshlrev_b32 v56, 2, v56\n" \
  "v_lshlrev_b32 v57, 2, v57\n" \
  "v_lshlrev_b32 v58, 2, v58\n" \
  "s_lshl_b32 s72, %[w], 3\n" \
  "s_lshl_b32 s63, 1, s72\n" \
  "v_mov_b32 v93, s63\n" \
  "s_and_b32 s74, s72, 24\n" \
  "s_lshl_b64 s[88:89], -1, s72\n" \
  "s_and_b32 s88, s88, 0x01010101\n" \
  "s_and_b32 s89, s89, 0x01010101\n" \
  "s_and_b32 s73, %[w], 4\n" \
  "s_lshl_b32 s75, %[w], 8\n" \
  "s_add_u32 s75, s75, 106816\n" \
  "s_mov_b32 s52, s96\n" \
  "s_or_b32 s53, s97, 0x4000000\n" \
  "s_mov_b32 s54, %[n]\n" \
  "s_mov_b32 s55, 0x20000\n" \
  "s_mov_b64 s[56:57], %[out]\n" \
  "s_lshl_b32 s63, s67, 10\n" \
  "s_add_u32 s56, s56, s63\n" \
  "s_addc_u32 s57, s57, 0\n" \
  "s_lshl_b32 s58, s68, 10\n" \
  "s_mov_b32 s59, 0x20000\n" \
  "s_lshl_b32 s67, s67, 24\n" \
  "s_mov_b32 s70, 32768\n" \
  "s_mul_i32 s63, %[w], 3328\n" \
  "s_add_u32 s63, s63, 37328\n" \
  "v_add_u32 v21, s63, v13\n" \
  "v_and_b32 v15, 0xf0, v54\n" \
  "v_add_u32 v15, s63, v15\n" \
  "v_and_b32 v23, 0xf0, v13\n" \
  "v_add3_u32 v16, v23, s63, 8\n" \
  "s_mul_i32 s63, %[w], 832\n" \
  "s_add_u32 s63, s63, 100096\n" \
  "v_mov_b32 v14, s63\n" \
  "v_and_b32 v50, 0x300, v13\n" \
  "v_and_or_b32 v19, v54, 60, v50\n" \
  "v_add_u32 v19, 110912, v19\n" \
  "v_lshl_add_u32 v23, v50, 2, v23\n" \
  "s_lshl_b32 s63, s73, 10\n" \
  "s_lshl_b32 s61, s74, 5\n" \
  "s_add_u32 s63, s63, s61\n" \
  "v_add_u32 v23, s63, v23\n" \
  "v_bfe_u32 v50, v86, 2, 2\n" \
  "v_mul_u32_u24 v22, 1056, v50\n" \
  "v_and_b32 v50, 48, v13\n" \
  "v_lshl_add_u32 v22, v50, 2, v22\n" \
  "v_and_b32 v50, 48, v86\n" \
  "s_mul_i32 s63, s73, 1056\n" \
  "s_add_u32 s63, s63, 110912\n" \
  "v_add3_u32 v22, v22, v50, s63\n" \
  "v_mul_u32_u24 v50, 26, v86\n" \
  "v_lshrrev_b32 v50, 8, v50\n" \
  "v_mad_i32_i24 v50, v50, -10, v86\n" \
  "v_lshlrev_b32 v42, 19, v50\n" \
  "s_mul_i32 s63, %[w], 11\n" \
  "v_mad_u32_u24 v50, v86, 37, s63\n" \
  "v_and_b32 v50, 63, v50\n" \
  "v_mul_u32_u24 v43, 0xffff, v50\n" \
  "v_add_u32 v43, 64, v43\n" \
  "s_lshl_b32 s63, %[n], 10\n" \
  "s_add_u32 s66, s66, 262144\n" \
  "s_cmp_ge_u32 %[w], 6\n" \
  "s_cselect_b32 s98, s63, 0\n" \
  "s_cmp_lt_u32 s92, 4096\n" \
  "s_cbranch_scc1 Lq2_%=\n" \
  "v_mov_b32 v53, 3\n" \
  "ds_or_b32 v94, v53\n" \
  "Lq2_%=:\n" \
  "v_mov_b32 v50, 2\n" \
  "s_waitcnt vmcnt(0)\n" \
  "v_subrev_u32 v60, s67, v60\n" \
  "s_mov_b64 exec, s[80:81]\n" \
  "v_lshlrev_b32_sdwa v64, v50, v60 dst_sel:DWORD dst_unused:UNUSED_PAD src0_sel:DWORD src1_sel:BYTE_3\n" \
  "ds_add_u32 v64, v92 offset:35584\n" \
  "v_subrev_u32 v61, s67, v61\n" \
  "s_mov_b64 exec, s[82:83]\n" \
  "v_lshlrev_b32_sdwa v65, v50, v61 dst_sel:DWORD dst_unused:UNUSED_PAD src0_sel:DWORD src1_sel:BYTE_3\n" \
  "ds_add_u32 v65, v92 offset:35584\n" \
  "v_subrev_u32 v62, s67, v62\n" \
  "s_mov_b64 exec, s[84:85]\n" \
  "v_lshlrev_b32_sdwa v66, v50, v62 dst_sel:DWORD dst_unused:UNUSED_PAD src0_sel:DWORD src1_sel:BYTE_3\n" \
  "ds_add_u32 v66, v92 offset:35584\n" \
  "s_cmp_le_i32 s62, 192\n" \
  "s_cbranch_scc1 Lk409_%=\n" \
  "v_subrev_u32 v63, s67, v63\n" \
  "s_mov_b64 exec, s[86:87]\n" \
  "v_lshlrev_b32_sdwa v67, v50, v63 dst_sel:DWORD dst_unused:UNUSED_PAD src0_sel:DWORD src1_sel:BYTE_3\n" \
  "ds_add_u32 v67, v92 offset:35584\n" \
  "Lk409_%=:\n" \
  "s_mov_b64 exec, -1\n" \
  "buffer_load_dwordx4 v[216:219], v56, %[wsd], s93 offen sc1\n" \
  "buffer_load_dwordx4 v[220:223], v57, %[wsd], s93 offen sc1\n" \
  "buffer_load_dwordx4 v[224:227], v58, %[wsd], s93 offen sc1\n" \
  "s_waitcnt lgkmcnt(0)\n" \
  "s_barrier\n" \
  "ds_read_b32 v85, v54 offset:35584\n" \
  "ds_read_b32 v86, v94 offset:4\n" \
  "ds_bpermute_b32 v76, v64, v42\n" \
  "ds_bpermute_b32 v77, v65, v42\n" \
  "ds_bpermute_b32 v78, v66, v42\n" \
  "ds_bpermute_b32 v79, v67, v42\n" \
  "buffer_load_dword v20, s[96:99], s66 offen lds\n" \
  "s_waitcnt lgkmcnt(4)\n" \
  "v_add_u32_dpp v87, v85, v85 row_shr:1 row_mask:0xf bank_mask:0xf bound_ctrl:1\n" \
  "v_cvt_f32_u32 v86, v86\n" \
  "v_rcp_f32 v86, v86\n" \
  "v_add_u32_dpp v87, v87, v87 row_shr:2 row_mask:0xf bank_mask:0xf bound_ctrl:1\n" \
  "v_mul_hi_u32_u24 v68, %[cb], v60\n" \
  "v_subrev_u32 v68, s71, v68\n" \
  "v_add_u32_dpp v87, v87, v87 row_shr:4 row_mask:0xf bank_mask:0xf bound_ctrl:1\n" \
  "v_and_b32 v68, 15, v68\n" \
  "v_lshl_add_u32 v72, v68, 1, s75\n" \
  "v_add_u32_dpp v87, v87, v87 row_shr:8 row_mask:0xf bank_mask:0xf bound_ctrl:1\n" \
  "v_lshl_add_u32 v68, v68, 3, s73\n" \
  "v_mul_hi_u32_u24 v69, %[cb], v61\n" \
  "v_add_u32_dpp v87, v87, v87 row_bcast:15 row_mask:0xa bank_mask:0xf\n" \
  "v_subrev_u32 v69, s71, v69\n" \
  "v_and_b32 v69, 15, v69\n" \
  "v_add_u32_dpp v87, v87, v87 row_bcast:31 row_mask:0xc bank_mask:0xf\n" \
  "v_sub_u32 v85, v87, v85\n" \
  "v_cvt_f32_u32 v87, v87\n" \
  "v_cvt_f32_u32 v85, v85\n" \
  "v_mul_f32 v87, v87, v86\n" \
  "v_mul_f32 v85, v85, v86\n" \
  "v_cvt_pknorm_u16_f32 v84, v85, v87\n" \
  "ds_bpermute_b32 v80, v64, v84\n" \
  "ds_bpermute_b32 v81, v65, v84\n" \
  "ds_bpermute_b32 v82, v66, v84\n" \
  "ds_bpermute_b32 v83, v67, v84\n" \
  "v_lshl_add_u32 v73, v69, 1, s75\n" \
  "v_lshl_add_u32 v69, v69, 3, s73\n" \
  "v_mul_hi_u32_u24 v70, %[cb], v62\n" \
  "v_subrev_u32 v70, s71, v70\n" \
  "v_and_b32 v70, 15, v70\n" \
  "v_lshl_add_u32 v74, v70, 1, s75\n" \
  "v_lshl_add_u32 v70, v70, 3, s73\n" \
  "v_mul_hi_u32_u24 v71, %[cb], v63\n" \
  "v_subrev_u32 v71, s71, v71\n" \
  "v_and_b32 v71, 15, v71\n" \
  "v_lshl_add_u32 v75, v71, 1, s75\n" \
  "v_lshl_add_u32 v71, v71, 3, s73\n" \
  "s_mov_b32 s63, 0xffff\n" \
  "s_waitcnt lgkmcnt(4)\n" \
  "v_and_or_b32 v76, v60, s63, v76\n" \
  "v_and_or_b32 v77, v61, s63, v77\n" \
  "v_and_or_b32 v78, v62, s63, v78\n" \
  "v_and_or_b32 v79, v63, s63, v79\n" \
  "s_waitcnt lgkmcnt(3)\n" \
  "v_dot2_u32_u16 v80, v80, v43, 0\n" \
  "s_waitcnt lgkmcnt(2)\n" \
  "v_dot2_u32_u16 v81, v81, v43, 0\n" \
  "s_waitcnt lgkmcnt(1)\n" \
  "v_dot2_u32_u16 v82, v82, v43, 0\n" \
  "s_waitcnt lgkmcnt(0)\n" \
  "v_dot2_u32_u16 v83, v83, v43, 0\n" \
  "v_lshrrev_b32 v60, 19, v80\n" \
  "s_mov_b64 exec, s[80:81]\n" \
  "v_lshl_add_u32 v68, v60, 7, v68\n" \
  "ds_add_rtn_u32 v80, v68, v93 offset:34560\n" \
  "v_lshrrev_b32 v61, 19, v81\n" \
  "s_mov_b64 exec, s[82:83]\n" \
  "v_lshl_add_u32 v69, v61, 7, v69\n" \
  "ds_add_rtn_u32 v81, v69, v93 offset:34560\n" \
  "v_lshrrev_b32 v62, 19, v82\n" \
  "s_mov_b64 exec, s[84:85]\n" \
  "v_lshl_add_u32 v70, v62, 7, v70\n" \
  "ds_add_rtn_u32 v82, v70, v93 offset:34560\n" \
  "s_cmp_le_i32 s62, 192\n" \
  "s_cbranch_scc1 Lk494_%=\n" \
  "v_lshrrev_b32 v63, 19, v83\n" \
  "s_mov_b64 exec, s[86:87]\n" \
  "v_lshl_add_u32 v71, v63, 7, v71\n" \
  "ds_add_rtn_u32 v83, v71, v93 offset:34560\n" \
  "Lk494_%=:\n" \
  "s_mov_b64 exec, -1\n" \
  "v_lshl_add_u32 v72, v60, 5, v72\n" \
  "v_lshl_add_u32 v73, v61, 5, v73\n" \
  "v_lshl_add_u32 v74, v62, 5, v74\n" \
  "v_lshl_add_u32 v75, v63, 5, v75\n" \
  "v_lshrrev_b32 v50, 1, v13\n" \
  "v_lshrrev_b32 v51, 3, v13\n" \
  "v_add_u32 v51, s75, v51\n" \
  "v_lshrrev_b32 v52, 8, v13\n" \
  "v_mul_u32_u24 v52, 0xd000d0, v52\n" \
  "v_add_u32 v52, 0x3400000, v52\n" \
  "s_mov_b32 s63, 0x01010101\n" \
  "s_waitcnt lgkmcnt(0)\n" \
  "s_barrier\n" \
  "ds_read_b64 v[88:89], v50 offset:34560\n" \
  "ds_read_b64 v[90:91], v50 offset:35072\n" \
  "v_bfe_u32 v80, v80, s74, 8\n" \
  "v_bfe_u32 v81, v81, s74, 8\n" \
  "v_bfe_u32 v82, v82, s74, 8\n" \
  "v_bfe_u32 v83, v83, s74, 8\n" \
  "s_lshl_b32 s72, %[w], 4\n" \
  "s_or_b32 s72, s72, 15\n" \
  "s_lshl_b32 s73, s73, 2\n" \
  "s_waitcnt lgkmcnt(0)\n" \
  "v_dot4_u32_u8 v44, v88, s63, 0\n" \
  "v_dot4_u32_u8 v45, v90, s63, 0\n" \
  "v_dot4_u32_u8 v44, v89, s63, v44\n" \
  "v_dot4_u32_u8 v45, v91, s63, v45\n" \
  "v_dot4_u32_u8 v46, v88, s88, 0\n" \
  "v_dot4_u32_u8 v47, v90, s88, 0\n" \
  "v_dot4_u32_u8 v46, v89, s89, v46\n" \
  "v_lshl_or_b32 v44, v45, 16, v44\n" \
  "v_dot4_u32_u8 v47, v91, s89, v47\n" \
  "s_nop 0\n" \
  "v_add_u32_dpp v87, v44, v44 row_shr:1 row_mask:0xf bank_mask:0xf bound_ctrl:1\n" \
  "s_nop 0\n" \
  "v_lshl_or_b32 v46, v47, 16, v46\n" \
  "v_add_u32_dpp v87, v87, v87 row_shr:2 row_mask:0xf bank_mask:0xf bound_ctrl:1\n" \
  "v_sub_u32 v52, v52, v46\n" \
  "s_nop 0\n" \
  "v_add_u32_dpp v87, v87, v87 row_shr:4 row_mask:0xf bank_mask:0xf bound_ctrl:1\n" \
  "s_nop 1\n" \
  "v_add_u32_dpp v87, v87, v87 row_shr:8 row_mask:0xf bank_mask:0xf bound_ctrl:1\n" \
  "v_add_u32 v52, v52, v87\n" \
  "ds_write_b16 v51, v52\n" \
  "ds_write_b16_d16_hi v51, v52 offset:128\n" \
  "ds_read_u16 v72, v72\n" \
  "ds_read_u16 v73, v73\n" \
  "ds_read_u16 v74, v74\n" \
  "ds_read_u16 v75, v75\n" \
  "v_readlane_b32 s60, v87, s72\n" \
  "s_movk_i32 s63, 200\n" \
  "v_cmp_lt_u32_sdwa s[90:91], s63, v87 src0_sel:DWORD src1_sel:WORD_0\n" \
  "v_cmp_lt_u32_sdwa vcc, s63, v87 src0_sel:DWORD src1_sel:WORD_1\n" \
  "s_lshr_b32 s72, s60, s73\n" \
  "s_and_b32 s72, s72, 0xffff\n" \
  "s_add_u32 s60, s72, 3\n" \
  "s_lshr_b32 s60, s60, 2\n" \
  "s_max_u32 s60, s60, 1\n" \
  "s_lshl2_add_u32 s73, s60, 8\n" \
  "s_sub_u32 s73, s73, s72\n" \
  "s_bfm_b64 s[88:89], s73, 0\n" \
  "s_or_b64 vcc, vcc, s[90:91]\n" \
  "s_cbranch_scc0 Lo1_%=\n" \
  "s_mov_b64 s[80:81], 0\n" \
  "s_mov_b64 s[82:83], 0\n" \
  "s_mov_b64 s[84:85], 0\n" \
  "s_mov_b64 s[86:87], 0\n" \
  "s_mov_b64 s[88:89], 0xfff\n" \
  "s_mov_b32 s72, 0\n" \
  "s_mov_b32 s60, 1\n" \
  "ds_or_b32 v94, v92\n" \
  "Lo1_%=:\n" \
  "v_lshrrev_b32 v50, 2, v13\n" \
  "s_lshl_b32 s72, s72, 2\n" \
  "v_add3_u32 v50, v50, s72, v14\n" \
  "v_mov_b32 v51, 0x50ffff\n" \
  "s_mov_b32 s63, 100096\n" \
  "s_mov_b64 exec, s[88:89]\n" \
  "ds_write_b32 v50, v51\n" \
  "s_waitcnt lgkmcnt(1)\n" \
  "s_mov_b64 exec, s[80:81]\n" \
  "v_add_u32 v80, v80, v72\n" \
  "v_lshl_add_u32 v72, v80, 2, s63\n" \
  "ds_write_b32 v72, v76\n" \
  "s_mov_b64 exec, s[82:83]\n" \
  "v_add_u32 v81, v81, v73\n" \
  "v_lshl_add_u32 v73, v81, 2, s63\n" \
  "ds_write_b32 v73, v77\n" \
  "s_mov_b64 exec, s[84:85]\n" \
  "v_add_u32 v82, v82, v74\n" \
  "v_lshl_add_u32 v74, v82, 2, s63\n" \
  "ds_write_b32 v74, v78\n" \
  "s_cmp_le_i32 s62, 192\n" \
  "s_cbranch_scc1 Lk594_%=\n" \
  "s_mov_b64 exec, s[86:87]\n" \
  "v_add_u32 v83, v83, v75\n" \
  "v_lshl_add_u32 v75, v83, 2, s63\n" \
  "ds_write_b32 v75, v79\n" \
  "Lk594_%=:\n" \
  "s_mov_b64 exec, -1\n" \
  "s_sub_u32 s60, s60, 1\n" \
  "s_lshl_b32 s63, s60, 4\n" \
  "v_sub_u32 v50, v14, v13\n" \
  "v_add_u32 v50, s63, v50\n" \
  "s_waitcnt lgkmcnt(0)\n" \
  "s_barrier\n" \
  "ds_read_b128 v[56:59], v14\n" \
  "ds_read_b128 v[60:63], v14 offset:16\n" \
  "ds_read_b128 v[64:67], v50\n" \
  "v_lshlrev_b32 v232, 4, v80\n" \
  "v_lshlrev_b32 v233, 4, v81\n" \
  "v_lshlrev_b32 v234, 4, v82\n" \
  "v_lshlrev_b32 v235, 4, v83\n" \
  "s_waitcnt lgkmcnt(1)\n" \
  "v_and_b32 v12, 0xffff, v56\n" \
  "buffer_load_dwordx4 v[96:99], v[12:13], s[52:55], 0 idxen offen\n" \
  "v_and_b32 v12, 0xffff, v57\n" \
  "buffer_load_dwordx4 v[100:103], v[12:13], s[52:55], 0 idxen offen\n" \
  "v_and_b32 v12, 0xffff, v58\n" \
  "buffer_load_dwordx4 v[104:107], v[12:13], s[52:55], 0 idxen offen\n" \
  "v_and_b32 v12, 0xffff, v59\n" \
  "buffer_load_dwordx4 v[108:111], v[12:13], s[52:55], 0 idxen offen\n" \
  "v_and_b32 v12, 0xffff, v60\n" \
  "buffer_load_dwordx4 v[112:115], v[12:13], s[52:55], 0 idxen offen\n" \
  "v_and_b32 v12, 0xffff, v61\n" \
  "buffer_load_dwordx4 v[116:119], v[12:13], s[52:55], 0 idxen offen\n" \
  "v_and_b32 v12, 0xffff, v62\n" \
  "buffer_load_dwordx4 v[120:123], v[12:13], s[52:55], 0 idxen offen\n" \
  "v_and_b32 v12, 0xffff, v63\n" \
  "buffer_load_dwordx4 v[124:127], v[12:13], s[52:55], 0 idxen offen\n" \
  "s_waitcnt vmcnt(9)\n" \
  "s_mov_b64 exec, s[80:81]\n" \
  "ds_write_b128 v232, v[216:219] offset:37328\n" \
  "s_mov_b64 exec, s[82:83]\n" \
  "ds_write_b128 v233, v[220:223] offset:37328\n" \
  "s_mov_b64 exec, s[84:85]\n" \
  "ds_write_b128 v234, v[224:227] offset:37328\n" \
  "s_cmp_le_i32 s62, 192\n" \
  "s_cbranch_scc1 Lk639_%=\n" \
  "s_mov_b64 exec, s[86:87]\n" \
  "ds_write_b128 v235, v[228:231] offset:37328\n" \
  "Lk639_%=:\n" \
  "s_mov_b64 exec, -1\n" \
  "s_mov_b32 s63, 0x003f0001\n" \
  "v_dot2_u32_u16 v52, v84, s63, 0\n" \
  "v_bfe_u32 v51, v84, 13, 3\n" \
  "s_bfm_b64 s[88:89], s68, 0\n" \
  "v_mov_b32 v18, v94\n" \
  "v_cmp_ge_u32 s[90:91], %[w], v51\n" \
  "v_lshrrev_b32 v52, 19, v52\n" \
  "s_and_b64 s[88:89], s[88:89], s[90:91]\n" \
  "v_cmp_le_u32 s[90:91], %[w], v52\n" \
  "s_and_b64 s[88:89], s[88:89], s[90:91]\n" \
  "s_ff1_i32_b64 s72, s[88:89]\n" \
  "s_flbit_i32_b64 s74, s[88:89]\n" \
  "s_xor_b32 s74, s74, 63\n" \
  "s_mov_b32 s63, 0x0c0c0602\n" \
  "s_waitcnt lgkmcnt(0)\n" \
  "s_barrier\n" \
  "ds_read_b64 v[92:93], v16\n" \
  "ds_read_b64 v[94:95], v15\n" \
  "ds_read_u16 v12, v14 offset:32\n" \
  "v_perm_b32 v64, v65, v64, s63\n" \
  "v_perm_b32 v66, v67, v66, s63\n" \
  "v_readlane_b32 s93, v51, s72\n" \
  "v_readlane_b32 s75, v52, s74\n" \
  "v_lshl_or_b32 v17, v66, 16, v64\n" \
  "v_readlane_b32 s63, v51, s74\n" \
  "v_readlane_b32 s73, v42, s72\n" \
  "v_readlane_b32 s61, v17, s60\n" \
  "s_sub_u32 s74, s74, s72\n" \
  "s_sub_u32 s75, s75, %[w]\n" \
  "s_cmp_eq_u32 s63, %[w]\n" \
  "s_cselect_b32 s75, s75, 0\n" \
  "s_lshr_b32 s73, s73, 16\n" \
  "s_mul_i32 s72, s72, 1056\n" \
  "s_or_b32 s63, %[blk], %[w]\n" \
  "s_cbranch_scc0 Lep0_%=\n" \
  "Lgo%=:\n" \
  "s_waitcnt lgkmcnt(0)\n" \
  "v_mfma_f32_4x4x4_16b_bf16 v[56:59], v[92:93], v[32:33], 0 cbsz:4\n" \
  "v_mfma_f32_4x4x4_16b_bf16 v[60:63], v[92:93], v[36:37], 0 cbsz:4\n" \
  "v_mfma_f32_4x4x4_16b_bf16 v[64:67], v[92:93], v[32:33], 0 cbsz:4 abid:1\n" \
  "v_mfma_f32_4x4x4_16b_bf16 v[68:71], v[92:93], v[36:37], 0 cbsz:4 abid:1\n" \
  "v_mfma_f32_4x4x4_16b_bf16 v[84:87], v[94:95], v[24:25], 0\n" \
  "v_mfma_f32_4x4x4_16b_bf16 v[88:91], v[94:95], v[26:27], 0\n" \
  "v_mfma_f32_4x4x4_16b_bf16 v[48:51], v[94:95], v[28:29], 0\n" \
  "v_mfma_f32_4x4x4_16b_bf16 v[52:55], v[94:95], v[30:31], 0\n" \
  "s_nop 1\n" \
  "v_cvt_pk_bf16_f32 v57, v56, v57\n" \
  "v_lshlrev_b32_sdwa v56, 1, v57 dst_sel:DWORD dst_unused:UNUSED_PAD src0_sel:DWORD src1_sel:WORD_0\n" \
  "v_lshlrev_b32_sdwa v57, 1, v57 dst_sel:DWORD dst_unused:UNUSED_PAD src0_sel:DWORD src1_sel:WORD_1\n" \
  "ds_read_u16_d16_hi v56, v56\n" \
  "ds_read_u16_d16_hi v57, v57\n" \
  "v_cvt_pk_bf16_f32 v59, v58, v59\n" \
  "v_lshlrev_b32_sdwa v58, 1, v59 dst_sel:DWORD dst_unused:UNUSED_PAD src0_sel:DWORD src1_sel:WORD_0\n" \
  "v_lshlrev_b32_sdwa v59, 1, v59 dst_sel:DWORD dst_unused:UNUSED_PAD src0_sel:DWORD src1_sel:WORD_1\n" \
  "ds_read_u16_d16_hi v58, v58\n" \
  "ds_read_u16_d16_hi v59, v59\n" \
  "v_cvt_pk_bf16_f32 v61, v60, v61\n" \
  "v_lshlrev_b32_sdwa v60, 1, v61 dst_sel:DWORD dst_unused:UNUSED_PAD src0_sel:DWORD src1_sel:WORD_0\n" \
  "v_lshlrev_b32_sdwa v61, 1, v61 dst_sel:DWORD dst_unused:UNUSED_PAD src0_sel:DWORD src1_sel:WORD_1\n" \
  "ds_read_u16_d16_hi v60, v60\n" \
  "ds_read_u16_d16_hi v61, v61\n" \
  "v_cvt_pk_bf16_f32 v63, v62, v63\n" \
  "v_lshlrev_b32_sdwa v62, 1, v63 dst_sel:DWORD dst_unused:UNUSED_PAD src0_sel:DWORD src1_sel:WORD_0\n" \
  "v_lshlrev_b32_sdwa v63, 1, v63 dst_sel:DWORD dst_unused:UNUSED_PAD src0_sel:DWORD src1_sel:WORD_1\n" \
  "ds_read_u16_d16_hi v62, v62\n" \
  "ds_read_u16_d16_hi v63, v63\n" \
  "v_cvt_pk_bf16_f32 v76, v84, v88\n" \
  "v_cvt_pk_bf16_f32 v77, v48, v52\n" \
  "s_mov_b32 s63, 0x7fff7fff\n" \
  "v_and_b32 v41, s63, v218\n" \
  "v_and_b32 v42, s63, v219\n" \
  "v_pk_max_u16 v40, v41, v42\n" \
  "v_and_b32 v41, s63, v222\n" \
  "v_and_b32 v42, s63, v223\n" \
  "v_pk_max_u16 v41, v41, v40\n" \
  "v_pk_max_u16 v40, v41, v42\n" \
  "v_and_b32 v41, s63, v226\n" \
  "v_and_b32 v42, s63, v227\n" \
  "v_pk_max_u16 v41, v41, v40\n" \
  "v_pk_max_u16 v40, v41, v42\n" \
  "s_cmp_le_i32 s62, 192\n" \
  "s_cbranch_scc1 Lk725_%=\n" \
  "v_and_b32 v41, s63, v230\n" \
  "v_and_b32 v42, s63, v231\n" \
  "v_pk_max_u16 v41, v41, v40\n" \
  "v_pk_max_u16 v40, v41, v42\n" \
  "Lk725_%=:\n" \
  "v_max_u16_sdwa v40, v40, v40 dst_sel:DWORD dst_unused:UNUSED_PAD src0_sel:WORD_0 src1_sel:WORD_1\n" \
  "s_nop 0\n" \
  "v_cmp_lt_i32 vcc, s69, v40\n" \
  "s_cmp_eq_u64 vcc, 0\n" \
  "s_cselect_b32 s74, s74, -1\n" \
  "s_waitcnt vmcnt(7) lgkmcnt(0)\n" \
  "v_mfma_f32_4x4x4_16b_bf16 v[48:51], v[76:77], v[96:97], 0 cbsz:4\n" \
  "v_mfma_f32_4x4x4_16b_bf16 v[52:55], v[76:77], v[98:99], 0 cbsz:4\n" \
  "s_nop 4\n" \
  "Lbody%=:\n" \
  "s_set_gpr_idx_on s61, gpr_idx(SRC2,DST)\n" \
  "v_pk_fma_f32 v[128:129], v[56:57], v[48:49], v[128:129] op_sel:[0,0,0] op_sel_hi:[0,1,1]\n" \
  "v_pk_fma_f32 v[130:131], v[56:57], v[50:51], v[130:131] op_sel:[0,0,0] op_sel_hi:[0,1,1]\n" \
  "v_pk_fma_f32 v[132:133], v[60:61], v[52:53], v[132:133] op_sel:[0,0,0] op_sel_hi:[0,1,1]\n" \
  "v_pk_fma_f32 v[134:135], v[60:61], v[54:55], v[134:135] op_sel:[0,0,0] op_sel_hi:[0,1,1]\n" \
  "s_set_gpr_idx_off\n" \
  "s_waitcnt vmcnt(6) lgkmcnt(2)\n" \
  "buffer_load_dwordx4 v[96:99], v[12:13], s[52:55], 0 idxen offen\n" \
  "ds_read_u16 v12, v14 offset:36\n" \
  "s_lshr_b32 s61, s61, 8\n" \
  "v_mfma_f32_4x4x4_16b_bf16 v[48:51], v[76:77], v[100:101], 0 cbsz:4 abid:1\n" \
  "v_mfma_f32_4x4x4_16b_bf16 v[52:55], v[76:77], v[102:103], 0 cbsz:4 abid:1\n" \
  "v_cvt_pk_bf16_f32 v65, v64, v65\n" \
  "v_lshlrev_b32_sdwa v64, 1, v65 dst_sel:DWORD dst_unused:UNUSED_PAD src0_sel:DWORD src1_sel:WORD_0\n" \
  "v_lshlrev_b32_sdwa v65, 1, v65 dst_sel:DWORD dst_unused:UNUSED_PAD src0_sel:DWORD src1_sel:WORD_1\n" \
  "ds_read_u16_d16_hi v64, v64\n" \
  "ds_read_u16_d16_hi v65, v65\n" \
  "s_set_gpr_idx_on s61, gpr_idx(SRC2,DST)\n" \
  "v_pk_fma_f32 v[128:129], v[56:57], v[48:49], v[128:129] op_sel:[1,0,0] op_sel_hi:[1,1,1]\n" \
  "v_pk_fma_f32 v[130:131], v[56:57], v[50:51], v[130:131] op_sel:[1,0,0] op_sel_hi:[1,1,1]\n" \
  "v_pk_fma_f32 v[132:133], v[60:61], v[52:53], v[132:133] op_sel:[1,0,0] op_sel_hi:[1,1,1]\n" \
  "v_pk_fma_f32 v[134:135], v[60:61], v[54:55], v[134:135] op_sel:[1,0,0] op_sel_hi:[1,1,1]\n" \
  "s_set_gpr_idx_off\n" \
  "s_waitcnt vmcnt(6) lgkmcnt(2)\n" \
  "buffer_load_dwordx4 v[100:103], v[12:13], s[52:55], 0 idxen offen\n" \
  "ds_read_u16 v12, v14 offset:40\n" \
  "s_lshr_b32 s61, s61, 8\n" \
  "v_mfma_f32_4x4x4_16b_bf16 v[48:51], v[76:77], v[104:105], 0 cbsz:4 abid:2\n" \
  "v_mfma_f32_4x4x4_16b_bf16 v[52:55], v[76:77], v[106:107], 0 cbsz:4 abid:2\n" \
  "v_cvt_pk_bf16_f32 v69, v68, v69\n" \
  "v_lshlrev_b32_sdwa v68, 1, v69 dst_sel:DWORD dst_unused:UNUSED_PAD src0_sel:DWORD src1_sel:WORD_0\n" \
  "v_lshlrev_b32_sdwa v69, 1, v69 dst_sel:DWORD dst_unused:UNUSED_PAD src0_sel:DWORD src1_sel:WORD_1\n" \
  "ds_read_u16_d16_hi v68, v68\n" \
  "ds_read_u16_d16_hi v69, v69\n" \
  "s_set_gpr_idx_on s61, gpr_idx(SRC2,DST)\n" \
  "v_pk_fma_f32 v[128:129], v[58:59], v[48:49], v[128:129] op_sel:[0,0,0] op_sel_hi:[0,1,1]\n" \
  "v_pk_fma_f32 v[130:131], v[58:59], v[50:51], v[130:131] op_sel:[0,0,0] op_sel_hi:[0,1,1]\n" \
  "v_pk_fma_f32 v[132:133], v[62:63], v[52:53], v[132:133] op_sel:[0,0,0] op_sel_hi:[0,1,1]\n" \
  "v_pk_fma_f32 v[134:135], v[62:63], v[54:55], v[134:135] op_sel:[0,0,0] op_sel_hi:[0,1,1]\n" \
  "s_set_gpr_idx_off\n" \
  "s_waitcnt vmcnt(6) lgkmcnt(2)\n" \
  "buffer_load_dwordx4 v[104:107], v[12:13], s[52:55], 0 idxen offen\n" \
  "ds_read_u16 v12, v14 offset:44\n" \
  "s_lshr_b32 s61, s61, 8\n" \
  "v_mfma_f32_4x4x4_16b_bf16 v[48:51], v[76:77], v[108:109], 0 cbsz:4 abid:3\n" \
  "v_mfma_f32_4x4x4_16b_bf16 v[52:55], v[76:77], v[110:111], 0 cbsz:4 abid:3\n" \
  "v_cvt_pk_bf16_f32 v67, v66, v67\n" \
  "v_lshlrev_b32_sdwa v66, 1, v67 dst_sel:DWORD dst_unused:UNUSED_PAD src0_sel:DWORD src1_sel:WORD_0\n" \
  "v_lshlrev_b32_sdwa v67, 1, v67 dst_sel:DWORD dst_unused:UNUSED_PAD src0_sel:DWORD src1_sel:WORD_1\n" \
  "ds_read_u16_d16_hi v66, v66\n" \
  "ds_read_u16_d16_hi v67, v67\n" \
  "s_set_gpr_idx_on s61, gpr_idx(SRC2,DST)\n" \
  "v_pk_fma_f32 v[128:129], v[58:59], v[48:49], v[128:129] op_sel:[1,0,0] op_sel_hi:[1,1,1]\n" \
  "v_pk_fma_f32 v[130:131], v[58:59], v[50:51], v[130:131] op_sel:[1,0,0] op_sel_hi:[1,1,1]\n" \
  "v_pk_fma_f32 v[132:133], v[62:63], v[52:53], v[132:133] op_sel:[1,0,0] op_sel_hi:[1,1,1]\n" \
  "v_pk_fma_f32 v[134:135], v[62:63], v[54:55], v[134:135] op_sel:[1,0,0] op_sel_hi:[1,1,1]\n" \
  "s_set_gpr_idx_off\n" \
  "s_waitcnt vmcnt(6) lgkmcnt(2)\n" \
  "buffer_load_dwordx4 v[108:111], v[12:13], s[52:55], 0 idxen offen\n" \
  "ds_read_u16 v12, v14 offset:48\n" \
  "s_sub_u32 s60, s60, 1\n" \
  "s_cbranch_scc1 Lend%=\n" \
  "v_readlane_b32 s61, v17, s60\n" \
  "s_setprio 2\n" \
  "v_mfma_f32_4x4x4_16b_bf16 v[56:59], v[92:93], v[32:33], 0 cbsz:4 abid:2\n" \
  "v_mfma_f32_4x4x4_16b_bf16 v[60:63], v[92:93], v[36:37], 0 cbsz:4 abid:2\n" \
  "v_mfma_f32_4x4x4_16b_bf16 v[48:51], v[76:77], v[112:113], 0 cbsz:4 abid:4\n" \
  "v_mfma_f32_4x4x4_16b_bf16 v[52:55], v[76:77], v[114:115], 0 cbsz:4 abid:4\n" \
  "v_cvt_pk_bf16_f32 v71, v70, v71\n" \
  "v_lshlrev_b32_sdwa v70, 1, v71 dst_sel:DWORD dst_unused:UNUSED_PAD src0_sel:DWORD src1_sel:WORD_0\n" \
  "v_lshlrev_b32_sdwa v71, 1, v71 dst_sel:DWORD dst_unused:UNUSED_PAD src0_sel:DWORD src1_sel:WORD_1\n" \
  "ds_read_u16_d16_hi v70, v70\n" \
  "ds_read_u16_d16_hi v71, v71\n" \
  "v_add_u32 v16, 0x100, v16\n" \
  "s_set_gpr_idx_on s61, gpr_idx(SRC2,DST)\n" \
  "v_pk_fma_f32 v[128:129], v[64:65], v[48:49], v[128:129] op_sel:[0,0,0] op_sel_hi:[0,1,1]\n" \
  "v_pk_fma_f32 v[130:131], v[64:65], v[50:51], v[130:131] op_sel:[0,0,0] op_sel_hi:[0,1,1]\n" \
  "v_pk_fma_f32 v[132:133], v[68:69], v[52:53], v[132:133] op_sel:[0,0,0] op_sel_hi:[0,1,1]\n" \
  "v_pk_fma_f32 v[134:135], v[68:69], v[54:55], v[134:135] op_sel:[0,0,0] op_sel_hi:[0,1,1]\n" \
  "s_set_gpr_idx_off\n" \
  "s_waitcnt vmcnt(6) lgkmcnt(2)\n" \
  "buffer_load_dwordx4 v[112:115], v[12:13], s[52:55], 0 idxen offen\n" \
  "ds_read_u16 v12, v14 offset:52\n" \
  "ds_read_b64 v[94:95], v15 offset:256\n" \
  "s_lshr_b32 s61, s61, 8\n" \
  "v_mfma_f32_4x4x4_16b_bf16 v[48:51], v[76:77], v[116:117], 0 cbsz:4 abid:5\n" \
  "v_mfma_f32_4x4x4_16b_bf16 v[52:55], v[76:77], v[118:119], 0 cbsz:4 abid:5\n" \
  "v_cvt_pk_bf16_f32 v57, v56, v57\n" \
  "v_lshlrev_b32_sdwa v56, 1, v57 dst_sel:DWORD dst_unused:UNUSED_PAD src0_sel:DWORD src1_sel:WORD_0\n" \
  "v_lshlrev_b32_sdwa v57, 1, v57 dst_sel:DWORD dst_unused:UNUSED_PAD src0_sel:DWORD src1_sel:WORD_1\n" \
  "ds_read_u16_d16_hi v56, v56\n" \
  "ds_read_u16_d16_hi v57, v57\n" \
  "s_set_gpr_idx_on s61, gpr_idx(SRC2,DST)\n" \
  "v_pk_fma_f32 v[128:129], v[64:65], v[48:49], v[128:129] op_sel:[1,0,0] op_sel_hi:[1,1,1]\n" \
  "v_pk_fma_f32 v[130:131], v[64:65], v[50:51], v[130:131] op_sel:[1,0,0] op_sel_hi:[1,1,1]\n" \
  "v_pk_fma_f32 v[132:133], v[68:69], v[52:53], v[132:133] op_sel:[1,0,0] op_sel_hi:[1,1,1]\n" \
  "v_pk_fma_f32 v[134:135], v[68:69], v[54:55], v[134:135] op_sel:[1,0,0] op_sel_hi:[1,1,1]\n" \
  "s_set_gpr_idx_off\n" \
  "s_waitcnt vmcnt(6) lgkmcnt(2)\n" \
  "buffer_load_dwordx4 v[116:119], v[12:13], s[52:55], 0 idxen offen\n" \
  "ds_read_u16 v12, v14 offset:56\n" \
  "s_lshr_b32 s61, s61, 8\n" \
  "v_mfma_f32_4x4x4_16b_bf16 v[48:51], v[76:77], v[120:121], 0 cbsz:4 abid:6\n" \
  "v_mfma_f32_4x4x4_16b_bf16 v[52:55], v[76:77], v[122:123], 0 cbsz:4 abid:6\n" \
  "v_cvt_pk_bf16_f32 v61, v60, v61\n" \
  "v_lshlrev_b32_sdwa v60, 1, v61 dst_sel:DWORD dst_unused:UNUSED_PAD src0_sel:DWORD src1_sel:WORD_0\n" \
  "v_lshlrev_b32_sdwa v61, 1, v61 dst_sel:DWORD dst_unused:UNUSED_PAD src0_sel:DWORD src1_sel:WORD_1\n" \
  "ds_read_u16_d16_hi v60, v60\n" \
  "ds_read_u16_d16_hi v61, v61\n" \
  "s_set_gpr_idx_on s61, gpr_idx(SRC2,DST)\n" \
  "v_pk_fma_f32 v[128:129], v[66:67], v[48:49], v[128:129] op_sel:[0,0,0] op_sel_hi:[0,1,1]\n" \
  "v_pk_fma_f32 v[130:131], v[66:67], v[50:51], v[130:131] op_sel:[0,0,0] op_sel_hi:[0,1,1]\n" \
  "v_pk_fma_f32 v[132:133], v[70:71], v[52:53], v[132:133] op_sel:[0,0,0] op_sel_hi:[0,1,1]\n" \
  "v_pk_fma_f32 v[134:135], v[70:71], v[54:55], v[134:135] op_sel:[0,0,0] op_sel_hi:[0,1,1]\n" \
  "s_set_gpr_idx_off\n" \
  "s_waitcnt vmcnt(6) lgkmcnt(2)\n" \
  "buffer_load_dwordx4 v[120:123], v[12:13], s[52:55], 0 idxen offen\n" \
  "ds_read_u16 v12, v14 offset:60\n" \
  "s_lshr_b32 s61, s61, 8\n" \
  "v_mfma_f32_4x4x4_16b_bf16 v[84:87], v[94:95], v[24:25], 0\n" \
  "v_mfma_f32_4x4x4_16b_bf16 v[48:51], v[76:77], v[124:125], 0 cbsz:4 abid:7\n" \
  "v_mfma_f32_4x4x4_16b_bf16 v[52:55], v[76:77], v[126:127], 0 cbsz:4 abid:7\n" \
  "v_cvt_pk_bf16_f32 v59, v58, v59\n" \
  "v_lshlrev_b32_sdwa v58, 1, v59 dst_sel:DWORD dst_unused:UNUSED_PAD src0_sel:DWORD src1_sel:WORD_0\n" \
  "v_lshlrev_b32_sdwa v59, 1, v59 dst_sel:DWORD dst_unused:UNUSED_PAD src0_sel:DWORD src1_sel:WORD_1\n" \
  "ds_read_u16_d16_hi v58, v58\n" \
  "ds_read_u16_d16_hi v59, v59\n" \
  "s_set_gpr_idx_on s61, gpr_idx(SRC2,DST)\n" \
  "v_pk_fma_f32 v[128:129], v[66:67], v[48:49], v[128:129] op_sel:[1,0,0] op_sel_hi:[1,1,1]\n" \
  "v_pk_fma_f32 v[130:131], v[66:67], v[50:51], v[130:131] op_sel:[1,0,0] op_sel_hi:[1,1,1]\n" \
  "v_pk_fma_f32 v[132:133], v[70:71], v[52:53], v[132:133] op_sel:[1,0,0] op_sel_hi:[1,1,1]\n" \
  "v_pk_fma_f32 v[134:135], v[70:71], v[54:55], v[134:135] op_sel:[1,0,0] op_sel_hi:[1,1,1]\n" \
  "s_set_gpr_idx_off\n" \
  "s_waitcnt vmcnt(6) lgkmcnt(2)\n" \
  "buffer_load_dwordx4 v[124:127], v[12:13], s[52:55], 0 idxen offen\n" \
  "ds_read_u16 v12, v14 offset:64\n" \
  "s_sub_u32 s60, s60, 1\n" \
  "s_cbranch_scc1 Lend%=\n" \
  "v_readlane_b32 s61, v17, s60\n" \
  "s_setprio 1\n" \
  "v_mfma_f32_4x4x4_16b_bf16 v[64:67], v[92:93], v[32:33], 0 cbsz:4 abid:3\n" \
  "v_mfma_f32_4x4x4_16b_bf16 v[68:71], v[92:93], v[36:37], 0 cbsz:4 abid:3\n" \
  "v_mfma_f32_4x4x4_16b_bf16 v[48:51], v[76:77], v[96:97], 0 cbsz:4 abid:8\n" \
  "v_mfma_f32_4x4x4_16b_bf16 v[52:55], v[76:77], v[98:99], 0 cbsz:4 abid:8\n" \
  "v_cvt_pk_bf16_f32 v63, v62, v63\n" \
  "v_lshlrev_b32_sdwa v62, 1, v63 dst_sel:DWORD dst_unused:UNUSED_PAD src0_sel:DWORD src1_sel:WORD_0\n" \
  "v_lshlrev_b32_sdwa v63, 1, v63 dst_sel:DWORD dst_unused:UNUSED_PAD src0_sel:DWORD src1_sel:WORD_1\n" \
  "ds_read_u16_d16_hi v62, v62\n" \
  "ds_read_u16_d16_hi v63, v63\n" \
  "v_add_u32 v15, 0x100, v15\n" \
  "s_set_gpr_idx_on s61, gpr_idx(SRC2,DST)\n" \
  "v_pk_fma_f32 v[128:129], v[56:57], v[48:49], v[128:129] op_sel:[0,0,0] op_sel_hi:[0,1,1]\n" \
  "v_pk_fma_f32 v[130:131], v[56:57], v[50:51], v[130:131] op_sel:[0,0,0] op_sel_hi:[0,1,1]\n" \
  "v_pk_fma_f32 v[132:133], v[60:61], v[52:53], v[132:133] op_sel:[0,0,0] op_sel_hi:[0,1,1]\n" \
  "v_pk_fma_f32 v[134:135], v[60:61], v[54:55], v[134:135] op_sel:[0,0,0] op_sel_hi:[0,1,1]\n" \
  "s_set_gpr_idx_off\n" \
  "s_waitcnt vmcnt(6) lgkmcnt(2)\n" \
  "buffer_load_dwordx4 v[96:99], v[12:13], s[52:55], 0 idxen offen\n" \
  "ds_read_u16 v12, v14 offset:68\n" \
  "s_lshr_b32 s61, s61, 8\n" \
  "v_mfma_f32_4x4x4_16b_bf16 v[48:51], v[76:77], v[100:101], 0 cbsz:4 abid:9\n" \
  "v_mfma_f32_4x4x4_16b_bf16 v[52:55], v[76:77], v[102:103], 0 cbsz:4 abid:9\n" \
  "v_cvt_pk_bf16_f32 v65, v64, v65\n" \
  "v_lshlrev_b32_sdwa v64, 1, v65 dst_sel:DWORD dst_unused:UNUSED_PAD src0_sel:DWORD src1_sel:WORD_0\n" \
  "v_lshlrev_b32_sdwa v65, 1, v65 dst_sel:DWORD dst_unused:UNUSED_PAD src0_sel:DWORD src1_sel:WORD_1\n" \
  "ds_read_u16_d16_hi v64, v64\n" \
  "ds_read_u16_d16_hi v65, v65\n" \
  "s_set_gpr_idx_on s61, gpr_idx(SRC2,DST)\n" \
  "v_pk_fma_f32 v[128:129], v[56:57], v[48:49], v[128:129] op_sel:[1,0,0] op_sel_hi:[1,1,1]\n" \
  "v_pk_fma_f32 v[130:131], v[56:57], v[50:51], v[130:131] op_sel:[1,0,0] op_sel_hi:[1,1,1]\n" \
  "v_pk_fma_f32 v[132:133], v[60:61], v[52:53], v[132:133] op_sel:[1,0,0] op_sel_hi:[1,1,1]\n" \
  "v_pk_fma_f32 v[134:135], v[60:61], v[54:55], v[134:135] op_sel:[1,0,0] op_sel_hi:[1,1,1]\n" \
  "s_set_gpr_idx_off\n" \
  "s_waitcnt vmcnt(6) lgkmcnt(2)\n" \
  "buffer_load_dwordx4 v[100:103], v[12:13], s[52:55], 0 idxen offen\n" \
  "ds_read_u16 v12, v14 offset:72\n" \
  "s_lshr_b32 s61, s61, 8\n" \
  "v_mfma_f32_4x4x4_16b_bf16 v[48:51], v[76:77], v[104:105], 0 cbsz:4 abid:10\n" \
  "v_mfma_f32_4x4x4_16b_bf16 v[52:55], v[76:77], v[106:107], 0 cbsz:4 abid:10\n" \
  "v_cvt_pk_bf16_f32 v69, v68, v69\n" \
  "v_lshlrev_b32_sdwa v68, 1, v69 dst_sel:DWORD dst_unused:UNUSED_PAD src0_sel:DWORD src1_sel:WORD_0\n" \
  "v_lshlrev_b32_sdwa v69, 1, v69 dst_sel:DWORD dst_unused:UNUSED_PAD src0_sel:DWORD src1_sel:WORD_1\n" \
  "ds_read_u16_d16_hi v68, v68\n" \
  "ds_read_u16_d16_hi v69, v69\n" \
  "s_set_gpr_idx_on s61, gpr_idx(SRC2,DST)\n" \
  "v_pk_fma_f32 v[128:129], v[58:59], v[48:49], v[128:129] op_sel:[0,0,0] op_sel_hi:[0,1,1]\n" \
  "v_pk_fma_f32 v[130:131], v[58:59], v[50:51], v[130:131] op_sel:[0,0,0] op_sel_hi:[0,1,1]\n" \
  "v_pk_fma_f32 v[132:133], v[62:63], v[52:53], v[132:133] op_sel:[0,0,0] op_sel_hi:[0,1,1]\n" \
  "v_pk_fma_f32 v[134:135], v[62:63], v[54:55], v[134:135] op_sel:[0,0,0] op_sel_hi:[0,1,1]\n" \
  "s_set_gpr_idx_off\n" \
  "s_waitcnt vmcnt(6) lgkmcnt(2)\n" \
  "buffer_load_dwordx4 v[104:107], v[12:13], s[52:55], 0 idxen offen\n" \
  "ds_read_u16 v12, v14 offset:76\n" \
  "ds_read_b64 v[92:93], v16\n" \
  "s_lshr_b32 s61, s61, 8\n" \
  "v_mfma_f32_4x4x4_16b_bf16 v[88:91], v[94:95], v[26:27], 0\n" \
  "v_mfma_f32_4x4x4_16b_bf16 v[48:51], v[76:77], v[108:109], 0 cbsz:4 abid:11\n" \
  "v_mfma_f32_4x4x4_16b_bf16 v[52:55], v[76:77], v[110:111], 0 cbsz:4 abid:11\n" \
  "v_cvt_pk_bf16_f32 v67, v66, v67\n" \
  "v_lshlrev_b32_sdwa v66, 1, v67 dst_sel:DWORD dst_unused:UNUSED_PAD src0_sel:DWORD src1_sel:WORD_0\n" \
  "v_lshlrev_b32_sdwa v67, 1, v67 dst_sel:DWORD dst_unused:UNUSED_PAD src0_sel:DWORD src1_sel:WORD_1\n" \
  "ds_read_u16_d16_hi v66, v66\n" \
  "ds_read_u16_d16_hi v67, v67\n" \
  "s_set_gpr_idx_on s61, gpr_idx(SRC2,DST)\n" \
  "v_pk_fma_f32 v[128:129], v[58:59], v[48:49], v[128:129] op_sel:[1,0,0] op_sel_hi:[1,1,1]\n" \
  "v_pk_fma_f32 v[130:131], v[58:59], v[50:51], v[130:131] op_sel:[1,0,0] op_sel_hi:[1,1,1]\n" \
  "v_pk_fma_f32 v[132:133], v[62:63], v[52:53], v[132:133] op_sel:[1,0,0] op_sel_hi:[1,1,1]\n" \
  "v_pk_fma_f32 v[134:135], v[62:63], v[54:55], v[134:135] op_sel:[1,0,0] op_sel_hi:[1,1,1]\n" \
  "s_set_gpr_idx_off\n" \
  "s_waitcnt vmcnt(6) lgkmcnt(2)\n" \
  "buffer_load_dwordx4 v[108:111], v[12:13], s[52:55], 0 idxen offen\n" \
  "ds_read_u16 v12, v14 offset:80\n" \
  "s_sub_u32 s60, s60, 1\n" \
  "s_cbranch_scc1 Lend%=\n" \
  "v_readlane_b32 s61, v17, s60\n" \
  "s_setprio 0\n" \
  "v_mfma_f32_4x4x4_16b_bf16 v[56:59], v[92:93], v[32:33], 0 cbsz:4\n" \
  "v_mfma_f32_4x4x4_16b_bf16 v[60:63], v[92:93], v[36:37], 0 cbsz:4\n" \
  "v_mfma_f32_4x4x4_16b_bf16 v[48:51], v[76:77], v[112:113], 0 cbsz:4 abid:12\n" \
  "v_mfma_f32_4x4x4_16b_bf16 v[52:55], v[76:77], v[114:115], 0 cbsz:4 abid:12\n" \
  "v_cvt_pk_bf16_f32 v71, v70, v71\n" \
  "v_lshlrev_b32_sdwa v70, 1, v71 dst_sel:DWORD dst_unused:UNUSED_PAD src0_sel:DWORD src1_sel:WORD_0\n" \
  "v_lshlrev_b32_sdwa v71, 1, v71 dst_sel:DWORD dst_unused:UNUSED_PAD src0_sel:DWORD src1_sel:WORD_1\n" \
  "ds_read_u16_d16_hi v70, v70\n" \
  "ds_read_u16_d16_hi v71, v71\n" \
  "v_cvt_pk_bf16_f32 v20, v84, v88\n" \
  "s_set_gpr_idx_on s61, gpr_idx(SRC2,DST)\n" \
  "v_pk_fma_f32 v[128:129], v[64:65], v[48:49], v[128:129] op_sel:[0,0,0] op_sel_hi:[0,1,1]\n" \
  "v_pk_fma_f32 v[130:131], v[64:65], v[50:51], v[130:131] op_sel:[0,0,0] op_sel_hi:[0,1,1]\n" \
  "v_pk_fma_f32 v[132:133], v[68:69], v[52:53], v[132:133] op_sel:[0,0,0] op_sel_hi:[0,1,1]\n" \
  "v_pk_fma_f32 v[134:135], v[68:69], v[54:55], v[134:135] op_sel:[0,0,0] op_sel_hi:[0,1,1]\n" \
  "s_set_gpr_idx_off\n" \
  "s_waitcnt vmcnt(6) lgkmcnt(2)\n" \
  "buffer_load_dwordx4 v[112:115], v[12:13], s[52:55], 0 idxen offen\n" \
  "ds_read_u16 v12, v14 offset:84\n" \
  "s_lshr_b32 s61, s61, 8\n" \
  "v_mfma_f32_4x4x4_16b_bf16 v[48:51], v[76:77], v[116:117], 0 cbsz:4 abid:13\n" \
  "v_mfma_f32_4x4x4_16b_bf16 v[52:55], v[76:77], v[118:119], 0 cbsz:4 abid:13\n" \
  "v_cvt_pk_bf16_f32 v57, v56, v57\n" \
  "v_lshlrev_b32_sdwa v56, 1, v57 dst_sel:DWORD dst_unused:UNUSED_PAD src0_sel:DWORD src1_sel:WORD_0\n" \
  "v_lshlrev_b32_sdwa v57, 1, v57 dst_sel:DWORD dst_unused:UNUSED_PAD src0_sel:DWORD src1_sel:WORD_1\n" \
  "ds_read_u16_d16_hi v56, v56\n" \
  "ds_read_u16_d16_hi v57, v57\n" \
  "s_set_gpr_idx_on s61, gpr_idx(SRC2,DST)\n" \
  "v_pk_fma_f32 v[128:129], v[64:65], v[48:49], v[128:129] op_sel:[1,0,0] op_sel_hi:[1,1,1]\n" \
  "v_pk_fma_f32 v[130:131], v[64:65], v[50:51], v[130:131] op_sel:[1,0,0] op_sel_hi:[1,1,1]\n" \
  "v_pk_fma_f32 v[132:133], v[68:69], v[52:53], v[132:133] op_sel:[1,0,0] op_sel_hi:[1,1,1]\n" \
  "v_pk_fma_f32 v[134:135], v[68:69], v[54:55], v[134:135] op_sel:[1,0,0] op_sel_hi:[1,1,1]\n" \
  "s_set_gpr_idx_off\n" \
  "s_waitcnt vmcnt(6) lgkmcnt(2)\n" \
  "buffer_load_dwordx4 v[116:119], v[12:13], s[52:55], 0 idxen offen\n" \
  "ds_read_u16 v12, v14 offset:88\n" \
  "s_lshr_b32 s61, s61, 8\n" \
  "v_mfma_f32_4x4x4_16b_bf16 v[48:51], v[76:77], v[120:121], 0 cbsz:4 abid:14\n" \
  "v_mfma_f32_4x4x4_16b_bf16 v[52:55], v[76:77], v[122:123], 0 cbsz:4 abid:14\n" \
  "v_cvt_pk_bf16_f32 v61, v60, v61\n" \
  "v_lshlrev_b32_sdwa v60, 1, v61 dst_sel:DWORD dst_unused:UNUSED_PAD src0_sel:DWORD src1_sel:WORD_0\n" \
  "v_lshlrev_b32_sdwa v61, 1, v61 dst_sel:DWORD dst_unused:UNUSED_PAD src0_sel:DWORD src1_sel:WORD_1\n" \
  "ds_read_u16_d16_hi v60, v60\n" \
  "ds_read_u16_d16_hi v61, v61\n" \
  "s_set_gpr_idx_on s61, gpr_idx(SRC2,DST)\n" \
  "v_pk_fma_f32 v[128:129], v[66:67], v[48:49], v[128:129] op_sel:[0,0,0] op_sel_hi:[0,1,1]\n" \
  "v_pk_fma_f32 v[130:131], v[66:67], v[50:51], v[130:131] op_sel:[0,0,0] op_sel_hi:[0,1,1]\n" \
  "v_pk_fma_f32 v[132:133], v[70:71], v[52:53], v[132:133] op_sel:[0,0,0] op_sel_hi:[0,1,1]\n" \
  "v_pk_fma_f32 v[134:135], v[70:71], v[54:55], v[134:135] op_sel:[0,0,0] op_sel_hi:[0,1,1]\n" \
  "s_set_gpr_idx_off\n" \
  "s_waitcnt vmcnt(6) lgkmcnt(2)\n" \
  "buffer_load_dwordx4 v[120:123], v[12:13], s[52:55], 0 idxen offen\n" \
  "ds_read_u16 v12, v14 offset:92\n" \
  "s_lshr_b32 s61, s61, 8\n" \
  "v_mfma_f32_4x4x4_16b_bf16 v[84:87], v[94:95], v[28:29], 0\n" \
  "v_mfma_f32_4x4x4_16b_bf16 v[88:91], v[94:95], v[30:31], 0\n" \
  "v_mfma_f32_4x4x4_16b_bf16 v[48:51], v[76:77], v[124:125], 0 cbsz:4 abid:15\n" \
  "v_mfma_f32_4x4x4_16b_bf16 v[52:55], v[76:77], v[126:127], 0 cbsz:4 abid:15\n" \
  "v_cvt_pk_bf16_f32 v59, v58, v59\n" \
  "v_lshlrev_b32_sdwa v58, 1, v59 dst_sel:DWORD dst_unused:UNUSED_PAD src0_sel:DWORD src1_sel:WORD_0\n" \
  "v_lshlrev_b32_sdwa v59, 1, v59 dst_sel:DWORD dst_unused:UNUSED_PAD src0_sel:DWORD src1_sel:WORD_1\n" \
  "ds_read_u16_d16_hi v58, v58\n" \
  "ds_read_u16_d16_hi v59, v59\n" \
  "v_cvt_pk_bf16_f32 v77, v84, v88\n" \
  "v_mov_b32 v76, v20\n" \
  "s_set_gpr_idx_on s61, gpr_idx(SRC2,DST)\n" \
  "v_pk_fma_f32 v[128:129], v[66:67], v[48:49], v[128:129] op_sel:[1,0,0] op_sel_hi:[1,1,1]\n" \
  "v_pk_fma_f32 v[130:131], v[66:67], v[50:51], v[130:131] op_sel:[1,0,0] op_sel_hi:[1,1,1]\n" \
  "v_pk_fma_f32 v[132:133], v[70:71], v[52:53], v[132:133] op_sel:[1,0,0] op_sel_hi:[1,1,1]\n" \
  "v_pk_fma_f32 v[134:135], v[70:71], v[54:55], v[134:135] op_sel:[1,0,0] op_sel_hi:[1,1,1]\n" \
  "s_set_gpr_idx_off\n" \
  "s_waitcnt vmcnt(6) lgkmcnt(2)\n" \
  "buffer_load_dwordx4 v[124:127], v[12:13], s[52:55], 0 idxen offen\n" \
  "ds_read_u16 v12, v14 offset:96\n" \
  "s_sub_u32 s60, s60, 1\n" \
  "s_cbranch_scc1 Lend%=\n" \
  "v_readlane_b32 s61, v17, s60\n" \
  "s_setprio 3\n" \
  "v_mfma_f32_4x4x4_16b_bf16 v[64:67], v[92:93], v[32:33], 0 cbsz:4 abid:1\n" \
  "v_mfma_f32_4x4x4_16b_bf16 v[68:71], v[92:93], v[36:37], 0 cbsz:4 abid:1\n" \
  "v_mfma_f32_4x4x4_16b_bf16 v[48:51], v[76:77], v[96:97], 0 cbsz:4\n" \
  "v_mfma_f32_4x4x4_16b_bf16 v[52:55], v[76:77], v[98:99], 0 cbsz:4\n" \
  "v_cvt_pk_bf16_f32 v63, v62, v63\n" \
  "v_lshlrev_b32_sdwa v62, 1, v63 dst_sel:DWORD dst_unused:UNUSED_PAD src0_sel:DWORD src1_sel:WORD_0\n" \
  "v_lshlrev_b32_sdwa v63, 1, v63 dst_sel:DWORD dst_unused:UNUSED_PAD src0_sel:DWORD src1_sel:WORD_1\n" \
  "ds_read_u16_d16_hi v62, v62\n" \
  "ds_read_u16_d16_hi v63, v63\n" \
  "v_add_u32 v14, 64, v14\n" \
  "s_branch Lbody%=\n" \
  "Lep0_%=:\n" \
  "ds_read_b32 v50, v18\n" \
  "s_waitcnt lgkmcnt(0)\n" \
  "v_readfirstlane_b32 s63, v50\n" \
  "s_bitcmp1_b32 s63, 1\n" \
  "s_cbranch_scc1 Lgo%=\n" \
  "v_mov_b64 v[50:51], s[50:51]\n" \
  "s_mov_b64 exec, 1\n" \
  "buffer_store_dwordx2 v[50:51], off, %[wsd], s70 sc1\n" \
  "s_mov_b64 exec, -1\n" \
  "s_nop 3\n" \
  "s_branch Lgo%=\n" \
  "Lbad%=:\n" \
  "v_mov_b32 v72, 1\n" \
  "v_mov_b32 v73, s62\n" \
  "ds_or_b32 v18, v72\n" \
  "ds_write_b32 v73, v73\n" \
  "s_branch Lf9_%=\n" \
  "Lbad2_%=:\n" \
  "s_waitcnt vmcnt(0) lgkmcnt(0)\n" \
  "s_bitcmp1_b32 %[bad], 1\n" \
  "s_cbranch_scc1 Lout%=\n" \
  "s_branch Lep%=\n" \
  "Lend%=:\n" \
  "s_and_b32 s63, %[w], 3\n" \
  "s_lshl_b32 s63, s63, 11\n" \
  "buffer_load_dwordx4 v[96:99], v13, %[wsd], s63 offen offset:0 sc1\n" \
  "buffer_load_dwordx4 v[100:103], v13, %[wsd], s63 offen offset:1024 sc1\n" \
  "s_add_u32 s63, s63, 8192\n" \
  "buffer_load_dwordx4 v[104:107], v13, %[wsd], s63 offen offset:0 sc1\n" \
  "buffer_load_dwordx4 v[108:111], v13, %[wsd], s63 offen offset:1024 sc1\n" \
  "s_add_u32 s63, s63, 8192\n" \
  "buffer_load_dwordx4 v[112:115], v13, %[wsd], s63 offen offset:0 sc1\n" \
  "buffer_load_dwordx4 v[116:119], v13, %[wsd], s63 offen offset:1024 sc1\n" \
  "s_add_u32 s63, s63, 8192\n" \
  "buffer_load_dwordx4 v[120:123], v13, %[wsd], s63 offen offset:0 sc1\n" \
  "buffer_load_dwordx4 v[124:127], v13, %[wsd], s63 offen offset:1024 sc1\n" \
  "buffer_load_dword v20, off, %[wsd], s70 sc1\n" \
  "s_lshl2_add_u32 s62, %[w], 35872\n" \
  "s_cmp_ge_u32 s74, 10\n" \
  "s_cbranch_scc1 Lbad%=\n" \
  "s_cmp_lt_u32 s93, %[w]\n" \
  "s_cbranch_scc0 Lf1_%=\n" \
  "s_set_gpr_idx_on s73, gpr_idx(SRC0)\n" \
  "v_mov_b32 v84, v128\n" \
  "v_mov_b32 v85, v129\n" \
  "v_mov_b32 v86, v130\n" \
  "v_mov_b32 v87, v131\n" \
  "v_mov_b32 v88, v132\n" \
  "v_mov_b32 v89, v133\n" \
  "v_mov_b32 v90, v134\n" \
  "v_mov_b32 v91, v135\n" \
  "s_set_gpr_idx_off\n" \
  "ds_write_b128 v21, v[84:87]\n" \
  "ds_write_b128 v21, v[88:91] offset:1024\n" \
  "v_mov_b32 v53, s62\n" \
  "ds_write_b32 v53, v53\n" \
  "s_add_u32 s73, s73, 8\n" \
  "s_cmp_eq_u32 s73, 80\n" \
  "s_cselect_b32 s73, 0, s73\n" \
  "s_add_u32 s72, s72, 1056\n" \
  "s_sub_u32 s74, s74, 1\n" \
  "s_cbranch_scc1 Lf9_%=\n" \
  "Lf1_%=:\n" \
  "s_min_u32 s63, s75, 1\n" \
  "s_sub_u32 s60, s74, s63\n" \
  "s_cbranch_scc1 Lf3_%=\n" \
  "Lf2_%=:\n" \
  "s_set_gpr_idx_on s73, gpr_idx(SRC0,SRC1)\n" \
  "v_cvt_pk_bf16_f32 v48, v128, v132\n" \
  "v_cvt_pk_bf16_f32 v49, v129, v133\n" \
  "v_cvt_pk_bf16_f32 v50, v130, v134\n" \
  "v_cvt_pk_bf16_f32 v51, v131, v135\n" \
  "s_set_gpr_idx_off\n" \
  "v_add_u32 v52, s72, v19\n" \
  "s_add_u32 s73, s73, 8\n" \
  "s_cmp_eq_u32 s73, 80\n" \
  "s_cselect_b32 s73, 0, s73\n" \
  "s_add_u32 s72, s72, 1056\n" \
  "ds_write2_b32 v52, v48, v49 offset1:16\n" \
  "ds_write2_b32 v52, v50, v51 offset0:32 offset1:48\n" \
  "s_sub_u32 s60, s60, 1\n" \
  "s_cbranch_scc0 Lf2_%=\n" \
  "Lf3_%=:\n" \
  "s_set_gpr_idx_on s73, gpr_idx(SRC0)\n" \
  "v_mov_b32 v40, v128\n" \
  "v_mov_b32 v41, v129\n" \
  "v_mov_b32 v42, v130\n" \
  "v_mov_b32 v43, v131\n" \
  "v_mov_b32 v44, v132\n" \
  "v_mov_b32 v45, v133\n" \
  "v_mov_b32 v46, v134\n" \
  "v_mov_b32 v47, v135\n" \
  "s_set_gpr_idx_off\n" \
  "s_cmp_eq_u32 s75, 0\n" \
  "s_cbranch_scc1 Lf9_%=\n" \
  "s_mov_b32 s60, s75\n" \
  "v_mov_b32 v72, s62\n" \
  "Lf4_%=:\n" \
  "ds_read_b32 v73, v72 offset:4\n" \
  "ds_read_b128 v[84:87], v21 offset:3328\n" \
  "ds_read_b128 v[88:91], v21 offset:4352\n" \
  "s_waitcnt lgkmcnt(0)\n" \
  "v_readfirstlane_b32 s63, v73\n" \
  "s_cmp_eq_u32 s63, 0\n" \
  "s_cbranch_scc0 Lf5_%=\n" \
  "s_sleep 1\n" \
  "s_branch Lf4_%=\n" \
  "Lf5_%=:\n" \
  "v_pk_add_f32 v[40:41], v[40:41], v[84:85]\n" \
  "v_pk_add_f32 v[42:43], v[42:43], v[86:87]\n" \
  "v_pk_add_f32 v[44:45], v[44:45], v[88:89]\n" \
  "v_pk_add_f32 v[46:47], v[46:47], v[90:91]\n" \
  "v_add_u32 v72, 4, v72\n" \
  "v_add_u32 v21, 3328, v21\n" \
  "s_sub_u32 s60, s60, 1\n" \
  "s_cmp_lg_u32 s60, 0\n" \
  "s_cbranch_scc1 Lf4_%=\n" \
  "v_cvt_pk_bf16_f32 v48, v40, v44\n" \
  "v_cvt_pk_bf16_f32 v49, v41, v45\n" \
  "v_cvt_pk_bf16_f32 v50, v42, v46\n" \
  "v_cvt_pk_bf16_f32 v51, v43, v47\n" \
  "v_add_u32 v52, s72, v19\n" \
  "ds_write2_b32 v52, v48, v49 offset1:16\n" \
  "ds_write2_b32 v52, v50, v51 offset0:32 offset1:48\n" \
  "Lf9_%=:\n" \
  "s_waitcnt lgkmcnt(0)\n" \
  "s_barrier\n" \
  "ds_read_b32 v18, v18\n" \
  "ds_read_b128 v[128:131], v22 offset:0\n" \
  "ds_read_b128 v[132:135], v22 offset:256\n" \
  "ds_read_b128 v[136:139], v22 offset:512\n" \
  "ds_read_b128 v[140:143], v22 offset:768\n" \
  "ds_read_b128 v[144:147], v22 offset:8448\n" \
  "ds_read_b128 v[148:151], v22 offset:8704\n" \
  "ds_read_b128 v[152:155], v22 offset:8960\n" \
  "ds_read_b128 v[156:159], v22 offset:9216\n" \
  "ds_read_b128 v[160:163], v22 offset:16896\n" \
  "ds_read_b128 v[164:167], v22 offset:17152\n" \
  "ds_read_b128 v[168:171], v22 offset:17408\n" \
  "ds_read_b128 v[172:175], v22 offset:17664\n" \
  "ds_read_b128 v[176:179], v22 offset:25344\n" \
  "ds_read_b128 v[180:183], v22 offset:25600\n" \
  "s_waitcnt lgkmcnt(14)\n" \
  "v_readfirstlane_b32 %[bad], v18\n" \
  "s_cmp_lg_u32 %[bad], 0\n" \
  "s_cbranch_scc1 Lbad2_%=\n" \
  "s_waitcnt vmcnt(7) lgkmcnt(13)\n" \
  "v_mfma_f32_16x16x32_bf16 v[40:43], v[128:131], v[96:99], 0\n" \
  "v_mfma_f32_16x16x32_bf16 v[44:47], v[128:131], v[100:103], 0\n" \
  "ds_read_b128 v[184:187], v22 offset:25856\n" \
  "s_waitcnt vmcnt(5) lgkmcnt(13)\n" \
  "v_mfma_f32_16x16x32_bf16 v[40:43], v[132:135], v[104:107], v[40:43]\n" \
  "v_mfma_f32_16x16x32_bf16 v[44:47], v[132:135], v[108:111], v[44:47]\n" \
  "ds_read_b128 v[188:191], v22 offset:26112\n" \
  "s_waitcnt vmcnt(3) lgkmcnt(13)\n" \
  "v_mfma_f32_16x16x32_bf16 v[40:43], v[136:139], v[112:115], v[40:43]\n" \
  "v_mfma_f32_16x16x32_bf16 v[44:47], v[136:139], v[116:119], v[44:47]\n" \
  "ds_read_b128 v[192:195], v22 offset:33792\n" \
  "s_waitcnt vmcnt(1) lgkmcnt(13)\n" \
  "v_mfma_f32_16x16x32_bf16 v[40:43], v[140:143], v[120:123], v[40:43]\n" \
  "v_mfma_f32_16x16x32_bf16 v[44:47], v[140:143], v[124:127], v[44:47]\n" \
  "ds_read_b128 v[196:199], v22 offset:34048\n" \
  "s_waitcnt lgkmcnt(13)\n" \
  "v_mfma_f32_16x16x32_bf16 v[48:51], v[144:147], v[96:99], 0\n" \
  "v_mfma_f32_16x16x32_bf16 v[52:55], v[144:147], v[100:103], 0\n" \
  "ds_read_b128 v[200:203], v22 offset:34304\n" \
  "s_waitcnt lgkmcnt(13)\n" \
  "v_mfma_f32_16x16x32_bf16 v[48:51], v[148:151], v[104:107], v[48:51]\n" \
  "v_mfma_f32_16x16x32_bf16 v[52:55], v[148:151], v[108:111], v[52:55]\n" \
  "ds_read_b128 v[204:207], v22 offset:34560\n" \
  "s_waitcnt lgkmcnt(13)\n" \
  "v_mfma_f32_16x16x32_bf16 v[48:51], v[152:155], v[112:115], v[48:51]\n" \
  "v_mfma_f32_16x16x32_bf16 v[52:55], v[152:155], v[116:119], v[52:55]\n" \
  "v_cvt_pk_bf16_f32 v40, v40, v41\n" \
  "v_cvt_pk_bf16_f32 v41, v42, v43\n" \
  "v_cvt_pk_bf16_f32 v42, v44, v45\n" \
  "v_cvt_pk_bf16_f32 v43, v46, v47\n" \
  "buffer_store_dwordx4 v[40:43], v23, s[56:59], 0 offen sc1\n" \
  "s_waitcnt lgkmcnt(12)\n" \
  "v_mfma_f32_16x16x32_bf16 v[48:51], v[156:159], v[120:123], v[48:51]\n" \
  "v_mfma_f32_16x16x32_bf16 v[52:55], v[156:159], v[124:127], v[52:55]\n" \
  "s_waitcnt lgkmcnt(11)\n" \
  "v_mfma_f32_16x16x32_bf16 v[56:59], v[160:163], v[96:99], 0\n" \
  "v_mfma_f32_16x16x32_bf16 v[60:63], v[160:163], v[100:103], 0\n" \
  "s_waitcnt lgkmcnt(10)\n" \
  "v_mfma_f32_16x16x32_bf16 v[56:59], v[164:167], v[104:107], v[56:59]\n" \
  "v_mfma_f32_16x16x32_bf16 v[60:63], v[164:167], v[108:111], v[60:63]\n" \
  "s_waitcnt lgkmcnt(9)\n" \
  "v_mfma_f32_16x16x32_bf16 v[56:59], v[168:171], v[112:115], v[56:59]\n" \
  "v_mfma_f32_16x16x32_bf16 v[60:63], v[168:171], v[116:119], v[60:63]\n" \
  "v_cvt_pk_bf16_f32 v48, v48, v49\n" \
  "v_cvt_pk_bf16_f32 v49, v50, v51\n" \
  "v_cvt_pk_bf16_f32 v50, v52, v53\n" \
  "v_cvt_pk_bf16_f32 v51, v54, v55\n" \
  "s_movk_i32 s63, 0x1000\n" \
  "s_lshl_b32 s63, s63, 1\n" \
  "buffer_store_dwordx4 v[48:51], v23, s[56:59], s63 offen sc1\n" \
  "s_waitcnt lgkmcnt(8)\n" \
  "v_mfma_f32_16x16x32_bf16 v[56:59], v[172:175], v[120:123], v[56:59]\n" \
  "v_mfma_f32_16x16x32_bf16 v[60:63], v[172:175], v[124:127], v[60:63]\n" \
  "s_waitcnt lgkmcnt(7)\n" \
  "v_mfma_f32_16x16x32_bf16 v[64:67], v[176:179], v[96:99], 0\n" \
  "v_mfma_f32_16x16x32_bf16 v[68:71], v[176:179], v[100:103], 0\n" \
  "s_waitcnt lgkmcnt(6)\n" \
  "v_mfma_f32_16x16x32_bf16 v[64:67], v[180:183], v[104:107], v[64:67]\n" \
  "v_mfma_f32_16x16x32_bf16 v[68:71], v[180:183], v[108:111], v[68:71]\n" \
  "s_waitcnt lgkmcnt(5)\n" \
  "v_mfma_f32_16x16x32_bf16 v[64:67], v[184:187], v[112:115], v[64:67]\n" \
  "v_mfma_f32_16x16x32_bf16 v[68:71], v[184:187], v[116:119], v[68:71]\n" \
  "v_cvt_pk_bf16_f32 v56, v56, v57\n" \
  "v_cvt_pk_bf16_f32 v57, v58, v59\n" \
  "v_cvt_pk_bf16_f32 v58, v60, v61\n" \
  "v_cvt_pk_bf16_f32 v59, v62, v63\n" \
  "s_movk_i32 s63, 0x2000\n" \
  "s_lshl_b32 s63, s63, 1\n" \
  "buffer_store_dwordx4 v[56:59], v23, s[56:59], s63 offen sc1\n" \
  "s_waitcnt lgkmcnt(4)\n" \
  "v_mfma_f32_16x16x32_bf16 v[64:67], v[188:191], v[120:123], v[64:67]\n" \
  "v_mfma_f32_16x16x32_bf16 v[68:71], v[188:191], v[124:127], v[68:71]\n" \
  "s_waitcnt lgkmcnt(3)\n" \
  "v_mfma_f32_16x16x32_bf16 v[72:75], v[192:195], v[96:99], 0\n" \
  "v_mfma_f32_16x16x32_bf16 v[76:79], v[192:195], v[100:103], 0\n" \
  "s_waitcnt lgkmcnt(2)\n" \
  "v_mfma_f32_16x16x32_bf16 v[72:75], v[196:199], v[104:107], v[72:75]\n" \
  "v_mfma_f32_16x16x32_bf16 v[76:79], v[196:199], v[108:111], v[76:79]\n" \
  "s_waitcnt lgkmcnt(1)\n" \
  "v_mfma_f32_16x16x32_bf16 v[72:75], v[200:203], v[112:115], v[72:75]\n" \
  "v_mfma_f32_16x16x32_bf16 v[76:79], v[200:203], v[116:119], v[76:79]\n" \
  "v_cvt_pk_bf16_f32 v64, v64, v65\n" \
  "v_cvt_pk_bf16_f32 v65, v66, v67\n" \
  "v_cvt_pk_bf16_f32 v66, v68, v69\n" \
  "v_cvt_pk_bf16_f32 v67, v70, v71\n" \
  "s_movk_i32 s63, 0x3000\n" \
  "s_lshl_b32 s63, s63, 1\n" \
  "buffer_store_dwordx4 v[64:67], v23, s[56:59], s63 offen sc1\n" \
  "s_waitcnt lgkmcnt(0)\n" \
  "v_mfma_f32_16x16x32_bf16 v[72:75], v[204:207], v[120:123], v[72:75]\n" \
  "v_mfma_f32_16x16x32_bf16 v[76:79], v[204:207], v[124:127], v[76:79]\n" \
  "s_nop 7\n" \
  "v_cvt_pk_bf16_f32 v72, v72, v73\n" \
  "v_cvt_pk_bf16_f32 v73, v74, v75\n" \
  "v_cvt_pk_bf16_f32 v74, v76, v77\n" \
  "v_cvt_pk_bf16_f32 v75, v78, v79\n" \
  "s_movk_i32 s63, 0x4000\n" \
  "s_lshl_b32 s63, s63, 1\n" \
  "buffer_store_dwordx4 v[72:75], v23, s[56:59], s63 offen sc1\n" \
  "s_waitcnt vmcnt(5)\n" \
  "v_readfirstlane_b32 s63, v20\n" \
  "s_cmp_eq_u32 s63, s50\n" \
  "s_cbranch_scc1 Lout%=\n" \
  "Lep%=:\n" \
  "s_cmp_lg_u32 %[w], 0\n" \
  "s_cbranch_scc1 Lout%=\n" \
  "v_mov_b64 v[84:85], s[50:51]\n" \
  "s_mov_b64 exec, 1\n" \
  "buffer_store_dwordx2 v[84:85], off, %[wsd], s70 sc1\n" \
  "s_mov_b64 exec, -1\n" \
  "Lout%=:\n" \
  "s_mov_b32 m0, s79\n"
#define STREAM_CLOBBERS \
  "v12", "v13", "v14", "v15", "v16", "v17", "v18", "v19", "v20", "v21", "v22", "v23", "v24", "v25", "v26", \
  "v27", "v28", "v29", "v30", "v31", "v32", "v33", "v34", "v35", "v36", "v37", "v38", "v39", "v40", "v41", \
  "v42", "v43", "v44", "v45", "v46", "v47", "v48", "v49", "v50", "v51", "v52", "v53", "v54", "v55", "v56", \
  "v57", "v58", "v59", "v60", "v61", "v62", "v63", "v64", "v65", "v66", "v67", "v68", "v69", "v70", "v71", \
  "v72", "v73", "v74", "v75", "v76", "v77", "v78", "v79", "v80", "v81", "v82", "v83", "v84", "v85", "v86", \
  "v87", "v88", "v89", "v90", "v91", "v92", "v93", "v94", "v95", "v96", "v97", "v98", "v99", "v100", "v101", \
  "v102", "v103", "v104", "v105", "v106", "v107", "v108", "v109", "v110", "v111", "v112", "v113", "v114", \
  "v115", "v116", "v117", "v118", "v119", "v120", "v121", "v122", "v123", "v124", "v125", "v126", "v127", \
  "v128", "v129", "v130", "v131", "v132", "v133", "v134", "v135", "v136", "v137", "v138", "v139", "v140", \
  "v141", "v142", "v143", "v144", "v145", "v146", "v147", "v148", "v149", "v150", "v151", "v152", "v153", \
  "v154", "v155", "v156", "v157", "v158", "v159", "v160", "v161", "v162", "v163", "v164", "v165", "v166", \
  "v167", "v168", "v169", "v170", "v171", "v172", "v173", "v174", "v175", "v176", "v177", "v178", "v179", \
  "v180", "v181", "v182", "v183", "v184", "v185", "v186", "v187", "v188", "v189", "v190", "v191", "v192", \
  "v193", "v194", "v195", "v196", "v197", "v198", "v199", "v200", "v201", "v202", "v203", "v204", "v205", \
  "v206", "v207", "v208", "v209", "v210", "v211", "v212", "v213", "v214", "v215", "v216", "v217", "v218", \
  "v219", "v220", "v221", "v222", "v223", "v224", "v225", "v226", "v227", "v228", "v229", "v230", "v231", \
  "v232", "v233", "v234", "v235", "vcc", "s50", "s51", "s52", "s53", "s54", "s55", "s56", "s57", "s58", "s59", \
  "s60", "s61", "s62", "s63", "s64", "s65", "s66", "s67", "s68", "s69", "s70", "s71", "s72", "s73", "s74", \
  "s75", "s76", "s77", "s78", "s79", "s80", "s81", "s82", "s83", "s84", "s85", "s86", "s87", "s88", "s89", \
  "s90", "s91", "s92", "s93", "s94", "s95", "s96", "s97", "s98", "s99", "scc", "memory"
#define LD_CELL 34560
#define LD_DEG 35584
#define LD_FLAG 35840
#define LD_TOT 35844
#define LD_CRD 35872
#define LD_SINK 35904
#define LD_PCNT 35848
#define LD_CGT 36160
#define LD_WQT 36288
#define LD_TAG 37312
#define LD_PAY 37328
#define LD_ENT 100096
#define LD_TB 106816
#define LD_WQ2 108864
#define LD_ROWS 110912
#define LD_SK 132416

namespace {

constexpr int C_ = 128;
constexpr int NMAX = 40;
constexpr int NB = 256;
#define LUT 1
constexpr int PW = 5;
constexpr int PT = PW * 64;
constexpr int PK = 4;
constexpr int CH = PT * PK;
constexpr int MT = 512;
constexpr int MW = MT / 64;
constexpr int MK = 4;
constexpr int KH = 10;
constexpr int SW = 208;
constexpr int RP = 1056;
constexpr int TN = 135 * 128;
constexpr unsigned OOB = 0x80000000u;
#define PNT " nt"
#define PFA 0
#define PFC 2
#define PUBB 0
#define ROT 1
#define POLLS 1
#define SPINS 4096
constexpr unsigned WS_WFRAG = 0;
constexpr unsigned WS_EP = WS_WFRAG + C_ * C_ * 2;
constexpr unsigned WS_FB = WS_EP + 256;
constexpr unsigned WS_GR = WS_FB + 4 * NB * 64;
#define NCP 3
#define GSV 2176
constexpr unsigned GS = GSV;
constexpr unsigned WS_RECK = WS_GR + 4 * NB * 2176;
constexpr unsigned WS_RECP = WS_RECK + NB * CH * 4;
constexpr unsigned WS_END = WS_RECP + NB * CH * 16;

__device__ __forceinline__ float bf2f(unsigned short v) { return __uint_as_float(((unsigned)v) << 16); }
__device__ __forceinline__ unsigned short f2bf(float f) {
  __hip_bfloat16 b = __float2bfloat16(f);
  return *reinterpret_cast<unsigned short*>(&b);
}
__device__ __forceinline__ unsigned pk2bf(float lo, float hi) {
  return __builtin_bit_cast(unsigned, __builtin_convertvector(f32x2{lo, hi}, bf16x2));
}
__device__ __forceinline__ unsigned row_incl_scan(unsigned x) {
  x += __builtin_amdgcn_update_dpp(0u, x, 0x111, 0xf, 0xf, false);
  x += __builtin_amdgcn_update_dpp(0u, x, 0x112, 0xf, 0xf, false);
  x += __builtin_amdgcn_update_dpp(0u, x, 0x114, 0xf, 0xf, false);
  x += __builtin_amdgcn_update_dpp(0u, x, 0x118, 0xf, 0xf, false);
  return x;
}
__device__ __forceinline__ unsigned wave_incl_scan(unsigned x) {
  x = row_incl_scan(x);
  x += __builtin_amdgcn_update_dpp(0u, x, 0x142, 0xa, 0xf, false);
  x += __builtin_amdgcn_update_dpp(0u, x, 0x143, 0xc, 0xf, false);
  return x;
}

struct Lds {
  unsigned tp[TN / 2];
  u32x4 cell[64];
  unsigned deg[64];
  unsigned flag, tot, pcnt, pad_[5];
  unsigned cready[8];
  u32x4 sink[16];
  u32x4 cgt[8];
  u32x4 wqt[64];
  unsigned long long tag;
  int thr, pad2_;
  union {
    unsigned ri[MW][MK * 64];
    u32x4 pay[MW * SW + 32];
  };
  char gap[65536 - TN * 2 - 1024 - 256 - 64 - 256 - 128 - 1024 - 16 - (MW * SW + 32) * 16];
  unsigned tn[TN / 2];
  __attribute__((aligned(16))) unsigned ent[MW * SW + 16];
  unsigned short tb[MW][128];
  u32x4 wq2[128];
  union {
    struct {
      u32x4 sp[CH + 64];
      unsigned sk[CH + 64];
      unsigned hist[PW][NB];
      unsigned hbase[PW][NB];
    };
    u32x4 rows[NMAX * RP / 16];
  };
};
static_assert(sizeof(Lds) <= 163840 && offsetof(Lds, tn) == 65536 && SW * 16 >= 2048, "LDS layout");
static_assert(offsetof(Lds, cell) == LD_CELL && offsetof(Lds, deg) == LD_DEG && offsetof(Lds, flag) == LD_FLAG &&
              offsetof(Lds, tot) == LD_TOT && offsetof(Lds, cready) == LD_CRD && offsetof(Lds, sink) == LD_SINK &&
              offsetof(Lds, pay) == LD_PAY && offsetof(Lds, ent) == LD_ENT && offsetof(Lds, tb) == LD_TB &&
              offsetof(Lds, pcnt) == LD_PCNT && offsetof(Lds, cgt) == LD_CGT && offsetof(Lds, wqt) == LD_WQT &&
              offsetof(Lds, tag) == LD_TAG && offsetof(Lds, wq2) == LD_WQ2 && offsetof(Lds, rows) == LD_ROWS && offsetof(Lds, sk) == LD_SK, "LDS layout");
static_assert(WS_FB == 33024 && WS_GR == 98560 && WS_RECK == 2326784 && WS_RECP == 3637504, "workspace layout");

__device__ __forceinline__ unsigned lds_addr(const void* p) {
  return (unsigned)(uintptr_t)(__attribute__((address_space(3))) const void*)p;
}
__device__ __forceinline__ u32x4 make_srd(const void* p, unsigned stride, unsigned num) {
  unsigned long long a = (unsigned long long)p;
  return u32x4{(unsigned)a, (unsigned)(a >> 32) | (stride << 16), num, 0x00020000u};
}

}

extern "C" __global__ __launch_bounds__(MT) void tfn_kernel(const int* __restrict__ dst, const int* __restrict__ src,
                                                            const u32x2* __restrict__ Y, const u32x2* __restrict__ ef,
                                                            char* ws, int E, int N, unsigned M2, unsigned cb,
                                                            const unsigned short* __restrict__ x, u32x4* __restrict__ out,
                                                            const unsigned short* __restrict__ wrad,
                                                            const unsigned short* __restrict__ cg,
                                                            const unsigned short* __restrict__ wmix, int nbk, unsigned npb) {
  __shared__ Lds L;
  const int tid = threadIdx.x, lane = tid & 63;
  const int w = __builtin_amdgcn_readfirstlane(tid >> 6);
  const int blk = blockIdx.x;
  const bool fast = (int)cb < 0;
  TDECL;
  MSTAMP(0);
  const u32x4 wsd = make_srd(ws, 0, WS_END);
  const int nper = (unsigned)nbk / 8;
  const int bucket = (blk & 7) * nper + (blk >> 3);
  const unsigned xo = ROT * (((blk & 7) * N) >> 3) * 1024;
  auto table1 = [&]() {
    if (!LUT) return;
    u32x4 t;
#pragma unroll
    for (int j = 0; j < 4; j++) t[j] = (2 * (tid * 4 + j) - 128) * 0x10001u + 0x10000u;
#pragma unroll
    for (int it = 0; it < 4; it++) {
      u32x4 u = t + it * (MT * 8 * 0x10001u);
      if (it == 0) {
#pragma unroll
        for (int j = 0; j < 4; j++) u[j] = tid < 32 ? (tid * 4 + j) * 0x10001u + ((j & 1) << 16) : u[j];
      }
      *reinterpret_cast<u32x4*>(&L.tp[(it * MT + tid) * 4]) = u;
      *reinterpret_cast<u32x4*>(&L.tn[(it * MT + tid) * 4]) = u | 0x80008000u;
    }
  };
  auto warm = [&](int n0, int n1, int r, int nr) {
    if (n0 >= n1) return;
    unsigned keep;
    asm volatile("s_mov_b32 %0, m0\n s_mov_b32 m0, %3\n s_mov_b32 s40, %6\n"
                 "1: s_cmp_ge_u32 s40, %5\n s_cbranch_scc1 2f\n buffer_load_dword %1, %2, s40 offen lds\n s_add_u32 s40, s40, %4\n s_branch 1b\n"
                 "2: s_mov_b32 m0, %0\n"
                 : "=&s"(keep) : "v"(xo + ((blk >> 3) * 64 + lane) * 128), "s"(make_srd(x, 0, N * 1024)), "n"(offsetof(Lds, sink)),
                   "s"(nr * nper * 8192), "s"(n1 * nper * 8192), "s"((n0 + r) * nper * 8192) : "memory", "s40", "scc");
  };
  auto totals = [&](u32x4& c, u32x4& p) {
    c = p = u32x4{0, 0, 0, 0};
#pragma unroll
    for (int ww = 0; ww < PW; ww++) {
      const u32x4 h = reinterpret_cast<const u32x4*>(L.hist[ww])[lane];
      c += h;
      if (ww < PW - 1) p += ww < w ? h : u32x4{0, 0, 0, 0};
    }
    const unsigned c01 = c[0] + c[1], c012 = c01 + c[2], b0 = wave_incl_scan(c012 + c[3]) - c012 - c[3];
    return u32x4{b0, b0 + c[0], b0 + c01, b0 + c012};
  };
  asm volatile("" ::"s"(x), "s"(out), "s"(nbk), "s"(npb));
  if (__builtin_expect(w < PW, 1)) {
    const int e0 = blk * CH + tid * 2;
    u32x2 d0, d1, s0, s1;
    u32x4 y0, y1, f0, f1;
    asm volatile(
        "buffer_load_dwordx2 %0, %4, %5, 0 offen" PNT "\n buffer_load_dwordx2 %1, %4, %5, %7 offen" PNT "\n"
        "buffer_load_dwordx2 %2, %4, %6, 0 offen" PNT "\n buffer_load_dwordx2 %3, %4, %6, %7 offen" PNT "\n s_barrier\n"
        : "=&v"(d0), "=&v"(d1), "=&v"(s0), "=&v"(s1)
        : "v"(e0 * 4), "s"(make_srd(dst, 0, E * 4)), "s"(make_srd(src, 0, E * 4)), "s"(CH * 2));
    asm volatile(
        "buffer_load_dwordx4 %0, %4, %5, 0 offen" PNT "\n buffer_load_dwordx4 %1, %4, %5, %7 offen" PNT "\n"
        "buffer_load_dwordx4 %2, %4, %6, 0 offen" PNT "\n buffer_load_dwordx4 %3, %4, %6, %7 offen" PNT "\n"
        : "=&v"(y0), "=&v"(y1), "=&v"(f0), "=&v"(f1)
        : "v"(e0 * 8), "s"(make_srd(Y, 0, E * 8)), "s"(make_srd(ef, 0, E * 8)), "s"(CH * 4));
    auto valid = [&](int k) { return e0 + (k >> 1) * (CH / 2) + (k & 1) < E; };
    reinterpret_cast<u32x4*>(L.hist[w])[lane] = u32x4{0, 0, 0, 0};
    table1();
    asm volatile("s_waitcnt vmcnt(6)" : "+v"(d0), "+v"(d1));
    const u32x4 dv = {d0[0], d0[1], d1[0], d1[1]};
    unsigned bk[PK], rk[PK];
#pragma unroll
    for (int k = 0; k < PK; k++) {
      bk[k] = valid(k) ? __umulhi(dv[k], M2) & (NB - 1) : lane;
      rk[k] = atomicAdd(&L.hist[w][bk[k]], valid(k) ? 1u : 0u);
    }
    __syncthreads();
    u32x4 c, p;
    const u32x4 b = totals(c, p);
    reinterpret_cast<u32x4*>(L.hbase[w])[lane] = b + p;
    asm volatile("s_waitcnt vmcnt(4)" : "+v"(s0), "+v"(s1));
    const u32x4 sv = {s0[0], s0[1], s1[0], s1[1]};
#pragma unroll
    for (int k = 0; k < PK; k++) {
      rk[k] += L.hbase[w][bk[k]];
      rk[k] = valid(k) ? rk[k] : CH + lane;
      L.sk[rk[k]] = (dv[k] << 24) | sv[k];
    }
    asm volatile("s_waitcnt vmcnt(0)" : "+v"(y0), "+v"(y1), "+v"(f0), "+v"(f1));
#pragma unroll
    for (int k = 0; k < PK; k++) {
      const u32x4 &yy = k < 2 ? y0 : y1, &ff = k < 2 ? f0 : f1;
      L.sp[rk[k]] = u32x4{yy[(k & 1) * 2], yy[(k & 1) * 2 + 1], ff[(k & 1) * 2], ff[(k & 1) * 2 + 1]};
    }
  } else if (w == PW + 1) {
    u32x2 ep;
    asm volatile("buffer_load_dwordx2 %0, off, %1, %2\n s_barrier" : "=&v"(ep) : "s"(wsd), "s"(WS_EP) : "memory");
    table1();
    __syncthreads();
    u32x4 c, p;
    u32x4 b = totals(c, p);
    asm volatile("s_waitcnt vmcnt(0)" : "+v"(ep) :: "memory");
    const unsigned long long tg = (((unsigned long long)ep[1] << 32) | ep[0]) + 1;
    b |= (c << 11) | ((unsigned)tg << 22);
    const unsigned th = tg >> 10;
    const auto grs = __builtin_amdgcn_make_buffer_rsrc((void*)(ws + WS_GR + blk * GS), 0, fast ? NB * 8 : 0, 0x00020000);
    __builtin_amdgcn_raw_buffer_store_b128(u32x4{b[0], th, b[1], th}, grs, lane * 16, 0, 16);
    __builtin_amdgcn_raw_buffer_store_b128(u32x4{b[2], th, b[3], th}, grs, lane * 16 + 1024, 0, 16);
    L.tag = tg;
    MSTAMP(1);
  } else if (w == PW) {
    unsigned q8[8], keep;
    asm volatile(
        "s_barrier\n s_cmp_ge_u32 %11, 16\n s_cbranch_scc1 1f\n"
        "buffer_load_dword %1, %9, %10, 0 offen\n buffer_load_dword %2, %9, %10, 0 offen offset:256\n"
        "buffer_load_dword %3, %9, %10, 0 offen offset:512\n buffer_load_dword %4, %9, %10, 0 offen offset:768\n"
        "buffer_load_dword %5, %9, %10, 0 offen offset:1024\n buffer_load_dword %6, %9, %10, 0 offen offset:1280\n"
        "buffer_load_dword %7, %9, %10, 0 offen offset:1536\n buffer_load_dword %8, %9, %10, 0 offen offset:1792\n s_branch 2f\n"
        "1: s_cmp_ge_u32 %11, 24\n s_cbranch_scc1 2f\n"
        "s_getpc_b64 s[40:41]\n .Ltfn_pc: s_sub_u32 s40, s40, .Ltfn_pc-tfn_kernel\n s_subb_u32 s41, s41, 0\n"
        "s_mov_b32 s42, .Ltfn_end-tfn_kernel\n s_mov_b32 s43, 0x20000\n s_mov_b32 %0, m0\n s_mov_b32 m0, %13\n s_nop 0\n"
        "buffer_load_dword %12, s[40:43], 0 offen lds\n buffer_load_dword %12, s[40:43], %14 offen lds\n s_mov_b32 m0, %0\n 2:\n"
        : "=&s"(keep), "=&v"(q8[0]), "=&v"(q8[1]), "=&v"(q8[2]), "=&v"(q8[3]), "=&v"(q8[4]), "=&v"(q8[5]), "=&v"(q8[6]), "=&v"(q8[7])
        : "v"((32 * (blk >> 2) + 8 * (lane >> 4)) * 256 + (blk & 3) * 64 + (lane & 15) * 4), "s"(make_srd(wmix, 0, C_ * C_ * 2)),
          "s"(blk), "v"(lane * 128), "n"(offsetof(Lds, sink)), "s"(8192)
        : "s40", "s41", "s42", "s43", "scc", "memory");
    table1();
    L.cell[lane] = u32x4{0, 0, 0, 0};
    if (lane < (256 + 64) / 16) L.cell[64 + lane] = u32x4{0, 0, 0, 0};
    __syncthreads();
    asm volatile("s_cmp_ge_u32 %8, 16\n s_cbranch_scc1 1f\n s_waitcnt vmcnt(0)\n 1:"
                 : "+v"(q8[0]), "+v"(q8[1]), "+v"(q8[2]), "+v"(q8[3]), "+v"(q8[4]), "+v"(q8[5]), "+v"(q8[6]), "+v"(q8[7]) : "s"(blk) : "scc");
    if (blk < 16) {
      const auto frs = __builtin_amdgcn_make_buffer_rsrc((void*)(ws + WS_WFRAG + blk * 2048), 0, 2048, 0x00020000);
      u32x4 v0, v1;
#pragma unroll
      for (int j = 0; j < 4; j++) {
        v0[j] = __builtin_amdgcn_perm(q8[2 * j + 1], q8[2 * j], 0x05040100u);
        v1[j] = __builtin_amdgcn_perm(q8[2 * j + 1], q8[2 * j], 0x07060302u);
      }
      __builtin_amdgcn_raw_buffer_store_b128(v0, frs, lane * 16, 0, 16);
      __builtin_amdgcn_raw_buffer_store_b128(v1, frs, lane * 16 + 1024, 0, 16);
    }
  } else {
    unsigned q[5];
    asm volatile(
        "s_barrier\n buffer_load_dword %0, %5, %6, 0 offen\n buffer_load_dword %1, %5, %6, 0 offen offset:256\n"
        "buffer_load_dword %2, %5, %6, 0 offen offset:512\n buffer_load_dword %3, %5, %6, 0 offen offset:768\n"
        "buffer_load_dword %4, %5, %7, 0 offen\n"
        : "=&v"(q[0]), "=&v"(q[1]), "=&v"(q[2]), "=&v"(q[3]), "=&v"(q[4])
        : "v"(lane * 4), "s"(make_srd(wrad, 0, 1024)), "s"(make_srd(cg, 0, 128)) : "memory");
    table1();
    __syncthreads();
    asm volatile("s_waitcnt vmcnt(0)" : "+v"(q[0]), "+v"(q[1]), "+v"(q[2]), "+v"(q[3]), "+v"(q[4]));
    warm(0, PFA, 0, 1);
    if (lane < 32) {
      reinterpret_cast<unsigned short*>(L.cgt)[(lane & 1) * 32 + (lane >> 1)] = q[4];
      reinterpret_cast<unsigned short*>(L.cgt)[(lane & 1) * 32 + 16 + (lane >> 1)] = q[4] >> 16;
    }
    float sm = 0.f;
#pragma unroll
    for (int h = 0; h < 2; h++) {
      float a = 0.f, v[4];
#pragma unroll
      for (int k = 0; k < 4; k++) {
        v[k] = __uint_as_float(h ? q[k] & 0xffff0000u : q[k] << 16);
        a += fabsf(v[k]);
      }
      sm = a <= sm ? sm : a < 1e30f ? a : 1e30f;
      if (!LUT) {
        u32x4 u;
#pragma unroll
        for (int k = 0; k < 4; k += 2) {
          const float a0 = v[k] * -1.4426950408889634f, a1 = v[k + 1] * -1.4426950408889634f;
          u[k >> 1] = pk2bf(a0, a1);
          u[2 + (k >> 1)] = pk2bf(a0 - __uint_as_float(u[k >> 1] << 16), a1 - __uint_as_float(u[k >> 1] & 0xffff0000u));
        }
        L.wq2[lane * 2 + h] = u;
      }
    }
    if (LUT)
      L.wqt[lane] = u32x4{__builtin_amdgcn_perm(q[1], q[0], 0x05040100u), __builtin_amdgcn_perm(q[3], q[2], 0x05040100u),
                          __builtin_amdgcn_perm(q[1], q[0], 0x07060302u), __builtin_amdgcn_perm(q[3], q[2], 0x07060302u)};
    int t = sm >= 1e30f ? -1 : (int)(__float_as_uint(250.f * __builtin_amdgcn_rcpf(fmaxf(sm, 1e-30f))) >> 16);
    t = min(t, __builtin_amdgcn_update_dpp(t, t, 0x111, 0xf, 0xf, false));
    t = min(t, __builtin_amdgcn_update_dpp(t, t, 0x112, 0xf, 0xf, false));
    t = min(t, __builtin_amdgcn_update_dpp(t, t, 0x114, 0xf, 0xf, false));
    t = min(t, __builtin_amdgcn_update_dpp(t, t, 0x118, 0xf, 0xf, false));
    t = min(t, __builtin_amdgcn_update_dpp(t, t, 0x142, 0xa, 0xf, false));
    t = min(t, __builtin_amdgcn_update_dpp(t, t, 0x143, 0xc, 0xf, false));
    if (lane == 63) L.thr = t;
  }
  unsigned bad = 3;
  if (fast) {
    asm volatile(STREAM_ASM
                 : [bad] "=&s"(bad)
                 : [wsd] "s"(wsd), [xp] "s"(x), [out] "s"(out), [n] "s"(N), [m2] "s"(M2), [npb] "s"(npb), [cb] "s"(cb),
                   [blk] "s"(blk), [w] "s"(w)
                 : STREAM_CLOBBERS);
  }
  const bool slow = bad != 0;
  MSTAMP(6);
  asm volatile(".Ltfn_end:");
  if (slow) {
    int n01[2];
#pragma unroll
    for (int h = 0; h < 2; h++) {
      unsigned c = (unsigned)(((unsigned long long)(bucket + h) * npb) >> 20);
#pragma unroll
      for (int r = 0; r < 2; r++) c += __umulhi(c, M2) < (unsigned)(bucket + h) ? 1 : 0;
      n01[h] = min(c, (unsigned)N);
    }
    const int node0 = n01[0], nn = n01[1] - n01[0];
    const unsigned mxa = lds_addr(L.rows) + ((w >> 2) * 4 + ((lane & 15) >> 2)) * RP + (lane & 3) * 64 + (lane >> 4) * 16;
    const unsigned mxo = ((w >> 2) * 4 + (lane >> 4)) * 1024 + (w & 3) * 256 + (lane & 15) * 16;
    __syncthreads();
#pragma unroll 1
    for (int it = w; it < 2 * nn; it += MW) {
      const int dl = it >> 1, ch = (it & 1) * 64 + lane;
      float acc[4] = {0.f, 0.f, 0.f, 0.f};
#pragma unroll 1
      for (int eb = 0; eb < E; eb += 64) {
        unsigned long long m = __ballot(eb + lane < E && dst[min(eb + lane, E - 1)] == node0 + dl);
#pragma unroll 1
        for (; m; m &= m - 1) {
          const int e = eb + __builtin_ctzll(m);
          const u32x2 ry = Y[e], rf = ef[e];
          float yy[4] = {bf2f(ry[0] & 0xffff), bf2f(ry[0] >> 16), bf2f(ry[1] & 0xffff), bf2f(ry[1] >> 16)};
          float ee[4] = {bf2f(rf[0] & 0xffff), bf2f(rf[0] >> 16), bf2f(rf[1] & 0xffff), bf2f(rf[1] >> 16)};
          float z = 0.f;
#pragma unroll
          for (int k = 0; k < 4; k++) z += ee[k] * bf2f(wrad[k * C_ + ch]);
          const float R = z / (1.f + __expf(-z));
          const unsigned short* xr = x + (size_t)src[e] * 512 + ch * 4;
#pragma unroll 1
          for (int o = 0; o < 4; o++) {
            float tp = 0.f;
#pragma unroll 1
            for (int i = 0; i < 4; i++) {
              float A = 0.f;
#pragma unroll
              for (int f = 0; f < 4; f++) A += yy[f] * bf2f(cg[i * 16 + f * 4 + o]);
              tp += bf2f(xr[i]) * bf2f(f2bf(A));
            }
            const float v = R * tp;
            acc[0] += o == 0 ? v : 0.f;
            acc[1] += o == 1 ? v : 0.f;
            acc[2] += o == 2 ? v : 0.f;
            acc[3] += o == 3 ? v : 0.f;
          }
        }
      }
      unsigned short* ar = reinterpret_cast<unsigned short*>(L.rows) + dl * (RP / 2) + (ch >> 5) * 128 + (ch & 31);
#pragma unroll
      for (int o = 0; o < 4; o++) ar[o * 32] = f2bf(acc[o]);
    }
    __syncthreads();
    const auto ors = __builtin_amdgcn_make_buffer_rsrc((void*)(out + (size_t)node0 * 64), 0, nn * 1024, 0x00020000);
#pragma unroll 1
    for (int t = 0; t < NMAX / 8; t++) {
      f32x4 a = {0.f, 0.f, 0.f, 0.f}, b = {0.f, 0.f, 0.f, 0.f};
#pragma unroll 1
      for (int k = 0; k < 4; k++) {
        const s16x8 f = *(const __attribute__((address_space(3))) s16x8*)(uintptr_t)(mxa + t * 8 * RP + k * 256);
        const unsigned short* wp = wmix + (32 * k + 8 * (lane >> 4)) * C_ + 32 * (w & 3) + 2 * (lane & 15);
        s16x8 w0, w1;
#pragma unroll
        for (int j = 0; j < 8; j++) {
          w0[j] = wp[j * C_];
          w1[j] = wp[j * C_ + 1];
        }
        a = __builtin_amdgcn_mfma_f32_16x16x32_bf16(f, w0, a, 0, 0, 0);
        b = __builtin_amdgcn_mfma_f32_16x16x32_bf16(f, w1, b, 0, 0, 0);
      }
      __builtin_amdgcn_raw_buffer_store_b128(u32x4{pk2bf(a[0], a[1]), pk2bf(a[2], a[3]), pk2bf(b[0], b[1]), pk2bf(b[2], b[3])},
                                             ors, mxo + t * 8192, 0, 0);
    }
  }
  MFLUSH;
}

extern "C" void kernel_launch(void* const* d_in, const int* in_sizes, int n_in, void* d_out, int out_size,
                              void* d_ws, size_t ws_size, hipStream_t stream) {
  const int N = in_sizes[0] / 512;
  const int E = in_sizes[1];
  int nbk = NB;
  while ((N + nbk - 1) / nbk > NMAX || (N % nbk == 0 && N / nbk >= NMAX)) nbk += NB;
  const unsigned long long M2l = (((unsigned long long)nbk) << 32) / (unsigned long long)N;
  const unsigned M2 = M2l > 0xffffffffull ? 0xffffffffu : (unsigned)M2l;
  const unsigned long long cb = (16ull << 32) / (unsigned long long)N;
  const unsigned fast = nbk == NB && E <= NB * CH ? 0x80000000u : 0u;
  tfn_kernel<<<nbk, MT, 0, stream>>>((const int*)d_in[2], (const int*)d_in[1], (const u32x2*)d_in[3], (const u32x2*)d_in[4],
                                     (char*)d_ws, E, N, M2, (cb > 0xffffffull ? 0xffffffu : (unsigned)cb) | fast,
                                     (const unsigned short*)d_in[0], (u32x4*)d_out, (const unsigned short*)d_in[5],
                                     (const unsigned short*)d_in[6], (const unsigned short*)d_in[7], nbk,
                                     (unsigned)((1ull << 52) / M2));
}
```

```cpp
#include <hip/hip_runtime.h>
#include <hip/hip_bf16.h>
#include <stdint.h>
#include <stddef.h>

typedef __attribute__((ext_vector_type(2))) float f32x2;
typedef __attribute__((ext_vector_type(4))) float f32x4;
typedef __attribute__((ext_vector_type(8))) short s16x8;
typedef __attribute__((ext_vector_type(2))) unsigned int u32x2;
typedef __attribute__((ext_vector_type(4))) unsigned int u32x4;
typedef __attribute__((ext_vector_type(16))) unsigned int u32x16;
typedef __attribute__((ext_vector_type(2))) __bf16 bf16x2;

#define TDECL
#define TSTAMP(i)
#define TFLUSH
#define MSTAMP(i) TSTAMP(i)
#define MFLUSH do { asm volatile("s_waitcnt vmcnt(0)" ::: "memory"); TSTAMP(15); TFLUSH; } while (0)
#undef MFLUSH
#define MFLUSH

#define STREAM_ASM \
  "s_load_dwordx16 s[8:23], %[ka], 0\n" \
  "s_load_dwordx16 s[24:39], %[ka], 64\n" \
  "s_getpc_b64 s[40:41]\n" \
  "Lpc%=:\n" \
  "v_lshlrev_b32 v50, 7, %[tid]\n" \
  "s_mov_b32 s42, Lout%=-Lpc%=\n" \
  "s_mov_b32 s43, 0x20000\n" \
  "buffer_load_dword v208, v50, s[40:43], 0 offen\n" \
  "v_lshlrev_b32 v52, 3, %[tid]\n" \
  "v_lshlrev_b32 v53, 2, %[tid]\n" \
  "v_mov_b32 v48, 0\n" \
  "v_readfirstlane_b32 s44, %[tid]\n" \
  "v_add_u32 v49, 134464, v53\n" \
  "ds_write_b32 v49, v48\n" \
  "s_mul_i32 s63, %[blk], 5120\n" \
  "s_add_u32 s61, s63, 3584\n" \
  "s_cmp_lt_u32 s44, 384\n" \
  "s_cselect_b32 s61, s61, 0x40000000\n" \
  "s_waitcnt lgkmcnt(0)\n" \
  "s_cmp_lt_u32 s44, s34\n" \
  "s_cbranch_scc0 Lh0_%=\n" \
  "buffer_load_dwordx2 v[100:101], v52, s[8:11], s63 offen nt\n" \
  "buffer_load_dword v102, v53, s[8:11], s61 offen nt\n" \
  "s_barrier\n" \
  "buffer_load_dwordx2 v[104:105], v52, s[12:15], s63 offen nt\n" \
  "buffer_load_dword v106, v53, s[12:15], s61 offen nt\n" \
  "v_lshlrev_b32 v49, 1, v52\n" \
  "s_lshl_b32 s93, s63, 1\n" \
  "s_lshl_b32 s62, s61, 1\n" \
  "buffer_load_dwordx4 v[108:111], v49, s[16:19], s93 offen nt\n" \
  "buffer_load_dwordx4 v[114:117], v49, s[20:23], s93 offen nt\n" \
  "buffer_load_dwordx2 v[112:113], v52, s[16:19], s62 offen nt\n" \
  "buffer_load_dwordx2 v[118:119], v52, s[20:23], s62 offen nt\n" \
  "s_sub_i32 s63, s10, s63\n" \
  "s_sub_i32 s61, s10, s61\n" \
  "v_cmp_gt_i32 s[80:81], s63, v52\n" \
  "s_sub_i32 s63, s63, 4\n" \
  "v_cmp_gt_i32 s[84:85], s61, v53\n" \
  "v_cmp_gt_i32 s[82:83], s63, v52\n" \
  "s_branch Lh1_%=\n" \
  "Lh0_%=:\n" \
  "s_mov_b32 s93, 32768\n" \
  "buffer_load_dwordx2 v[88:89], off, s[24:27], s93\n" \
  "s_barrier\n" \
  "s_load_dwordx8 s[8:15], %[ka], 128\n" \
  "s_load_dwordx4 s[16:19], %[ka], 160\n" \
  "s_mov_b64 s[80:81], 0\n" \
  "s_mov_b64 s[82:83], 0\n" \
  "s_mov_b64 s[84:85], 0\n" \
  "s_cmp_eq_u32 s34, 0\n" \
  "s_cbranch_scc1 Lnf%=\n" \
  "Lh1_%=:\n" \
  "ds_write_b32 v53, v48 offset:34560\n" \
  "s_lshr_b32 s44, s44, 6\n" \
  "v_and_b32 v86, 63, %[tid]\n" \
  "s_lshl_b32 s72, s44, 3\n" \
  "s_lshl_b32 s63, 1, s72\n" \
  "v_mov_b32 v93, s63\n" \
  "s_and_b32 s45, s72, 24\n" \
  "s_lshl_b64 s[46:47], -1, s72\n" \
  "s_and_b32 s46, s46, 0x01010101\n" \
  "s_and_b32 s47, s47, 0x01010101\n" \
  "s_and_b32 s73, s44, 4\n" \
  "s_add_u32 s74, s73, 134464\n" \
  "s_lshl_b32 s75, s44, 9\n" \
  "s_add_u32 s75, s75, 136512\n" \
  "v_lshlrev_b32 v13, 4, v86\n" \
  "v_lshlrev_b32 v54, 2, v86\n" \
  "v_lshl_add_u32 v52, v86, 3, s75\n" \
  "v_lshlrev_b32 v50, 5, v86\n" \
  "v_add_u32 v50, 134464, v50\n" \
  "s_and_b32 s71, %[blk], 7\n" \
  "s_lshr_b32 s66, %[blk], 3\n" \
  "s_lshl_b32 s68, s71, 5\n" \
  "s_add_u32 s68, s68, s66\n" \
  "s_and_b32 s61, s68, 2\n" \
  "s_lshl_b32 s61, s61, 9\n" \
  "v_and_b32 v55, 0xf8, v54\n" \
  "s_and_b32 s63, s68, 0xfc\n" \
  "s_lshl2_add_u32 s61, s63, s61\n" \
  "v_add_u32 v55, s44, v55\n" \
  "s_and_b32 s63, s68, 1\n" \
  "s_lshl3_add_u32 s61, s63, s61\n" \
  "v_mul_u32_u24 v17, 2176, v55\n" \
  "s_add_u32 s61, s61, 49408\n" \
  "v_mov_b32 v94, 35840\n" \
  "s_mov_b32 s63, 0x01010101\n" \
  "s_waitcnt vmcnt(6)\n" \
  "v_mul_hi_u32 v96, v100, s36\n" \
  "v_mul_hi_u32 v97, v101, s36\n" \
  "v_mul_hi_u32 v98, v102, s36\n" \
  "s_mov_b64 exec, s[80:81]\n" \
  "v_lshl_add_u32 v120, v96, 3, s74\n" \
  "ds_add_rtn_u32 v124, v120, v93\n" \
  "s_mov_b64 exec, s[82:83]\n" \
  "v_lshl_add_u32 v121, v97, 3, s74\n" \
  "ds_add_rtn_u32 v125, v121, v93\n" \
  "s_mov_b64 exec, s[84:85]\n" \
  "v_lshl_add_u32 v122, v98, 3, s74\n" \
  "ds_add_rtn_u32 v126, v122, v93\n" \
  "s_mov_b64 exec, -1\n" \
  "v_lshl_add_u32 v96, v96, 1, s75\n" \
  "v_lshl_add_u32 v97, v97, 1, s75\n" \
  "v_lshl_add_u32 v98, v98, 1, s75\n" \
  "s_waitcnt lgkmcnt(0)\n" \
  "s_barrier\n" \
  "ds_read_b128 v[128:131], v50\n" \
  "ds_read_b128 v[132:135], v50 offset:16\n" \
  "v_bfe_u32 v124, v124, s45, 8\n" \
  "v_bfe_u32 v125, v125, s45, 8\n" \
  "v_bfe_u32 v126, v126, s45, 8\n" \
  "s_waitcnt lgkmcnt(0)\n" \
  "v_dot4_u32_u8 v136, v128, s63, 0\n" \
  "v_dot4_u32_u8 v136, v129, s63, v136\n" \
  "v_dot4_u32_u8 v137, v130, s63, v136\n" \
  "v_dot4_u32_u8 v137, v131, s63, v137\n" \
  "v_dot4_u32_u8 v138, v132, s63, v137\n" \
  "v_dot4_u32_u8 v138, v133, s63, v138\n" \
  "v_dot4_u32_u8 v139, v134, s63, v138\n" \
  "v_dot4_u32_u8 v139, v135, s63, v139\n" \
  "v_dot4_u32_u8 v140, v128, s46, 0\n" \
  "v_dot4_u32_u8 v141, v130, s46, 0\n" \
  "v_dot4_u32_u8 v142, v132, s46, 0\n" \
  "v_add_u32_dpp v87, v139, v139 row_shr:1 row_mask:0xf bank_mask:0xf bound_ctrl:1\n" \
  "v_dot4_u32_u8 v143, v134, s46, 0\n" \
  "v_dot4_u32_u8 v140, v129, s47, v140\n" \
  "v_add_u32_dpp v87, v87, v87 row_shr:2 row_mask:0xf bank_mask:0xf bound_ctrl:1\n" \
  "v_dot4_u32_u8 v141, v131, s47, v141\n" \
  "v_dot4_u32_u8 v142, v133, s47, v142\n" \
  "v_add_u32_dpp v87, v87, v87 row_shr:4 row_mask:0xf bank_mask:0xf bound_ctrl:1\n" \
  "v_dot4_u32_u8 v143, v135, s47, v143\n" \
  "s_mov_b32 s63, 0x10001\n" \
  "v_add_u32_dpp v87, v87, v87 row_shr:8 row_mask:0xf bank_mask:0xf bound_ctrl:1\n" \
  "v_sub_u32 v140, v136, v140\n" \
  "v_sub_u32 v141, v137, v141\n" \
  "v_add_u32_dpp v87, v87, v87 row_bcast:15 row_mask:0xa bank_mask:0xf\n" \
  "v_sub_u32 v142, v138, v142\n" \
  "v_sub_u32 v143, v139, v143\n" \
  "v_add_u32_dpp v87, v87, v87 row_bcast:31 row_mask:0xc bank_mask:0xf\n" \
  "v_lshl_or_b32 v140, v141, 16, v140\n" \
  "v_sub_u32 v87, v87, v139\n" \
  "v_lshl_or_b32 v141, v143, 16, v142\n" \
  "s_cmp_eq_u32 s44, 7\n" \
  "v_mad_u32_u24 v140, v87, s63, v140\n" \
  "v_mad_u32_u24 v141, v87, s63, v141\n" \
  "s_cbranch_scc1 Lh2_%=\n" \
  "ds_write_b64 v52, v[140:141]\n" \
  "ds_read_u16 v96, v96\n" \
  "ds_read_u16 v97, v97\n" \
  "ds_read_u16 v98, v98\n" \
  "s_mov_b32 s63, 129344\n" \
  "s_mov_b32 s93, 108864\n" \
  "s_lshl_b32 s74, s44, 10\n" \
  "v_add_u32 v50, s74, v13\n" \
  "v_add_u32 v51, s93, v50\n" \
  "s_waitcnt vmcnt(4)\n" \
  "v_lshl_or_b32 v104, v100, 24, v104\n" \
  "v_lshl_or_b32 v105, v101, 24, v105\n" \
  "v_lshl_or_b32 v106, v102, 24, v106\n" \
  "s_waitcnt lgkmcnt(0)\n" \
  "s_mov_b64 exec, s[80:81]\n" \
  "v_add_u32 v124, v124, v96\n" \
  "v_lshl_add_u32 v120, v124, 2, s63\n" \
  "v_lshl_add_u32 v124, v124, 4, s93\n" \
  "ds_write_b32 v120, v104\n" \
  "s_mov_b64 exec, s[82:83]\n" \
  "v_add_u32 v125, v125, v97\n" \
  "v_lshl_add_u32 v121, v125, 2, s63\n" \
  "v_lshl_add_u32 v125, v125, 4, s93\n" \
  "ds_write_b32 v121, v105\n" \
  "s_mov_b64 exec, s[84:85]\n" \
  "v_add_u32 v126, v126, v98\n" \
  "v_lshl_add_u32 v122, v126, 2, s63\n" \
  "v_lshl_add_u32 v126, v126, 4, s93\n" \
  "ds_write_b32 v122, v106\n" \
  "s_mul_i32 s72, %[blk], 20480\n" \
  "s_lshr_b32 s62, s72, 2\n" \
  "s_add_u32 s72, s72, 1917184\n" \
  "s_add_u32 s62, s62, 607488\n" \
  "s_add_u32 s74, s72, 7168\n" \
  "s_add_u32 s75, s72, 14336\n" \
  "s_sub_u32 s63, s62, 7168\n" \
  "s_cmp_eq_u32 s44, 6\n" \
  "s_cselect_b32 s75, s63, s75\n" \
  "s_waitcnt vmcnt(0)\n" \
  "ds_write2_b64 v126, v[112:113], v[118:119] offset1:1\n" \
  "s_mov_b64 exec, s[82:83]\n" \
  "ds_write2_b64 v125, v[110:111], v[116:117] offset1:1\n" \
  "s_mov_b64 exec, s[80:81]\n" \
  "ds_write2_b64 v124, v[108:109], v[114:115] offset1:1\n" \
  "s_mov_b64 exec, -1\n" \
  "s_cmp_gt_u32 s44, 3\n" \
  "s_waitcnt lgkmcnt(0)\n" \
  "s_barrier\n" \
  "ds_read_b128 v[216:219], v51 offset:0\n" \
  "ds_read_b128 v[220:223], v51 offset:7168\n" \
  "ds_read_b128 v[224:227], v51 offset:14336\n" \
  "ds_read_b128 v[228:231], v51 offset:21504\n" \
  "s_waitcnt lgkmcnt(3)\n" \
  "buffer_store_dwordx4 v[216:219], v50, s[24:27], s72 offen sc1\n" \
  "s_waitcnt lgkmcnt(2)\n" \
  "buffer_store_dwordx4 v[220:223], v50, s[24:27], s74 offen sc1\n" \
  "s_waitcnt lgkmcnt(1)\n" \
  "buffer_store_dwordx4 v[224:227], v50, s[24:27], s75 offen sc1\n" \
  "s_cbranch_scc1 Lc1_%=\n" \
  "s_waitcnt lgkmcnt(0)\n" \
  "buffer_store_dwordx4 v[228:231], v50, s[24:27], s62 offen sc1\n" \
  "Lc1_%=:\n" \
  "s_mov_b32 s63, 0x80008\n" \
  "s_mov_b32 s93, 0x80008000\n" \
  "v_mul_lo_u32 v52, %[tid], s63\n" \
  "v_lshlrev_b32 v53, 4, %[tid]\n" \
  "v_add_u32 v49, 0x10000, v53\n" \
  "v_cmp_gt_u32 s[90:91], 32, %[tid]\n" \
  "v_cmp_gt_u32 vcc, 368, %[tid]\n" \
  "v_add_u32 v104, 0xf810f80, v52\n" \
  "v_add_u32 v105, 0xf830f82, v52\n" \
  "v_add_u32 v106, 0xf850f84, v52\n" \
  "v_add_u32 v107, 0xf870f86, v52\n" \
  "v_or_b32 v108, s93, v104\n" \
  "v_or_b32 v109, s93, v105\n" \
  "v_or_b32 v110, s93, v106\n" \
  "v_or_b32 v111, s93, v107\n" \
  "ds_write_b128 v53, v[104:107] offset:8192\n" \
  "ds_write_b128 v49, v[108:111] offset:8192\n" \
  "v_add_u32 v112, 0x1f811f80, v52\n" \
  "v_add_u32 v113, 0x1f831f82, v52\n" \
  "v_add_u32 v114, 0x1f851f84, v52\n" \
  "v_add_u32 v115, 0x1f871f86, v52\n" \
  "v_or_b32 v116, s93, v112\n" \
  "v_or_b32 v117, s93, v113\n" \
  "v_or_b32 v118, s93, v114\n" \
  "v_or_b32 v119, s93, v115\n" \
  "ds_write_b128 v53, v[112:115] offset:16384\n" \
  "ds_write_b128 v49, v[116:119] offset:16384\n" \
  "s_mov_b64 exec, vcc\n" \
  "v_add_u32 v120, 0x2f812f80, v52\n" \
  "v_add_u32 v121, 0x2f832f82, v52\n" \
  "v_add_u32 v122, 0x2f852f84, v52\n" \
  "v_add_u32 v123, 0x2f872f86, v52\n" \
  "v_or_b32 v124, s93, v120\n" \
  "v_or_b32 v125, s93, v121\n" \
  "v_or_b32 v126, s93, v122\n" \
  "v_or_b32 v127, s93, v123\n" \
  "ds_write_b128 v53, v[120:123] offset:24576\n" \
  "ds_write_b128 v49, v[124:127] offset:24576\n" \
  "s_mov_b64 exec, -1\n" \
  "v_add_u32 v96, 0xff80ff80, v52\n" \
  "v_add_u32 v97, 0xff82ff82, v52\n" \
  "v_add_u32 v98, 0xff84ff84, v52\n" \
  "v_add_u32 v99, 0xff86ff86, v52\n" \
  "v_lshrrev_b32 v52, 1, v52\n" \
  "s_mov_b64 exec, s[90:91]\n" \
  "v_add_u32 v96, 0x0, v52\n" \
  "v_add_u32 v97, 0x20001, v52\n" \
  "v_add_u32 v98, 0x20002, v52\n" \
  "v_add_u32 v99, 0x40003, v52\n" \
  "s_mov_b64 exec, -1\n" \
  "v_or_b32 v100, s93, v96\n" \
  "v_or_b32 v101, s93, v97\n" \
  "v_or_b32 v102, s93, v98\n" \
  "v_or_b32 v103, s93, v99\n" \
  "ds_write_b128 v53, v[96:99] offset:0\n" \
  "ds_write_b128 v49, v[100:103] offset:0\n" \
  "buffer_load_dwordx2 v[44:45], v17, s[24:27], s61 offen sc1\n" \
  "s_mov_b32 s79, m0\n" \
  "s_mov_b64 s[88:89], 0\n" \
  "s_mov_b32 s92, 0\n" \
  "v_mov_b32 v92, 1\n" \
  "v_and_b32 v41, 4, v54\n" \
  "s_movk_i32 s63, 5120\n" \
  "v_mad_u32_u24 v40, v55, s63, v41\n" \
  "s_lshl_b32 s63, s44, 10\n" \
  "s_add_u32 s63, s63, 37328\n" \
  "v_add_u32 v41, s63, v41\n" \
  "s_mul_i32 s90, s68, s39\n" \
  "s_mul_hi_u32 s91, s68, s39\n" \
  "s_lshr_b64 s[90:91], s[90:91], 20\n" \
  "s_mul_hi_u32 s63, s90, s36\n" \
  "s_cmp_lt_u32 s63, s68\n" \
  "s_addc_u32 s90, s90, 0\n" \
  "s_mul_hi_u32 s63, s90, s36\n" \
  "s_cmp_lt_u32 s63, s68\n" \
  "s_addc_u32 s90, s90, 0\n" \
  "s_min_u32 s67, s90, s35\n" \
  "s_add_u32 s68, s68, 1\n" \
  "s_mul_i32 s90, s68, s39\n" \
  "s_mul_hi_u32 s91, s68, s39\n" \
  "s_lshr_b64 s[90:91], s[90:91], 20\n" \
  "s_mul_hi_u32 s63, s90, s36\n" \
  "s_cmp_lt_u32 s63, s68\n" \
  "s_addc_u32 s90, s90, 0\n" \
  "s_mul_hi_u32 s63, s90, s36\n" \
  "s_cmp_lt_u32 s63, s68\n" \
  "s_addc_u32 s90, s90, 0\n" \
  "s_min_u32 s68, s90, s35\n" \
  "s_sub_u32 s68, s68, s67\n" \
  "s_mul_i32 s63, s71, s35\n" \
  "s_lshr_b32 s63, s63, 3\n" \
  "s_lshl3_add_u32 s63, s66, s63\n" \
  "s_sub_u32 s66, 7, s44\n" \
  "s_lshl_b32 s66, s66, 8\n" \
  "s_add_u32 s66, s66, s63\n" \
  "s_lshl_b32 s66, s66, 10\n" \
  "v_lshlrev_b32 v20, 7, v86\n" \
  "s_lshl_b32 s71, s71, 1\n" \
  "s_mov_b32 m0, 35904\n" \
  "v_lshlrev_b32 v50, 5, v86\n" \
  "v_and_b32 v50, 0x60, v50\n" \
  "v_bfrev_b32 v51, 1\n" \
  "s_lshl_b32 s63, %[blk], 6\n" \
  "s_add_u32 s63, s63, 33024\n" \
  "s_cmp_lg_u32 s44, 7\n" \
  "s_waitcnt vmcnt(1) lgkmcnt(0)\n" \
  "s_barrier\n" \
  "s_cbranch_scc1 Ls1_%=\n" \
  "s_mov_b64 exec, 1\n" \
  "buffer_store_dwordx2 v[88:89], off, s[24:27], s63 sc1\n" \
  "s_mov_b64 exec, -1\n" \
  "Ls1_%=:\n" \
  "ds_read_b96 v[56:58], v94 offset:1472\n" \
  "ds_read_b128 v[24:27], v50 offset:36160\n" \
  "ds_read_b128 v[28:31], v50 offset:36176\n" \
  "ds_read_b64 v[32:33], v13 offset:36288\n" \
  "ds_read_b64 v[36:37], v13 offset:36296\n" \
  "s_waitcnt lgkmcnt(4)\n" \
  "v_readfirstlane_b32 s50, v56\n" \
  "v_readfirstlane_b32 s51, v57\n" \
  "v_readfirstlane_b32 s69, v58\n" \
  "s_lshl_b32 s64, s50, 22\n" \
  "s_lshr_b64 s[90:91], s[50:51], 10\n" \
  "s_mov_b32 s65, s90\n" \
  "s_cmp_lg_u32 s44, 7\n" \
  "s_cbranch_scc1 Lp2_%=\n" \
  "s_waitcnt vmcnt(1)\n" \
  "s_branch Lchk%=\n" \
  "Lpoll%=:\n" \
  "buffer_load_dwordx2 v[44:45], v17, s[24:27], s61 offen sc1\n" \
  "Lp2_%=:\n" \
  "s_waitcnt vmcnt(0)\n" \
  "Lchk%=:\n" \
  "v_and_b32 v50, 0xffc00000, v44\n" \
  "v_cmp_eq_u32 s[90:91], s65, v45\n" \
  "v_cmp_eq_u32 vcc, s64, v50\n" \
  "s_and_b64 vcc, vcc, s[90:91]\n" \
  "s_andn2_b64 vcc, vcc, s[88:89]\n" \
  "s_or_b64 s[88:89], s[88:89], vcc\n" \
  "v_cndmask_b32 v48, v48, v44, vcc\n" \
  "s_cmp_eq_u64 s[88:89], -1\n" \
  "v_cndmask_b32 v17, v17, v51, vcc\n" \
  "s_cbranch_scc1 Lseen%=\n" \
  "s_add_u32 s92, s92, 1\n" \
  "s_cmp_lt_u32 s92, 4096\n" \
  "s_cbranch_scc1 Lpoll%=\n" \
  "v_mov_b32 v48, 0\n" \
  "Lseen%=:\n" \
  "s_cmp_lt_u32 s44, 6\n" \
  "s_cbranch_scc1 Lq3_%=\n" \
  "buffer_load_dword v20, s[28:31], s66 offen lds\n" \
  "Lq3_%=:\n" \
  "v_bfe_u32 v49, v48, 11, 11\n" \
  "v_and_b32 v50, 0x7ff, v48\n" \
  "v_bfe_u32 v52, v13, 4, 1\n" \
  "v_add_u32_dpp v87, v49, v49 row_shr:1 row_mask:0xf bank_mask:0xf bound_ctrl:1\n" \
  "v_lshl_add_u32 v50, v50, 2, v40\n" \
  "v_lshlrev_b32 v53, v52, v49\n" \
  "v_add_u32_dpp v87, v87, v87 row_shr:2 row_mask:0xf bank_mask:0xf bound_ctrl:1\n" \
  "v_sub_u32 v49, v49, v52\n" \
  "v_and_b32 v85, -8, v41\n" \
  "v_add_u32_dpp v87, v87, v87 row_shr:4 row_mask:0xf bank_mask:0xf bound_ctrl:1\n" \
  "v_add_u32 v85, v85, v54\n" \
  "s_nop 0\n" \
  "v_add_u32_dpp v87, v87, v87 row_shr:8 row_mask:0xf bank_mask:0xf bound_ctrl:1\n" \
  "s_nop 1\n" \
  "v_add_u32_dpp v87, v87, v87 row_bcast:15 row_mask:0xa bank_mask:0xf\n" \
  "s_nop 1\n" \
  "v_add_u32_dpp v87, v87, v87 row_bcast:31 row_mask:0xc bank_mask:0xf\n" \
  "v_sub_u32 v53, v87, v53\n" \
  "v_readlane_b32 s62, v87, 63\n" \
  "v_lshl_add_u32 v53, v53, 1, v41\n" \
  "v_lshl_add_u32 v49, v49, 2, v53\n" \
  "s_lshr_b32 s62, s62, 1\n" \
  "s_cmp_lt_u32 s62, 256\n" \
  "s_cbranch_scc1 Ll0_%=\n" \
  "s_mov_b32 s62, 0\n" \
  "ds_or_b32 v94, v92\n" \
  "s_branch Ll2_%=\n" \
  "Ll0_%=:\n" \
  "v_cmp_lt_i32 vcc, v53, v49\n" \
  "s_mov_b64 exec, vcc\n" \
  "Ll1_%=:\n" \
  "ds_write_b32 v53, v50\n" \
  "v_add_u32 v53, 8, v53\n" \
  "v_cmp_lt_i32 vcc, v53, v49\n" \
  "v_add_u32 v50, 8, v50\n" \
  "s_and_b64 exec, vcc, vcc\n" \
  "s_cbranch_scc1 Ll1_%=\n" \
  "s_mov_b64 exec, -1\n" \
  "Ll2_%=:\n" \
  "ds_read2st64_b32 v[56:57], v85 offset1:1\n" \
  "ds_read2st64_b32 v[58:59], v85 offset0:2 offset1:3\n" \
  "v_mov_b32 v50, s62\n" \
  "s_mov_b64 exec, 1\n" \
  "ds_add_u32 v94, v50 offset:4\n" \
  "s_mov_b64 exec, -1\n" \
  "v_cmp_gt_i32 s[80:81], s62, v86\n" \
  "s_sub_i32 s63, s62, 64\n" \
  "v_cmp_gt_i32 s[82:83], s63, v86\n" \
  "s_sub_i32 s63, s62, 128\n" \
  "v_cmp_gt_i32 s[84:85], s63, v86\n" \
  "s_sub_i32 s63, s62, 192\n" \
  "v_cmp_gt_i32 s[86:87], s63, v86\n" \
  "v_bfrev_b32 v50, 4\n" \
  "s_waitcnt lgkmcnt(1)\n" \
  "v_cndmask_b32 v56, v50, v56, s[80:81]\n" \
  "v_cndmask_b32 v57, v50, v57, s[82:83]\n" \
  "v_cndmask_b32 v58, v50, v58, s[84:85]\n" \
  "v_cndmask_b32 v59, v50, v59, s[86:87]\n" \
  "s_sleep 4\n" \
  "v_mul_u32_u24 v18, 64, v55\n" \
  "s_mov_b32 s93, 33024\n" \
  "s_mov_b64 s[88:89], 0\n" \
  "s_mov_b32 s63, 0xbfb8aa3b\n" \
  "v_lshlrev_b32 v100, 17, %[tid]\n" \
  "v_add_u32 v103, 0x3f810000, v100\n" \
  "v_add_u32 v102, 0x3f800000, v100\n" \
  "v_add_u32 v101, 0x3b810000, v100\n" \
  "v_add_u32 v100, 0x3b800000, v100\n" \
  "v_mul_f32 v104, s63, v100\n" \
  "v_mul_f32 v105, s63, v101\n" \
  "v_mul_f32 v106, s63, v102\n" \
  "v_mul_f32 v107, s63, v103\n" \
  "v_exp_f32 v104, v104\n" \
  "v_exp_f32 v105, v105\n" \
  "v_exp_f32 v106, v106\n" \
  "v_exp_f32 v107, v107\n" \
  "v_lshlrev_b32 v96, 2, %[tid]\n" \
  "v_add_f32 v108, 1.0, v104\n" \
  "v_add_f32 v109, 1.0, v105\n" \
  "v_add_f32 v110, 1.0, v106\n" \
  "v_add_f32 v111, 1.0, v107\n" \
  "v_rcp_f32 v108, v108\n" \
  "v_rcp_f32 v109, v109\n" \
  "v_rcp_f32 v110, v110\n" \
  "v_rcp_f32 v111, v111\n" \
  "v_add_u32 v97, 0x10000, v96\n" \
  "v_pk_mul_f32 v[100:101], v[100:101], v[108:109]\n" \
  "v_pk_mul_f32 v[102:103], v[102:103], v[110:111]\n" \
  "v_pk_mul_f32 v[104:105], v[104:105], v[100:101] neg_lo:[0,1] neg_hi:[0,1]\n" \
  "v_pk_mul_f32 v[106:107], v[106:107], v[102:103] neg_lo:[0,1] neg_hi:[0,1]\n" \
  "v_cvt_pk_bf16_f32 v100, v100, v101\n" \
  "v_cvt_pk_bf16_f32 v102, v102, v103\n" \
  "v_cvt_pk_bf16_f32 v104, v104, v105\n" \
  "v_cvt_pk_bf16_f32 v106, v106, v107\n" \
  "ds_write2st64_b32 v96, v100, v102 offset0:119 offset1:127\n" \
  "ds_write2st64_b32 v97, v104, v106 offset0:119 offset1:127\n" \
  "buffer_load_dwordx2 v[44:45], v18, s[24:27], s93 offen sc1\n" \
  "v_mov_b64 v[128:129], 0\n" \
  "v_mov_b64 v[130:131], 0\n" \
  "v_mov_b64 v[132:133], 0\n" \
  "v_mov_b64 v[134:135], 0\n" \
  "v_mov_b64 v[136:137], 0\n" \
  "v_mov_b64 v[138:139], 0\n" \
  "v_mov_b64 v[140:141], 0\n" \
  "v_mov_b64 v[142:143], 0\n" \
  "v_mov_b64 v[144:145], 0\n" \
  "v_mov_b64 v[146:147], 0\n" \
  "v_mov_b64 v[148:149], 0\n" \
  "v_mov_b64 v[150:151], 0\n" \
  "v_mov_b64 v[152:153], 0\n" \
  "v_mov_b64 v[154:155], 0\n" \
  "v_mov_b64 v[156:157], 0\n" \
  "v_mov_b64 v[158:159], 0\n" \
  "v_mov_b64 v[160:161], 0\n" \
  "v_mov_b64 v[162:163], 0\n" \
  "v_mov_b64 v[164:165], 0\n" \
  "v_mov_b64 v[166:167], 0\n" \
  "v_mov_b64 v[168:169], 0\n" \
  "v_mov_b64 v[170:171], 0\n" \
  "v_mov_b64 v[172:173], 0\n" \
  "v_mov_b64 v[174:175], 0\n" \
  "v_mov_b64 v[176:177], 0\n" \
  "v_mov_b64 v[178:179], 0\n" \
  "v_mov_b64 v[180:181], 0\n" \
  "v_mov_b64 v[182:183], 0\n" \
  "v_mov_b64 v[184:185], 0\n" \
  "v_mov_b64 v[186:187], 0\n" \
  "v_mov_b64 v[188:189], 0\n" \
  "v_mov_b64 v[190:191], 0\n" \
  "v_mov_b64 v[192:193], 0\n" \
  "v_mov_b64 v[194:195], 0\n" \
  "v_mov_b64 v[196:197], 0\n" \
  "v_mov_b64 v[198:199], 0\n" \
  "v_mov_b64 v[200:201], 0\n" \
  "v_mov_b64 v[202:203], 0\n" \
  "v_mov_b64 v[204:205], 0\n" \
  "v_mov_b64 v[206:207], 0\n" \
  "s_sleep 4\n" \
  "Ld1_%=:\n" \
  "buffer_load_dwordx2 v[46:47], v18, s[24:27], s93 offen sc1\n" \
  "s_waitcnt vmcnt(1)\n" \
  "v_cmp_eq_u32 s[90:91], s65, v45\n" \
  "v_cmp_eq_u32 vcc, s64, v44\n" \
  "s_mov_b32 s63, 606464\n" \
  "s_and_b64 vcc, vcc, s[90:91]\n" \
  "s_or_b64 s[88:89], s[88:89], vcc\n" \
  "s_cmp_eq_u64 s[88:89], -1\n" \
  "s_cbranch_scc1 Ld2_%=\n" \
  "v_cndmask_b32 v18, v18, v51, vcc\n" \
  "buffer_load_dwordx2 v[44:45], v18, s[24:27], s93 offen sc1\n" \
  "s_waitcnt vmcnt(1)\n" \
  "v_cmp_eq_u32 s[90:91], s65, v47\n" \
  "v_cmp_eq_u32 vcc, s64, v46\n" \
  "s_mov_b32 s63, 606464\n" \
  "s_and_b64 vcc, vcc, s[90:91]\n" \
  "s_or_b64 s[88:89], s[88:89], vcc\n" \
  "s_cmp_eq_u64 s[88:89], -1\n" \
  "s_cbranch_scc1 Ld2_%=\n" \
  "v_cndmask_b32 v18, v18, v51, vcc\n" \
  "s_add_u32 s92, s92, 1\n" \
  "s_cmp_lt_u32 s92, 4096\n" \
  "s_cbranch_scc1 Ld1_%=\n" \
  "s_mov_b64 s[80:81], 0\n" \
  "s_mov_b64 s[82:83], 0\n" \
  "s_mov_b64 s[84:85], 0\n" \
  "s_mov_b64 s[86:87], 0\n" \
  "Ld2_%=:\n" \
  "s_mov_b32 s93, 1917184\n" \
  "s_cmp_le_i32 s62, 192\n" \
  "s_cbranch_scc1 Lk529_%=\n" \
  "buffer_load_dword v63, v59, s[24:27], s63 offen sc1\n" \
  "v_lshlrev_b32 v59, 2, v59\n" \
  "buffer_load_dwordx4 v[228:231], v59, s[24:27], s93 offen sc1\n" \
  "Lk529_%=:\n" \
  "buffer_load_dword v60, v56, s[24:27], s63 offen sc1\n" \
  "buffer_load_dword v61, v57, s[24:27], s63 offen sc1\n" \
  "buffer_load_dword v62, v58, s[24:27], s63 offen sc1\n" \
  "v_lshlrev_b32 v56, 2, v56\n" \
  "v_lshlrev_b32 v57, 2, v57\n" \
  "v_lshlrev_b32 v58, 2, v58\n" \
  "s_lshl_b32 s75, s44, 8\n" \
  "s_add_u32 s75, s75, 106816\n" \
  "s_mov_b32 s52, s28\n" \
  "s_or_b32 s53, s29, 0x4000000\n" \
  "s_mov_b32 s54, s35\n" \
  "s_mov_b32 s55, 0x20000\n" \
  "s_lshl_b32 s63, s67, 10\n" \
  "s_add_u32 s56, s32, s63\n" \
  "s_addc_u32 s57, s33, 0\n" \
  "s_lshl_b32 s58, s68, 10\n" \
  "s_mov_b32 s59, 0x20000\n" \
  "s_lshl_b32 s67, s67, 24\n" \
  "s_mov_b32 s70, 32768\n" \
  "s_mul_i32 s63, s44, 3328\n" \
  "s_add_u32 s63, s63, 37328\n" \
  "v_add_u32 v21, s63, v13\n" \
  "v_and_b32 v15, 0xf0, v54\n" \
  "v_add_u32 v15, s63, v15\n" \
  "v_and_b32 v23, 0xf0, v13\n" \
  "v_add3_u32 v16, v23, s63, 8\n" \
  "s_mul_i32 s63, s44, 832\n" \
  "s_add_u32 s63, s63, 100096\n" \
  "v_mov_b32 v14, s63\n" \
  "v_and_b32 v50, 0x300, v13\n" \
  "v_and_or_b32 v19, v54, 60, v50\n" \
  "v_add_u32 v19, 108864, v19\n" \
  "v_lshl_add_u32 v23, v50, 2, v23\n" \
  "s_lshl_b32 s63, s73, 10\n" \
  "s_lshl_b32 s61, s45, 5\n" \
  "s_add_u32 s63, s63, s61\n" \
  "v_add_u32 v23, s63, v23\n" \
  "v_bfe_u32 v50, v86, 2, 2\n" \
  "v_mul_u32_u24 v22, 1056, v50\n" \
  "v_and_b32 v50, 48, v13\n" \
  "v_lshl_add_u32 v22, v50, 2, v22\n" \
  "v_and_b32 v50, 48, v86\n" \
  "s_mul_i32 s63, s73, 1056\n" \
  "s_add_u32 s63, s63, 108864\n" \
  "v_add3_u32 v22, v22, v50, s63\n" \
  "v_mul_u32_u24 v50, 26, v86\n" \
  "v_lshrrev_b32 v50, 8, v50\n" \
  "v_mad_i32_i24 v50, v50, -10, v86\n" \
  "v_lshlrev_b32 v42, 19, v50\n" \
  "s_mul_i32 s63, s44, 11\n" \
  "v_mad_u32_u24 v50, v86, 37, s63\n" \
  "v_and_b32 v50, 63, v50\n" \
  "v_mul_u32_u24 v43, 0xffff, v50\n" \
  "v_add_u32 v43, 64, v43\n" \
  "s_add_u32 s66, s66, 524288\n" \
  "s_cmp_ge_u32 s44, 5\n" \
  "s_cselect_b32 s30, s30, 0\n" \
  "s_cmp_lt_u32 s92, 4096\n" \
  "s_cbranch_scc1 Lq2_%=\n" \
  "v_mov_b32 v53, 3\n" \
  "ds_or_b32 v94, v53\n" \
  "Lq2_%=:\n" \
  "v_mov_b32 v50, 2\n" \
  "s_waitcnt vmcnt(0)\n" \
  "v_subrev_u32 v60, s67, v60\n" \
  "s_mov_b64 exec, s[80:81]\n" \
  "v_lshlrev_b32_sdwa v64, v50, v60 dst_sel:DWORD dst_unused:UNUSED_PAD src0_sel:DWORD src1_sel:BYTE_3\n" \
  "ds_add_u32 v64, v92 offset:35584\n" \
  "v_subrev_u32 v61, s67, v61\n" \
  "s_mov_b64 exec, s[82:83]\n" \
  "v_lshlrev_b32_sdwa v65, v50, v61 dst_sel:DWORD dst_unused:UNUSED_PAD src0_sel:DWORD src1_sel:BYTE_3\n" \
  "ds_add_u32 v65, v92 offset:35584\n" \
  "v_subrev_u32 v62, s67, v62\n" \
  "s_mov_b64 exec, s[84:85]\n" \
  "v_lshlrev_b32_sdwa v66, v50, v62 dst_sel:DWORD dst_unused:UNUSED_PAD src0_sel:DWORD src1_sel:BYTE_3\n" \
  "ds_add_u32 v66, v92 offset:35584\n" \
  "s_cmp_le_i32 s62, 192\n" \
  "s_cbranch_scc1 Lk611_%=\n" \
  "v_subrev_u32 v63, s67, v63\n" \
  "s_mov_b64 exec, s[86:87]\n" \
  "v_lshlrev_b32_sdwa v67, v50, v63 dst_sel:DWORD dst_unused:UNUSED_PAD src0_sel:DWORD src1_sel:BYTE_3\n" \
  "ds_add_u32 v67, v92 offset:35584\n" \
  "Lk611_%=:\n" \
  "s_mov_b64 exec, -1\n" \
  "buffer_load_dwordx4 v[216:219], v56, s[24:27], s93 offen sc1\n" \
  "buffer_load_dwordx4 v[220:223], v57, s[24:27], s93 offen sc1\n" \
  "buffer_load_dwordx4 v[224:227], v58, s[24:27], s93 offen sc1\n" \
  "s_waitcnt lgkmcnt(0)\n" \
  "s_barrier\n" \
  "ds_read_b32 v85, v54 offset:35584\n" \
  "ds_read_b32 v86, v94 offset:4\n" \
  "ds_bpermute_b32 v76, v64, v42\n" \
  "ds_bpermute_b32 v77, v65, v42\n" \
  "ds_bpermute_b32 v78, v66, v42\n" \
  "ds_bpermute_b32 v79, v67, v42\n" \
  "buffer_load_dword v20, s[28:31], s66 offen lds\n" \
  "s_waitcnt lgkmcnt(4)\n" \
  "v_add_u32_dpp v87, v85, v85 row_shr:1 row_mask:0xf bank_mask:0xf bound_ctrl:1\n" \
  "v_cvt_f32_u32 v86, v86\n" \
  "v_rcp_f32 v86, v86\n" \
  "v_add_u32_dpp v87, v87, v87 row_shr:2 row_mask:0xf bank_mask:0xf bound_ctrl:1\n" \
  "v_mul_hi_u32_u24 v68, s37, v60\n" \
  "v_subrev_u32 v68, s71, v68\n" \
  "v_add_u32_dpp v87, v87, v87 row_shr:4 row_mask:0xf bank_mask:0xf bound_ctrl:1\n" \
  "v_and_b32 v68, 15, v68\n" \
  "v_lshl_add_u32 v72, v68, 1, s75\n" \
  "v_add_u32_dpp v87, v87, v87 row_shr:8 row_mask:0xf bank_mask:0xf bound_ctrl:1\n" \
  "v_lshl_add_u32 v68, v68, 3, s73\n" \
  "v_mul_hi_u32_u24 v69, s37, v61\n" \
  "v_add_u32_dpp v87, v87, v87 row_bcast:15 row_mask:0xa bank_mask:0xf\n" \
  "v_subrev_u32 v69, s71, v69\n" \
  "v_and_b32 v69, 15, v69\n" \
  "v_add_u32_dpp v87, v87, v87 row_bcast:31 row_mask:0xc bank_mask:0xf\n" \
  "v_sub_u32 v85, v87, v85\n" \
  "v_cvt_f32_u32 v87, v87\n" \
  "v_cvt_f32_u32 v85, v85\n" \
  "v_mul_f32 v87, v87, v86\n" \
  "v_mul_f32 v85, v85, v86\n" \
  "v_cvt_pknorm_u16_f32 v84, v85, v87\n" \
  "ds_bpermute_b32 v80, v64, v84\n" \
  "ds_bpermute_b32 v81, v65, v84\n" \
  "ds_bpermute_b32 v82, v66, v84\n" \
  "ds_bpermute_b32 v83, v67, v84\n" \
  "v_lshl_add_u32 v73, v69, 1, s75\n" \
  "v_lshl_add_u32 v69, v69, 3, s73\n" \
  "v_mul_hi_u32_u24 v70, s37, v62\n" \
  "v_subrev_u32 v70, s71, v70\n" \
  "v_and_b32 v70, 15, v70\n" \
  "v_lshl_add_u32 v74, v70, 1, s75\n" \
  "v_lshl_add_u32 v70, v70, 3, s73\n" \
  "v_mul_hi_u32_u24 v71, s37, v63\n" \
  "v_subrev_u32 v71, s71, v71\n" \
  "v_and_b32 v71, 15, v71\n" \
  "v_lshl_add_u32 v75, v71, 1, s75\n" \
  "v_lshl_add_u32 v71, v71, 3, s73\n" \
  "s_mov_b32 s63, 0xffff\n" \
  "s_waitcnt lgkmcnt(4)\n" \
  "v_and_or_b32 v76, v60, s63, v76\n" \
  "v_and_or_b32 v77, v61, s63, v77\n" \
  "v_and_or_b32 v78, v62, s63, v78\n" \
  "v_and_or_b32 v79, v63, s63, v79\n" \
  "s_waitcnt lgkmcnt(3)\n" \
  "v_dot2_u32_u16 v80, v80, v43, 0\n" \
  "s_waitcnt lgkmcnt(2)\n" \
  "v_dot2_u32_u16 v81, v81, v43, 0\n" \
  "s_waitcnt lgkmcnt(1)\n" \
  "v_dot2_u32_u16 v82, v82, v43, 0\n" \
  "s_waitcnt lgkmcnt(0)\n" \
  "v_dot2_u32_u16 v83, v83, v43, 0\n" \
  "v_lshrrev_b32 v60, 19, v80\n" \
  "s_mov_b64 exec, s[80:81]\n" \
  "v_lshl_add_u32 v68, v60, 7, v68\n" \
  "ds_add_rtn_u32 v80, v68, v93 offset:34560\n" \
  "v_lshrrev_b32 v61, 19, v81\n" \
  "s_mov_b64 exec, s[82:83]\n" \
  "v_lshl_add_u32 v69, v61, 7, v69\n" \
  "ds_add_rtn_u32 v81, v69, v93 offset:34560\n" \
  "v_lshrrev_b32 v62, 19, v82\n" \
  "s_mov_b64 exec, s[84:85]\n" \
  "v_lshl_add_u32 v70, v62, 7, v70\n" \
  "ds_add_rtn_u32 v82, v70, v93 offset:34560\n" \
  "s_cmp_le_i32 s62, 192\n" \
  "s_cbranch_scc1 Lk696_%=\n" \
  "v_lshrrev_b32 v63, 19, v83\n" \
  "s_mov_b64 exec, s[86:87]\n" \
  "v_lshl_add_u32 v71, v63, 7, v71\n" \
  "ds_add_rtn_u32 v83, v71, v93 offset:34560\n" \
  "Lk696_%=:\n" \
  "s_mov_b64 exec, -1\n" \
  "v_lshl_add_u32 v72, v60, 5, v72\n" \
  "v_lshl_add_u32 v73, v61, 5, v73\n" \
  "v_lshl_add_u32 v74, v62, 5, v74\n" \
  "v_lshl_add_u32 v75, v63, 5, v75\n" \
  "v_lshrrev_b32 v50, 1, v13\n" \
  "v_lshrrev_b32 v51, 3, v13\n" \
  "v_add_u32 v51, s75, v51\n" \
  "v_lshrrev_b32 v52, 8, v13\n" \
  "v_mul_u32_u24 v52, 0xd000d0, v52\n" \
  "v_add_u32 v52, 0x3400000, v52\n" \
  "s_mov_b32 s63, 0x01010101\n" \
  "s_waitcnt lgkmcnt(0)\n" \
  "s_barrier\n" \
  "ds_read_b64 v[88:89], v50 offset:34560\n" \
  "ds_read_b64 v[90:91], v50 offset:35072\n" \
  "v_bfe_u32 v80, v80, s45, 8\n" \
  "v_bfe_u32 v81, v81, s45, 8\n" \
  "v_bfe_u32 v82, v82, s45, 8\n" \
  "v_bfe_u32 v83, v83, s45, 8\n" \
  "s_lshl_b32 s72, s44, 4\n" \
  "s_or_b32 s72, s72, 15\n" \
  "s_lshl_b32 s73, s73, 2\n" \
  "s_waitcnt lgkmcnt(0)\n" \
  "v_dot4_u32_u8 v44, v88, s63, 0\n" \
  "v_dot4_u32_u8 v45, v90, s63, 0\n" \
  "v_dot4_u32_u8 v44, v89, s63, v44\n" \
  "v_dot4_u32_u8 v45, v91, s63, v45\n" \
  "v_dot4_u32_u8 v46, v88, s46, 0\n" \
  "v_dot4_u32_u8 v47, v90, s46, 0\n" \
  "v_dot4_u32_u8 v46, v89, s47, v46\n" \
  "v_lshl_or_b32 v44, v45, 16, v44\n" \
  "v_dot4_u32_u8 v47, v91, s47, v47\n" \
  "s_nop 0\n" \
  "v_add_u32_dpp v87, v44, v44 row_shr:1 row_mask:0xf bank_mask:0xf bound_ctrl:1\n" \
  "s_nop 0\n" \
  "v_lshl_or_b32 v46, v47, 16, v46\n" \
  "v_add_u32_dpp v87, v87, v87 row_shr:2 row_mask:0xf bank_mask:0xf bound_ctrl:1\n" \
  "v_sub_u32 v52, v52, v46\n" \
  "s_nop 0\n" \
  "v_add_u32_dpp v87, v87, v87 row_shr:4 row_mask:0xf bank_mask:0xf bound_ctrl:1\n" \
  "s_nop 1\n" \
  "v_add_u32_dpp v87, v87, v87 row_shr:8 row_mask:0xf bank_mask:0xf bound_ctrl:1\n" \
  "v_add_u32 v52, v52, v87\n" \
  "ds_write_b16 v51, v52\n" \
  "ds_write_b16_d16_hi v51, v52 offset:128\n" \
  "ds_read_u16 v72, v72\n" \
  "ds_read_u16 v73, v73\n" \
  "ds_read_u16 v74, v74\n" \
  "ds_read_u16 v75, v75\n" \
  "v_readlane_b32 s60, v87, s72\n" \
  "s_movk_i32 s63, 200\n" \
  "v_cmp_lt_u32_sdwa s[90:91], s63, v87 src0_sel:DWORD src1_sel:WORD_0\n" \
  "v_cmp_lt_u32_sdwa vcc, s63, v87 src0_sel:DWORD src1_sel:WORD_1\n" \
  "s_lshr_b32 s72, s60, s73\n" \
  "s_and_b32 s72, s72, 0xffff\n" \
  "s_add_u32 s60, s72, 3\n" \
  "s_lshr_b32 s60, s60, 2\n" \
  "s_max_u32 s60, s60, 1\n" \
  "s_lshl2_add_u32 s73, s60, 8\n" \
  "s_sub_u32 s73, s73, s72\n" \
  "s_bfm_b64 s[88:89], s73, 0\n" \
  "s_or_b64 vcc, vcc, s[90:91]\n" \
  "s_cbranch_scc0 Lo1_%=\n" \
  "s_mov_b64 s[80:81], 0\n" \
  "s_mov_b64 s[82:83], 0\n" \
  "s_mov_b64 s[84:85], 0\n" \
  "s_mov_b64 s[86:87], 0\n" \
  "s_mov_b64 s[88:89], 0xfff\n" \
  "s_mov_b32 s72, 0\n" \
  "s_mov_b32 s60, 1\n" \
  "ds_or_b32 v94, v92\n" \
  "Lo1_%=:\n" \
  "v_lshrrev_b32 v50, 2, v13\n" \
  "s_lshl_b32 s72, s72, 2\n" \
  "v_add3_u32 v50, v50, s72, v14\n" \
  "v_mov_b32 v51, 0x50ffff\n" \
  "s_mov_b32 s63, 100096\n" \
  "s_mov_b64 exec, s[88:89]\n" \
  "ds_write_b32 v50, v51\n" \
  "s_waitcnt lgkmcnt(1)\n" \
  "s_mov_b64 exec, s[80:81]\n" \
  "v_add_u32 v80, v80, v72\n" \
  "v_lshl_add_u32 v72, v80, 2, s63\n" \
  "ds_write_b32 v72, v76\n" \
  "s_mov_b64 exec, s[82:83]\n" \
  "v_add_u32 v81, v81, v73\n" \
  "v_lshl_add_u32 v73, v81, 2, s63\n" \
  "ds_write_b32 v73, v77\n" \
  "s_mov_b64 exec, s[84:85]\n" \
  "v_add_u32 v82, v82, v74\n" \
  "v_lshl_add_u32 v74, v82, 2, s63\n" \
  "ds_write_b32 v74, v78\n" \
  "s_cmp_le_i32 s62, 192\n" \
  "s_cbranch_scc1 Lk796_%=\n" \
  "s_mov_b64 exec, s[86:87]\n" \
  "v_add_u32 v83, v83, v75\n" \
  "v_lshl_add_u32 v75, v83, 2, s63\n" \
  "ds_write_b32 v75, v79\n" \
  "Lk796_%=:\n" \
  "s_mov_b64 exec, -1\n" \
  "s_sub_u32 s60, s60, 1\n" \
  "s_lshl_b32 s63, s60, 4\n" \
  "v_sub_u32 v50, v14, v13\n" \
  "v_add_u32 v50, s63, v50\n" \
  "s_waitcnt lgkmcnt(0)\n" \
  "s_barrier\n" \
  "ds_read_b128 v[56:59], v14\n" \
  "ds_read_b128 v[60:63], v14 offset:16\n" \
  "ds_read_b128 v[64:67], v50\n" \
  "v_lshlrev_b32 v232, 4, v80\n" \
  "v_lshlrev_b32 v233, 4, v81\n" \
  "v_lshlrev_b32 v234, 4, v82\n" \
  "v_lshlrev_b32 v235, 4, v83\n" \
  "s_waitcnt lgkmcnt(1)\n" \
  "v_and_b32 v12, 0xffff, v56\n" \
  "buffer_load_dwordx4 v[96:99], v[12:13], s[52:55], 0 idxen offen\n" \
  "v_and_b32 v12, 0xffff, v57\n" \
  "buffer_load_dwordx4 v[100:103], v[12:13], s[52:55], 0 idxen offen\n" \
  "v_and_b32 v12, 0xffff, v58\n" \
  "buffer_load_dwordx4 v[104:107], v[12:13], s[52:55], 0 idxen offen\n" \
  "v_and_b32 v12, 0xffff, v59\n" \
  "buffer_load_dwordx4 v[108:111], v[12:13], s[52:55], 0 idxen offen\n" \
  "v_and_b32 v12, 0xffff, v60\n" \
  "buffer_load_dwordx4 v[112:115], v[12:13], s[52:55], 0 idxen offen\n" \
  "v_and_b32 v12, 0xffff, v61\n" \
  "buffer_load_dwordx4 v[116:119], v[12:13], s[52:55], 0 idxen offen\n" \
  "v_and_b32 v12, 0xffff, v62\n" \
  "buffer_load_dwordx4 v[120:123], v[12:13], s[52:55], 0 idxen offen\n" \
  "v_and_b32 v12, 0xffff, v63\n" \
  "buffer_load_dwordx4 v[124:127], v[12:13], s[52:55], 0 idxen offen\n" \
  "s_waitcnt vmcnt(9)\n" \
  "s_mov_b64 exec, s[80:81]\n" \
  "ds_write_b128 v232, v[216:219] offset:37328\n" \
  "s_mov_b64 exec, s[82:83]\n" \
  "ds_write_b128 v233, v[220:223] offset:37328\n" \
  "s_mov_b64 exec, s[84:85]\n" \
  "ds_write_b128 v234, v[224:227] offset:37328\n" \
  "s_cmp_le_i32 s62, 192\n" \
  "s_cbranch_scc1 Lk841_%=\n" \
  "s_mov_b64 exec, s[86:87]\n" \
  "ds_write_b128 v235, v[228:231] offset:37328\n" \
  "Lk841_%=:\n" \
  "s_mov_b64 exec, -1\n" \
  "s_mov_b32 s63, 0x003f0001\n" \
  "v_dot2_u32_u16 v52, v84, s63, 0\n" \
  "v_bfe_u32 v51, v84, 13, 3\n" \
  "s_bfm_b64 s[88:89], s68, 0\n" \
  "v_mov_b32 v18, v94\n" \
  "v_cmp_ge_u32 s[90:91], s44, v51\n" \
  "v_lshrrev_b32 v52, 19, v52\n" \
  "s_and_b64 s[88:89], s[88:89], s[90:91]\n" \
  "v_cmp_le_u32 s[90:91], s44, v52\n" \
  "s_and_b64 s[88:89], s[88:89], s[90:91]\n" \
  "s_ff1_i32_b64 s72, s[88:89]\n" \
  "s_flbit_i32_b64 s74, s[88:89]\n" \
  "s_xor_b32 s74, s74, 63\n" \
  "s_mov_b32 s63, 0x0c0c0602\n" \
  "s_waitcnt lgkmcnt(0)\n" \
  "s_barrier\n" \
  "ds_read_b64 v[92:93], v16\n" \
  "ds_read_b64 v[94:95], v15\n" \
  "ds_read_u16 v12, v14 offset:32\n" \
  "v_perm_b32 v64, v65, v64, s63\n" \
  "v_perm_b32 v66, v67, v66, s63\n" \
  "v_readlane_b32 s93, v51, s72\n" \
  "v_readlane_b32 s75, v52, s74\n" \
  "v_lshl_or_b32 v17, v66, 16, v64\n" \
  "v_readlane_b32 s63, v51, s74\n" \
  "v_readlane_b32 s73, v42, s72\n" \
  "v_readlane_b32 s61, v17, s60\n" \
  "s_sub_u32 s74, s74, s72\n" \
  "s_sub_u32 s75, s75, s44\n" \
  "s_cmp_eq_u32 s63, s44\n" \
  "s_cselect_b32 s75, s75, 0\n" \
  "s_lshr_b32 s73, s73, 16\n" \
  "s_mul_i32 s72, s72, 1056\n" \
  "s_or_b32 s63, %[blk], s44\n" \
  "s_cbranch_scc0 Lep0_%=\n" \
  "Lgo%=:\n" \
  "s_waitcnt lgkmcnt(0)\n" \
  "v_mfma_f32_4x4x4_16b_bf16 v[56:59], v[92:93], v[32:33], 0 cbsz:4\n" \
  "v_mfma_f32_4x4x4_16b_bf16 v[60:63], v[92:93], v[36:37], 0 cbsz:4\n" \
  "v_mfma_f32_4x4x4_16b_bf16 v[64:67], v[92:93], v[32:33], 0 cbsz:4 abid:1\n" \
  "v_mfma_f32_4x4x4_16b_bf16 v[68:71], v[92:93], v[36:37], 0 cbsz:4 abid:1\n" \
  "v_mfma_f32_4x4x4_16b_bf16 v[84:87], v[94:95], v[24:25], 0\n" \
  "v_mfma_f32_4x4x4_16b_bf16 v[88:91], v[94:95], v[26:27], 0\n" \
  "v_mfma_f32_4x4x4_16b_bf16 v[48:51], v[94:95], v[28:29], 0\n" \
  "v_mfma_f32_4x4x4_16b_bf16 v[52:55], v[94:95], v[30:31], 0\n" \
  "s_nop 1\n" \
  "v_cvt_pk_bf16_f32 v57, v56, v57\n" \
  "v_lshlrev_b32_sdwa v56, 1, v57 dst_sel:DWORD dst_unused:UNUSED_PAD src0_sel:DWORD src1_sel:WORD_0\n" \
  "v_lshlrev_b32_sdwa v57, 1, v57 dst_sel:DWORD dst_unused:UNUSED_PAD src0_sel:DWORD src1_sel:WORD_1\n" \
  "ds_read_u16_d16_hi v56, v56\n" \
  "ds_read_u16_d16_hi v57, v57\n" \
  "v_cvt_pk_bf16_f32 v59, v58, v59\n" \
  "v_lshlrev_b32_sdwa v58, 1, v59 dst_sel:DWORD dst_unused:UNUSED_PAD src0_sel:DWORD src1_sel:WORD_0\n" \
  "v_lshlrev_b32_sdwa v59, 1, v59 dst_sel:DWORD dst_unused:UNUSED_PAD src0_sel:DWORD src1_sel:WORD_1\n" \
  "ds_read_u16_d16_hi v58, v58\n" \
  "ds_read_u16_d16_hi v59, v59\n" \
  "v_cvt_pk_bf16_f32 v61, v60, v61\n" \
  "v_lshlrev_b32_sdwa v60, 1, v61 dst_sel:DWORD dst_unused:UNUSED_PAD src0_sel:DWORD src1_sel:WORD_0\n" \
  "v_lshlrev_b32_sdwa v61, 1, v61 dst_sel:DWORD dst_unused:UNUSED_PAD src0_sel:DWORD src1_sel:WORD_1\n" \
  "ds_read_u16_d16_hi v60, v60\n" \
  "ds_read_u16_d16_hi v61, v61\n" \
  "v_cvt_pk_bf16_f32 v63, v62, v63\n" \
  "v_lshlrev_b32_sdwa v62, 1, v63 dst_sel:DWORD dst_unused:UNUSED_PAD src0_sel:DWORD src1_sel:WORD_0\n" \
  "v_lshlrev_b32_sdwa v63, 1, v63 dst_sel:DWORD dst_unused:UNUSED_PAD src0_sel:DWORD src1_sel:WORD_1\n" \
  "ds_read_u16_d16_hi v62, v62\n" \
  "ds_read_u16_d16_hi v63, v63\n" \
  "v_cvt_pk_bf16_f32 v76, v84, v88\n" \
  "v_cvt_pk_bf16_f32 v77, v48, v52\n" \
  "s_mov_b32 s63, 0x7fff7fff\n" \
  "v_and_b32 v41, s63, v218\n" \
  "v_and_b32 v42, s63, v219\n" \
  "v_pk_max_u16 v40, v41, v42\n" \
  "v_and_b32 v41, s63, v222\n" \
  "v_and_b32 v42, s63, v223\n" \
  "v_pk_max_u16 v41, v41, v40\n" \
  "v_pk_max_u16 v40, v41, v42\n" \
  "v_and_b32 v41, s63, v226\n" \
  "v_and_b32 v42, s63, v227\n" \
  "v_pk_max_u16 v41, v41, v40\n" \
  "v_pk_max_u16 v40, v41, v42\n" \
  "s_cmp_le_i32 s62, 192\n" \
  "s_cbranch_scc1 Lk927_%=\n" \
  "v_and_b32 v41, s63, v230\n" \
  "v_and_b32 v42, s63, v231\n" \
  "v_pk_max_u16 v41, v41, v40\n" \
  "v_pk_max_u16 v40, v41, v42\n" \
  "Lk927_%=:\n" \
  "v_max_u16_sdwa v40, v40, v40 dst_sel:DWORD dst_unused:UNUSED_PAD src0_sel:WORD_0 src1_sel:WORD_1\n" \
  "s_nop 0\n" \
  "v_cmp_lt_i32 vcc, s69, v40\n" \
  "s_cmp_eq_u64 vcc, 0\n" \
  "s_cselect_b32 s74, s74, -1\n" \
  "s_waitcnt vmcnt(7) lgkmcnt(0)\n" \
  "v_mfma_f32_4x4x4_16b_bf16 v[48:51], v[76:77], v[96:97], 0 cbsz:4\n" \
  "v_mfma_f32_4x4x4_16b_bf16 v[52:55], v[76:77], v[98:99], 0 cbsz:4\n" \
  "s_nop 4\n" \
  "Lbody%=:\n" \
  "s_set_gpr_idx_on s61, gpr_idx(SRC2,DST)\n" \
  "v_pk_fma_f32 v[128:129], v[56:57], v[48:49], v[128:129] op_sel:[0,0,0] op_sel_hi:[0,1,1]\n" \
  "v_pk_fma_f32 v[130:131], v[56:57], v[50:51], v[130:131] op_sel:[0,0,0] op_sel_hi:[0,1,1]\n" \
  "v_pk_fma_f32 v[132:133], v[60:61], v[52:53], v[132:133] op_sel:[0,0,0] op_sel_hi:[0,1,1]\n" \
  "v_pk_fma_f32 v[134:135], v[60:61], v[54:55], v[134:135] op_sel:[0,0,0] op_sel_hi:[0,1,1]\n" \
  "s_set_gpr_idx_off\n" \
  "s_waitcnt vmcnt(6) lgkmcnt(2)\n" \
  "buffer_load_dwordx4 v[96:99], v[12:13], s[52:55], 0 idxen offen\n" \
  "ds_read_u16 v12, v14 offset:36\n" \
  "s_lshr_b32 s61, s61, 8\n" \
  "v_mfma_f32_4x4x4_16b_bf16 v[48:51], v[76:77], v[100:101], 0 cbsz:4 abid:1\n" \
  "v_mfma_f32_4x4x4_16b_bf16 v[52:55], v[76:77], v[102:103], 0 cbsz:4 abid:1\n" \
  "v_cvt_pk_bf16_f32 v65, v64, v65\n" \
  "v_lshlrev_b32_sdwa v64, 1, v65 dst_sel:DWORD dst_unused:UNUSED_PAD src0_sel:DWORD src1_sel:WORD_0\n" \
  "v_lshlrev_b32_sdwa v65, 1, v65 dst_sel:DWORD dst_unused:UNUSED_PAD src0_sel:DWORD src1_sel:WORD_1\n" \
  "ds_read_u16_d16_hi v64, v64\n" \
  "ds_read_u16_d16_hi v65, v65\n" \
  "s_set_gpr_idx_on s61, gpr_idx(SRC2,DST)\n" \
  "v_pk_fma_f32 v[128:129], v[56:57], v[48:49], v[128:129] op_sel:[1,0,0] op_sel_hi:[1,1,1]\n" \
  "v_pk_fma_f32 v[130:131], v[56:57], v[50:51], v[130:131] op_sel:[1,0,0] op_sel_hi:[1,1,1]\n" \
  "v_pk_fma_f32 v[132:133], v[60:61], v[52:53], v[132:133] op_sel:[1,0,0] op_sel_hi:[1,1,1]\n" \
  "v_pk_fma_f32 v[134:135], v[60:61], v[54:55], v[134:135] op_sel:[1,0,0] op_sel_hi:[1,1,1]\n" \
  "s_set_gpr_idx_off\n" \
  "s_waitcnt vmcnt(6) lgkmcnt(2)\n" \
  "buffer_load_dwordx4 v[100:103], v[12:13], s[52:55], 0 idxen offen\n" \
  "ds_read_u16 v12, v14 offset:40\n" \
  "s_lshr_b32 s61, s61, 8\n" \
  "v_mfma_f32_4x4x4_16b_bf16 v[48:51], v[76:77], v[104:105], 0 cbsz:4 abid:2\n" \
  "v_mfma_f32_4x4x4_16b_bf16 v[52:55], v[76:77], v[106:107], 0 cbsz:4 abid:2\n" \
  "v_cvt_pk_bf16_f32 v69, v68, v69\n" \
  "v_lshlrev_b32_sdwa v68, 1, v69 dst_sel:DWORD dst_unused:UNUSED_PAD src0_sel:DWORD src1_sel:WORD_0\n" \
  "v_lshlrev_b32_sdwa v69, 1, v69 dst_sel:DWORD dst_unused:UNUSED_PAD src0_sel:DWORD src1_sel:WORD_1\n" \
  "ds_read_u16_d16_hi v68, v68\n" \
  "ds_read_u16_d16_hi v69, v69\n" \
  "s_set_gpr_idx_on s61, gpr_idx(SRC2,DST)\n" \
  "v_pk_fma_f32 v[128:129], v[58:59], v[48:49], v[128:129] op_sel:[0,0,0] op_sel_hi:[0,1,1]\n" \
  "v_pk_fma_f32 v[130:131], v[58:59], v[50:51], v[130:131] op_sel:[0,0,0] op_sel_hi:[0,1,1]\n" \
  "v_pk_fma_f32 v[132:133], v[62:63], v[52:53], v[132:133] op_sel:[0,0,0] op_sel_hi:[0,1,1]\n" \
  "v_pk_fma_f32 v[134:135], v[62:63], v[54:55], v[134:135] op_sel:[0,0,0] op_sel_hi:[0,1,1]\n" \
  "s_set_gpr_idx_off\n" \
  "s_waitcnt vmcnt(6) lgkmcnt(2)\n" \
  "buffer_load_dwordx4 v[104:107], v[12:13], s[52:55], 0 idxen offen\n" \
  "ds_read_u16 v12, v14 offset:44\n" \
  "s_lshr_b32 s61, s61, 8\n" \
  "v_mfma_f32_4x4x4_16b_bf16 v[48:51], v[76:77], v[108:109], 0 cbsz:4 abid:3\n" \
  "v_mfma_f32_4x4x4_16b_bf16 v[52:55], v[76:77], v[110:111], 0 cbsz:4 abid:3\n" \
  "v_cvt_pk_bf16_f32 v67, v66, v67\n" \
  "v_lshlrev_b32_sdwa v66, 1, v67 dst_sel:DWORD dst_unused:UNUSED_PAD src0_sel:DWORD src1_sel:WORD_0\n" \
  "v_lshlrev_b32_sdwa v67, 1, v67 dst_sel:DWORD dst_unused:UNUSED_PAD src0_sel:DWORD src1_sel:WORD_1\n" \
  "ds_read_u16_d16_hi v66, v66\n" \
  "ds_read_u16_d16_hi v67, v67\n" \
  "s_set_gpr_idx_on s61, gpr_idx(SRC2,DST)\n" \
  "v_pk_fma_f32 v[128:129], v[58:59], v[48:49], v[128:129] op_sel:[1,0,0] op_sel_hi:[1,1,1]\n" \
  "v_pk_fma_f32 v[130:131], v[58:59], v[50:51], v[130:131] op_sel:[1,0,0] op_sel_hi:[1,1,1]\n" \
  "v_pk_fma_f32 v[132:133], v[62:63], v[52:53], v[132:133] op_sel:[1,0,0] op_sel_hi:[1,1,1]\n" \
  "v_pk_fma_f32 v[134:135], v[62:63], v[54:55], v[134:135] op_sel:[1,0,0] op_sel_hi:[1,1,1]\n" \
  "s_set_gpr_idx_off\n" \
  "s_waitcnt vmcnt(6) lgkmcnt(2)\n" \
  "buffer_load_dwordx4 v[108:111], v[12:13], s[52:55], 0 idxen offen\n" \
  "ds_read_u16 v12, v14 offset:48\n" \
  "s_sub_u32 s60, s60, 1\n" \
  "s_cbranch_scc1 Lend%=\n" \
  "v_readlane_b32 s61, v17, s60\n" \
  "s_setprio 2\n" \
  "v_mfma_f32_4x4x4_16b_bf16 v[56:59], v[92:93], v[32:33], 0 cbsz:4 abid:2\n" \
  "v_mfma_f32_4x4x4_16b_bf16 v[60:63], v[92:93], v[36:37], 0 cbsz:4 abid:2\n" \
  "v_mfma_f32_4x4x4_16b_bf16 v[48:51], v[76:77], v[112:113], 0 cbsz:4 abid:4\n" \
  "v_mfma_f32_4x4x4_16b_bf16 v[52:55], v[76:77], v[114:115], 0 cbsz:4 abid:4\n" \
  "v_cvt_pk_bf16_f32 v71, v70, v71\n" \
  "v_lshlrev_b32_sdwa v70, 1, v71 dst_sel:DWORD dst_unused:UNUSED_PAD src0_sel:DWORD src1_sel:WORD_0\n" \
  "v_lshlrev_b32_sdwa v71, 1, v71 dst_sel:DWORD dst_unused:UNUSED_PAD src0_sel:DWORD src1_sel:WORD_1\n" \
  "ds_read_u16_d16_hi v70, v70\n" \
  "ds_read_u16_d16_hi v71, v71\n" \
  "v_add_u32 v16, 0x100, v16\n" \
  "s_set_gpr_idx_on s61, gpr_idx(SRC2,DST)\n" \
  "v_pk_fma_f32 v[128:129], v[64:65], v[48:49], v[128:129] op_sel:[0,0,0] op_sel_hi:[0,1,1]\n" \
  "v_pk_fma_f32 v[130:131], v[64:65], v[50:51], v[130:131] op_sel:[0,0,0] op_sel_hi:[0,1,1]\n" \
  "v_pk_fma_f32 v[132:133], v[68:69], v[52:53], v[132:133] op_sel:[0,0,0] op_sel_hi:[0,1,1]\n" \
  "v_pk_fma_f32 v[134:135], v[68:69], v[54:55], v[134:135] op_sel:[0,0,0] op_sel_hi:[0,1,1]\n" \
  "s_set_gpr_idx_off\n" \
  "s_waitcnt vmcnt(6) lgkmcnt(2)\n" \
  "buffer_load_dwordx4 v[112:115], v[12:13], s[52:55], 0 idxen offen\n" \
  "ds_read_u16 v12, v14 offset:52\n" \
  "ds_read_b64 v[94:95], v15 offset:256\n" \
  "s_lshr_b32 s61, s61, 8\n" \
  "v_mfma_f32_4x4x4_16b_bf16 v[48:51], v[76:77], v[116:117], 0 cbsz:4 abid:5\n" \
  "v_mfma_f32_4x4x4_16b_bf16 v[52:55], v[76:77], v[118:119], 0 cbsz:4 abid:5\n" \
  "v_cvt_pk_bf16_f32 v57, v56, v57\n" \
  "v_lshlrev_b32_sdwa v56, 1, v57 dst_sel:DWORD dst_unused:UNUSED_PAD src0_sel:DWORD src1_sel:WORD_0\n" \
  "v_lshlrev_b32_sdwa v57, 1, v57 dst_sel:DWORD dst_unused:UNUSED_PAD src0_sel:DWORD src1_sel:WORD_1\n" \
  "ds_read_u16_d16_hi v56, v56\n" \
  "ds_read_u16_d16_hi v57, v57\n" \
  "s_set_gpr_idx_on s61, gpr_idx(SRC2,DST)\n" \
  "v_pk_fma_f32 v[128:129], v[64:65], v[48:49], v[128:129] op_sel:[1,0,0] op_sel_hi:[1,1,1]\n" \
  "v_pk_fma_f32 v[130:131], v[64:65], v[50:51], v[130:131] op_sel:[1,0,0] op_sel_hi:[1,1,1]\n" \
  "v_pk_fma_f32 v[132:133], v[68:69], v[52:53], v[132:133] op_sel:[1,0,0] op_sel_hi:[1,1,1]\n" \
  "v_pk_fma_f32 v[134:135], v[68:69], v[54:55], v[134:135] op_sel:[1,0,0] op_sel_hi:[1,1,1]\n" \
  "s_set_gpr_idx_off\n" \
  "s_waitcnt vmcnt(6) lgkmcnt(2)\n" \
  "buffer_load_dwordx4 v[116:119], v[12:13], s[52:55], 0 idxen offen\n" \
  "ds_read_u16 v12, v14 offset:56\n" \
  "s_lshr_b32 s61, s61, 8\n" \
  "v_mfma_f32_4x4x4_16b_bf16 v[48:51], v[76:77], v[120:121], 0 cbsz:4 abid:6\n" \
  "v_mfma_f32_4x4x4_16b_bf16 v[52:55], v[76:77], v[122:123], 0 cbsz:4 abid:6\n" \
  "v_cvt_pk_bf16_f32 v61, v60, v61\n" \
  "v_lshlrev_b32_sdwa v60, 1, v61 dst_sel:DWORD dst_unused:UNUSED_PAD src0_sel:DWORD src1_sel:WORD_0\n" \
  "v_lshlrev_b32_sdwa v61, 1, v61 dst_sel:DWORD dst_unused:UNUSED_PAD src0_sel:DWORD src1_sel:WORD_1\n" \
  "ds_read_u16_d16_hi v60, v60\n" \
  "ds_read_u16_d16_hi v61, v61\n" \
  "s_set_gpr_idx_on s61, gpr_idx(SRC2,DST)\n" \
  "v_pk_fma_f32 v[128:129], v[66:67], v[48:49], v[128:129] op_sel:[0,0,0] op_sel_hi:[0,1,1]\n" \
  "v_pk_fma_f32 v[130:131], v[66:67], v[50:51], v[130:131] op_sel:[0,0,0] op_sel_hi:[0,1,1]\n" \
  "v_pk_fma_f32 v[132:133], v[70:71], v[52:53], v[132:133] op_sel:[0,0,0] op_sel_hi:[0,1,1]\n" \
  "v_pk_fma_f32 v[134:135], v[70:71], v[54:55], v[134:135] op_sel:[0,0,0] op_sel_hi:[0,1,1]\n" \
  "s_set_gpr_idx_off\n" \
  "s_waitcnt vmcnt(6) lgkmcnt(2)\n" \
  "buffer_load_dwordx4 v[120:123], v[12:13], s[52:55], 0 idxen offen\n" \
  "ds_read_u16 v12, v14 offset:60\n" \
  "s_lshr_b32 s61, s61, 8\n" \
  "v_mfma_f32_4x4x4_16b_bf16 v[84:87], v[94:95], v[24:25], 0\n" \
  "v_mfma_f32_4x4x4_16b_bf16 v[48:51], v[76:77], v[124:125], 0 cbsz:4 abid:7\n" \
  "v_mfma_f32_4x4x4_16b_bf16 v[52:55], v[76:77], v[126:127], 0 cbsz:4 abid:7\n" \
  "v_cvt_pk_bf16_f32 v59, v58, v59\n" \
  "v_lshlrev_b32_sdwa v58, 1, v59 dst_sel:DWORD dst_unused:UNUSED_PAD src0_sel:DWORD src1_sel:WORD_0\n" \
  "v_lshlrev_b32_sdwa v59, 1, v59 dst_sel:DWORD dst_unused:UNUSED_PAD src0_sel:DWORD src1_sel:WORD_1\n" \
  "ds_read_u16_d16_hi v58, v58\n" \
  "ds_read_u16_d16_hi v59, v59\n" \
  "s_set_gpr_idx_on s61, gpr_idx(SRC2,DST)\n" \
  "v_pk_fma_f32 v[128:129], v[66:67], v[48:49], v[128:129] op_sel:[1,0,0] op_sel_hi:[1,1,1]\n" \
  "v_pk_fma_f32 v[130:131], v[66:67], v[50:51], v[130:131] op_sel:[1,0,0] op_sel_hi:[1,1,1]\n" \
  "v_pk_fma_f32 v[132:133], v[70:71], v[52:53], v[132:133] op_sel:[1,0,0] op_sel_hi:[1,1,1]\n" \
  "v_pk_fma_f32 v[134:135], v[70:71], v[54:55], v[134:135] op_sel:[1,0,0] op_sel_hi:[1,1,1]\n" \
  "s_set_gpr_idx_off\n" \
  "s_waitcnt vmcnt(6) lgkmcnt(2)\n" \
  "buffer_load_dwordx4 v[124:127], v[12:13], s[52:55], 0 idxen offen\n" \
  "ds_read_u16 v12, v14 offset:64\n" \
  "s_sub_u32 s60, s60, 1\n" \
  "s_cbranch_scc1 Lend%=\n" \
  "v_readlane_b32 s61, v17, s60\n" \
  "s_setprio 1\n" \
  "v_mfma_f32_4x4x4_16b_bf16 v[64:67], v[92:93], v[32:33], 0 cbsz:4 abid:3\n" \
  "v_mfma_f32_4x4x4_16b_bf16 v[68:71], v[92:93], v[36:37], 0 cbsz:4 abid:3\n" \
  "v_mfma_f32_4x4x4_16b_bf16 v[48:51], v[76:77], v[96:97], 0 cbsz:4 abid:8\n" \
  "v_mfma_f32_4x4x4_16b_bf16 v[52:55], v[76:77], v[98:99], 0 cbsz:4 abid:8\n" \
  "v_cvt_pk_bf16_f32 v63, v62, v63\n" \
  "v_lshlrev_b32_sdwa v62, 1, v63 dst_sel:DWORD dst_unused:UNUSED_PAD src0_sel:DWORD src1_sel:WORD_0\n" \
  "v_lshlrev_b32_sdwa v63, 1, v63 dst_sel:DWORD dst_unused:UNUSED_PAD src0_sel:DWORD src1_sel:WORD_1\n" \
  "ds_read_u16_d16_hi v62, v62\n" \
  "ds_read_u16_d16_hi v63, v63\n" \
  "v_add_u32 v15, 0x100, v15\n" \
  "s_set_gpr_idx_on s61, gpr_idx(SRC2,DST)\n" \
  "v_pk_fma_f32 v[128:129], v[56:57], v[48:49], v[128:129] op_sel:[0,0,0] op_sel_hi:[0,1,1]\n" \
  "v_pk_fma_f32 v[130:131], v[56:57], v[50:51], v[130:131] op_sel:[0,0,0] op_sel_hi:[0,1,1]\n" \
  "v_pk_fma_f32 v[132:133], v[60:61], v[52:53], v[132:133] op_sel:[0,0,0] op_sel_hi:[0,1,1]\n" \
  "v_pk_fma_f32 v[134:135], v[60:61], v[54:55], v[134:135] op_sel:[0,0,0] op_sel_hi:[0,1,1]\n" \
  "s_set_gpr_idx_off\n" \
  "s_waitcnt vmcnt(6) lgkmcnt(2)\n" \
  "buffer_load_dwordx4 v[96:99], v[12:13], s[52:55], 0 idxen offen\n" \
  "ds_read_u16 v12, v14 offset:68\n" \
  "s_lshr_b32 s61, s61, 8\n" \
  "v_mfma_f32_4x4x4_16b_bf16 v[48:51], v[76:77], v[100:101], 0 cbsz:4 abid:9\n" \
  "v_mfma_f32_4x4x4_16b_bf16 v[52:55], v[76:77], v[102:103], 0 cbsz:4 abid:9\n" \
  "v_cvt_pk_bf16_f32 v65, v64, v65\n" \
  "v_lshlrev_b32_sdwa v64, 1, v65 dst_sel:DWORD dst_unused:UNUSED_PAD src0_sel:DWORD src1_sel:WORD_0\n" \
  "v_lshlrev_b32_sdwa v65, 1, v65 dst_sel:DWORD dst_unused:UNUSED_PAD src0_sel:DWORD src1_sel:WORD_1\n" \
  "ds_read_u16_d16_hi v64, v64\n" \
  "ds_read_u16_d16_hi v65, v65\n" \
  "s_set_gpr_idx_on s61, gpr_idx(SRC2,DST)\n" \
  "v_pk_fma_f32 v[128:129], v[56:57], v[48:49], v[128:129] op_sel:[1,0,0] op_sel_hi:[1,1,1]\n" \
  "v_pk_fma_f32 v[130:131], v[56:57], v[50:51], v[130:131] op_sel:[1,0,0] op_sel_hi:[1,1,1]\n" \
  "v_pk_fma_f32 v[132:133], v[60:61], v[52:53], v[132:133] op_sel:[1,0,0] op_sel_hi:[1,1,1]\n" \
  "v_pk_fma_f32 v[134:135], v[60:61], v[54:55], v[134:135] op_sel:[1,0,0] op_sel_hi:[1,1,1]\n" \
  "s_set_gpr_idx_off\n" \
  "s_waitcnt vmcnt(6) lgkmcnt(2)\n" \
  "buffer_load_dwordx4 v[100:103], v[12:13], s[52:55], 0 idxen offen\n" \
  "ds_read_u16 v12, v14 offset:72\n" \
  "s_lshr_b32 s61, s61, 8\n" \
  "v_mfma_f32_4x4x4_16b_bf16 v[48:51], v[76:77], v[104:105], 0 cbsz:4 abid:10\n" \
  "v_mfma_f32_4x4x4_16b_bf16 v[52:55], v[76:77], v[106:107], 0 cbsz:4 abid:10\n" \
  "v_cvt_pk_bf16_f32 v69, v68, v69\n" \
  "v_lshlrev_b32_sdwa v68, 1, v69 dst_sel:DWORD dst_unused:UNUSED_PAD src0_sel:DWORD src1_sel:WORD_0\n" \
  "v_lshlrev_b32_sdwa v69, 1, v69 dst_sel:DWORD dst_unused:UNUSED_PAD src0_sel:DWORD src1_sel:WORD_1\n" \
  "ds_read_u16_d16_hi v68, v68\n" \
  "ds_read_u16_d16_hi v69, v69\n" \
  "s_set_gpr_idx_on s61, gpr_idx(SRC2,DST)\n" \
  "v_pk_fma_f32 v[128:129], v[58:59], v[48:49], v[128:129] op_sel:[0,0,0] op_sel_hi:[0,1,1]\n" \
  "v_pk_fma_f32 v[130:131], v[58:59], v[50:51], v[130:131] op_sel:[0,0,0] op_sel_hi:[0,1,1]\n" \
  "v_pk_fma_f32 v[132:133], v[62:63], v[52:53], v[132:133] op_sel:[0,0,0] op_sel_hi:[0,1,1]\n" \
  "v_pk_fma_f32 v[134:135], v[62:63], v[54:55], v[134:135] op_sel:[0,0,0] op_sel_hi:[0,1,1]\n" \
  "s_set_gpr_idx_off\n" \
  "s_waitcnt vmcnt(6) lgkmcnt(2)\n" \
  "buffer_load_dwordx4 v[104:107], v[12:13], s[52:55], 0 idxen offen\n" \
  "ds_read_u16 v12, v14 offset:76\n" \
  "ds_read_b64 v[92:93], v16\n" \
  "s_lshr_b32 s61, s61, 8\n" \
  "v_mfma_f32_4x4x4_16b_bf16 v[88:91], v[94:95], v[26:27], 0\n" \
  "v_mfma_f32_4x4x4_16b_bf16 v[48:51], v[76:77], v[108:109], 0 cbsz:4 abid:11\n" \
  "v_mfma_f32_4x4x4_16b_bf16 v[52:55], v[76:77], v[110:111], 0 cbsz:4 abid:11\n" \
  "v_cvt_pk_bf16_f32 v67, v66, v67\n" \
  "v_lshlrev_b32_sdwa v66, 1, v67 dst_sel:DWORD dst_unused:UNUSED_PAD src0_sel:DWORD src1_sel:WORD_0\n" \
  "v_lshlrev_b32_sdwa v67, 1, v67 dst_sel:DWORD dst_unused:UNUSED_PAD src0_sel:DWORD src1_sel:WORD_1\n" \
  "ds_read_u16_d16_hi v66, v66\n" \
  "ds_read_u16_d16_hi v67, v67\n" \
  "s_set_gpr_idx_on s61, gpr_idx(SRC2,DST)\n" \
  "v_pk_fma_f32 v[128:129], v[58:59], v[48:49], v[128:129] op_sel:[1,0,0] op_sel_hi:[1,1,1]\n" \
  "v_pk_fma_f32 v[130:131], v[58:59], v[50:51], v[130:131] op_sel:[1,0,0] op_sel_hi:[1,1,1]\n" \
  "v_pk_fma_f32 v[132:133], v[62:63], v[52:53], v[132:133] op_sel:[1,0,0] op_sel_hi:[1,1,1]\n" \
  "v_pk_fma_f32 v[134:135], v[62:63], v[54:55], v[134:135] op_sel:[1,0,0] op_sel_hi:[1,1,1]\n" \
  "s_set_gpr_idx_off\n" \
  "s_waitcnt vmcnt(6) lgkmcnt(2)\n" \
  "buffer_load_dwordx4 v[108:111], v[12:13], s[52:55], 0 idxen offen\n" \
  "ds_read_u16 v12, v14 offset:80\n" \
  "s_sub_u32 s60, s60, 1\n" \
  "s_cbranch_scc1 Lend%=\n" \
  "v_readlane_b32 s61, v17, s60\n" \
  "s_setprio 0\n" \
  "v_mfma_f32_4x4x4_16b_bf16 v[56:59], v[92:93], v[32:33], 0 cbsz:4\n" \
  "v_mfma_f32_4x4x4_16b_bf16 v[60:63], v[92:93], v[36:37], 0 cbsz:4\n" \
  "v_mfma_f32_4x4x4_16b_bf16 v[48:51], v[76:77], v[112:113], 0 cbsz:4 abid:12\n" \
  "v_mfma_f32_4x4x4_16b_bf16 v[52:55], v[76:77], v[114:115], 0 cbsz:4 abid:12\n" \
  "v_cvt_pk_bf16_f32 v71, v70, v71\n" \
  "v_lshlrev_b32_sdwa v70, 1, v71 dst_sel:DWORD dst_unused:UNUSED_PAD src0_sel:DWORD src1_sel:WORD_0\n" \
  "v_lshlrev_b32_sdwa v71, 1, v71 dst_sel:DWORD dst_unused:UNUSED_PAD src0_sel:DWORD src1_sel:WORD_1\n" \
  "ds_read_u16_d16_hi v70, v70\n" \
  "ds_read_u16_d16_hi v71, v71\n" \
  "v_cvt_pk_bf16_f32 v20, v84, v88\n" \
  "s_set_gpr_idx_on s61, gpr_idx(SRC2,DST)\n" \
  "v_pk_fma_f32 v[128:129], v[64:65], v[48:49], v[128:129] op_sel:[0,0,0] op_sel_hi:[0,1,1]\n" \
  "v_pk_fma_f32 v[130:131], v[64:65], v[50:51], v[130:131] op_sel:[0,0,0] op_sel_hi:[0,1,1]\n" \
  "v_pk_fma_f32 v[132:133], v[68:69], v[52:53], v[132:133] op_sel:[0,0,0] op_sel_hi:[0,1,1]\n" \
  "v_pk_fma_f32 v[134:135], v[68:69], v[54:55], v[134:135] op_sel:[0,0,0] op_sel_hi:[0,1,1]\n" \
  "s_set_gpr_idx_off\n" \
  "s_waitcnt vmcnt(6) lgkmcnt(2)\n" \
  "buffer_load_dwordx4 v[112:115], v[12:13], s[52:55], 0 idxen offen\n" \
  "ds_read_u16 v12, v14 offset:84\n" \
  "s_lshr_b32 s61, s61, 8\n" \
  "v_mfma_f32_4x4x4_16b_bf16 v[48:51], v[76:77], v[116:117], 0 cbsz:4 abid:13\n" \
  "v_mfma_f32_4x4x4_16b_bf16 v[52:55], v[76:77], v[118:119], 0 cbsz:4 abid:13\n" \
  "v_cvt_pk_bf16_f32 v57, v56, v57\n" \
  "v_lshlrev_b32_sdwa v56, 1, v57 dst_sel:DWORD dst_unused:UNUSED_PAD src0_sel:DWORD src1_sel:WORD_0\n" \
  "v_lshlrev_b32_sdwa v57, 1, v57 dst_sel:DWORD dst_unused:UNUSED_PAD src0_sel:DWORD src1_sel:WORD_1\n" \
  "ds_read_u16_d16_hi v56, v56\n" \
  "ds_read_u16_d16_hi v57, v57\n" \
  "s_set_gpr_idx_on s61, gpr_idx(SRC2,DST)\n" \
  "v_pk_fma_f32 v[128:129], v[64:65], v[48:49], v[128:129] op_sel:[1,0,0] op_sel_hi:[1,1,1]\n" \
  "v_pk_fma_f32 v[130:131], v[64:65], v[50:51], v[130:131] op_sel:[1,0,0] op_sel_hi:[1,1,1]\n" \
  "v_pk_fma_f32 v[132:133], v[68:69], v[52:53], v[132:133] op_sel:[1,0,0] op_sel_hi:[1,1,1]\n" \
  "v_pk_fma_f32 v[134:135], v[68:69], v[54:55], v[134:135] op_sel:[1,0,0] op_sel_hi:[1,1,1]\n" \
  "s_set_gpr_idx_off\n" \
  "s_waitcnt vmcnt(6) lgkmcnt(2)\n" \
  "buffer_load_dwordx4 v[116:119], v[12:13], s[52:55], 0 idxen offen\n" \
  "ds_read_u16 v12, v14 offset:88\n" \
  "s_lshr_b32 s61, s61, 8\n" \
  "v_mfma_f32_4x4x4_16b_bf16 v[48:51], v[76:77], v[120:121], 0 cbsz:4 abid:14\n" \
  "v_mfma_f32_4x4x4_16b_bf16 v[52:55], v[76:77], v[122:123], 0 cbsz:4 abid:14\n" \
  "v_cvt_pk_bf16_f32 v61, v60, v61\n" \
  "v_lshlrev_b32_sdwa v60, 1, v61 dst_sel:DWORD dst_unused:UNUSED_PAD src0_sel:DWORD src1_sel:WORD_0\n" \
  "v_lshlrev_b32_sdwa v61, 1, v61 dst_sel:DWORD dst_unused:UNUSED_PAD src0_sel:DWORD src1_sel:WORD_1\n" \
  "ds_read_u16_d16_hi v60, v60\n" \
  "ds_read_u16_d16_hi v61, v61\n" \
  "s_set_gpr_idx_on s61, gpr_idx(SRC2,DST)\n" \
  "v_pk_fma_f32 v[128:129], v[66:67], v[48:49], v[128:129] op_sel:[0,0,0] op_sel_hi:[0,1,1]\n" \
  "v_pk_fma_f32 v[130:131], v[66:67], v[50:51], v[130:131] op_sel:[0,0,0] op_sel_hi:[0,1,1]\n" \
  "v_pk_fma_f32 v[132:133], v[70:71], v[52:53], v[132:133] op_sel:[0,0,0] op_sel_hi:[0,1,1]\n" \
  "v_pk_fma_f32 v[134:135], v[70:71], v[54:55], v[134:135] op_sel:[0,0,0] op_sel_hi:[0,1,1]\n" \
  "s_set_gpr_idx_off\n" \
  "s_waitcnt vmcnt(6) lgkmcnt(2)\n" \
  "buffer_load_dwordx4 v[120:123], v[12:13], s[52:55], 0 idxen offen\n" \
  "ds_read_u16 v12, v14 offset:92\n" \
  "s_lshr_b32 s61, s61, 8\n" \
  "v_mfma_f32_4x4x4_16b_bf16 v[84:87], v[94:95], v[28:29], 0\n" \
  "v_mfma_f32_4x4x4_16b_bf16 v[88:91], v[94:95], v[30:31], 0\n" \
  "v_mfma_f32_4x4x4_16b_bf16 v[48:51], v[76:77], v[124:125], 0 cbsz:4 abid:15\n" \
  "v_mfma_f32_4x4x4_16b_bf16 v[52:55], v[76:77], v[126:127], 0 cbsz:4 abid:15\n" \
  "v_cvt_pk_bf16_f32 v59, v58, v59\n" \
  "v_lshlrev_b32_sdwa v58, 1, v59 dst_sel:DWORD dst_unused:UNUSED_PAD src0_sel:DWORD src1_sel:WORD_0\n" \
  "v_lshlrev_b32_sdwa v59, 1, v59 dst_sel:DWORD dst_unused:UNUSED_PAD src0_sel:DWORD src1_sel:WORD_1\n" \
  "ds_read_u16_d16_hi v58, v58\n" \
  "ds_read_u16_d16_hi v59, v59\n" \
  "v_cvt_pk_bf16_f32 v77, v84, v88\n" \
  "v_mov_b32 v76, v20\n" \
  "s_set_gpr_idx_on s61, gpr_idx(SRC2,DST)\n" \
  "v_pk_fma_f32 v[128:129], v[66:67], v[48:49], v[128:129] op_sel:[1,0,0] op_sel_hi:[1,1,1]\n" \
  "v_pk_fma_f32 v[130:131], v[66:67], v[50:51], v[130:131] op_sel:[1,0,0] op_sel_hi:[1,1,1]\n" \
  "v_pk_fma_f32 v[132:133], v[70:71], v[52:53], v[132:133] op_sel:[1,0,0] op_sel_hi:[1,1,1]\n" \
  "v_pk_fma_f32 v[134:135], v[70:71], v[54:55], v[134:135] op_sel:[1,0,0] op_sel_hi:[1,1,1]\n" \
  "s_set_gpr_idx_off\n" \
  "s_waitcnt vmcnt(6) lgkmcnt(2)\n" \
  "buffer_load_dwordx4 v[124:127], v[12:13], s[52:55], 0 idxen offen\n" \
  "ds_read_u16 v12, v14 offset:96\n" \
  "s_sub_u32 s60, s60, 1\n" \
  "s_cbranch_scc1 Lend%=\n" \
  "v_readlane_b32 s61, v17, s60\n" \
  "s_setprio 3\n" \
  "v_mfma_f32_4x4x4_16b_bf16 v[64:67], v[92:93], v[32:33], 0 cbsz:4 abid:1\n" \
  "v_mfma_f32_4x4x4_16b_bf16 v[68:71], v[92:93], v[36:37], 0 cbsz:4 abid:1\n" \
  "v_mfma_f32_4x4x4_16b_bf16 v[48:51], v[76:77], v[96:97], 0 cbsz:4\n" \
  "v_mfma_f32_4x4x4_16b_bf16 v[52:55], v[76:77], v[98:99], 0 cbsz:4\n" \
  "v_cvt_pk_bf16_f32 v63, v62, v63\n" \
  "v_lshlrev_b32_sdwa v62, 1, v63 dst_sel:DWORD dst_unused:UNUSED_PAD src0_sel:DWORD src1_sel:WORD_0\n" \
  "v_lshlrev_b32_sdwa v63, 1, v63 dst_sel:DWORD dst_unused:UNUSED_PAD src0_sel:DWORD src1_sel:WORD_1\n" \
  "ds_read_u16_d16_hi v62, v62\n" \
  "ds_read_u16_d16_hi v63, v63\n" \
  "v_add_u32 v14, 64, v14\n" \
  "s_branch Lbody%=\n" \
  "Lep0_%=:\n" \
  "ds_read_b32 v50, v18\n" \
  "s_waitcnt lgkmcnt(0)\n" \
  "v_readfirstlane_b32 s63, v50\n" \
  "s_bitcmp1_b32 s63, 1\n" \
  "s_cbranch_scc1 Lgo%=\n" \
  "v_mov_b64 v[50:51], s[50:51]\n" \
  "s_mov_b64 exec, 1\n" \
  "buffer_store_dwordx2 v[50:51], off, s[24:27], s70 sc1\n" \
  "s_mov_b64 exec, -1\n" \
  "s_nop 3\n" \
  "s_branch Lgo%=\n" \
  "Lbad%=:\n" \
  "v_mov_b32 v72, 1\n" \
  "v_mov_b32 v73, s62\n" \
  "ds_or_b32 v18, v72\n" \
  "ds_write_b32 v73, v73\n" \
  "s_branch Lf9_%=\n" \
  "Lnf%=:\n" \
  "s_mov_b32 %[bad], 3\n" \
  "s_mov_b32 s79, m0\n" \
  "Lbad2_%=:\n" \
  "s_waitcnt vmcnt(0) lgkmcnt(0)\n" \
  "s_bitcmp1_b32 %[bad], 1\n" \
  "s_cbranch_scc1 Lout%=\n" \
  "s_branch Lep%=\n" \
  "Lh2_%=:\n" \
  "s_waitcnt vmcnt(0)\n" \
  "v_add_co_u32 v90, vcc, 1, v88\n" \
  "s_mul_i32 s63, %[blk], 2176\n" \
  "s_add_u32 s63, s63, 49408\n" \
  "v_addc_co_u32 v91, vcc, 0, v89, vcc\n" \
  "v_lshlrev_b32 v88, 22, v90\n" \
  "v_alignbit_b32 v89, v91, v90, 10\n" \
  "v_add_u32 v87, v87, v88\n" \
  "v_lshl_add_u32 v128, v136, 11, v87\n" \
  "v_mov_b32 v129, v89\n" \
  "v_sub_u32 v51, v137, v136\n" \
  "v_lshl_add_u32 v51, v51, 11, v136\n" \
  "v_add_u32 v130, v51, v87\n" \
  "v_mov_b32 v131, v89\n" \
  "v_sub_u32 v51, v138, v137\n" \
  "v_lshl_add_u32 v51, v51, 11, v137\n" \
  "v_add_u32 v132, v51, v87\n" \
  "v_mov_b32 v133, v89\n" \
  "v_sub_u32 v51, v139, v138\n" \
  "v_lshl_add_u32 v51, v51, 11, v138\n" \
  "v_add_u32 v134, v51, v87\n" \
  "v_mov_b32 v135, v89\n" \
  "buffer_store_dwordx4 v[128:131], v13, s[24:27], s63 offen sc1\n" \
  "buffer_store_dwordx4 v[132:135], v13, s[24:27], s63 offen offset:1024 sc1\n" \
  "buffer_load_dword v100, v54, s[8:11], 0 offen offset:0\n" \
  "buffer_load_dword v101, v54, s[8:11], 0 offen offset:256\n" \
  "buffer_load_dword v102, v54, s[8:11], 0 offen offset:512\n" \
  "buffer_load_dword v103, v54, s[8:11], 0 offen offset:768\n" \
  "buffer_load_dword v104, v54, s[12:15], 0 offen\n" \
  "s_lshr_b32 s63, %[blk], 2\n" \
  "s_lshl_b32 s63, s63, 13\n" \
  "s_and_b32 s93, %[blk], 3\n" \
  "s_lshl_b32 s93, s93, 6\n" \
  "s_add_u32 s63, s63, s93\n" \
  "v_and_b32 v50, 0x300, v13\n" \
  "v_and_b32 v51, 60, v54\n" \
  "v_lshl_add_u32 v50, v50, 3, v51\n" \
  "s_cmp_ge_u32 %[blk], 16\n" \
  "s_cbranch_scc1 Lh4_%=\n" \
  "buffer_load_dword v108, v50, s[16:19], s63 offen offset:0\n" \
  "buffer_load_dword v109, v50, s[16:19], s63 offen offset:256\n" \
  "buffer_load_dword v110, v50, s[16:19], s63 offen offset:512\n" \
  "buffer_load_dword v111, v50, s[16:19], s63 offen offset:768\n" \
  "buffer_load_dword v112, v50, s[16:19], s63 offen offset:1024\n" \
  "buffer_load_dword v113, v50, s[16:19], s63 offen offset:1280\n" \
  "buffer_load_dword v114, v50, s[16:19], s63 offen offset:1536\n" \
  "buffer_load_dword v115, v50, s[16:19], s63 offen offset:1792\n" \
  "Lh4_%=:\n" \
  "s_barrier\n" \
  "ds_write_b64 v94, v[90:91] offset:1472\n" \
  "s_mov_b32 s63, 0x05040100\n" \
  "s_mov_b32 s93, 0x07060302\n" \
  "s_cmp_ge_u32 %[blk], 16\n" \
  "s_waitcnt vmcnt(0)\n" \
  "s_cbranch_scc1 Lh5_%=\n" \
  "v_perm_b32 v216, v109, v108, s63\n" \
  "v_perm_b32 v217, v111, v110, s63\n" \
  "v_perm_b32 v218, v113, v112, s63\n" \
  "v_perm_b32 v219, v115, v114, s63\n" \
  "v_perm_b32 v220, v109, v108, s93\n" \
  "v_perm_b32 v221, v111, v110, s93\n" \
  "v_perm_b32 v222, v113, v112, s93\n" \
  "v_perm_b32 v223, v115, v114, s93\n" \
  "s_lshl_b32 s72, %[blk], 11\n" \
  "buffer_store_dwordx4 v[216:219], v13, s[24:27], s72 offen sc1\n" \
  "buffer_store_dwordx4 v[220:223], v13, s[24:27], s72 offen offset:1024 sc1\n" \
  "Lh5_%=:\n" \
  "v_perm_b32 v120, v101, v100, s63\n" \
  "v_perm_b32 v121, v103, v102, s63\n" \
  "v_perm_b32 v122, v101, v100, s93\n" \
  "v_perm_b32 v123, v103, v102, s93\n" \
  "ds_write_b128 v13, v[120:123] offset:36288\n" \
  "v_and_b32 v50, 30, v86\n" \
  "v_and_b32 v51, 16, v13\n" \
  "v_lshl_add_u32 v50, v51, 2, v50\n" \
  "s_mov_b64 exec, 0xffffffff\n" \
  "ds_write_b16 v50, v104 offset:36160\n" \
  "ds_write_b16_d16_hi v50, v104 offset:36192\n" \
  "s_mov_b64 exec, -1\n" \
  "v_and_b32 v100, 0x7fff7fff, v100\n" \
  "v_max_u16_sdwa v100, v100, v100 dst_sel:WORD_1 dst_unused:UNUSED_PAD src0_sel:WORD_0 src1_sel:WORD_1\n" \
  "v_and_b32 v101, 0x7fff7fff, v101\n" \
  "v_max_u16_sdwa v101, v101, v101 dst_sel:WORD_1 dst_unused:UNUSED_PAD src0_sel:WORD_0 src1_sel:WORD_1\n" \
  "v_and_b32 v102, 0x7fff7fff, v102\n" \
  "v_max_u16_sdwa v102, v102, v102 dst_sel:WORD_1 dst_unused:UNUSED_PAD src0_sel:WORD_0 src1_sel:WORD_1\n" \
  "v_and_b32 v103, 0x7fff7fff, v103\n" \
  "v_max_u16_sdwa v103, v103, v103 dst_sel:WORD_1 dst_unused:UNUSED_PAD src0_sel:WORD_0 src1_sel:WORD_1\n" \
  "v_add_f32 v100, v100, v101\n" \
  "v_add_f32 v102, v102, v103\n" \
  "v_add_f32 v100, v100, v102\n" \
  "v_and_b32 v100, 0x7fffffff, v100\n" \
  "s_nop 1\n" \
  "v_max_u32_dpp v100, v100, v100 row_shr:1 row_mask:0xf bank_mask:0xf bound_ctrl:1\n" \
  "s_nop 1\n" \
  "v_max_u32_dpp v100, v100, v100 row_shr:2 row_mask:0xf bank_mask:0xf bound_ctrl:1\n" \
  "s_nop 1\n" \
  "v_max_u32_dpp v100, v100, v100 row_shr:4 row_mask:0xf bank_mask:0xf bound_ctrl:1\n" \
  "s_nop 1\n" \
  "v_max_u32_dpp v100, v100, v100 row_shr:8 row_mask:0xf bank_mask:0xf bound_ctrl:1\n" \
  "s_nop 1\n" \
  "v_max_u32_dpp v100, v100, v100 row_bcast:15 row_mask:0xa bank_mask:0xf\n" \
  "s_nop 1\n" \
  "v_max_u32_dpp v100, v100, v100 row_bcast:31 row_mask:0xc bank_mask:0xf\n" \
  "v_max_u32 v101, 0x0d000000, v100\n" \
  "v_rcp_f32 v101, v101\n" \
  "v_cmp_gt_u32 vcc, 0x71000000, v100\n" \
  "v_mul_f32 v101, 0x437a0000, v101\n" \
  "v_lshrrev_b32 v101, 16, v101\n" \
  "v_cndmask_b32 v101, -1, v101, vcc\n" \
  "s_brev_b64 exec, 1\n" \
  "ds_write_b32 v94, v101 offset:1480\n" \
  "s_mov_b64 exec, -1\n" \
  "s_branch Lc1_%=\n" \
  "Lend%=:\n" \
  "s_and_b32 s63, s44, 3\n" \
  "s_lshl_b32 s63, s63, 11\n" \
  "buffer_load_dwordx4 v[96:99], v13, s[24:27], s63 offen offset:0 sc1\n" \
  "buffer_load_dwordx4 v[100:103], v13, s[24:27], s63 offen offset:1024 sc1\n" \
  "s_add_u32 s63, s63, 8192\n" \
  "buffer_load_dwordx4 v[104:107], v13, s[24:27], s63 offen offset:0 sc1\n" \
  "buffer_load_dwordx4 v[108:111], v13, s[24:27], s63 offen offset:1024 sc1\n" \
  "s_add_u32 s63, s63, 8192\n" \
  "buffer_load_dwordx4 v[112:115], v13, s[24:27], s63 offen offset:0 sc1\n" \
  "buffer_load_dwordx4 v[116:119], v13, s[24:27], s63 offen offset:1024 sc1\n" \
  "s_add_u32 s63, s63, 8192\n" \
  "buffer_load_dwordx4 v[120:123], v13, s[24:27], s63 offen offset:0 sc1\n" \
  "buffer_load_dwordx4 v[124:127], v13, s[24:27], s63 offen offset:1024 sc1\n" \
  "buffer_load_dword v20, off, s[24:27], s70 sc1\n" \
  "s_lshl2_add_u32 s62, s44, 35872\n" \
  "s_cmp_ge_u32 s74, 10\n" \
  "s_cbranch_scc1 Lbad%=\n" \
  "s_cmp_lt_u32 s93, s44\n" \
  "s_cbranch_scc0 Lf1_%=\n" \
  "s_set_gpr_idx_on s73, gpr_idx(SRC0)\n" \
  "v_mov_b32 v84, v128\n" \
  "v_mov_b32 v85, v129\n" \
  "v_mov_b32 v86, v130\n" \
  "v_mov_b32 v87, v131\n" \
  "v_mov_b32 v88, v132\n" \
  "v_mov_b32 v89, v133\n" \
  "v_mov_b32 v90, v134\n" \
  "v_mov_b32 v91, v135\n" \
  "s_set_gpr_idx_off\n" \
  "ds_write_b128 v21, v[84:87]\n" \
  "ds_write_b128 v21, v[88:91] offset:1024\n" \
  "v_mov_b32 v53, s62\n" \
  "ds_write_b32 v53, v53\n" \
  "s_add_u32 s73, s73, 8\n" \
  "s_cmp_eq_u32 s73, 80\n" \
  "s_cselect_b32 s73, 0, s73\n" \
  "s_add_u32 s72, s72, 1056\n" \
  "s_sub_u32 s74, s74, 1\n" \
  "s_cbranch_scc1 Lf9_%=\n" \
  "Lf1_%=:\n" \
  "s_min_u32 s63, s75, 1\n" \
  "s_sub_u32 s60, s74, s63\n" \
  "s_cbranch_scc1 Lf3_%=\n" \
  "Lf2_%=:\n" \
  "s_set_gpr_idx_on s73, gpr_idx(SRC0,SRC1)\n" \
  "v_cvt_pk_bf16_f32 v48, v128, v132\n" \
  "v_cvt_pk_bf16_f32 v49, v129, v133\n" \
  "v_cvt_pk_bf16_f32 v50, v130, v134\n" \
  "v_cvt_pk_bf16_f32 v51, v131, v135\n" \
  "s_set_gpr_idx_off\n" \
  "v_add_u32 v52, s72, v19\n" \
  "s_add_u32 s73, s73, 8\n" \
  "s_cmp_eq_u32 s73, 80\n" \
  "s_cselect_b32 s73, 0, s73\n" \
  "s_add_u32 s72, s72, 1056\n" \
  "ds_write2_b32 v52, v48, v49 offset1:16\n" \
  "ds_write2_b32 v52, v50, v51 offset0:32 offset1:48\n" \
  "s_sub_u32 s60, s60, 1\n" \
  "s_cbranch_scc0 Lf2_%=\n" \
  "Lf3_%=:\n" \
  "s_set_gpr_idx_on s73, gpr_idx(SRC0)\n" \
  "v_mov_b32 v40, v128\n" \
  "v_mov_b32 v41, v129\n" \
  "v_mov_b32 v42, v130\n" \
  "v_mov_b32 v43, v131\n" \
  "v_mov_b32 v44, v132\n" \
  "v_mov_b32 v45, v133\n" \
  "v_mov_b32 v46, v134\n" \
  "v_mov_b32 v47, v135\n" \
  "s_set_gpr_idx_off\n" \
  "s_cmp_eq_u32 s75, 0\n" \
  "s_cbranch_scc1 Lf9_%=\n" \
  "s_mov_b32 s60, s75\n" \
  "v_mov_b32 v72, s62\n" \
  "Lf4_%=:\n" \
  "ds_read_b32 v73, v72 offset:4\n" \
  "ds_read_b128 v[84:87], v21 offset:3328\n" \
  "ds_read_b128 v[88:91], v21 offset:4352\n" \
  "s_waitcnt lgkmcnt(0)\n" \
  "v_readfirstlane_b32 s63, v73\n" \
  "s_cmp_eq_u32 s63, 0\n" \
  "s_cbranch_scc0 Lf5_%=\n" \
  "s_sleep 1\n" \
  "s_branch Lf4_%=\n" \
  "Lf5_%=:\n" \
  "v_pk_add_f32 v[40:41], v[40:41], v[84:85]\n" \
  "v_pk_add_f32 v[42:43], v[42:43], v[86:87]\n" \
  "v_pk_add_f32 v[44:45], v[44:45], v[88:89]\n" \
  "v_pk_add_f32 v[46:47], v[46:47], v[90:91]\n" \
  "v_add_u32 v72, 4, v72\n" \
  "v_add_u32 v21, 3328, v21\n" \
  "s_sub_u32 s60, s60, 1\n" \
  "s_cmp_lg_u32 s60, 0\n" \
  "s_cbranch_scc1 Lf4_%=\n" \
  "v_cvt_pk_bf16_f32 v48, v40, v44\n" \
  "v_cvt_pk_bf16_f32 v49, v41, v45\n" \
  "v_cvt_pk_bf16_f32 v50, v42, v46\n" \
  "v_cvt_pk_bf16_f32 v51, v43, v47\n" \
  "v_add_u32 v52, s72, v19\n" \
  "ds_write2_b32 v52, v48, v49 offset1:16\n" \
  "ds_write2_b32 v52, v50, v51 offset0:32 offset1:48\n" \
  "Lf9_%=:\n" \
  "s_waitcnt lgkmcnt(0)\n" \
  "s_barrier\n" \
  "ds_read_b32 v18, v18\n" \
  "ds_read_b128 v[128:131], v22 offset:0\n" \
  "ds_read_b128 v[132:135], v22 offset:256\n" \
  "ds_read_b128 v[136:139], v22 offset:512\n" \
  "ds_read_b128 v[140:143], v22 offset:768\n" \
  "ds_read_b128 v[144:147], v22 offset:8448\n" \
  "ds_read_b128 v[148:151], v22 offset:8704\n" \
  "ds_read_b128 v[152:155], v22 offset:8960\n" \
  "ds_read_b128 v[156:159], v22 offset:9216\n" \
  "ds_read_b128 v[160:163], v22 offset:16896\n" \
  "ds_read_b128 v[164:167], v22 offset:17152\n" \
  "ds_read_b128 v[168:171], v22 offset:17408\n" \
  "ds_read_b128 v[172:175], v22 offset:17664\n" \
  "ds_read_b128 v[176:179], v22 offset:25344\n" \
  "ds_read_b128 v[180:183], v22 offset:25600\n" \
  "s_waitcnt lgkmcnt(14)\n" \
  "v_readfirstlane_b32 %[bad], v18\n" \
  "s_cmp_lg_u32 %[bad], 0\n" \
  "s_cbranch_scc1 Lbad2_%=\n" \
  "s_waitcnt vmcnt(7) lgkmcnt(13)\n" \
  "v_mfma_f32_16x16x32_bf16 v[40:43], v[128:131], v[96:99], 0\n" \
  "v_mfma_f32_16x16x32_bf16 v[44:47], v[128:131], v[100:103], 0\n" \
  "ds_read_b128 v[184:187], v22 offset:25856\n" \
  "s_waitcnt vmcnt(5) lgkmcnt(13)\n" \
  "v_mfma_f32_16x16x32_bf16 v[40:43], v[132:135], v[104:107], v[40:43]\n" \
  "v_mfma_f32_16x16x32_bf16 v[44:47], v[132:135], v[108:111], v[44:47]\n" \
  "ds_read_b128 v[188:191], v22 offset:26112\n" \
  "s_waitcnt vmcnt(3) lgkmcnt(13)\n" \
  "v_mfma_f32_16x16x32_bf16 v[40:43], v[136:139], v[112:115], v[40:43]\n" \
  "v_mfma_f32_16x16x32_bf16 v[44:47], v[136:139], v[116:119], v[44:47]\n" \
  "ds_read_b128 v[192:195], v22 offset:33792\n" \
  "s_waitcnt vmcnt(1) lgkmcnt(13)\n" \
  "v_mfma_f32_16x16x32_bf16 v[40:43], v[140:143], v[120:123], v[40:43]\n" \
  "v_mfma_f32_16x16x32_bf16 v[44:47], v[140:143], v[124:127], v[44:47]\n" \
  "ds_read_b128 v[196:199], v22 offset:34048\n" \
  "s_waitcnt lgkmcnt(13)\n" \
  "v_mfma_f32_16x16x32_bf16 v[48:51], v[144:147], v[96:99], 0\n" \
  "v_mfma_f32_16x16x32_bf16 v[52:55], v[144:147], v[100:103], 0\n" \
  "ds_read_b128 v[200:203], v22 offset:34304\n" \
  "s_waitcnt lgkmcnt(13)\n" \
  "v_mfma_f32_16x16x32_bf16 v[48:51], v[148:151], v[104:107], v[48:51]\n" \
  "v_mfma_f32_16x16x32_bf16 v[52:55], v[148:151], v[108:111], v[52:55]\n" \
  "ds_read_b128 v[204:207], v22 offset:34560\n" \
  "s_waitcnt lgkmcnt(13)\n" \
  "v_mfma_f32_16x16x32_bf16 v[48:51], v[152:155], v[112:115], v[48:51]\n" \
  "v_mfma_f32_16x16x32_bf16 v[52:55], v[152:155], v[116:119], v[52:55]\n" \
  "v_cvt_pk_bf16_f32 v40, v40, v41\n" \
  "v_cvt_pk_bf16_f32 v41, v42, v43\n" \
  "v_cvt_pk_bf16_f32 v42, v44, v45\n" \
  "v_cvt_pk_bf16_f32 v43, v46, v47\n" \
  "buffer_store_dwordx4 v[40:43], v23, s[56:59], 0 offen sc1\n" \
  "s_waitcnt lgkmcnt(12)\n" \
  "v_mfma_f32_16x16x32_bf16 v[48:51], v[156:159], v[120:123], v[48:51]\n" \
  "v_mfma_f32_16x16x32_bf16 v[52:55], v[156:159], v[124:127], v[52:55]\n" \
  "s_waitcnt lgkmcnt(11)\n" \
  "v_mfma_f32_16x16x32_bf16 v[56:59], v[160:163], v[96:99], 0\n" \
  "v_mfma_f32_16x16x32_bf16 v[60:63], v[160:163], v[100:103], 0\n" \
  "s_waitcnt lgkmcnt(10)\n" \
  "v_mfma_f32_16x16x32_bf16 v[56:59], v[164:167], v[104:107], v[56:59]\n" \
  "v_mfma_f32_16x16x32_bf16 v[60:63], v[164:167], v[108:111], v[60:63]\n" \
  "s_waitcnt lgkmcnt(9)\n" \
  "v_mfma_f32_16x16x32_bf16 v[56:59], v[168:171], v[112:115], v[56:59]\n" \
  "v_mfma_f32_16x16x32_bf16 v[60:63], v[168:171], v[116:119], v[60:63]\n" \
  "v_cvt_pk_bf16_f32 v48, v48, v49\n" \
  "v_cvt_pk_bf16_f32 v49, v50, v51\n" \
  "v_cvt_pk_bf16_f32 v50, v52, v53\n" \
  "v_cvt_pk_bf16_f32 v51, v54, v55\n" \
  "s_movk_i32 s63, 0x1000\n" \
  "s_lshl_b32 s63, s63, 1\n" \
  "buffer_store_dwordx4 v[48:51], v23, s[56:59], s63 offen sc1\n" \
  "s_waitcnt lgkmcnt(8)\n" \
  "v_mfma_f32_16x16x32_bf16 v[56:59], v[172:175], v[120:123], v[56:59]\n" \
  "v_mfma_f32_16x16x32_bf16 v[60:63], v[172:175], v[124:127], v[60:63]\n" \
  "s_waitcnt lgkmcnt(7)\n" \
  "v_mfma_f32_16x16x32_bf16 v[64:67], v[176:179], v[96:99], 0\n" \
  "v_mfma_f32_16x16x32_bf16 v[68:71], v[176:179], v[100:103], 0\n" \
  "s_waitcnt lgkmcnt(6)\n" \
  "v_mfma_f32_16x16x32_bf16 v[64:67], v[180:183], v[104:107], v[64:67]\n" \
  "v_mfma_f32_16x16x32_bf16 v[68:71], v[180:183], v[108:111], v[68:71]\n" \
  "s_waitcnt lgkmcnt(5)\n" \
  "v_mfma_f32_16x16x32_bf16 v[64:67], v[184:187], v[112:115], v[64:67]\n" \
  "v_mfma_f32_16x16x32_bf16 v[68:71], v[184:187], v[116:119], v[68:71]\n" \
  "v_cvt_pk_bf16_f32 v56, v56, v57\n" \
  "v_cvt_pk_bf16_f32 v57, v58, v59\n" \
  "v_cvt_pk_bf16_f32 v58, v60, v61\n" \
  "v_cvt_pk_bf16_f32 v59, v62, v63\n" \
  "s_movk_i32 s63, 0x2000\n" \
  "s_lshl_b32 s63, s63, 1\n" \
  "buffer_store_dwordx4 v[56:59], v23, s[56:59], s63 offen sc1\n" \
  "s_waitcnt lgkmcnt(4)\n" \
  "v_mfma_f32_16x16x32_bf16 v[64:67], v[188:191], v[120:123], v[64:67]\n" \
  "v_mfma_f32_16x16x32_bf16 v[68:71], v[188:191], v[124:127], v[68:71]\n" \
  "s_waitcnt lgkmcnt(3)\n" \
  "v_mfma_f32_16x16x32_bf16 v[72:75], v[192:195], v[96:99], 0\n" \
  "v_mfma_f32_16x16x32_bf16 v[76:79], v[192:195], v[100:103], 0\n" \
  "s_waitcnt lgkmcnt(2)\n" \
  "v_mfma_f32_16x16x32_bf16 v[72:75], v[196:199], v[104:107], v[72:75]\n" \
  "v_mfma_f32_16x16x32_bf16 v[76:79], v[196:199], v[108:111], v[76:79]\n" \
  "s_waitcnt lgkmcnt(1)\n" \
  "v_mfma_f32_16x16x32_bf16 v[72:75], v[200:203], v[112:115], v[72:75]\n" \
  "v_mfma_f32_16x16x32_bf16 v[76:79], v[200:203], v[116:119], v[76:79]\n" \
  "v_cvt_pk_bf16_f32 v64, v64, v65\n" \
  "v_cvt_pk_bf16_f32 v65, v66, v67\n" \
  "v_cvt_pk_bf16_f32 v66, v68, v69\n" \
  "v_cvt_pk_bf16_f32 v67, v70, v71\n" \
  "s_movk_i32 s63, 0x3000\n" \
  "s_lshl_b32 s63, s63, 1\n" \
  "buffer_store_dwordx4 v[64:67], v23, s[56:59], s63 offen sc1\n" \
  "s_waitcnt lgkmcnt(0)\n" \
  "v_mfma_f32_16x16x32_bf16 v[72:75], v[204:207], v[120:123], v[72:75]\n" \
  "v_mfma_f32_16x16x32_bf16 v[76:79], v[204:207], v[124:127], v[76:79]\n" \
  "s_nop 7\n" \
  "v_cvt_pk_bf16_f32 v72, v72, v73\n" \
  "v_cvt_pk_bf16_f32 v73, v74, v75\n" \
  "v_cvt_pk_bf16_f32 v74, v76, v77\n" \
  "v_cvt_pk_bf16_f32 v75, v78, v79\n" \
  "s_movk_i32 s63, 0x4000\n" \
  "s_lshl_b32 s63, s63, 1\n" \
  "buffer_store_dwordx4 v[72:75], v23, s[56:59], s63 offen sc1\n" \
  "s_waitcnt vmcnt(5)\n" \
  "v_readfirstlane_b32 s63, v20\n" \
  "s_cmp_eq_u32 s63, s50\n" \
  "s_cbranch_scc1 Lout%=\n" \
  "Lep%=:\n" \
  "s_cmp_lg_u32 s44, 0\n" \
  "s_cbranch_scc1 Lout%=\n" \
  "v_mov_b64 v[84:85], s[50:51]\n" \
  "s_mov_b64 exec, 1\n" \
  "buffer_store_dwordx2 v[84:85], off, s[24:27], s70 sc1\n" \
  "s_mov_b64 exec, -1\n" \
  "Lout%=:\n" \
  "s_mov_b32 m0, s79\n"
#define STREAM_CLOBBERS \
  "v12", "v13", "v14", "v15", "v16", "v17", "v18", "v19", "v20", "v21", "v22", "v23", "v24", "v25", "v26", \
  "v27", "v28", "v29", "v30", "v31", "v32", "v33", "v34", "v35", "v36", "v37", "v38", "v39", "v40", "v41", \
  "v42", "v43", "v44", "v45", "v46", "v47", "v48", "v49", "v50", "v51", "v52", "v53", "v54", "v55", "v56", \
  "v57", "v58", "v59", "v60", "v61", "v62", "v63", "v64", "v65", "v66", "v67", "v68", "v69", "v70", "v71", \
  "v72", "v73", "v74", "v75", "v76", "v77", "v78", "v79", "v80", "v81", "v82", "v83", "v84", "v85", "v86", \
  "v87", "v88", "v89", "v90", "v91", "v92", "v93", "v94", "v95", "v96", "v97", "v98", "v99", "v100", "v101", \
  "v102", "v103", "v104", "v105", "v106", "v107", "v108", "v109", "v110", "v111", "v112", "v113", "v114", \
  "v115", "v116", "v117", "v118", "v119", "v120", "v121", "v122", "v123", "v124", "v125", "v126", "v127", \
  "v128", "v129", "v130", "v131", "v132", "v133", "v134", "v135", "v136", "v137", "v138", "v139", "v140", \
  "v141", "v142", "v143", "v144", "v145", "v146", "v147", "v148", "v149", "v150", "v151", "v152", "v153", \
  "v154", "v155", "v156", "v157", "v158", "v159", "v160", "v161", "v162", "v163", "v164", "v165", "v166", \
  "v167", "v168", "v169", "v170", "v171", "v172", "v173", "v174", "v175", "v176", "v177", "v178", "v179", \
  "v180", "v181", "v182", "v183", "v184", "v185", "v186", "v187", "v188", "v189", "v190", "v191", "v192", \
  "v193", "v194", "v195", "v196", "v197", "v198", "v199", "v200", "v201", "v202", "v203", "v204", "v205", \
  "v206", "v207", "v208", "v209", "v210", "v211", "v212", "v213", "v214", "v215", "v216", "v217", "v218", \
  "v219", "v220", "v221", "v222", "v223", "v224", "v225", "v226", "v227", "v228", "v229", "v230", "v231", \
  "v232", "v233", "v234", "v235", "vcc", "s8", "s9", "s10", "s11", "s12", "s13", "s14", "s15", "s16", "s17", \
  "s18", "s19", "s20", "s21", "s22", "s23", "s24", "s25", "s26", "s27", "s28", "s29", "s30", "s31", "s33", \
  "s34", "s35", "s36", "s37", "s38", "s39", "s40", "s41", "s42", "s43", "s44", "s45", "s46", "s47", "s48", \
  "s49", "s50", "s51", "s52", "s53", "s54", "s55", "s56", "s57", "s58", "s59", "s60", "s61", "s62", "s63", \
  "s64", "s65", "s66", "s67", "s68", "s69", "s70", "s71", "s72", "s73", "s74", "s75", "s76", "s77", "s78", \
  "s79", "s80", "s81", "s82", "s83", "s84", "s85", "s86", "s87", "s88", "s89", "s90", "s91", "s92", "s93", \
  "s94", "s95", "scc", "memory"
#define LD_CELL 34560
#define LD_DEG 35584
#define LD_FLAG 35840
#define LD_CRD 35872
#define LD_SINK 35904
#define LD_CGT 36160
#define LD_WQT 36288
#define LD_TAG 37312
#define LD_PAY 37328
#define LD_ENT 100096
#define LD_TB 106816
#define LD_ROWS 108864
#define LD_SK 129344
#define LD_HIST 134464
#define LD_HB 136512
#define WS_EP 32768
#define WS_FB 33024
#define WS_GR 49408
#define WS_RECK 606464
#define WS_RECP 1917184
#define WS_END 7160064

namespace {

constexpr int C_ = 128;
constexpr int NMAX = 40;
constexpr int NB = 256;
constexpr int CH = 1280;
constexpr int MT = 512;
constexpr int MW = MT / 64;
constexpr int RP = 1056;
constexpr unsigned W3 = 0x00020000u;

__device__ __forceinline__ float bf2f(unsigned short v) { return __uint_as_float(((unsigned)v) << 16); }
__device__ __forceinline__ unsigned short f2bf(float f) {
  __hip_bfloat16 b = __float2bfloat16(f);
  return *reinterpret_cast<unsigned short*>(&b);
}
__device__ __forceinline__ unsigned pk2bf(float lo, float hi) {
  return __builtin_bit_cast(unsigned, __builtin_convertvector(f32x2{lo, hi}, bf16x2));
}

}

extern "C" __global__ __launch_bounds__(MT) void tfn_kernel(
    const int* __restrict__ dst, unsigned dn, unsigned dw, const int* __restrict__ src, unsigned sn, unsigned sw,
    const u32x2* __restrict__ Y, unsigned yn, unsigned yw, const u32x2* __restrict__ ef, unsigned fn, unsigned fw,
    char* ws, unsigned wn, unsigned ww, const unsigned short* __restrict__ x, unsigned xn, unsigned xw,
    u32x4* __restrict__ out, unsigned pt, int N, unsigned M2, unsigned cb, int nbk, unsigned npb,
    const unsigned short* __restrict__ wrad, unsigned rn, unsigned rw, const unsigned short* __restrict__ cg, unsigned cn,
    unsigned cw, const unsigned short* __restrict__ wmix, unsigned mn, unsigned mw) {
  __shared__ __attribute__((aligned(16))) char lds[LD_ROWS + NMAX * RP];
  TDECL;
  MSTAMP(0);
  unsigned bad;
  asm volatile(STREAM_ASM
               : [bad] "=&s"(bad)
               : [ka] "s"(__builtin_amdgcn_kernarg_segment_ptr()), [blk] "s"(blockIdx.x), [tid] "v"(threadIdx.x)
               : STREAM_CLOBBERS);
  const bool slow = bad != 0;
  MSTAMP(6);
  if (slow) {
    const int lane = threadIdx.x & 63, w = __builtin_amdgcn_readfirstlane(threadIdx.x >> 6), E = dn / 4;
    const int bucket = (blockIdx.x & 7) * (nbk / 8) + (blockIdx.x >> 3);
    int n01[2];
#pragma unroll
    for (int h = 0; h < 2; h++) {
      unsigned c = (unsigned)(((unsigned long long)(bucket + h) * npb) >> 20);
#pragma unroll
      for (int r = 0; r < 2; r++) c += __umulhi(c, M2) < (unsigned)(bucket + h) ? 1 : 0;
      n01[h] = min(c, (unsigned)N);
    }
    const int node0 = n01[0], nn = n01[1] - n01[0];
    const unsigned mxa = LD_ROWS + ((w >> 2) * 4 + ((lane & 15) >> 2)) * RP + (lane & 3) * 64 + (lane >> 4) * 16;
    const unsigned mxo = ((w >> 2) * 4 + (lane >> 4)) * 1024 + (w & 3) * 256 + (lane & 15) * 16;
    __syncthreads();
#pragma unroll 1
    for (int it = w; it < 2 * nn; it += MW) {
      const int dl = it >> 1, ch = (it & 1) * 64 + lane;
      float acc[4] = {0.f, 0.f, 0.f, 0.f};
#pragma unroll 1
      for (int eb = 0; eb < E; eb += 64) {
        unsigned long long m = __ballot(eb + lane < E && dst[min(eb + lane, E - 1)] == node0 + dl);
#pragma unroll 1
        for (; m; m &= m - 1) {
          const int e = eb + __builtin_ctzll(m);
          const u32x2 ry = Y[e], rf = ef[e];
          float yy[4] = {bf2f(ry[0] & 0xffff), bf2f(ry[0] >> 16), bf2f(ry[1] & 0xffff), bf2f(ry[1] >> 16)};
          float ee[4] = {bf2f(rf[0] & 0xffff), bf2f(rf[0] >> 16), bf2f(rf[1] & 0xffff), bf2f(rf[1] >> 16)};
          float z = 0.f;
#pragma unroll
          for (int k = 0; k < 4; k++) z += ee[k] * bf2f(wrad[k * C_ + ch]);
          const float R = z / (1.f + __expf(-z));
          const unsigned short* xr = x + (size_t)src[e] * 512 + ch * 4;
#pragma unroll 1
          for (int o = 0; o < 4; o++) {
            float tp = 0.f;
#pragma unroll 1
            for (int i = 0; i < 4; i++) {
              float A = 0.f;
#pragma unroll
              for (int f = 0; f < 4; f++) A += yy[f] * bf2f(cg[i * 16 + f * 4 + o]);
              tp += bf2f(xr[i]) * bf2f(f2bf(A));
            }
            const float v = R * tp;
            acc[0] += o == 0 ? v : 0.f;
            acc[1] += o == 1 ? v : 0.f;
            acc[2] += o == 2 ? v : 0.f;
            acc[3] += o == 3 ? v : 0.f;
          }
        }
      }
      unsigned short* ar = reinterpret_cast<unsigned short*>(lds + LD_ROWS) + dl * (RP / 2) + (ch >> 5) * 128 + (ch & 31);
#pragma unroll
      for (int o = 0; o < 4; o++) ar[o * 32] = f2bf(acc[o]);
    }
    __syncthreads();
    const auto ors = __builtin_amdgcn_make_buffer_rsrc((void*)(out + (size_t)node0 * 64), 0, nn * 1024, 0x00020000);
#pragma unroll 1
    for (int t = 0; t < NMAX / 8; t++) {
      f32x4 a = {0.f, 0.f, 0.f, 0.f}, b = {0.f, 0.f, 0.f, 0.f};
#pragma unroll 1
      for (int k = 0; k < 4; k++) {
        const s16x8 f = *reinterpret_cast<const s16x8*>(lds + mxa + t * 8 * RP + k * 256);
        const unsigned short* wp = wmix + (32 * k + 8 * (lane >> 4)) * C_ + 32 * (w & 3) + 2 * (lane & 15);
        s16x8 w0, w1;
#pragma unroll
        for (int j = 0; j < 8; j++) {
          w0[j] = wp[j * C_];
          w1[j] = wp[j * C_ + 1];
        }
        a = __builtin_amdgcn_mfma_f32_16x16x32_bf16(f, w0, a, 0, 0, 0);
        b = __builtin_amdgcn_mfma_f32_16x16x32_bf16(f, w1, b, 0, 0, 0);
      }
      __builtin_amdgcn_raw_buffer_store_b128(u32x4{pk2bf(a[0], a[1]), pk2bf(a[2], a[3]), pk2bf(b[0], b[1]), pk2bf(b[2], b[3])},
                                             ors, mxo + t * 8192, 0, 0);
    }
  }
  MFLUSH;
}

extern "C" void kernel_launch(void* const* d_in, const int* in_sizes, int n_in, void* d_out, int out_size,
                              void* d_ws, size_t ws_size, hipStream_t stream) {
  const int N = in_sizes[0] / 512;
  const int E = in_sizes[1];
  int nbk = NB;
  while ((N + nbk - 1) / nbk > NMAX || (N % nbk == 0 && N / nbk >= NMAX)) nbk += NB;
  const unsigned long long M2l = (((unsigned long long)nbk) << 32) / (unsigned long long)N;
  const unsigned M2 = M2l > 0xffffffffull ? 0xffffffffu : (unsigned)M2l;
  const unsigned long long cb = (16ull << 32) / (unsigned long long)N;
  const bool fast = nbk == NB && E <= NB * CH && ws_size >= WS_END;
  const unsigned e4 = E * 4u;
  tfn_kernel<<<nbk, MT, 0, stream>>>(
      (const int*)d_in[2], e4, W3, (const int*)d_in[1], e4, W3, (const u32x2*)d_in[3], 2 * e4, W3, (const u32x2*)d_in[4], 2 * e4, W3,
      (char*)d_ws, WS_END, W3, (const unsigned short*)d_in[0], N * 1024u, W3, (u32x4*)d_out, fast ? 448u : 0u, N, M2,
      cb > 0xffffffull ? 0xffffffu : (unsigned)cb, nbk, (unsigned)((1ull << 52) / M2), (const unsigned short*)d_in[5], 4 * C_ * 2,
      W3, (const unsigned short*)d_in[6], 128, W3, (const unsigned short*)d_in[7], C_ * C_ * 2, W3);
}
```

```cpp
#include <hip/hip_runtime.h>
#include <hip/hip_bf16.h>
#include <stdint.h>
#include <stddef.h>

typedef __attribute__((ext_vector_type(2))) float f32x2;
typedef __attribute__((ext_vector_type(4))) float f32x4;
typedef __attribute__((ext_vector_type(8))) short s16x8;
typedef __attribute__((ext_vector_type(2))) unsigned int u32x2;
typedef __attribute__((ext_vector_type(4))) unsigned int u32x4;
typedef __attribute__((ext_vector_type(16))) unsigned int u32x16;
typedef __attribute__((ext_vector_type(2))) __bf16 bf16x2;

#define TDECL
#define TSTAMP(i)
#define TFLUSH
#define MSTAMP(i) TSTAMP(i)
#define MFLUSH do { asm volatile("s_waitcnt vmcnt(0)" ::: "memory"); TSTAMP(15); TFLUSH; } while (0)
#undef MFLUSH
#define MFLUSH

#define STREAM_ASM \
  "s_load_dwordx16 s[8:23], %[ka], 0\n" \
  "s_load_dwordx16 s[24:39], %[ka], 64\n" \
  "s_memrealtime s[76:77]\n" \
  "s_getpc_b64 s[40:41]\n" \
  "Lpc%=:\n" \
  "v_lshlrev_b32 v50, 7, %[tid]\n" \
  "s_mov_b32 s42, Lout%=-Lpc%=\n" \
  "s_mov_b32 s43, 0x20000\n" \
  "buffer_load_dword v208, v50, s[40:43], 0 offen\n" \
  "v_lshlrev_b32 v52, 3, %[tid]\n" \
  "v_lshlrev_b32 v53, 2, %[tid]\n" \
  "v_mov_b32 v48, 0\n" \
  "v_readfirstlane_b32 s44, %[tid]\n" \
  "v_add_u32 v49, 134464, v53\n" \
  "ds_write_b32 v49, v48\n" \
  "s_mul_i32 s63, %[blk], 5120\n" \
  "s_add_u32 s61, s63, 3584\n" \
  "s_cmp_lt_u32 s44, 384\n" \
  "s_cselect_b32 s61, s61, 0x40000000\n" \
  "s_waitcnt lgkmcnt(0)\n" \
  "s_cmp_lt_u32 s44, s34\n" \
  "s_cbranch_scc0 Lh0_%=\n" \
  "buffer_load_dwordx2 v[100:101], v52, s[8:11], s63 offen nt\n" \
  "buffer_load_dword v102, v53, s[8:11], s61 offen nt\n" \
  "s_barrier\n" \
  "buffer_load_dwordx2 v[104:105], v52, s[12:15], s63 offen nt\n" \
  "buffer_load_dword v106, v53, s[12:15], s61 offen nt\n" \
  "v_lshlrev_b32 v49, 1, v52\n" \
  "s_lshl_b32 s93, s63, 1\n" \
  "s_lshl_b32 s62, s61, 1\n" \
  "buffer_load_dwordx4 v[108:111], v49, s[16:19], s93 offen nt\n" \
  "buffer_load_dwordx4 v[114:117], v49, s[20:23], s93 offen nt\n" \
  "buffer_load_dwordx2 v[112:113], v52, s[16:19], s62 offen nt\n" \
  "buffer_load_dwordx2 v[118:119], v52, s[20:23], s62 offen nt\n" \
  "s_sub_i32 s63, s10, s63\n" \
  "s_sub_i32 s61, s10, s61\n" \
  "v_cmp_gt_i32 s[80:81], s63, v52\n" \
  "s_sub_i32 s63, s63, 4\n" \
  "v_cmp_gt_i32 s[84:85], s61, v53\n" \
  "v_cmp_gt_i32 s[82:83], s63, v52\n" \
  "s_branch Lh1_%=\n" \
  "Lh0_%=:\n" \
  "s_mov_b32 s93, 32768\n" \
  "buffer_load_dwordx2 v[88:89], off, s[24:27], s93\n" \
  "s_barrier\n" \
  "s_load_dwordx8 s[8:15], %[ka], 128\n" \
  "s_load_dwordx4 s[16:19], %[ka], 160\n" \
  "s_mov_b64 s[80:81], 0\n" \
  "s_mov_b64 s[82:83], 0\n" \
  "s_mov_b64 s[84:85], 0\n" \
  "s_cmp_eq_u32 s34, 0\n" \
  "s_cbranch_scc1 Lnf%=\n" \
  "Lh1_%=:\n" \
  "ds_write_b32 v53, v48 offset:34560\n" \
  "s_lshr_b32 s44, s44, 6\n" \
  "v_and_b32 v86, 63, %[tid]\n" \
  "s_lshl_b32 s72, s44, 3\n" \
  "s_lshl_b32 s63, 1, s72\n" \
  "v_mov_b32 v93, s63\n" \
  "s_and_b32 s45, s72, 24\n" \
  "s_lshl_b64 s[46:47], -1, s72\n" \
  "s_and_b32 s46, s46, 0x01010101\n" \
  "s_and_b32 s47, s47, 0x01010101\n" \
  "s_and_b32 s73, s44, 4\n" \
  "s_add_u32 s74, s73, 134464\n" \
  "s_lshl_b32 s75, s44, 9\n" \
  "s_add_u32 s75, s75, 136512\n" \
  "v_lshlrev_b32 v13, 4, v86\n" \
  "v_lshlrev_b32 v54, 2, v86\n" \
  "v_lshl_add_u32 v52, v86, 3, s75\n" \
  "v_lshlrev_b32 v50, 5, v86\n" \
  "v_add_u32 v50, 134464, v50\n" \
  "s_and_b32 s71, %[blk], 7\n" \
  "s_lshr_b32 s66, %[blk], 3\n" \
  "s_lshl_b32 s68, s71, 5\n" \
  "s_add_u32 s68, s68, s66\n" \
  "s_and_b32 s61, s68, 2\n" \
  "s_lshl_b32 s61, s61, 9\n" \
  "v_and_b32 v55, 0xf8, v54\n" \
  "s_and_b32 s63, s68, 0xfc\n" \
  "s_lshl2_add_u32 s61, s63, s61\n" \
  "v_add_u32 v55, s44, v55\n" \
  "s_and_b32 s63, s68, 1\n" \
  "s_lshl3_add_u32 s61, s63, s61\n" \
  "v_mul_u32_u24 v17, 2176, v55\n" \
  "s_add_u32 s61, s61, 49408\n" \
  "v_mov_b32 v94, 35840\n" \
  "s_mov_b32 s63, 0x01010101\n" \
  "s_waitcnt vmcnt(6)\n" \
  "v_mul_hi_u32 v96, v100, s36\n" \
  "v_mul_hi_u32 v97, v101, s36\n" \
  "v_mul_hi_u32 v98, v102, s36\n" \
  "s_mov_b64 exec, s[80:81]\n" \
  "v_lshl_add_u32 v120, v96, 3, s74\n" \
  "ds_add_rtn_u32 v124, v120, v93\n" \
  "s_mov_b64 exec, s[82:83]\n" \
  "v_lshl_add_u32 v121, v97, 3, s74\n" \
  "ds_add_rtn_u32 v125, v121, v93\n" \
  "s_mov_b64 exec, s[84:85]\n" \
  "v_lshl_add_u32 v122, v98, 3, s74\n" \
  "ds_add_rtn_u32 v126, v122, v93\n" \
  "s_mov_b64 exec, -1\n" \
  "v_lshl_add_u32 v96, v96, 1, s75\n" \
  "v_lshl_add_u32 v97, v97, 1, s75\n" \
  "v_lshl_add_u32 v98, v98, 1, s75\n" \
  "s_waitcnt lgkmcnt(0)\n" \
  "s_barrier\n" \
  "ds_read_b128 v[128:131], v50\n" \
  "ds_read_b128 v[132:135], v50 offset:16\n" \
  "v_bfe_u32 v124, v124, s45, 8\n" \
  "v_bfe_u32 v125, v125, s45, 8\n" \
  "v_bfe_u32 v126, v126, s45, 8\n" \
  "s_waitcnt lgkmcnt(0)\n" \
  "v_dot4_u32_u8 v136, v128, s63, 0\n" \
  "v_dot4_u32_u8 v136, v129, s63, v136\n" \
  "v_dot4_u32_u8 v137, v130, s63, v136\n" \
  "v_dot4_u32_u8 v137, v131, s63, v137\n" \
  "v_dot4_u32_u8 v138, v132, s63, v137\n" \
  "v_dot4_u32_u8 v138, v133, s63, v138\n" \
  "v_dot4_u32_u8 v139, v134, s63, v138\n" \
  "v_dot4_u32_u8 v139, v135, s63, v139\n" \
  "v_dot4_u32_u8 v140, v128, s46, 0\n" \
  "v_dot4_u32_u8 v141, v130, s46, 0\n" \
  "v_dot4_u32_u8 v142, v132, s46, 0\n" \
  "v_add_u32_dpp v87, v139, v139 row_shr:1 row_mask:0xf bank_mask:0xf bound_ctrl:1\n" \
  "v_dot4_u32_u8 v143, v134, s46, 0\n" \
  "v_dot4_u32_u8 v140, v129, s47, v140\n" \
  "v_add_u32_dpp v87, v87, v87 row_shr:2 row_mask:0xf bank_mask:0xf bound_ctrl:1\n" \
  "v_dot4_u32_u8 v141, v131, s47, v141\n" \
  "v_dot4_u32_u8 v142, v133, s47, v142\n" \
  "v_add_u32_dpp v87, v87, v87 row_shr:4 row_mask:0xf bank_mask:0xf bound_ctrl:1\n" \
  "v_dot4_u32_u8 v143, v135, s47, v143\n" \
  "s_mov_b32 s63, 0x10001\n" \
  "v_add_u32_dpp v87, v87, v87 row_shr:8 row_mask:0xf bank_mask:0xf bound_ctrl:1\n" \
  "v_sub_u32 v140, v136, v140\n" \
  "v_sub_u32 v141, v137, v141\n" \
  "v_add_u32_dpp v87, v87, v87 row_bcast:15 row_mask:0xa bank_mask:0xf\n" \
  "v_sub_u32 v142, v138, v142\n" \
  "v_sub_u32 v143, v139, v143\n" \
  "v_add_u32_dpp v87, v87, v87 row_bcast:31 row_mask:0xc bank_mask:0xf\n" \
  "v_lshl_or_b32 v140, v141, 16, v140\n" \
  "v_sub_u32 v87, v87, v139\n" \
  "v_lshl_or_b32 v141, v143, 16, v142\n" \
  "s_cmp_eq_u32 s44, 7\n" \
  "v_mad_u32_u24 v140, v87, s63, v140\n" \
  "v_mad_u32_u24 v141, v87, s63, v141\n" \
  "s_cbranch_scc1 Lh2_%=\n" \
  "ds_write_b64 v52, v[140:141]\n" \
  "ds_read_u16 v96, v96\n" \
  "ds_read_u16 v97, v97\n" \
  "ds_read_u16 v98, v98\n" \
  "s_mov_b32 s63, 129344\n" \
  "s_mov_b32 s93, 108864\n" \
  "s_lshl_b32 s74, s44, 10\n" \
  "v_add_u32 v50, s74, v13\n" \
  "v_add_u32 v51, s93, v50\n" \
  "s_waitcnt vmcnt(4)\n" \
  "v_lshl_or_b32 v104, v100, 24, v104\n" \
  "v_lshl_or_b32 v105, v101, 24, v105\n" \
  "v_lshl_or_b32 v106, v102, 24, v106\n" \
  "s_waitcnt lgkmcnt(0)\n" \
  "s_mov_b64 exec, s[80:81]\n" \
  "v_add_u32 v124, v124, v96\n" \
  "v_lshl_add_u32 v120, v124, 2, s63\n" \
  "v_lshl_add_u32 v124, v124, 4, s93\n" \
  "ds_write_b32 v120, v104\n" \
  "s_mov_b64 exec, s[82:83]\n" \
  "v_add_u32 v125, v125, v97\n" \
  "v_lshl_add_u32 v121, v125, 2, s63\n" \
  "v_lshl_add_u32 v125, v125, 4, s93\n" \
  "ds_write_b32 v121, v105\n" \
  "s_mov_b64 exec, s[84:85]\n" \
  "v_add_u32 v126, v126, v98\n" \
  "v_lshl_add_u32 v122, v126, 2, s63\n" \
  "v_lshl_add_u32 v126, v126, 4, s93\n" \
  "ds_write_b32 v122, v106\n" \
  "s_mul_i32 s72, %[blk], 20480\n" \
  "s_lshr_b32 s62, s72, 2\n" \
  "s_add_u32 s72, s72, 1917184\n" \
  "s_add_u32 s62, s62, 607488\n" \
  "s_add_u32 s74, s72, 7168\n" \
  "s_add_u32 s75, s72, 14336\n" \
  "s_sub_u32 s63, s62, 7168\n" \
  "s_cmp_eq_u32 s44, 6\n" \
  "s_cselect_b32 s75, s63, s75\n" \
  "s_waitcnt vmcnt(0)\n" \
  "ds_write2_b64 v126, v[112:113], v[118:119] offset1:1\n" \
  "s_mov_b64 exec, s[82:83]\n" \
  "ds_write2_b64 v125, v[110:111], v[116:117] offset1:1\n" \
  "s_mov_b64 exec, s[80:81]\n" \
  "ds_write2_b64 v124, v[108:109], v[114:115] offset1:1\n" \
  "s_mov_b64 exec, -1\n" \
  "s_cmp_gt_u32 s44, 3\n" \
  "s_waitcnt lgkmcnt(0)\n" \
  "s_barrier\n" \
  "ds_read_b128 v[216:219], v51 offset:0\n" \
  "ds_read_b128 v[220:223], v51 offset:7168\n" \
  "ds_read_b128 v[224:227], v51 offset:14336\n" \
  "ds_read_b128 v[228:231], v51 offset:21504\n" \
  "s_waitcnt lgkmcnt(3)\n" \
  "buffer_store_dwordx4 v[216:219], v50, s[24:27], s72 offen sc1\n" \
  "s_waitcnt lgkmcnt(2)\n" \
  "buffer_store_dwordx4 v[220:223], v50, s[24:27], s74 offen sc1\n" \
  "s_waitcnt lgkmcnt(1)\n" \
  "buffer_store_dwordx4 v[224:227], v50, s[24:27], s75 offen sc1\n" \
  "s_cbranch_scc1 Lc1_%=\n" \
  "s_waitcnt lgkmcnt(0)\n" \
  "buffer_store_dwordx4 v[228:231], v50, s[24:27], s62 offen sc1\n" \
  "Lc1_%=:\n" \
  "s_mov_b32 s63, 0x80008\n" \
  "s_mov_b32 s93, 0x80008000\n" \
  "v_mul_lo_u32 v52, %[tid], s63\n" \
  "v_lshlrev_b32 v53, 4, %[tid]\n" \
  "v_add_u32 v49, 0x10000, v53\n" \
  "v_cmp_gt_u32 s[90:91], 32, %[tid]\n" \
  "v_cmp_gt_u32 vcc, 368, %[tid]\n" \
  "v_add_u32 v104, 0xf810f80, v52\n" \
  "v_add_u32 v105, 0xf830f82, v52\n" \
  "v_add_u32 v106, 0xf850f84, v52\n" \
  "v_add_u32 v107, 0xf870f86, v52\n" \
  "v_or_b32 v108, s93, v104\n" \
  "v_or_b32 v109, s93, v105\n" \
  "v_or_b32 v110, s93, v106\n" \
  "v_or_b32 v111, s93, v107\n" \
  "ds_write_b128 v53, v[104:107] offset:8192\n" \
  "ds_write_b128 v49, v[108:111] offset:8192\n" \
  "v_add_u32 v112, 0x1f811f80, v52\n" \
  "v_add_u32 v113, 0x1f831f82, v52\n" \
  "v_add_u32 v114, 0x1f851f84, v52\n" \
  "v_add_u32 v115, 0x1f871f86, v52\n" \
  "v_or_b32 v116, s93, v112\n" \
  "v_or_b32 v117, s93, v113\n" \
  "v_or_b32 v118, s93, v114\n" \
  "v_or_b32 v119, s93, v115\n" \
  "ds_write_b128 v53, v[112:115] offset:16384\n" \
  "ds_write_b128 v49, v[116:119] offset:16384\n" \
  "s_mov_b64 exec, vcc\n" \
  "v_add_u32 v120, 0x2f812f80, v52\n" \
  "v_add_u32 v121, 0x2f832f82, v52\n" \
  "v_add_u32 v122, 0x2f852f84, v52\n" \
  "v_add_u32 v123, 0x2f872f86, v52\n" \
  "v_or_b32 v124, s93, v120\n" \
  "v_or_b32 v125, s93, v121\n" \
  "v_or_b32 v126, s93, v122\n" \
  "v_or_b32 v127, s93, v123\n" \
  "ds_write_b128 v53, v[120:123] offset:24576\n" \
  "ds_write_b128 v49, v[124:127] offset:24576\n" \
  "s_mov_b64 exec, -1\n" \
  "v_add_u32 v96, 0xff80ff80, v52\n" \
  "v_add_u32 v97, 0xff82ff82, v52\n" \
  "v_add_u32 v98, 0xff84ff84, v52\n" \
  "v_add_u32 v99, 0xff86ff86, v52\n" \
  "v_lshrrev_b32 v52, 1, v52\n" \
  "s_mov_b64 exec, s[90:91]\n" \
  "v_add_u32 v96, 0x0, v52\n" \
  "v_add_u32 v97, 0x20001, v52\n" \
  "v_add_u32 v98, 0x20002, v52\n" \
  "v_add_u32 v99, 0x40003, v52\n" \
  "s_mov_b64 exec, -1\n" \
  "v_or_b32 v100, s93, v96\n" \
  "v_or_b32 v101, s93, v97\n" \
  "v_or_b32 v102, s93, v98\n" \
  "v_or_b32 v103, s93, v99\n" \
  "ds_write_b128 v53, v[96:99] offset:0\n" \
  "ds_write_b128 v49, v[100:103] offset:0\n" \
  "buffer_load_dwordx2 v[44:45], v17, s[24:27], s61 offen sc1\n" \
  "s_mov_b32 s79, m0\n" \
  "s_mov_b64 s[88:89], 0\n" \
  "s_mov_b32 s92, 0\n" \
  "v_mov_b32 v92, 1\n" \
  "v_and_b32 v41, 4, v54\n" \
  "s_movk_i32 s63, 5120\n" \
  "v_mad_u32_u24 v40, v55, s63, v41\n" \
  "s_lshl_b32 s63, s44, 10\n" \
  "s_add_u32 s63, s63, 37328\n" \
  "v_add_u32 v41, s63, v41\n" \
  "s_mul_i32 s90, s68, s39\n" \
  "s_mul_hi_u32 s91, s68, s39\n" \
  "s_lshr_b64 s[90:91], s[90:91], 20\n" \
  "s_mul_hi_u32 s63, s90, s36\n" \
  "s_cmp_lt_u32 s63, s68\n" \
  "s_addc_u32 s90, s90, 0\n" \
  "s_mul_hi_u32 s63, s90, s36\n" \
  "s_cmp_lt_u32 s63, s68\n" \
  "s_addc_u32 s90, s90, 0\n" \
  "s_min_u32 s67, s90, s35\n" \
  "s_add_u32 s68, s68, 1\n" \
  "s_mul_i32 s90, s68, s39\n" \
  "s_mul_hi_u32 s91, s68, s39\n" \
  "s_lshr_b64 s[90:91], s[90:91], 20\n" \
  "s_mul_hi_u32 s63, s90, s36\n" \
  "s_cmp_lt_u32 s63, s68\n" \
  "s_addc_u32 s90, s90, 0\n" \
  "s_mul_hi_u32 s63, s90, s36\n" \
  "s_cmp_lt_u32 s63, s68\n" \
  "s_addc_u32 s90, s90, 0\n" \
  "s_min_u32 s68, s90, s35\n" \
  "s_sub_u32 s68, s68, s67\n" \
  "s_mul_i32 s63, s71, s35\n" \
  "s_lshr_b32 s63, s63, 3\n" \
  "s_lshl3_add_u32 s63, s66, s63\n" \
  "s_add_u32 s66, s63, s44\n" \
  "s_lshl_b32 s66, s66, 10\n" \
  "v_lshlrev_b32 v20, 3, v13\n" \
  "v_cmp_gt_u32 vcc, 8, v86\n" \
  "s_lshl_b32 s71, s71, 1\n" \
  "s_mov_b32 m0, 35904\n" \
  "v_lshlrev_b32 v50, 5, v86\n" \
  "v_and_b32 v50, 0x60, v50\n" \
  "v_bfrev_b32 v51, 1\n" \
  "v_cndmask_b32 v20, v51, v20, vcc\n" \
  "s_lshl_b32 s63, %[blk], 6\n" \
  "s_add_u32 s63, s63, 33024\n" \
  "s_cmp_lg_u32 s44, 7\n" \
  "s_waitcnt vmcnt(1) lgkmcnt(0)\n" \
  "s_barrier\n" \
  "s_cbranch_scc1 Ls1_%=\n" \
  "s_mov_b64 exec, 1\n" \
  "buffer_store_dwordx2 v[88:89], off, s[24:27], s63 sc1\n" \
  "s_mov_b64 exec, -1\n" \
  "Ls1_%=:\n" \
  "ds_read_b96 v[56:58], v94 offset:1472\n" \
  "ds_read_b128 v[24:27], v50 offset:36160\n" \
  "ds_read_b128 v[28:31], v50 offset:36176\n" \
  "ds_read_b64 v[32:33], v13 offset:36288\n" \
  "ds_read_b64 v[36:37], v13 offset:36296\n" \
  "s_waitcnt lgkmcnt(4)\n" \
  "v_readfirstlane_b32 s50, v56\n" \
  "v_readfirstlane_b32 s51, v57\n" \
  "v_readfirstlane_b32 s69, v58\n" \
  "s_lshl_b32 s64, s50, 22\n" \
  "s_lshr_b64 s[90:91], s[50:51], 10\n" \
  "s_mov_b32 s65, s90\n" \
  "s_cmp_lg_u32 s44, 7\n" \
  "s_cbranch_scc1 Lp2_%=\n" \
  "s_waitcnt vmcnt(1)\n" \
  "s_branch Lchk%=\n" \
  "Lpoll%=:\n" \
  "buffer_load_dwordx2 v[44:45], v17, s[24:27], s61 offen sc1\n" \
  "Lp2_%=:\n" \
  "s_waitcnt vmcnt(0)\n" \
  "Lchk%=:\n" \
  "v_and_b32 v50, 0xffc00000, v44\n" \
  "v_cmp_eq_u32 s[90:91], s65, v45\n" \
  "v_cmp_eq_u32 vcc, s64, v50\n" \
  "s_and_b64 vcc, vcc, s[90:91]\n" \
  "s_andn2_b64 vcc, vcc, s[88:89]\n" \
  "s_or_b64 s[88:89], s[88:89], vcc\n" \
  "v_cndmask_b32 v48, v48, v44, vcc\n" \
  "s_cmp_eq_u64 s[88:89], -1\n" \
  "v_cndmask_b32 v17, v17, v51, vcc\n" \
  "s_cbranch_scc1 Lseen%=\n" \
  "s_add_u32 s92, s92, 1\n" \
  "s_cmp_lt_u32 s92, 4096\n" \
  "s_cbranch_scc1 Lpoll%=\n" \
  "v_mov_b32 v48, 0\n" \
  "Lseen%=:\n" \
  "buffer_load_dword v20, s[28:31], s66 offen lds\n" \
  "s_add_u32 s66, s66, 0x40000\n" \
  "v_bfe_u32 v49, v48, 11, 11\n" \
  "v_and_b32 v50, 0x7ff, v48\n" \
  "v_bfe_u32 v52, v13, 4, 1\n" \
  "v_add_u32_dpp v87, v49, v49 row_shr:1 row_mask:0xf bank_mask:0xf bound_ctrl:1\n" \
  "v_lshl_add_u32 v50, v50, 2, v40\n" \
  "v_lshlrev_b32 v53, v52, v49\n" \
  "v_add_u32_dpp v87, v87, v87 row_shr:2 row_mask:0xf bank_mask:0xf bound_ctrl:1\n" \
  "v_sub_u32 v49, v49, v52\n" \
  "v_and_b32 v85, -8, v41\n" \
  "v_add_u32_dpp v87, v87, v87 row_shr:4 row_mask:0xf bank_mask:0xf bound_ctrl:1\n" \
  "v_add_u32 v85, v85, v54\n" \
  "s_nop 0\n" \
  "v_add_u32_dpp v87, v87, v87 row_shr:8 row_mask:0xf bank_mask:0xf bound_ctrl:1\n" \
  "s_nop 1\n" \
  "v_add_u32_dpp v87, v87, v87 row_bcast:15 row_mask:0xa bank_mask:0xf\n" \
  "s_nop 1\n" \
  "v_add_u32_dpp v87, v87, v87 row_bcast:31 row_mask:0xc bank_mask:0xf\n" \
  "v_sub_u32 v53, v87, v53\n" \
  "v_readlane_b32 s62, v87, 63\n" \
  "v_lshl_add_u32 v53, v53, 1, v41\n" \
  "v_lshl_add_u32 v49, v49, 2, v53\n" \
  "s_lshr_b32 s62, s62, 1\n" \
  "s_cmp_lt_u32 s62, 256\n" \
  "s_cbranch_scc1 Ll0_%=\n" \
  "s_mov_b32 s62, 0\n" \
  "ds_or_b32 v94, v92\n" \
  "s_branch Ll2_%=\n" \
  "Ll0_%=:\n" \
  "v_cmp_lt_i32 vcc, v53, v49\n" \
  "s_mov_b64 exec, vcc\n" \
  "Ll1_%=:\n" \
  "ds_write_b32 v53, v50\n" \
  "v_add_u32 v53, 8, v53\n" \
  "v_cmp_lt_i32 vcc, v53, v49\n" \
  "v_add_u32 v50, 8, v50\n" \
  "s_and_b64 exec, vcc, vcc\n" \
  "s_cbranch_scc1 Ll1_%=\n" \
  "s_mov_b64 exec, -1\n" \
  "Ll2_%=:\n" \
  "ds_read2st64_b32 v[56:57], v85 offset1:1\n" \
  "ds_read2st64_b32 v[58:59], v85 offset0:2 offset1:3\n" \
  "v_mov_b32 v50, s62\n" \
  "s_mov_b64 exec, 1\n" \
  "ds_add_u32 v94, v50 offset:4\n" \
  "s_mov_b64 exec, -1\n" \
  "v_cmp_gt_i32 s[80:81], s62, v86\n" \
  "s_sub_i32 s63, s62, 64\n" \
  "v_cmp_gt_i32 s[82:83], s63, v86\n" \
  "s_sub_i32 s63, s62, 128\n" \
  "v_cmp_gt_i32 s[84:85], s63, v86\n" \
  "s_sub_i32 s63, s62, 192\n" \
  "v_cmp_gt_i32 s[86:87], s63, v86\n" \
  "v_bfrev_b32 v50, 4\n" \
  "s_waitcnt lgkmcnt(1)\n" \
  "v_cndmask_b32 v56, v50, v56, s[80:81]\n" \
  "v_cndmask_b32 v57, v50, v57, s[82:83]\n" \
  "v_cndmask_b32 v58, v50, v58, s[84:85]\n" \
  "v_cndmask_b32 v59, v50, v59, s[86:87]\n" \
  "s_sleep 4\n" \
  "v_mul_u32_u24 v18, 64, v55\n" \
  "s_mov_b32 s93, 33024\n" \
  "s_mov_b64 s[88:89], 0\n" \
  "s_mov_b32 s63, 0xbfb8aa3b\n" \
  "v_lshlrev_b32 v100, 17, %[tid]\n" \
  "v_add_u32 v103, 0x3f810000, v100\n" \
  "v_add_u32 v102, 0x3f800000, v100\n" \
  "v_add_u32 v101, 0x3b810000, v100\n" \
  "v_add_u32 v100, 0x3b800000, v100\n" \
  "v_mul_f32 v104, s63, v100\n" \
  "v_mul_f32 v105, s63, v101\n" \
  "v_mul_f32 v106, s63, v102\n" \
  "v_mul_f32 v107, s63, v103\n" \
  "v_exp_f32 v104, v104\n" \
  "v_exp_f32 v105, v105\n" \
  "v_exp_f32 v106, v106\n" \
  "v_exp_f32 v107, v107\n" \
  "v_lshlrev_b32 v96, 2, %[tid]\n" \
  "v_add_f32 v108, 1.0, v104\n" \
  "v_add_f32 v109, 1.0, v105\n" \
  "v_add_f32 v110, 1.0, v106\n" \
  "v_add_f32 v111, 1.0, v107\n" \
  "v_rcp_f32 v108, v108\n" \
  "v_rcp_f32 v109, v109\n" \
  "v_rcp_f32 v110, v110\n" \
  "v_rcp_f32 v111, v111\n" \
  "v_add_u32 v97, 0x10000, v96\n" \
  "v_pk_mul_f32 v[100:101], v[100:101], v[108:109]\n" \
  "v_pk_mul_f32 v[102:103], v[102:103], v[110:111]\n" \
  "v_pk_mul_f32 v[104:105], v[104:105], v[100:101] neg_lo:[0,1] neg_hi:[0,1]\n" \
  "v_pk_mul_f32 v[106:107], v[106:107], v[102:103] neg_lo:[0,1] neg_hi:[0,1]\n" \
  "v_cvt_pk_bf16_f32 v100, v100, v101\n" \
  "v_cvt_pk_bf16_f32 v102, v102, v103\n" \
  "v_cvt_pk_bf16_f32 v104, v104, v105\n" \
  "v_cvt_pk_bf16_f32 v106, v106, v107\n" \
  "ds_write2st64_b32 v96, v100, v102 offset0:119 offset1:127\n" \
  "ds_write2st64_b32 v97, v104, v106 offset0:119 offset1:127\n" \
  "buffer_load_dwordx2 v[44:45], v18, s[24:27], s93 offen sc1\n" \
  "v_mov_b64 v[128:129], 0\n" \
  "v_mov_b64 v[130:131], 0\n" \
  "v_mov_b64 v[132:133], 0\n" \
  "v_mov_b64 v[134:135], 0\n" \
  "v_mov_b64 v[136:137], 0\n" \
  "v_mov_b64 v[138:139], 0\n" \
  "v_mov_b64 v[140:141], 0\n" \
  "v_mov_b64 v[142:143], 0\n" \
  "v_mov_b64 v[144:145], 0\n" \
  "v_mov_b64 v[146:147], 0\n" \
  "v_mov_b64 v[148:149], 0\n" \
  "v_mov_b64 v[150:151], 0\n" \
  "v_mov_b64 v[152:153], 0\n" \
  "v_mov_b64 v[154:155], 0\n" \
  "v_mov_b64 v[156:157], 0\n" \
  "v_mov_b64 v[158:159], 0\n" \
  "v_mov_b64 v[160:161], 0\n" \
  "v_mov_b64 v[162:163], 0\n" \
  "v_mov_b64 v[164:165], 0\n" \
  "v_mov_b64 v[166:167], 0\n" \
  "v_mov_b64 v[168:169], 0\n" \
  "v_mov_b64 v[170:171], 0\n" \
  "v_mov_b64 v[172:173], 0\n" \
  "v_mov_b64 v[174:175], 0\n" \
  "v_mov_b64 v[176:177], 0\n" \
  "v_mov_b64 v[178:179], 0\n" \
  "v_mov_b64 v[180:181], 0\n" \
  "v_mov_b64 v[182:183], 0\n" \
  "v_mov_b64 v[184:185], 0\n" \
  "v_mov_b64 v[186:187], 0\n" \
  "v_mov_b64 v[188:189], 0\n" \
  "v_mov_b64 v[190:191], 0\n" \
  "v_mov_b64 v[192:193], 0\n" \
  "v_mov_b64 v[194:195], 0\n" \
  "v_mov_b64 v[196:197], 0\n" \
  "v_mov_b64 v[198:199], 0\n" \
  "v_mov_b64 v[200:201], 0\n" \
  "v_mov_b64 v[202:203], 0\n" \
  "v_mov_b64 v[204:205], 0\n" \
  "v_mov_b64 v[206:207], 0\n" \
  "s_sleep 4\n" \
  "Ld1_%=:\n" \
  "buffer_load_dwordx2 v[46:47], v18, s[24:27], s93 offen sc1\n" \
  "s_waitcnt vmcnt(1)\n" \
  "v_cmp_eq_u32 s[90:91], s65, v45\n" \
  "v_cmp_eq_u32 vcc, s64, v44\n" \
  "s_mov_b32 s63, 606464\n" \
  "s_and_b64 vcc, vcc, s[90:91]\n" \
  "s_or_b64 s[88:89], s[88:89], vcc\n" \
  "s_cmp_eq_u64 s[88:89], -1\n" \
  "s_cbranch_scc1 Ld2_%=\n" \
  "v_cndmask_b32 v18, v18, v51, vcc\n" \
  "buffer_load_dwordx2 v[44:45], v18, s[24:27], s93 offen sc1\n" \
  "s_waitcnt vmcnt(1)\n" \
  "v_cmp_eq_u32 s[90:91], s65, v47\n" \
  "v_cmp_eq_u32 vcc, s64, v46\n" \
  "s_mov_b32 s63, 606464\n" \
  "s_and_b64 vcc, vcc, s[90:91]\n" \
  "s_or_b64 s[88:89], s[88:89], vcc\n" \
  "s_cmp_eq_u64 s[88:89], -1\n" \
  "s_cbranch_scc1 Ld2_%=\n" \
  "v_cndmask_b32 v18, v18, v51, vcc\n" \
  "s_add_u32 s92, s92, 1\n" \
  "s_cmp_lt_u32 s92, 4096\n" \
  "s_cbranch_scc1 Ld1_%=\n" \
  "s_mov_b64 s[80:81], 0\n" \
  "s_mov_b64 s[82:83], 0\n" \
  "s_mov_b64 s[84:85], 0\n" \
  "s_mov_b64 s[86:87], 0\n" \
  "Ld2_%=:\n" \
  "s_mov_b32 s93, 1917184\n" \
  "s_cmp_le_i32 s62, 192\n" \
  "s_cbranch_scc1 Lk528_%=\n" \
  "buffer_load_dword v63, v59, s[24:27], s63 offen sc1\n" \
  "v_lshlrev_b32 v59, 2, v59\n" \
  "buffer_load_dwordx4 v[228:231], v59, s[24:27], s93 offen sc1\n" \
  "Lk528_%=:\n" \
  "buffer_load_dword v60, v56, s[24:27], s63 offen sc1\n" \
  "buffer_load_dword v61, v57, s[24:27], s63 offen sc1\n" \
  "buffer_load_dword v62, v58, s[24:27], s63 offen sc1\n" \
  "buffer_load_dword v20, s[28:31], s66 offen lds\n" \
  "s_add_u32 s66, s66, 0x40000\n" \
  "v_lshlrev_b32 v56, 2, v56\n" \
  "v_lshlrev_b32 v57, 2, v57\n" \
  "v_lshlrev_b32 v58, 2, v58\n" \
  "s_lshl_b32 s75, s44, 8\n" \
  "s_add_u32 s75, s75, 106816\n" \
  "s_mov_b32 s52, s28\n" \
  "s_or_b32 s53, s29, 0x4000000\n" \
  "s_mov_b32 s54, s35\n" \
  "s_mov_b32 s55, 0x20000\n" \
  "s_lshl_b32 s63, s67, 10\n" \
  "s_add_u32 s56, s32, s63\n" \
  "s_addc_u32 s57, s33, 0\n" \
  "s_lshl_b32 s58, s68, 10\n" \
  "s_mov_b32 s59, 0x20000\n" \
  "s_lshl_b32 s67, s67, 24\n" \
  "s_mov_b32 s70, 32768\n" \
  "s_mul_i32 s63, s44, 3328\n" \
  "s_add_u32 s63, s63, 37328\n" \
  "v_add_u32 v21, s63, v13\n" \
  "v_and_b32 v15, 0xf0, v54\n" \
  "v_add_u32 v15, s63, v15\n" \
  "v_and_b32 v23, 0xf0, v13\n" \
  "v_add3_u32 v16, v23, s63, 8\n" \
  "s_mul_i32 s63, s44, 832\n" \
  "s_add_u32 s63, s63, 100096\n" \
  "v_mov_b32 v14, s63\n" \
  "v_and_b32 v50, 0x300, v13\n" \
  "v_and_or_b32 v19, v54, 60, v50\n" \
  "v_add_u32 v19, 108864, v19\n" \
  "v_lshl_add_u32 v23, v50, 2, v23\n" \
  "s_lshl_b32 s63, s73, 10\n" \
  "s_lshl_b32 s61, s45, 5\n" \
  "s_add_u32 s63, s63, s61\n" \
  "v_add_u32 v23, s63, v23\n" \
  "v_bfe_u32 v50, v86, 2, 2\n" \
  "v_mul_u32_u24 v22, 1056, v50\n" \
  "v_and_b32 v50, 48, v13\n" \
  "v_lshl_add_u32 v22, v50, 2, v22\n" \
  "v_and_b32 v50, 48, v86\n" \
  "s_mul_i32 s63, s73, 1056\n" \
  "s_add_u32 s63, s63, 108864\n" \
  "v_add3_u32 v22, v22, v50, s63\n" \
  "v_mul_u32_u24 v50, 26, v86\n" \
  "v_lshrrev_b32 v50, 8, v50\n" \
  "v_mad_i32_i24 v50, v50, -10, v86\n" \
  "v_lshlrev_b32 v42, 19, v50\n" \
  "s_mul_i32 s63, s44, 11\n" \
  "v_mad_u32_u24 v50, v86, 37, s63\n" \
  "v_and_b32 v50, 63, v50\n" \
  "v_mul_u32_u24 v43, 0xffff, v50\n" \
  "v_add_u32 v43, 64, v43\n" \
  "s_cmp_lt_u32 s92, 4096\n" \
  "s_cbranch_scc1 Lq2_%=\n" \
  "v_mov_b32 v53, 3\n" \
  "ds_or_b32 v94, v53\n" \
  "Lq2_%=:\n" \
  "v_mov_b32 v50, 2\n" \
  "s_waitcnt vmcnt(1)\n" \
  "v_subrev_u32 v60, s67, v60\n" \
  "s_mov_b64 exec, s[80:81]\n" \
  "v_lshlrev_b32_sdwa v64, v50, v60 dst_sel:DWORD dst_unused:UNUSED_PAD src0_sel:DWORD src1_sel:BYTE_3\n" \
  "ds_add_u32 v64, v92 offset:35584\n" \
  "v_subrev_u32 v61, s67, v61\n" \
  "s_mov_b64 exec, s[82:83]\n" \
  "v_lshlrev_b32_sdwa v65, v50, v61 dst_sel:DWORD dst_unused:UNUSED_PAD src0_sel:DWORD src1_sel:BYTE_3\n" \
  "ds_add_u32 v65, v92 offset:35584\n" \
  "v_subrev_u32 v62, s67, v62\n" \
  "s_mov_b64 exec, s[84:85]\n" \
  "v_lshlrev_b32_sdwa v66, v50, v62 dst_sel:DWORD dst_unused:UNUSED_PAD src0_sel:DWORD src1_sel:BYTE_3\n" \
  "ds_add_u32 v66, v92 offset:35584\n" \
  "s_cmp_le_i32 s62, 192\n" \
  "s_cbranch_scc1 Lk609_%=\n" \
  "v_subrev_u32 v63, s67, v63\n" \
  "s_mov_b64 exec, s[86:87]\n" \
  "v_lshlrev_b32_sdwa v67, v50, v63 dst_sel:DWORD dst_unused:UNUSED_PAD src0_sel:DWORD src1_sel:BYTE_3\n" \
  "ds_add_u32 v67, v92 offset:35584\n" \
  "Lk609_%=:\n" \
  "s_mov_b64 exec, -1\n" \
  "buffer_load_dwordx4 v[216:219], v56, s[24:27], s93 offen sc1\n" \
  "buffer_load_dwordx4 v[220:223], v57, s[24:27], s93 offen sc1\n" \
  "buffer_load_dwordx4 v[224:227], v58, s[24:27], s93 offen sc1\n" \
  "buffer_load_dword v20, s[28:31], s66 offen lds\n" \
  "s_add_u32 s66, s66, 0x40000\n" \
  "buffer_load_dword v20, s[28:31], s66 offen lds\n" \
  "s_add_u32 s66, s66, 0x40000\n" \
  "s_waitcnt lgkmcnt(0)\n" \
  "s_barrier\n" \
  "ds_read_b32 v85, v54 offset:35584\n" \
  "ds_read_b32 v86, v94 offset:4\n" \
  "ds_bpermute_b32 v76, v64, v42\n" \
  "ds_bpermute_b32 v77, v65, v42\n" \
  "ds_bpermute_b32 v78, v66, v42\n" \
  "ds_bpermute_b32 v79, v67, v42\n" \
  "s_waitcnt lgkmcnt(4)\n" \
  "v_add_u32_dpp v87, v85, v85 row_shr:1 row_mask:0xf bank_mask:0xf bound_ctrl:1\n" \
  "v_cvt_f32_u32 v86, v86\n" \
  "v_rcp_f32 v86, v86\n" \
  "v_add_u32_dpp v87, v87, v87 row_shr:2 row_mask:0xf bank_mask:0xf bound_ctrl:1\n" \
  "v_mul_hi_u32_u24 v68, s37, v60\n" \
  "v_subrev_u32 v68, s71, v68\n" \
  "v_add_u32_dpp v87, v87, v87 row_shr:4 row_mask:0xf bank_mask:0xf bound_ctrl:1\n" \
  "v_and_b32 v68, 15, v68\n" \
  "v_lshl_add_u32 v72, v68, 1, s75\n" \
  "v_add_u32_dpp v87, v87, v87 row_shr:8 row_mask:0xf bank_mask:0xf bound_ctrl:1\n" \
  "v_lshl_add_u32 v68, v68, 3, s73\n" \
  "v_mul_hi_u32_u24 v69, s37, v61\n" \
  "v_add_u32_dpp v87, v87, v87 row_bcast:15 row_mask:0xa bank_mask:0xf\n" \
  "v_subrev_u32 v69, s71, v69\n" \
  "v_and_b32 v69, 15, v69\n" \
  "v_add_u32_dpp v87, v87, v87 row_bcast:31 row_mask:0xc bank_mask:0xf\n" \
  "v_sub_u32 v85, v87, v85\n" \
  "v_cvt_f32_u32 v87, v87\n" \
  "v_cvt_f32_u32 v85, v85\n" \
  "v_mul_f32 v87, v87, v86\n" \
  "v_mul_f32 v85, v85, v86\n" \
  "v_cvt_pknorm_u16_f32 v84, v85, v87\n" \
  "ds_bpermute_b32 v80, v64, v84\n" \
  "ds_bpermute_b32 v81, v65, v84\n" \
  "ds_bpermute_b32 v82, v66, v84\n" \
  "ds_bpermute_b32 v83, v67, v84\n" \
  "v_lshl_add_u32 v73, v69, 1, s75\n" \
  "v_lshl_add_u32 v69, v69, 3, s73\n" \
  "v_mul_hi_u32_u24 v70, s37, v62\n" \
  "v_subrev_u32 v70, s71, v70\n" \
  "v_and_b32 v70, 15, v70\n" \
  "v_lshl_add_u32 v74, v70, 1, s75\n" \
  "v_lshl_add_u32 v70, v70, 3, s73\n" \
  "v_mul_hi_u32_u24 v71, s37, v63\n" \
  "v_subrev_u32 v71, s71, v71\n" \
  "v_and_b32 v71, 15, v71\n" \
  "v_lshl_add_u32 v75, v71, 1, s75\n" \
  "v_lshl_add_u32 v71, v71, 3, s73\n" \
  "s_mov_b32 s63, 0xffff\n" \
  "s_waitcnt lgkmcnt(4)\n" \
  "v_and_or_b32 v76, v60, s63, v76\n" \
  "v_and_or_b32 v77, v61, s63, v77\n" \
  "v_and_or_b32 v78, v62, s63, v78\n" \
  "v_and_or_b32 v79, v63, s63, v79\n" \
  "s_waitcnt lgkmcnt(3)\n" \
  "v_dot2_u32_u16 v80, v80, v43, 0\n" \
  "s_waitcnt lgkmcnt(2)\n" \
  "v_dot2_u32_u16 v81, v81, v43, 0\n" \
  "s_waitcnt lgkmcnt(1)\n" \
  "v_dot2_u32_u16 v82, v82, v43, 0\n" \
  "s_waitcnt lgkmcnt(0)\n" \
  "v_dot2_u32_u16 v83, v83, v43, 0\n" \
  "v_lshrrev_b32 v60, 19, v80\n" \
  "s_mov_b64 exec, s[80:81]\n" \
  "v_lshl_add_u32 v68, v60, 7, v68\n" \
  "ds_add_rtn_u32 v80, v68, v93 offset:34560\n" \
  "v_lshrrev_b32 v61, 19, v81\n" \
  "s_mov_b64 exec, s[82:83]\n" \
  "v_lshl_add_u32 v69, v61, 7, v69\n" \
  "ds_add_rtn_u32 v81, v69, v93 offset:34560\n" \
  "v_lshrrev_b32 v62, 19, v82\n" \
  "s_mov_b64 exec, s[84:85]\n" \
  "v_lshl_add_u32 v70, v62, 7, v70\n" \
  "ds_add_rtn_u32 v82, v70, v93 offset:34560\n" \
  "s_cmp_le_i32 s62, 192\n" \
  "s_cbranch_scc1 Lk697_%=\n" \
  "v_lshrrev_b32 v63, 19, v83\n" \
  "s_mov_b64 exec, s[86:87]\n" \
  "v_lshl_add_u32 v71, v63, 7, v71\n" \
  "ds_add_rtn_u32 v83, v71, v93 offset:34560\n" \
  "Lk697_%=:\n" \
  "s_mov_b64 exec, -1\n" \
  "v_lshl_add_u32 v72, v60, 5, v72\n" \
  "v_lshl_add_u32 v73, v61, 5, v73\n" \
  "v_lshl_add_u32 v74, v62, 5, v74\n" \
  "v_lshl_add_u32 v75, v63, 5, v75\n" \
  "v_lshrrev_b32 v50, 1, v13\n" \
  "v_lshrrev_b32 v51, 3, v13\n" \
  "v_add_u32 v51, s75, v51\n" \
  "v_lshrrev_b32 v52, 8, v13\n" \
  "v_mul_u32_u24 v52, 0xd000d0, v52\n" \
  "v_add_u32 v52, 0x3400000, v52\n" \
  "s_mov_b32 s63, 0x01010101\n" \
  "s_waitcnt lgkmcnt(0)\n" \
  "s_barrier\n" \
  "ds_read_b64 v[88:89], v50 offset:34560\n" \
  "ds_read_b64 v[90:91], v50 offset:35072\n" \
  "buffer_load_dword v20, s[28:31], s66 offen lds\n" \
  "s_add_u32 s66, s66, 0x40000\n" \
  "v_bfe_u32 v80, v80, s45, 8\n" \
  "v_bfe_u32 v81, v81, s45, 8\n" \
  "v_bfe_u32 v82, v82, s45, 8\n" \
  "v_bfe_u32 v83, v83, s45, 8\n" \
  "s_lshl_b32 s72, s44, 4\n" \
  "s_or_b32 s72, s72, 15\n" \
  "s_lshl_b32 s73, s73, 2\n" \
  "s_waitcnt lgkmcnt(0)\n" \
  "v_dot4_u32_u8 v44, v88, s63, 0\n" \
  "v_dot4_u32_u8 v45, v90, s63, 0\n" \
  "v_dot4_u32_u8 v44, v89, s63, v44\n" \
  "v_dot4_u32_u8 v45, v91, s63, v45\n" \
  "v_dot4_u32_u8 v46, v88, s46, 0\n" \
  "v_dot4_u32_u8 v47, v90, s46, 0\n" \
  "v_dot4_u32_u8 v46, v89, s47, v46\n" \
  "v_lshl_or_b32 v44, v45, 16, v44\n" \
  "v_dot4_u32_u8 v47, v91, s47, v47\n" \
  "s_nop 0\n" \
  "v_add_u32_dpp v87, v44, v44 row_shr:1 row_mask:0xf bank_mask:0xf bound_ctrl:1\n" \
  "s_nop 0\n" \
  "v_lshl_or_b32 v46, v47, 16, v46\n" \
  "v_add_u32_dpp v87, v87, v87 row_shr:2 row_mask:0xf bank_mask:0xf bound_ctrl:1\n" \
  "v_sub_u32 v52, v52, v46\n" \
  "s_nop 0\n" \
  "v_add_u32_dpp v87, v87, v87 row_shr:4 row_mask:0xf bank_mask:0xf bound_ctrl:1\n" \
  "s_nop 1\n" \
  "v_add_u32_dpp v87, v87, v87 row_shr:8 row_mask:0xf bank_mask:0xf bound_ctrl:1\n" \
  "v_add_u32 v52, v52, v87\n" \
  "ds_write_b16 v51, v52\n" \
  "ds_write_b16_d16_hi v51, v52 offset:128\n" \
  "ds_read_u16 v72, v72\n" \
  "ds_read_u16 v73, v73\n" \
  "ds_read_u16 v74, v74\n" \
  "ds_read_u16 v75, v75\n" \
  "v_readlane_b32 s60, v87, s72\n" \
  "s_movk_i32 s63, 200\n" \
  "v_cmp_lt_u32_sdwa s[90:91], s63, v87 src0_sel:DWORD src1_sel:WORD_0\n" \
  "v_cmp_lt_u32_sdwa vcc, s63, v87 src0_sel:DWORD src1_sel:WORD_1\n" \
  "s_lshr_b32 s72, s60, s73\n" \
  "s_and_b32 s72, s72, 0xffff\n" \
  "s_add_u32 s60, s72, 3\n" \
  "s_lshr_b32 s60, s60, 2\n" \
  "s_max_u32 s60, s60, 1\n" \
  "s_lshl2_add_u32 s73, s60, 8\n" \
  "s_sub_u32 s73, s73, s72\n" \
  "s_bfm_b64 s[88:89], s73, 0\n" \
  "s_or_b64 vcc, vcc, s[90:91]\n" \
  "s_cbranch_scc0 Lo1_%=\n" \
  "s_mov_b64 s[80:81], 0\n" \
  "s_mov_b64 s[82:83], 0\n" \
  "s_mov_b64 s[84:85], 0\n" \
  "s_mov_b64 s[86:87], 0\n" \
  "s_mov_b64 s[88:89], 0xfff\n" \
  "s_mov_b32 s72, 0\n" \
  "s_mov_b32 s60, 1\n" \
  "ds_or_b32 v94, v92\n" \
  "Lo1_%=:\n" \
  "v_lshrrev_b32 v50, 2, v13\n" \
  "s_lshl_b32 s72, s72, 2\n" \
  "v_add3_u32 v50, v50, s72, v14\n" \
  "v_mov_b32 v51, 0x50ffff\n" \
  "s_mov_b32 s63, 100096\n" \
  "s_mov_b64 exec, s[88:89]\n" \
  "ds_write_b32 v50, v51\n" \
  "s_waitcnt lgkmcnt(1)\n" \
  "s_mov_b64 exec, s[80:81]\n" \
  "v_add_u32 v80, v80, v72\n" \
  "v_lshl_add_u32 v72, v80, 2, s63\n" \
  "ds_write_b32 v72, v76\n" \
  "s_mov_b64 exec, s[82:83]\n" \
  "v_add_u32 v81, v81, v73\n" \
  "v_lshl_add_u32 v73, v81, 2, s63\n" \
  "ds_write_b32 v73, v77\n" \
  "s_mov_b64 exec, s[84:85]\n" \
  "v_add_u32 v82, v82, v74\n" \
  "v_lshl_add_u32 v74, v82, 2, s63\n" \
  "ds_write_b32 v74, v78\n" \
  "s_cmp_le_i32 s62, 192\n" \
  "s_cbranch_scc1 Lk799_%=\n" \
  "s_mov_b64 exec, s[86:87]\n" \
  "v_add_u32 v83, v83, v75\n" \
  "v_lshl_add_u32 v75, v83, 2, s63\n" \
  "ds_write_b32 v75, v79\n" \
  "Lk799_%=:\n" \
  "s_mov_b64 exec, -1\n" \
  "s_sub_u32 s60, s60, 1\n" \
  "s_lshl_b32 s63, s60, 4\n" \
  "v_sub_u32 v50, v14, v13\n" \
  "v_add_u32 v50, s63, v50\n" \
  "s_waitcnt lgkmcnt(0)\n" \
  "s_barrier\n" \
  "ds_read_b128 v[56:59], v14\n" \
  "ds_read_b128 v[60:63], v14 offset:16\n" \
  "ds_read_b128 v[64:67], v50\n" \
  "v_lshlrev_b32 v232, 4, v80\n" \
  "v_lshlrev_b32 v233, 4, v81\n" \
  "v_lshlrev_b32 v234, 4, v82\n" \
  "v_lshlrev_b32 v235, 4, v83\n" \
  "s_waitcnt vmcnt(3)\n" \
  "s_mov_b64 exec, s[80:81]\n" \
  "ds_write_b128 v232, v[216:219] offset:37328\n" \
  "s_mov_b64 exec, s[82:83]\n" \
  "ds_write_b128 v233, v[220:223] offset:37328\n" \
  "s_mov_b64 exec, s[84:85]\n" \
  "ds_write_b128 v234, v[224:227] offset:37328\n" \
  "s_cmp_le_i32 s62, 192\n" \
  "s_cbranch_scc1 Lk827_%=\n" \
  "s_mov_b64 exec, s[86:87]\n" \
  "ds_write_b128 v235, v[228:231] offset:37328\n" \
  "Lk827_%=:\n" \
  "s_mov_b64 exec, -1\n" \
  "s_waitcnt lgkmcnt(4)\n" \
  "v_and_b32 v12, 0xffff, v56\n" \
  "buffer_load_dwordx4 v[96:99], v[12:13], s[52:55], 0 idxen offen\n" \
  "v_and_b32 v12, 0xffff, v57\n" \
  "buffer_load_dwordx4 v[100:103], v[12:13], s[52:55], 0 idxen offen\n" \
  "v_and_b32 v12, 0xffff, v58\n" \
  "buffer_load_dwordx4 v[104:107], v[12:13], s[52:55], 0 idxen offen\n" \
  "v_and_b32 v12, 0xffff, v59\n" \
  "buffer_load_dwordx4 v[108:111], v[12:13], s[52:55], 0 idxen offen\n" \
  "s_mov_b32 s63, 0x003f0001\n" \
  "v_dot2_u32_u16 v52, v84, s63, 0\n" \
  "v_bfe_u32 v51, v84, 13, 3\n" \
  "s_bfm_b64 s[88:89], s68, 0\n" \
  "v_mov_b32 v18, v94\n" \
  "v_cmp_ge_u32 s[90:91], s44, v51\n" \
  "v_lshrrev_b32 v52, 19, v52\n" \
  "s_and_b64 s[88:89], s[88:89], s[90:91]\n" \
  "v_cmp_le_u32 s[90:91], s44, v52\n" \
  "s_and_b64 s[88:89], s[88:89], s[90:91]\n" \
  "s_ff1_i32_b64 s72, s[88:89]\n" \
  "s_flbit_i32_b64 s74, s[88:89]\n" \
  "s_xor_b32 s74, s74, 63\n" \
  "s_mov_b32 s63, 0x0c0c0602\n" \
  "s_waitcnt lgkmcnt(0)\n" \
  "s_barrier\n" \
  "ds_read_b64 v[92:93], v16\n" \
  "ds_read_b64 v[94:95], v15\n" \
  "v_and_b32 v12, 0xffff, v60\n" \
  "buffer_load_dwordx4 v[112:115], v[12:13], s[52:55], 0 idxen offen\n" \
  "v_and_b32 v12, 0xffff, v61\n" \
  "buffer_load_dwordx4 v[116:119], v[12:13], s[52:55], 0 idxen offen\n" \
  "v_and_b32 v12, 0xffff, v62\n" \
  "buffer_load_dwordx4 v[120:123], v[12:13], s[52:55], 0 idxen offen\n" \
  "v_and_b32 v12, 0xffff, v63\n" \
  "buffer_load_dwordx4 v[124:127], v[12:13], s[52:55], 0 idxen offen\n" \
  "ds_read_u16 v12, v14 offset:32\n" \
  "v_perm_b32 v64, v65, v64, s63\n" \
  "v_perm_b32 v66, v67, v66, s63\n" \
  "v_readlane_b32 s93, v51, s72\n" \
  "v_readlane_b32 s75, v52, s74\n" \
  "v_lshl_or_b32 v17, v66, 16, v64\n" \
  "v_readlane_b32 s63, v51, s74\n" \
  "v_readlane_b32 s73, v42, s72\n" \
  "v_readlane_b32 s61, v17, s60\n" \
  "s_sub_u32 s74, s74, s72\n" \
  "s_sub_u32 s75, s75, s44\n" \
  "s_cmp_eq_u32 s63, s44\n" \
  "s_cselect_b32 s75, s75, 0\n" \
  "s_lshr_b32 s73, s73, 16\n" \
  "s_mul_i32 s72, s72, 1056\n" \
  "s_or_b32 s63, %[blk], s44\n" \
  "s_cbranch_scc0 Lep0_%=\n" \
  "Lgo%=:\n" \
  "s_waitcnt lgkmcnt(0)\n" \
  "v_mfma_f32_4x4x4_16b_bf16 v[56:59], v[92:93], v[32:33], 0 cbsz:4\n" \
  "v_mfma_f32_4x4x4_16b_bf16 v[60:63], v[92:93], v[36:37], 0 cbsz:4\n" \
  "v_mfma_f32_4x4x4_16b_bf16 v[64:67], v[92:93], v[32:33], 0 cbsz:4 abid:1\n" \
  "v_mfma_f32_4x4x4_16b_bf16 v[68:71], v[92:93], v[36:37], 0 cbsz:4 abid:1\n" \
  "v_mfma_f32_4x4x4_16b_bf16 v[84:87], v[94:95], v[24:25], 0\n" \
  "v_mfma_f32_4x4x4_16b_bf16 v[88:91], v[94:95], v[26:27], 0\n" \
  "v_mfma_f32_4x4x4_16b_bf16 v[48:51], v[94:95], v[28:29], 0\n" \
  "v_mfma_f32_4x4x4_16b_bf16 v[52:55], v[94:95], v[30:31], 0\n" \
  "s_nop 1\n" \
  "v_cvt_pk_bf16_f32 v57, v56, v57\n" \
  "v_lshlrev_b32_sdwa v56, 1, v57 dst_sel:DWORD dst_unused:UNUSED_PAD src0_sel:DWORD src1_sel:WORD_0\n" \
  "v_lshlrev_b32_sdwa v57, 1, v57 dst_sel:DWORD dst_unused:UNUSED_PAD src0_sel:DWORD src1_sel:WORD_1\n" \
  "ds_read_u16_d16_hi v56, v56\n" \
  "ds_read_u16_d16_hi v57, v57\n" \
  "v_cvt_pk_bf16_f32 v59, v58, v59\n" \
  "v_lshlrev_b32_sdwa v58, 1, v59 dst_sel:DWORD dst_unused:UNUSED_PAD src0_sel:DWORD src1_sel:WORD_0\n" \
  "v_lshlrev_b32_sdwa v59, 1, v59 dst_sel:DWORD dst_unused:UNUSED_PAD src0_sel:DWORD src1_sel:WORD_1\n" \
  "ds_read_u16_d16_hi v58, v58\n" \
  "ds_read_u16_d16_hi v59, v59\n" \
  "v_cvt_pk_bf16_f32 v61, v60, v61\n" \
  "v_lshlrev_b32_sdwa v60, 1, v61 dst_sel:DWORD dst_unused:UNUSED_PAD src0_sel:DWORD src1_sel:WORD_0\n" \
  "v_lshlrev_b32_sdwa v61, 1, v61 dst_sel:DWORD dst_unused:UNUSED_PAD src0_sel:DWORD src1_sel:WORD_1\n" \
  "ds_read_u16_d16_hi v60, v60\n" \
  "ds_read_u16_d16_hi v61, v61\n" \
  "v_cvt_pk_bf16_f32 v63, v62, v63\n" \
  "v_lshlrev_b32_sdwa v62, 1, v63 dst_sel:DWORD dst_unused:UNUSED_PAD src0_sel:DWORD src1_sel:WORD_0\n" \
  "v_lshlrev_b32_sdwa v63, 1, v63 dst_sel:DWORD dst_unused:UNUSED_PAD src0_sel:DWORD src1_sel:WORD_1\n" \
  "ds_read_u16_d16_hi v62, v62\n" \
  "ds_read_u16_d16_hi v63, v63\n" \
  "v_cvt_pk_bf16_f32 v76, v84, v88\n" \
  "v_cvt_pk_bf16_f32 v77, v48, v52\n" \
  "s_mov_b32 s63, 0x7fff7fff\n" \
  "v_and_b32 v41, s63, v218\n" \
  "v_and_b32 v42, s63, v219\n" \
  "v_pk_max_u16 v40, v41, v42\n" \
  "v_and_b32 v41, s63, v222\n" \
  "v_and_b32 v42, s63, v223\n" \
  "v_pk_max_u16 v41, v41, v40\n" \
  "v_pk_max_u16 v40, v41, v42\n" \
  "v_and_b32 v41, s63, v226\n" \
  "v_and_b32 v42, s63, v227\n" \
  "v_pk_max_u16 v41, v41, v40\n" \
  "v_pk_max_u16 v40, v41, v42\n" \
  "s_cmp_le_i32 s62, 192\n" \
  "s_cbranch_scc1 Lk930_%=\n" \
  "v_and_b32 v41, s63, v230\n" \
  "v_and_b32 v42, s63, v231\n" \
  "v_pk_max_u16 v41, v41, v40\n" \
  "v_pk_max_u16 v40, v41, v42\n" \
  "Lk930_%=:\n" \
  "v_max_u16_sdwa v40, v40, v40 dst_sel:DWORD dst_unused:UNUSED_PAD src0_sel:WORD_0 src1_sel:WORD_1\n" \
  "s_nop 0\n" \
  "v_cmp_lt_i32 vcc, s69, v40\n" \
  "s_cmp_eq_u64 vcc, 0\n" \
  "s_cselect_b32 s74, s74, -1\n" \
  "s_waitcnt vmcnt(7) lgkmcnt(0)\n" \
  "v_mfma_f32_4x4x4_16b_bf16 v[48:51], v[76:77], v[96:97], 0 cbsz:4\n" \
  "v_mfma_f32_4x4x4_16b_bf16 v[52:55], v[76:77], v[98:99], 0 cbsz:4\n" \
  "s_nop 4\n" \
  "Lbody%=:\n" \
  "s_set_gpr_idx_on s61, gpr_idx(SRC2,DST)\n" \
  "v_pk_fma_f32 v[128:129], v[56:57], v[48:49], v[128:129] op_sel:[0,0,0] op_sel_hi:[0,1,1]\n" \
  "v_pk_fma_f32 v[130:131], v[56:57], v[50:51], v[130:131] op_sel:[0,0,0] op_sel_hi:[0,1,1]\n" \
  "v_pk_fma_f32 v[132:133], v[60:61], v[52:53], v[132:133] op_sel:[0,0,0] op_sel_hi:[0,1,1]\n" \
  "v_pk_fma_f32 v[134:135], v[60:61], v[54:55], v[134:135] op_sel:[0,0,0] op_sel_hi:[0,1,1]\n" \
  "s_set_gpr_idx_off\n" \
  "s_waitcnt vmcnt(6) lgkmcnt(2)\n" \
  "buffer_load_dwordx4 v[96:99], v[12:13], s[52:55], 0 idxen offen\n" \
  "ds_read_u16 v12, v14 offset:36\n" \
  "s_lshr_b32 s61, s61, 8\n" \
  "v_mfma_f32_4x4x4_16b_bf16 v[48:51], v[76:77], v[100:101], 0 cbsz:4 abid:1\n" \
  "v_mfma_f32_4x4x4_16b_bf16 v[52:55], v[76:77], v[102:103], 0 cbsz:4 abid:1\n" \
  "v_cvt_pk_bf16_f32 v65, v64, v65\n" \
  "v_lshlrev_b32_sdwa v64, 1, v65 dst_sel:DWORD dst_unused:UNUSED_PAD src0_sel:DWORD src1_sel:WORD_0\n" \
  "v_lshlrev_b32_sdwa v65, 1, v65 dst_sel:DWORD dst_unused:UNUSED_PAD src0_sel:DWORD src1_sel:WORD_1\n" \
  "ds_read_u16_d16_hi v64, v64\n" \
  "ds_read_u16_d16_hi v65, v65\n" \
  "s_set_gpr_idx_on s61, gpr_idx(SRC2,DST)\n" \
  "v_pk_fma_f32 v[128:129], v[56:57], v[48:49], v[128:129] op_sel:[1,0,0] op_sel_hi:[1,1,1]\n" \
  "v_pk_fma_f32 v[130:131], v[56:57], v[50:51], v[130:131] op_sel:[1,0,0] op_sel_hi:[1,1,1]\n" \
  "v_pk_fma_f32 v[132:133], v[60:61], v[52:53], v[132:133] op_sel:[1,0,0] op_sel_hi:[1,1,1]\n" \
  "v_pk_fma_f32 v[134:135], v[60:61], v[54:55], v[134:135] op_sel:[1,0,0] op_sel_hi:[1,1,1]\n" \
  "s_set_gpr_idx_off\n" \
  "s_waitcnt vmcnt(6) lgkmcnt(2)\n" \
  "buffer_load_dwordx4 v[100:103], v[12:13], s[52:55], 0 idxen offen\n" \
  "ds_read_u16 v12, v14 offset:40\n" \
  "s_lshr_b32 s61, s61, 8\n" \
  "v_mfma_f32_4x4x4_16b_bf16 v[48:51], v[76:77], v[104:105], 0 cbsz:4 abid:2\n" \
  "v_mfma_f32_4x4x4_16b_bf16 v[52:55], v[76:77], v[106:107], 0 cbsz:4 abid:2\n" \
  "v_cvt_pk_bf16_f32 v69, v68, v69\n" \
  "v_lshlrev_b32_sdwa v68, 1, v69 dst_sel:DWORD dst_unused:UNUSED_PAD src0_sel:DWORD src1_sel:WORD_0\n" \
  "v_lshlrev_b32_sdwa v69, 1, v69 dst_sel:DWORD dst_unused:UNUSED_PAD src0_sel:DWORD src1_sel:WORD_1\n" \
  "ds_read_u16_d16_hi v68, v68\n" \
  "ds_read_u16_d16_hi v69, v69\n" \
  "s_set_gpr_idx_on s61, gpr_idx(SRC2,DST)\n" \
  "v_pk_fma_f32 v[128:129], v[58:59], v[48:49], v[128:129] op_sel:[0,0,0] op_sel_hi:[0,1,1]\n" \
  "v_pk_fma_f32 v[130:131], v[58:59], v[50:51], v[130:131] op_sel:[0,0,0] op_sel_hi:[0,1,1]\n" \
  "v_pk_fma_f32 v[132:133], v[62:63], v[52:53], v[132:133] op_sel:[0,0,0] op_sel_hi:[0,1,1]\n" \
  "v_pk_fma_f32 v[134:135], v[62:63], v[54:55], v[134:135] op_sel:[0,0,0] op_sel_hi:[0,1,1]\n" \
  "s_set_gpr_idx_off\n" \
  "s_waitcnt vmcnt(6) lgkmcnt(2)\n" \
  "buffer_load_dwordx4 v[104:107], v[12:13], s[52:55], 0 idxen offen\n" \
  "ds_read_u16 v12, v14 offset:44\n" \
  "s_lshr_b32 s61, s61, 8\n" \
  "v_mfma_f32_4x4x4_16b_bf16 v[48:51], v[76:77], v[108:109], 0 cbsz:4 abid:3\n" \
  "v_mfma_f32_4x4x4_16b_bf16 v[52:55], v[76:77], v[110:111], 0 cbsz:4 abid:3\n" \
  "v_cvt_pk_bf16_f32 v67, v66, v67\n" \
  "v_lshlrev_b32_sdwa v66, 1, v67 dst_sel:DWORD dst_unused:UNUSED_PAD src0_sel:DWORD src1_sel:WORD_0\n" \
  "v_lshlrev_b32_sdwa v67, 1, v67 dst_sel:DWORD dst_unused:UNUSED_PAD src0_sel:DWORD src1_sel:WORD_1\n" \
  "ds_read_u16_d16_hi v66, v66\n" \
  "ds_read_u16_d16_hi v67, v67\n" \
  "s_set_gpr_idx_on s61, gpr_idx(SRC2,DST)\n" \
  "v_pk_fma_f32 v[128:129], v[58:59], v[48:49], v[128:129] op_sel:[1,0,0] op_sel_hi:[1,1,1]\n" \
  "v_pk_fma_f32 v[130:131], v[58:59], v[50:51], v[130:131] op_sel:[1,0,0] op_sel_hi:[1,1,1]\n" \
  "v_pk_fma_f32 v[132:133], v[62:63], v[52:53], v[132:133] op_sel:[1,0,0] op_sel_hi:[1,1,1]\n" \
  "v_pk_fma_f32 v[134:135], v[62:63], v[54:55], v[134:135] op_sel:[1,0,0] op_sel_hi:[1,1,1]\n" \
  "s_set_gpr_idx_off\n" \
  "s_waitcnt vmcnt(6) lgkmcnt(2)\n" \
  "buffer_load_dwordx4 v[108:111], v[12:13], s[52:55], 0 idxen offen\n" \
  "ds_read_u16 v12, v14 offset:48\n" \
  "s_sub_u32 s60, s60, 1\n" \
  "s_cbranch_scc1 Lend%=\n" \
  "v_readlane_b32 s61, v17, s60\n" \
  "s_setprio 2\n" \
  "v_mfma_f32_4x4x4_16b_bf16 v[56:59], v[92:93], v[32:33], 0 cbsz:4 abid:2\n" \
  "v_mfma_f32_4x4x4_16b_bf16 v[60:63], v[92:93], v[36:37], 0 cbsz:4 abid:2\n" \
  "v_mfma_f32_4x4x4_16b_bf16 v[48:51], v[76:77], v[112:113], 0 cbsz:4 abid:4\n" \
  "v_mfma_f32_4x4x4_16b_bf16 v[52:55], v[76:77], v[114:115], 0 cbsz:4 abid:4\n" \
  "v_cvt_pk_bf16_f32 v71, v70, v71\n" \
  "v_lshlrev_b32_sdwa v70, 1, v71 dst_sel:DWORD dst_unused:UNUSED_PAD src0_sel:DWORD src1_sel:WORD_0\n" \
  "v_lshlrev_b32_sdwa v71, 1, v71 dst_sel:DWORD dst_unused:UNUSED_PAD src0_sel:DWORD src1_sel:WORD_1\n" \
  "ds_read_u16_d16_hi v70, v70\n" \
  "ds_read_u16_d16_hi v71, v71\n" \
  "v_add_u32 v16, 0x100, v16\n" \
  "s_set_gpr_idx_on s61, gpr_idx(SRC2,DST)\n" \
  "v_pk_fma_f32 v[128:129], v[64:65], v[48:49], v[128:129] op_sel:[0,0,0] op_sel_hi:[0,1,1]\n" \
  "v_pk_fma_f32 v[130:131], v[64:65], v[50:51], v[130:131] op_sel:[0,0,0] op_sel_hi:[0,1,1]\n" \
  "v_pk_fma_f32 v[132:133], v[68:69], v[52:53], v[132:133] op_sel:[0,0,0] op_sel_hi:[0,1,1]\n" \
  "v_pk_fma_f32 v[134:135], v[68:69], v[54:55], v[134:135] op_sel:[0,0,0] op_sel_hi:[0,1,1]\n" \
  "s_set_gpr_idx_off\n" \
  "s_waitcnt vmcnt(6) lgkmcnt(2)\n" \
  "buffer_load_dwordx4 v[112:115], v[12:13], s[52:55], 0 idxen offen\n" \
  "ds_read_u16 v12, v14 offset:52\n" \
  "ds_read_b64 v[94:95], v15 offset:256\n" \
  "s_lshr_b32 s61, s61, 8\n" \
  "v_mfma_f32_4x4x4_16b_bf16 v[48:51], v[76:77], v[116:117], 0 cbsz:4 abid:5\n" \
  "v_mfma_f32_4x4x4_16b_bf16 v[52:55], v[76:77], v[118:119], 0 cbsz:4 abid:5\n" \
  "v_cvt_pk_bf16_f32 v57, v56, v57\n" \
  "v_lshlrev_b32_sdwa v56, 1, v57 dst_sel:DWORD dst_unused:UNUSED_PAD src0_sel:DWORD src1_sel:WORD_0\n" \
  "v_lshlrev_b32_sdwa v57, 1, v57 dst_sel:DWORD dst_unused:UNUSED_PAD src0_sel:DWORD src1_sel:WORD_1\n" \
  "ds_read_u16_d16_hi v56, v56\n" \
  "ds_read_u16_d16_hi v57, v57\n" \
  "s_set_gpr_idx_on s61, gpr_idx(SRC2,DST)\n" \
  "v_pk_fma_f32 v[128:129], v[64:65], v[48:49], v[128:129] op_sel:[1,0,0] op_sel_hi:[1,1,1]\n" \
  "v_pk_fma_f32 v[130:131], v[64:65], v[50:51], v[130:131] op_sel:[1,0,0] op_sel_hi:[1,1,1]\n" \
  "v_pk_fma_f32 v[132:133], v[68:69], v[52:53], v[132:133] op_sel:[1,0,0] op_sel_hi:[1,1,1]\n" \
  "v_pk_fma_f32 v[134:135], v[68:69], v[54:55], v[134:135] op_sel:[1,0,0] op_sel_hi:[1,1,1]\n" \
  "s_set_gpr_idx_off\n" \
  "s_waitcnt vmcnt(6) lgkmcnt(2)\n" \
  "buffer_load_dwordx4 v[116:119], v[12:13], s[52:55], 0 idxen offen\n" \
  "ds_read_u16 v12, v14 offset:56\n" \
  "s_lshr_b32 s61, s61, 8\n" \
  "v_mfma_f32_4x4x4_16b_bf16 v[48:51], v[76:77], v[120:121], 0 cbsz:4 abid:6\n" \
  "v_mfma_f32_4x4x4_16b_bf16 v[52:55], v[76:77], v[122:123], 0 cbsz:4 abid:6\n" \
  "v_cvt_pk_bf16_f32 v61, v60, v61\n" \
  "v_lshlrev_b32_sdwa v60, 1, v61 dst_sel:DWORD dst_unused:UNUSED_PAD src0_sel:DWORD src1_sel:WORD_0\n" \
  "v_lshlrev_b32_sdwa v61, 1, v61 dst_sel:DWORD dst_unused:UNUSED_PAD src0_sel:DWORD src1_sel:WORD_1\n" \
  "ds_read_u16_d16_hi v60, v60\n" \
  "ds_read_u16_d16_hi v61, v61\n" \
  "s_set_gpr_idx_on s61, gpr_idx(SRC2,DST)\n" \
  "v_pk_fma_f32 v[128:129], v[66:67], v[48:49], v[128:129] op_sel:[0,0,0] op_sel_hi:[0,1,1]\n" \
  "v_pk_fma_f32 v[130:131], v[66:67], v[50:51], v[130:131] op_sel:[0,0,0] op_sel_hi:[0,1,1]\n" \
  "v_pk_fma_f32 v[132:133], v[70:71], v[52:53], v[132:133] op_sel:[0,0,0] op_sel_hi:[0,1,1]\n" \
  "v_pk_fma_f32 v[134:135], v[70:71], v[54:55], v[134:135] op_sel:[0,0,0] op_sel_hi:[0,1,1]\n" \
  "s_set_gpr_idx_off\n" \
  "s_waitcnt vmcnt(6) lgkmcnt(2)\n" \
  "buffer_load_dwordx4 v[120:123], v[12:13], s[52:55], 0 idxen offen\n" \
  "ds_read_u16 v12, v14 offset:60\n" \
  "s_lshr_b32 s61, s61, 8\n" \
  "v_mfma_f32_4x4x4_16b_bf16 v[84:87], v[94:95], v[24:25], 0\n" \
  "v_mfma_f32_4x4x4_16b_bf16 v[48:51], v[76:77], v[124:125], 0 cbsz:4 abid:7\n" \
  "v_mfma_f32_4x4x4_16b_bf16 v[52:55], v[76:77], v[126:127], 0 cbsz:4 abid:7\n" \
  "v_cvt_pk_bf16_f32 v59, v58, v59\n" \
  "v_lshlrev_b32_sdwa v58, 1, v59 dst_sel:DWORD dst_unused:UNUSED_PAD src0_sel:DWORD src1_sel:WORD_0\n" \
  "v_lshlrev_b32_sdwa v59, 1, v59 dst_sel:DWORD dst_unused:UNUSED_PAD src0_sel:DWORD src1_sel:WORD_1\n" \
  "ds_read_u16_d16_hi v58, v58\n" \
  "ds_read_u16_d16_hi v59, v59\n" \
  "s_set_gpr_idx_on s61, gpr_idx(SRC2,DST)\n" \
  "v_pk_fma_f32 v[128:129], v[66:67], v[48:49], v[128:129] op_sel:[1,0,0] op_sel_hi:[1,1,1]\n" \
  "v_pk_fma_f32 v[130:131], v[66:67], v[50:51], v[130:131] op_sel:[1,0,0] op_sel_hi:[1,1,1]\n" \
  "v_pk_fma_f32 v[132:133], v[70:71], v[52:53], v[132:133] op_sel:[1,0,0] op_sel_hi:[1,1,1]\n" \
  "v_pk_fma_f32 v[134:135], v[70:71], v[54:55], v[134:135] op_sel:[1,0,0] op_sel_hi:[1,1,1]\n" \
  "s_set_gpr_idx_off\n" \
  "s_waitcnt vmcnt(6) lgkmcnt(2)\n" \
  "buffer_load_dwordx4 v[124:127], v[12:13], s[52:55], 0 idxen offen\n" \
  "ds_read_u16 v12, v14 offset:64\n" \
  "s_sub_u32 s60, s60, 1\n" \
  "s_cbranch_scc1 Lend%=\n" \
  "v_readlane_b32 s61, v17, s60\n" \
  "s_setprio 1\n" \
  "v_mfma_f32_4x4x4_16b_bf16 v[64:67], v[92:93], v[32:33], 0 cbsz:4 abid:3\n" \
  "v_mfma_f32_4x4x4_16b_bf16 v[68:71], v[92:93], v[36:37], 0 cbsz:4 abid:3\n" \
  "v_mfma_f32_4x4x4_16b_bf16 v[48:51], v[76:77], v[96:97], 0 cbsz:4 abid:8\n" \
  "v_mfma_f32_4x4x4_16b_bf16 v[52:55], v[76:77], v[98:99], 0 cbsz:4 abid:8\n" \
  "v_cvt_pk_bf16_f32 v63, v62, v63\n" \
  "v_lshlrev_b32_sdwa v62, 1, v63 dst_sel:DWORD dst_unused:UNUSED_PAD src0_sel:DWORD src1_sel:WORD_0\n" \
  "v_lshlrev_b32_sdwa v63, 1, v63 dst_sel:DWORD dst_unused:UNUSED_PAD src0_sel:DWORD src1_sel:WORD_1\n" \
  "ds_read_u16_d16_hi v62, v62\n" \
  "ds_read_u16_d16_hi v63, v63\n" \
  "v_add_u32 v15, 0x100, v15\n" \
  "s_set_gpr_idx_on s61, gpr_idx(SRC2,DST)\n" \
  "v_pk_fma_f32 v[128:129], v[56:57], v[48:49], v[128:129] op_sel:[0,0,0] op_sel_hi:[0,1,1]\n" \
  "v_pk_fma_f32 v[130:131], v[56:57], v[50:51], v[130:131] op_sel:[0,0,0] op_sel_hi:[0,1,1]\n" \
  "v_pk_fma_f32 v[132:133], v[60:61], v[52:53], v[132:133] op_sel:[0,0,0] op_sel_hi:[0,1,1]\n" \
  "v_pk_fma_f32 v[134:135], v[60:61], v[54:55], v[134:135] op_sel:[0,0,0] op_sel_hi:[0,1,1]\n" \
  "s_set_gpr_idx_off\n" \
  "s_waitcnt vmcnt(6) lgkmcnt(2)\n" \
  "buffer_load_dwordx4 v[96:99], v[12:13], s[52:55], 0 idxen offen\n" \
  "ds_read_u16 v12, v14 offset:68\n" \
  "s_lshr_b32 s61, s61, 8\n" \
  "v_mfma_f32_4x4x4_16b_bf16 v[48:51], v[76:77], v[100:101], 0 cbsz:4 abid:9\n" \
  "v_mfma_f32_4x4x4_16b_bf16 v[52:55], v[76:77], v[102:103], 0 cbsz:4 abid:9\n" \
  "v_cvt_pk_bf16_f32 v65, v64, v65\n" \
  "v_lshlrev_b32_sdwa v64, 1, v65 dst_sel:DWORD dst_unused:UNUSED_PAD src0_sel:DWORD src1_sel:WORD_0\n" \
  "v_lshlrev_b32_sdwa v65, 1, v65 dst_sel:DWORD dst_unused:UNUSED_PAD src0_sel:DWORD src1_sel:WORD_1\n" \
  "ds_read_u16_d16_hi v64, v64\n" \
  "ds_read_u16_d16_hi v65, v65\n" \
  "s_set_gpr_idx_on s61, gpr_idx(SRC2,DST)\n" \
  "v_pk_fma_f32 v[128:129], v[56:57], v[48:49], v[128:129] op_sel:[1,0,0] op_sel_hi:[1,1,1]\n" \
  "v_pk_fma_f32 v[130:131], v[56:57], v[50:51], v[130:131] op_sel:[1,0,0] op_sel_hi:[1,1,1]\n" \
  "v_pk_fma_f32 v[132:133], v[60:61], v[52:53], v[132:133] op_sel:[1,0,0] op_sel_hi:[1,1,1]\n" \
  "v_pk_fma_f32 v[134:135], v[60:61], v[54:55], v[134:135] op_sel:[1,0,0] op_sel_hi:[1,1,1]\n" \
  "s_set_gpr_idx_off\n" \
  "s_waitcnt vmcnt(6) lgkmcnt(2)\n" \
  "buffer_load_dwordx4 v[100:103], v[12:13], s[52:55], 0 idxen offen\n" \
  "ds_read_u16 v12, v14 offset:72\n" \
  "s_lshr_b32 s61, s61, 8\n" \
  "v_mfma_f32_4x4x4_16b_bf16 v[48:51], v[76:77], v[104:105], 0 cbsz:4 abid:10\n" \
  "v_mfma_f32_4x4x4_16b_bf16 v[52:55], v[76:77], v[106:107], 0 cbsz:4 abid:10\n" \
  "v_cvt_pk_bf16_f32 v69, v68, v69\n" \
  "v_lshlrev_b32_sdwa v68, 1, v69 dst_sel:DWORD dst_unused:UNUSED_PAD src0_sel:DWORD src1_sel:WORD_0\n" \
  "v_lshlrev_b32_sdwa v69, 1, v69 dst_sel:DWORD dst_unused:UNUSED_PAD src0_sel:DWORD src1_sel:WORD_1\n" \
  "ds_read_u16_d16_hi v68, v68\n" \
  "ds_read_u16_d16_hi v69, v69\n" \
  "s_set_gpr_idx_on s61, gpr_idx(SRC2,DST)\n" \
  "v_pk_fma_f32 v[128:129], v[58:59], v[48:49], v[128:129] op_sel:[0,0,0] op_sel_hi:[0,1,1]\n" \
  "v_pk_fma_f32 v[130:131], v[58:59], v[50:51], v[130:131] op_sel:[0,0,0] op_sel_hi:[0,1,1]\n" \
  "v_pk_fma_f32 v[132:133], v[62:63], v[52:53], v[132:133] op_sel:[0,0,0] op_sel_hi:[0,1,1]\n" \
  "v_pk_fma_f32 v[134:135], v[62:63], v[54:55], v[134:135] op_sel:[0,0,0] op_sel_hi:[0,1,1]\n" \
  "s_set_gpr_idx_off\n" \
  "s_waitcnt vmcnt(6) lgkmcnt(2)\n" \
  "buffer_load_dwordx4 v[104:107], v[12:13], s[52:55], 0 idxen offen\n" \
  "ds_read_u16 v12, v14 offset:76\n" \
  "ds_read_b64 v[92:93], v16\n" \
  "s_lshr_b32 s61, s61, 8\n" \
  "v_mfma_f32_4x4x4_16b_bf16 v[88:91], v[94:95], v[26:27], 0\n" \
  "v_mfma_f32_4x4x4_16b_bf16 v[48:51], v[76:77], v[108:109], 0 cbsz:4 abid:11\n" \
  "v_mfma_f32_4x4x4_16b_bf16 v[52:55], v[76:77], v[110:111], 0 cbsz:4 abid:11\n" \
  "v_cvt_pk_bf16_f32 v67, v66, v67\n" \
  "v_lshlrev_b32_sdwa v66, 1, v67 dst_sel:DWORD dst_unused:UNUSED_PAD src0_sel:DWORD src1_sel:WORD_0\n" \
  "v_lshlrev_b32_sdwa v67, 1, v67 dst_sel:DWORD dst_unused:UNUSED_PAD src0_sel:DWORD src1_sel:WORD_1\n" \
  "ds_read_u16_d16_hi v66, v66\n" \
  "ds_read_u16_d16_hi v67, v67\n" \
  "s_set_gpr_idx_on s61, gpr_idx(SRC2,DST)\n" \
  "v_pk_fma_f32 v[128:129], v[58:59], v[48:49], v[128:129] op_sel:[1,0,0] op_sel_hi:[1,1,1]\n" \
  "v_pk_fma_f32 v[130:131], v[58:59], v[50:51], v[130:131] op_sel:[1,0,0] op_sel_hi:[1,1,1]\n" \
  "v_pk_fma_f32 v[132:133], v[62:63], v[52:53], v[132:133] op_sel:[1,0,0] op_sel_hi:[1,1,1]\n" \
  "v_pk_fma_f32 v[134:135], v[62:63], v[54:55], v[134:135] op_sel:[1,0,0] op_sel_hi:[1,1,1]\n" \
  "s_set_gpr_idx_off\n" \
  "s_waitcnt vmcnt(6) lgkmcnt(2)\n" \
  "buffer_load_dwordx4 v[108:111], v[12:13], s[52:55], 0 idxen offen\n" \
  "ds_read_u16 v12, v14 offset:80\n" \
  "s_sub_u32 s60, s60, 1\n" \
  "s_cbranch_scc1 Lend%=\n" \
  "v_readlane_b32 s61, v17, s60\n" \
  "s_setprio 0\n" \
  "v_mfma_f32_4x4x4_16b_bf16 v[56:59], v[92:93], v[32:33], 0 cbsz:4\n" \
  "v_mfma_f32_4x4x4_16b_bf16 v[60:63], v[92:93], v[36:37], 0 cbsz:4\n" \
  "v_mfma_f32_4x4x4_16b_bf16 v[48:51], v[76:77], v[112:113], 0 cbsz:4 abid:12\n" \
  "v_mfma_f32_4x4x4_16b_bf16 v[52:55], v[76:77], v[114:115], 0 cbsz:4 abid:12\n" \
  "v_cvt_pk_bf16_f32 v71, v70, v71\n" \
  "v_lshlrev_b32_sdwa v70, 1, v71 dst_sel:DWORD dst_unused:UNUSED_PAD src0_sel:DWORD src1_sel:WORD_0\n" \
  "v_lshlrev_b32_sdwa v71, 1, v71 dst_sel:DWORD dst_unused:UNUSED_PAD src0_sel:DWORD src1_sel:WORD_1\n" \
  "ds_read_u16_d16_hi v70, v70\n" \
  "ds_read_u16_d16_hi v71, v71\n" \
  "v_cvt_pk_bf16_f32 v20, v84, v88\n" \
  "s_set_gpr_idx_on s61, gpr_idx(SRC2,DST)\n" \
  "v_pk_fma_f32 v[128:129], v[64:65], v[48:49], v[128:129] op_sel:[0,0,0] op_sel_hi:[0,1,1]\n" \
  "v_pk_fma_f32 v[130:131], v[64:65], v[50:51], v[130:131] op_sel:[0,0,0] op_sel_hi:[0,1,1]\n" \
  "v_pk_fma_f32 v[132:133], v[68:69], v[52:53], v[132:133] op_sel:[0,0,0] op_sel_hi:[0,1,1]\n" \
  "v_pk_fma_f32 v[134:135], v[68:69], v[54:55], v[134:135] op_sel:[0,0,0] op_sel_hi:[0,1,1]\n" \
  "s_set_gpr_idx_off\n" \
  "s_waitcnt vmcnt(6) lgkmcnt(2)\n" \
  "buffer_load_dwordx4 v[112:115], v[12:13], s[52:55], 0 idxen offen\n" \
  "ds_read_u16 v12, v14 offset:84\n" \
  "s_lshr_b32 s61, s61, 8\n" \
  "v_mfma_f32_4x4x4_16b_bf16 v[48:51], v[76:77], v[116:117], 0 cbsz:4 abid:13\n" \
  "v_mfma_f32_4x4x4_16b_bf16 v[52:55], v[76:77], v[118:119], 0 cbsz:4 abid:13\n" \
  "v_cvt_pk_bf16_f32 v57, v56, v57\n" \
  "v_lshlrev_b32_sdwa v56, 1, v57 dst_sel:DWORD dst_unused:UNUSED_PAD src0_sel:DWORD src1_sel:WORD_0\n" \
  "v_lshlrev_b32_sdwa v57, 1, v57 dst_sel:DWORD dst_unused:UNUSED_PAD src0_sel:DWORD src1_sel:WORD_1\n" \
  "ds_read_u16_d16_hi v56, v56\n" \
  "ds_read_u16_d16_hi v57, v57\n" \
  "s_set_gpr_idx_on s61, gpr_idx(SRC2,DST)\n" \
  "v_pk_fma_f32 v[128:129], v[64:65], v[48:49], v[128:129] op_sel:[1,0,0] op_sel_hi:[1,1,1]\n" \
  "v_pk_fma_f32 v[130:131], v[64:65], v[50:51], v[130:131] op_sel:[1,0,0] op_sel_hi:[1,1,1]\n" \
  "v_pk_fma_f32 v[132:133], v[68:69], v[52:53], v[132:133] op_sel:[1,0,0] op_sel_hi:[1,1,1]\n" \
  "v_pk_fma_f32 v[134:135], v[68:69], v[54:55], v[134:135] op_sel:[1,0,0] op_sel_hi:[1,1,1]\n" \
  "s_set_gpr_idx_off\n" \
  "s_waitcnt vmcnt(6) lgkmcnt(2)\n" \
  "buffer_load_dwordx4 v[116:119], v[12:13], s[52:55], 0 idxen offen\n" \
  "ds_read_u16 v12, v14 offset:88\n" \
  "s_lshr_b32 s61, s61, 8\n" \
  "v_mfma_f32_4x4x4_16b_bf16 v[48:51], v[76:77], v[120:121], 0 cbsz:4 abid:14\n" \
  "v_mfma_f32_4x4x4_16b_bf16 v[52:55], v[76:77], v[122:123], 0 cbsz:4 abid:14\n" \
  "v_cvt_pk_bf16_f32 v61, v60, v61\n" \
  "v_lshlrev_b32_sdwa v60, 1, v61 dst_sel:DWORD dst_unused:UNUSED_PAD src0_sel:DWORD src1_sel:WORD_0\n" \
  "v_lshlrev_b32_sdwa v61, 1, v61 dst_sel:DWORD dst_unused:UNUSED_PAD src0_sel:DWORD src1_sel:WORD_1\n" \
  "ds_read_u16_d16_hi v60, v60\n" \
  "ds_read_u16_d16_hi v61, v61\n" \
  "s_set_gpr_idx_on s61, gpr_idx(SRC2,DST)\n" \
  "v_pk_fma_f32 v[128:129], v[66:67], v[48:49], v[128:129] op_sel:[0,0,0] op_sel_hi:[0,1,1]\n" \
  "v_pk_fma_f32 v[130:131], v[66:67], v[50:51], v[130:131] op_sel:[0,0,0] op_sel_hi:[0,1,1]\n" \
  "v_pk_fma_f32 v[132:133], v[70:71], v[52:53], v[132:133] op_sel:[0,0,0] op_sel_hi:[0,1,1]\n" \
  "v_pk_fma_f32 v[134:135], v[70:71], v[54:55], v[134:135] op_sel:[0,0,0] op_sel_hi:[0,1,1]\n" \
  "s_set_gpr_idx_off\n" \
  "s_waitcnt vmcnt(6) lgkmcnt(2)\n" \
  "buffer_load_dwordx4 v[120:123], v[12:13], s[52:55], 0 idxen offen\n" \
  "ds_read_u16 v12, v14 offset:92\n" \
  "s_lshr_b32 s61, s61, 8\n" \
  "v_mfma_f32_4x4x4_16b_bf16 v[84:87], v[94:95], v[28:29], 0\n" \
  "v_mfma_f32_4x4x4_16b_bf16 v[88:91], v[94:95], v[30:31], 0\n" \
  "v_mfma_f32_4x4x4_16b_bf16 v[48:51], v[76:77], v[124:125], 0 cbsz:4 abid:15\n" \
  "v_mfma_f32_4x4x4_16b_bf16 v[52:55], v[76:77], v[126:127], 0 cbsz:4 abid:15\n" \
  "v_cvt_pk_bf16_f32 v59, v58, v59\n" \
  "v_lshlrev_b32_sdwa v58, 1, v59 dst_sel:DWORD dst_unused:UNUSED_PAD src0_sel:DWORD src1_sel:WORD_0\n" \
  "v_lshlrev_b32_sdwa v59, 1, v59 dst_sel:DWORD dst_unused:UNUSED_PAD src0_sel:DWORD src1_sel:WORD_1\n" \
  "ds_read_u16_d16_hi v58, v58\n" \
  "ds_read_u16_d16_hi v59, v59\n" \
  "v_cvt_pk_bf16_f32 v77, v84, v88\n" \
  "v_mov_b32 v76, v20\n" \
  "s_set_gpr_idx_on s61, gpr_idx(SRC2,DST)\n" \
  "v_pk_fma_f32 v[128:129], v[66:67], v[48:49], v[128:129] op_sel:[1,0,0] op_sel_hi:[1,1,1]\n" \
  "v_pk_fma_f32 v[130:131], v[66:67], v[50:51], v[130:131] op_sel:[1,0,0] op_sel_hi:[1,1,1]\n" \
  "v_pk_fma_f32 v[132:133], v[70:71], v[52:53], v[132:133] op_sel:[1,0,0] op_sel_hi:[1,1,1]\n" \
  "v_pk_fma_f32 v[134:135], v[70:71], v[54:55], v[134:135] op_sel:[1,0,0] op_sel_hi:[1,1,1]\n" \
  "s_set_gpr_idx_off\n" \
  "s_waitcnt vmcnt(6) lgkmcnt(2)\n" \
  "buffer_load_dwordx4 v[124:127], v[12:13], s[52:55], 0 idxen offen\n" \
  "ds_read_u16 v12, v14 offset:96\n" \
  "s_sub_u32 s60, s60, 1\n" \
  "s_cbranch_scc1 Lend%=\n" \
  "v_readlane_b32 s61, v17, s60\n" \
  "s_setprio 3\n" \
  "v_mfma_f32_4x4x4_16b_bf16 v[64:67], v[92:93], v[32:33], 0 cbsz:4 abid:1\n" \
  "v_mfma_f32_4x4x4_16b_bf16 v[68:71], v[92:93], v[36:37], 0 cbsz:4 abid:1\n" \
  "v_mfma_f32_4x4x4_16b_bf16 v[48:51], v[76:77], v[96:97], 0 cbsz:4\n" \
  "v_mfma_f32_4x4x4_16b_bf16 v[52:55], v[76:77], v[98:99], 0 cbsz:4\n" \
  "v_cvt_pk_bf16_f32 v63, v62, v63\n" \
  "v_lshlrev_b32_sdwa v62, 1, v63 dst_sel:DWORD dst_unused:UNUSED_PAD src0_sel:DWORD src1_sel:WORD_0\n" \
  "v_lshlrev_b32_sdwa v63, 1, v63 dst_sel:DWORD dst_unused:UNUSED_PAD src0_sel:DWORD src1_sel:WORD_1\n" \
  "ds_read_u16_d16_hi v62, v62\n" \
  "ds_read_u16_d16_hi v63, v63\n" \
  "v_add_u32 v14, 64, v14\n" \
  "s_branch Lbody%=\n" \
  "Lep0_%=:\n" \
  "ds_read_b32 v50, v18\n" \
  "s_waitcnt lgkmcnt(0)\n" \
  "v_readfirstlane_b32 s63, v50\n" \
  "s_bitcmp1_b32 s63, 1\n" \
  "s_cbranch_scc1 Lgo%=\n" \
  "v_mov_b64 v[50:51], s[50:51]\n" \
  "s_mov_b64 exec, 1\n" \
  "buffer_store_dwordx2 v[50:51], off, s[24:27], s70 sc1\n" \
  "s_mov_b64 exec, -1\n" \
  "s_nop 3\n" \
  "s_branch Lgo%=\n" \
  "Lbad%=:\n" \
  "v_mov_b32 v72, 1\n" \
  "v_mov_b32 v73, s62\n" \
  "ds_or_b32 v18, v72\n" \
  "ds_write_b32 v73, v73\n" \
  "s_branch Lf9_%=\n" \
  "Lnf%=:\n" \
  "s_mov_b32 %[bad], 3\n" \
  "s_mov_b32 s79, m0\n" \
  "Lbad2_%=:\n" \
  "s_waitcnt vmcnt(0) lgkmcnt(0)\n" \
  "s_bitcmp1_b32 %[bad], 1\n" \
  "s_cbranch_scc1 Lout%=\n" \
  "s_branch Lep%=\n" \
  "Lh2_%=:\n" \
  "s_waitcnt vmcnt(0)\n" \
  "v_add_co_u32 v90, vcc, 1, v88\n" \
  "s_mul_i32 s63, %[blk], 2176\n" \
  "s_add_u32 s63, s63, 49408\n" \
  "v_addc_co_u32 v91, vcc, 0, v89, vcc\n" \
  "v_lshlrev_b32 v88, 22, v90\n" \
  "v_alignbit_b32 v89, v91, v90, 10\n" \
  "v_add_u32 v87, v87, v88\n" \
  "v_lshl_add_u32 v128, v136, 11, v87\n" \
  "v_mov_b32 v129, v89\n" \
  "v_sub_u32 v51, v137, v136\n" \
  "v_lshl_add_u32 v51, v51, 11, v136\n" \
  "v_add_u32 v130, v51, v87\n" \
  "v_mov_b32 v131, v89\n" \
  "v_sub_u32 v51, v138, v137\n" \
  "v_lshl_add_u32 v51, v51, 11, v137\n" \
  "v_add_u32 v132, v51, v87\n" \
  "v_mov_b32 v133, v89\n" \
  "v_sub_u32 v51, v139, v138\n" \
  "v_lshl_add_u32 v51, v51, 11, v138\n" \
  "v_add_u32 v134, v51, v87\n" \
  "v_mov_b32 v135, v89\n" \
  "buffer_store_dwordx4 v[128:131], v13, s[24:27], s63 offen sc1\n" \
  "buffer_store_dwordx4 v[132:135], v13, s[24:27], s63 offen offset:1024 sc1\n" \
  "buffer_load_dword v100, v54, s[8:11], 0 offen offset:0\n" \
  "buffer_load_dword v101, v54, s[8:11], 0 offen offset:256\n" \
  "buffer_load_dword v102, v54, s[8:11], 0 offen offset:512\n" \
  "buffer_load_dword v103, v54, s[8:11], 0 offen offset:768\n" \
  "buffer_load_dword v104, v54, s[12:15], 0 offen\n" \
  "s_lshr_b32 s63, %[blk], 2\n" \
  "s_lshl_b32 s63, s63, 13\n" \
  "s_and_b32 s93, %[blk], 3\n" \
  "s_lshl_b32 s93, s93, 6\n" \
  "s_add_u32 s63, s63, s93\n" \
  "v_and_b32 v50, 0x300, v13\n" \
  "v_and_b32 v51, 60, v54\n" \
  "v_lshl_add_u32 v50, v50, 3, v51\n" \
  "s_cmp_ge_u32 %[blk], 16\n" \
  "s_cbranch_scc1 Lh4_%=\n" \
  "buffer_load_dword v108, v50, s[16:19], s63 offen offset:0\n" \
  "buffer_load_dword v109, v50, s[16:19], s63 offen offset:256\n" \
  "buffer_load_dword v110, v50, s[16:19], s63 offen offset:512\n" \
  "buffer_load_dword v111, v50, s[16:19], s63 offen offset:768\n" \
  "buffer_load_dword v112, v50, s[16:19], s63 offen offset:1024\n" \
  "buffer_load_dword v113, v50, s[16:19], s63 offen offset:1280\n" \
  "buffer_load_dword v114, v50, s[16:19], s63 offen offset:1536\n" \
  "buffer_load_dword v115, v50, s[16:19], s63 offen offset:1792\n" \
  "Lh4_%=:\n" \
  "Lw1_%=:\n" \
  "s_memrealtime s[90:91]\n" \
  "s_waitcnt lgkmcnt(0)\n" \
  "s_sub_u32 s90, s90, s76\n" \
  "s_cmp_lt_u32 s90, 170\n" \
  "s_cbranch_scc1 Lw1_%=\n" \
  "s_barrier\n" \
  "ds_write_b64 v94, v[90:91] offset:1472\n" \
  "s_mov_b32 s63, 0x05040100\n" \
  "s_mov_b32 s93, 0x07060302\n" \
  "s_cmp_ge_u32 %[blk], 16\n" \
  "s_waitcnt vmcnt(0)\n" \
  "s_cbranch_scc1 Lh5_%=\n" \
  "v_perm_b32 v216, v109, v108, s63\n" \
  "v_perm_b32 v217, v111, v110, s63\n" \
  "v_perm_b32 v218, v113, v112, s63\n" \
  "v_perm_b32 v219, v115, v114, s63\n" \
  "v_perm_b32 v220, v109, v108, s93\n" \
  "v_perm_b32 v221, v111, v110, s93\n" \
  "v_perm_b32 v222, v113, v112, s93\n" \
  "v_perm_b32 v223, v115, v114, s93\n" \
  "s_lshl_b32 s72, %[blk], 11\n" \
  "buffer_store_dwordx4 v[216:219], v13, s[24:27], s72 offen sc1\n" \
  "buffer_store_dwordx4 v[220:223], v13, s[24:27], s72 offen offset:1024 sc1\n" \
  "Lh5_%=:\n" \
  "v_perm_b32 v120, v101, v100, s63\n" \
  "v_perm_b32 v121, v103, v102, s63\n" \
  "v_perm_b32 v122, v101, v100, s93\n" \
  "v_perm_b32 v123, v103, v102, s93\n" \
  "ds_write_b128 v13, v[120:123] offset:36288\n" \
  "v_and_b32 v50, 30, v86\n" \
  "v_and_b32 v51, 16, v13\n" \
  "v_lshl_add_u32 v50, v51, 2, v50\n" \
  "s_mov_b64 exec, 0xffffffff\n" \
  "ds_write_b16 v50, v104 offset:36160\n" \
  "ds_write_b16_d16_hi v50, v104 offset:36192\n" \
  "s_mov_b64 exec, -1\n" \
  "v_and_b32 v100, 0x7fff7fff, v100\n" \
  "v_max_u16_sdwa v100, v100, v100 dst_sel:WORD_1 dst_unused:UNUSED_PAD src0_sel:WORD_0 src1_sel:WORD_1\n" \
  "v_and_b32 v101, 0x7fff7fff, v101\n" \
  "v_max_u16_sdwa v101, v101, v101 dst_sel:WORD_1 dst_unused:UNUSED_PAD src0_sel:WORD_0 src1_sel:WORD_1\n" \
  "v_and_b32 v102, 0x7fff7fff, v102\n" \
  "v_max_u16_sdwa v102, v102, v102 dst_sel:WORD_1 dst_unused:UNUSED_PAD src0_sel:WORD_0 src1_sel:WORD_1\n" \
  "v_and_b32 v103, 0x7fff7fff, v103\n" \
  "v_max_u16_sdwa v103, v103, v103 dst_sel:WORD_1 dst_unused:UNUSED_PAD src0_sel:WORD_0 src1_sel:WORD_1\n" \
  "v_add_f32 v100, v100, v101\n" \
  "v_add_f32 v102, v102, v103\n" \
  "v_add_f32 v100, v100, v102\n" \
  "v_and_b32 v100, 0x7fffffff, v100\n" \
  "s_nop 1\n" \
  "v_max_u32_dpp v100, v100, v100 row_shr:1 row_mask:0xf bank_mask:0xf bound_ctrl:1\n" \
  "s_nop 1\n" \
  "v_max_u32_dpp v100, v100, v100 row_shr:2 row_mask:0xf bank_mask:0xf bound_ctrl:1\n" \
  "s_nop 1\n" \
  "v_max_u32_dpp v100, v100, v100 row_shr:4 row_mask:0xf bank_mask:0xf bound_ctrl:1\n" \
  "s_nop 1\n" \
  "v_max_u32_dpp v100, v100, v100 row_shr:8 row_mask:0xf bank_mask:0xf bound_ctrl:1\n" \
  "s_nop 1\n" \
  "v_max_u32_dpp v100, v100, v100 row_bcast:15 row_mask:0xa bank_mask:0xf\n" \
  "s_nop 1\n" \
  "v_max_u32_dpp v100, v100, v100 row_bcast:31 row_mask:0xc bank_mask:0xf\n" \
  "v_max_u32 v101, 0x0d000000, v100\n" \
  "v_rcp_f32 v101, v101\n" \
  "v_cmp_gt_u32 vcc, 0x71000000, v100\n" \
  "v_mul_f32 v101, 0x437a0000, v101\n" \
  "v_lshrrev_b32 v101, 16, v101\n" \
  "v_cndmask_b32 v101, -1, v101, vcc\n" \
  "s_brev_b64 exec, 1\n" \
  "ds_write_b32 v94, v101 offset:1480\n" \
  "s_mov_b64 exec, -1\n" \
  "s_branch Lc1_%=\n" \
  "Lend%=:\n" \
  "s_and_b32 s63, s44, 3\n" \
  "s_lshl_b32 s63, s63, 11\n" \
  "buffer_load_dwordx4 v[96:99], v13, s[24:27], s63 offen offset:0 sc1\n" \
  "buffer_load_dwordx4 v[100:103], v13, s[24:27], s63 offen offset:1024 sc1\n" \
  "s_add_u32 s63, s63, 8192\n" \
  "buffer_load_dwordx4 v[104:107], v13, s[24:27], s63 offen offset:0 sc1\n" \
  "buffer_load_dwordx4 v[108:111], v13, s[24:27], s63 offen offset:1024 sc1\n" \
  "s_add_u32 s63, s63, 8192\n" \
  "buffer_load_dwordx4 v[112:115], v13, s[24:27], s63 offen offset:0 sc1\n" \
  "buffer_load_dwordx4 v[116:119], v13, s[24:27], s63 offen offset:1024 sc1\n" \
  "s_add_u32 s63, s63, 8192\n" \
  "buffer_load_dwordx4 v[120:123], v13, s[24:27], s63 offen offset:0 sc1\n" \
  "buffer_load_dwordx4 v[124:127], v13, s[24:27], s63 offen offset:1024 sc1\n" \
  "buffer_load_dword v20, off, s[24:27], s70 sc1\n" \
  "s_lshl2_add_u32 s62, s44, 35872\n" \
  "s_cmp_ge_u32 s74, 10\n" \
  "s_cbranch_scc1 Lbad%=\n" \
  "s_cmp_lt_u32 s93, s44\n" \
  "s_cbranch_scc0 Lf1_%=\n" \
  "s_set_gpr_idx_on s73, gpr_idx(SRC0)\n" \
  "v_mov_b32 v84, v128\n" \
  "v_mov_b32 v85, v129\n" \
  "v_mov_b32 v86, v130\n" \
  "v_mov_b32 v87, v131\n" \
  "v_mov_b32 v88, v132\n" \
  "v_mov_b32 v89, v133\n" \
  "v_mov_b32 v90, v134\n" \
  "v_mov_b32 v91, v135\n" \
  "s_set_gpr_idx_off\n" \
  "ds_write_b128 v21, v[84:87]\n" \
  "ds_write_b128 v21, v[88:91] offset:1024\n" \
  "v_mov_b32 v53, s62\n" \
  "ds_write_b32 v53, v53\n" \
  "s_add_u32 s73, s73, 8\n" \
  "s_cmp_eq_u32 s73, 80\n" \
  "s_cselect_b32 s73, 0, s73\n" \
  "s_add_u32 s72, s72, 1056\n" \
  "s_sub_u32 s74, s74, 1\n" \
  "s_cbranch_scc1 Lf9_%=\n" \
  "Lf1_%=:\n" \
  "s_min_u32 s63, s75, 1\n" \
  "s_sub_u32 s60, s74, s63\n" \
  "s_cbranch_scc1 Lf3_%=\n" \
  "Lf2_%=:\n" \
  "s_set_gpr_idx_on s73, gpr_idx(SRC0,SRC1)\n" \
  "v_cvt_pk_bf16_f32 v48, v128, v132\n" \
  "v_cvt_pk_bf16_f32 v49, v129, v133\n" \
  "v_cvt_pk_bf16_f32 v50, v130, v134\n" \
  "v_cvt_pk_bf16_f32 v51, v131, v135\n" \
  "s_set_gpr_idx_off\n" \
  "v_add_u32 v52, s72, v19\n" \
  "s_add_u32 s73, s73, 8\n" \
  "s_cmp_eq_u32 s73, 80\n" \
  "s_cselect_b32 s73, 0, s73\n" \
  "s_add_u32 s72, s72, 1056\n" \
  "ds_write2_b32 v52, v48, v49 offset1:16\n" \
  "ds_write2_b32 v52, v50, v51 offset0:32 offset1:48\n" \
  "s_sub_u32 s60, s60, 1\n" \
  "s_cbranch_scc0 Lf2_%=\n" \
  "Lf3_%=:\n" \
  "s_set_gpr_idx_on s73, gpr_idx(SRC0)\n" \
  "v_mov_b32 v40, v128\n" \
  "v_mov_b32 v41, v129\n" \
  "v_mov_b32 v42, v130\n" \
  "v_mov_b32 v43, v131\n" \
  "v_mov_b32 v44, v132\n" \
  "v_mov_b32 v45, v133\n" \
  "v_mov_b32 v46, v134\n" \
  "v_mov_b32 v47, v135\n" \
  "s_set_gpr_idx_off\n" \
  "s_cmp_eq_u32 s75, 0\n" \
  "s_cbranch_scc1 Lf9_%=\n" \
  "s_mov_b32 s60, s75\n" \
  "v_mov_b32 v72, s62\n" \
  "Lf4_%=:\n" \
  "ds_read_b32 v73, v72 offset:4\n" \
  "ds_read_b128 v[84:87], v21 offset:3328\n" \
  "ds_read_b128 v[88:91], v21 offset:4352\n" \
  "s_waitcnt lgkmcnt(0)\n" \
  "v_readfirstlane_b32 s63, v73\n" \
  "s_cmp_eq_u32 s63, 0\n" \
  "s_cbranch_scc0 Lf5_%=\n" \
  "s_sleep 1\n" \
  "s_branch Lf4_%=\n" \
  "Lf5_%=:\n" \
  "v_pk_add_f32 v[40:41], v[40:41], v[84:85]\n" \
  "v_pk_add_f32 v[42:43], v[42:43], v[86:87]\n" \
  "v_pk_add_f32 v[44:45], v[44:45], v[88:89]\n" \
  "v_pk_add_f32 v[46:47], v[46:47], v[90:91]\n" \
  "v_add_u32 v72, 4, v72\n" \
  "v_add_u32 v21, 3328, v21\n" \
  "s_sub_u32 s60, s60, 1\n" \
  "s_cmp_lg_u32 s60, 0\n" \
  "s_cbranch_scc1 Lf4_%=\n" \
  "v_cvt_pk_bf16_f32 v48, v40, v44\n" \
  "v_cvt_pk_bf16_f32 v49, v41, v45\n" \
  "v_cvt_pk_bf16_f32 v50, v42, v46\n" \
  "v_cvt_pk_bf16_f32 v51, v43, v47\n" \
  "v_add_u32 v52, s72, v19\n" \
  "ds_write2_b32 v52, v48, v49 offset1:16\n" \
  "ds_write2_b32 v52, v50, v51 offset0:32 offset1:48\n" \
  "Lf9_%=:\n" \
  "s_waitcnt lgkmcnt(0)\n" \
  "s_barrier\n" \
  "ds_read_b32 v18, v18\n" \
  "ds_read_b128 v[128:131], v22 offset:0\n" \
  "ds_read_b128 v[132:135], v22 offset:256\n" \
  "ds_read_b128 v[136:139], v22 offset:512\n" \
  "ds_read_b128 v[140:143], v22 offset:768\n" \
  "ds_read_b128 v[144:147], v22 offset:8448\n" \
  "ds_read_b128 v[148:151], v22 offset:8704\n" \
  "ds_read_b128 v[152:155], v22 offset:8960\n" \
  "ds_read_b128 v[156:159], v22 offset:9216\n" \
  "ds_read_b128 v[160:163], v22 offset:16896\n" \
  "ds_read_b128 v[164:167], v22 offset:17152\n" \
  "ds_read_b128 v[168:171], v22 offset:17408\n" \
  "ds_read_b128 v[172:175], v22 offset:17664\n" \
  "ds_read_b128 v[176:179], v22 offset:25344\n" \
  "ds_read_b128 v[180:183], v22 offset:25600\n" \
  "s_waitcnt lgkmcnt(14)\n" \
  "v_readfirstlane_b32 %[bad], v18\n" \
  "s_cmp_lg_u32 %[bad], 0\n" \
  "s_cbranch_scc1 Lbad2_%=\n" \
  "s_waitcnt vmcnt(7) lgkmcnt(13)\n" \
  "v_mfma_f32_16x16x32_bf16 v[40:43], v[128:131], v[96:99], 0\n" \
  "v_mfma_f32_16x16x32_bf16 v[44:47], v[128:131], v[100:103], 0\n" \
  "ds_read_b128 v[184:187], v22 offset:25856\n" \
  "s_waitcnt vmcnt(5) lgkmcnt(13)\n" \
  "v_mfma_f32_16x16x32_bf16 v[40:43], v[132:135], v[104:107], v[40:43]\n" \
  "v_mfma_f32_16x16x32_bf16 v[44:47], v[132:135], v[108:111], v[44:47]\n" \
  "ds_read_b128 v[188:191], v22 offset:26112\n" \
  "s_waitcnt vmcnt(3) lgkmcnt(13)\n" \
  "v_mfma_f32_16x16x32_bf16 v[40:43], v[136:139], v[112:115], v[40:43]\n" \
  "v_mfma_f32_16x16x32_bf16 v[44:47], v[136:139], v[116:119], v[44:47]\n" \
  "ds_read_b128 v[192:195], v22 offset:33792\n" \
  "s_waitcnt vmcnt(1) lgkmcnt(13)\n" \
  "v_mfma_f32_16x16x32_bf16 v[40:43], v[140:143], v[120:123], v[40:43]\n" \
  "v_mfma_f32_16x16x32_bf16 v[44:47], v[140:143], v[124:127], v[44:47]\n" \
  "ds_read_b128 v[196:199], v22 offset:34048\n" \
  "s_waitcnt lgkmcnt(13)\n" \
  "v_mfma_f32_16x16x32_bf16 v[48:51], v[144:147], v[96:99], 0\n" \
  "v_mfma_f32_16x16x32_bf16 v[52:55], v[144:147], v[100:103], 0\n" \
  "ds_read_b128 v[200:203], v22 offset:34304\n" \
  "s_waitcnt lgkmcnt(13)\n" \
  "v_mfma_f32_16x16x32_bf16 v[48:51], v[148:151], v[104:107], v[48:51]\n" \
  "v_mfma_f32_16x16x32_bf16 v[52:55], v[148:151], v[108:111], v[52:55]\n" \
  "ds_read_b128 v[204:207], v22 offset:34560\n" \
  "s_waitcnt lgkmcnt(13)\n" \
  "v_mfma_f32_16x16x32_bf16 v[48:51], v[152:155], v[112:115], v[48:51]\n" \
  "v_mfma_f32_16x16x32_bf16 v[52:55], v[152:155], v[116:119], v[52:55]\n" \
  "v_cvt_pk_bf16_f32 v40, v40, v41\n" \
  "v_cvt_pk_bf16_f32 v41, v42, v43\n" \
  "v_cvt_pk_bf16_f32 v42, v44, v45\n" \
  "v_cvt_pk_bf16_f32 v43, v46, v47\n" \
  "buffer_store_dwordx4 v[40:43], v23, s[56:59], 0 offen sc1\n" \
  "s_waitcnt lgkmcnt(12)\n" \
  "v_mfma_f32_16x16x32_bf16 v[48:51], v[156:159], v[120:123], v[48:51]\n" \
  "v_mfma_f32_16x16x32_bf16 v[52:55], v[156:159], v[124:127], v[52:55]\n" \
  "s_waitcnt lgkmcnt(11)\n" \
  "v_mfma_f32_16x16x32_bf16 v[56:59], v[160:163], v[96:99], 0\n" \
  "v_mfma_f32_16x16x32_bf16 v[60:63], v[160:163], v[100:103], 0\n" \
  "s_waitcnt lgkmcnt(10)\n" \
  "v_mfma_f32_16x16x32_bf16 v[56:59], v[164:167], v[104:107], v[56:59]\n" \
  "v_mfma_f32_16x16x32_bf16 v[60:63], v[164:167], v[108:111], v[60:63]\n" \
  "s_waitcnt lgkmcnt(9)\n" \
  "v_mfma_f32_16x16x32_bf16 v[56:59], v[168:171], v[112:115], v[56:59]\n" \
  "v_mfma_f32_16x16x32_bf16 v[60:63], v[168:171], v[116:119], v[60:63]\n" \
  "v_cvt_pk_bf16_f32 v48, v48, v49\n" \
  "v_cvt_pk_bf16_f32 v49, v50, v51\n" \
  "v_cvt_pk_bf16_f32 v50, v52, v53\n" \
  "v_cvt_pk_bf16_f32 v51, v54, v55\n" \
  "s_movk_i32 s63, 0x1000\n" \
  "s_lshl_b32 s63, s63, 1\n" \
  "buffer_store_dwordx4 v[48:51], v23, s[56:59], s63 offen sc1\n" \
  "s_waitcnt lgkmcnt(8)\n" \
  "v_mfma_f32_16x16x32_bf16 v[56:59], v[172:175], v[120:123], v[56:59]\n" \
  "v_mfma_f32_16x16x32_bf16 v[60:63], v[172:175], v[124:127], v[60:63]\n" \
  "s_waitcnt lgkmcnt(7)\n" \
  "v_mfma_f32_16x16x32_bf16 v[64:67], v[176:179], v[96:99], 0\n" \
  "v_mfma_f32_16x16x32_bf16 v[68:71], v[176:179], v[100:103], 0\n" \
  "s_waitcnt lgkmcnt(6)\n" \
  "v_mfma_f32_16x16x32_bf16 v[64:67], v[180:183], v[104:107], v[64:67]\n" \
  "v_mfma_f32_16x16x32_bf16 v[68:71], v[180:183], v[108:111], v[68:71]\n" \
  "s_waitcnt lgkmcnt(5)\n" \
  "v_mfma_f32_16x16x32_bf16 v[64:67], v[184:187], v[112:115], v[64:67]\n" \
  "v_mfma_f32_16x16x32_bf16 v[68:71], v[184:187], v[116:119], v[68:71]\n" \
  "v_cvt_pk_bf16_f32 v56, v56, v57\n" \
  "v_cvt_pk_bf16_f32 v57, v58, v59\n" \
  "v_cvt_pk_bf16_f32 v58, v60, v61\n" \
  "v_cvt_pk_bf16_f32 v59, v62, v63\n" \
  "s_movk_i32 s63, 0x2000\n" \
  "s_lshl_b32 s63, s63, 1\n" \
  "buffer_store_dwordx4 v[56:59], v23, s[56:59], s63 offen sc1\n" \
  "s_waitcnt lgkmcnt(4)\n" \
  "v_mfma_f32_16x16x32_bf16 v[64:67], v[188:191], v[120:123], v[64:67]\n" \
  "v_mfma_f32_16x16x32_bf16 v[68:71], v[188:191], v[124:127], v[68:71]\n" \
  "s_waitcnt lgkmcnt(3)\n" \
  "v_mfma_f32_16x16x32_bf16 v[72:75], v[192:195], v[96:99], 0\n" \
  "v_mfma_f32_16x16x32_bf16 v[76:79], v[192:195], v[100:103], 0\n" \
  "s_waitcnt lgkmcnt(2)\n" \
  "v_mfma_f32_16x16x32_bf16 v[72:75], v[196:199], v[104:107], v[72:75]\n" \
  "v_mfma_f32_16x16x32_bf16 v[76:79], v[196:199], v[108:111], v[76:79]\n" \
  "s_waitcnt lgkmcnt(1)\n" \
  "v_mfma_f32_16x16x32_bf16 v[72:75], v[200:203], v[112:115], v[72:75]\n" \
  "v_mfma_f32_16x16x32_bf16 v[76:79], v[200:203], v[116:119], v[76:79]\n" \
  "v_cvt_pk_bf16_f32 v64, v64, v65\n" \
  "v_cvt_pk_bf16_f32 v65, v66, v67\n" \
  "v_cvt_pk_bf16_f32 v66, v68, v69\n" \
  "v_cvt_pk_bf16_f32 v67, v70, v71\n" \
  "s_movk_i32 s63, 0x3000\n" \
  "s_lshl_b32 s63, s63, 1\n" \
  "buffer_store_dwordx4 v[64:67], v23, s[56:59], s63 offen sc1\n" \
  "s_waitcnt lgkmcnt(0)\n" \
  "v_mfma_f32_16x16x32_bf16 v[72:75], v[204:207], v[120:123], v[72:75]\n" \
  "v_mfma_f32_16x16x32_bf16 v[76:79], v[204:207], v[124:127], v[76:79]\n" \
  "s_nop 7\n" \
  "v_cvt_pk_bf16_f32 v72, v72, v73\n" \
  "v_cvt_pk_bf16_f32 v73, v74, v75\n" \
  "v_cvt_pk_bf16_f32 v74, v76, v77\n" \
  "v_cvt_pk_bf16_f32 v75, v78, v79\n" \
  "s_movk_i32 s63, 0x4000\n" \
  "s_lshl_b32 s63, s63, 1\n" \
  "buffer_store_dwordx4 v[72:75], v23, s[56:59], s63 offen sc1\n" \
  "s_waitcnt vmcnt(5)\n" \
  "v_readfirstlane_b32 s63, v20\n" \
  "s_cmp_eq_u32 s63, s50\n" \
  "s_cbranch_scc1 Lout%=\n" \
  "Lep%=:\n" \
  "s_cmp_lg_u32 s44, 0\n" \
  "s_cbranch_scc1 Lout%=\n" \
  "v_mov_b64 v[84:85], s[50:51]\n" \
  "s_mov_b64 exec, 1\n" \
  "buffer_store_dwordx2 v[84:85], off, s[24:27], s70 sc1\n" \
  "s_mov_b64 exec, -1\n" \
  "Lout%=:\n" \
  "s_mov_b32 m0, s79\n"
#define STREAM_CLOBBERS \
  "v12", "v13", "v14", "v15", "v16", "v17", "v18", "v19", "v20", "v21", "v22", "v23", "v24", "v25", "v26", \
  "v27", "v28", "v29", "v30", "v31", "v32", "v33", "v34", "v35", "v36", "v37", "v38", "v39", "v40", "v41", \
  "v42", "v43", "v44", "v45", "v46", "v47", "v48", "v49", "v50", "v51", "v52", "v53", "v54", "v55", "v56", \
  "v57", "v58", "v59", "v60", "v61", "v62", "v63", "v64", "v65", "v66", "v67", "v68", "v69", "v70", "v71", \
  "v72", "v73", "v74", "v75", "v76", "v77", "v78", "v79", "v80", "v81", "v82", "v83", "v84", "v85", "v86", \
  "v87", "v88", "v89", "v90", "v91", "v92", "v93", "v94", "v95", "v96", "v97", "v98", "v99", "v100", "v101", \
  "v102", "v103", "v104", "v105", "v106", "v107", "v108", "v109", "v110", "v111", "v112", "v113", "v114", \
  "v115", "v116", "v117", "v118", "v119", "v120", "v121", "v122", "v123", "v124", "v125", "v126", "v127", \
  "v128", "v129", "v130", "v131", "v132", "v133", "v134", "v135", "v136", "v137", "v138", "v139", "v140", \
  "v141", "v142", "v143", "v144", "v145", "v146", "v147", "v148", "v149", "v150", "v151", "v152", "v153", \
  "v154", "v155", "v156", "v157", "v158", "v159", "v160", "v161", "v162", "v163", "v164", "v165", "v166", \
  "v167", "v168", "v169", "v170", "v171", "v172", "v173", "v174", "v175", "v176", "v177", "v178", "v179", \
  "v180", "v181", "v182", "v183", "v184", "v185", "v186", "v187", "v188", "v189", "v190", "v191", "v192", \
  "v193", "v194", "v195", "v196", "v197", "v198", "v199", "v200", "v201", "v202", "v203", "v204", "v205", \
  "v206", "v207", "v208", "v209", "v210", "v211", "v212", "v213", "v214", "v215", "v216", "v217", "v218", \
  "v219", "v220", "v221", "v222", "v223", "v224", "v225", "v226", "v227", "v228", "v229", "v230", "v231", \
  "v232", "v233", "v234", "v235", "vcc", "s8", "s9", "s10", "s11", "s12", "s13", "s14", "s15", "s16", "s17", \
  "s18", "s19", "s20", "s21", "s22", "s23", "s24", "s25", "s26", "s27", "s28", "s29", "s30", "s31", "s33", \
  "s34", "s35", "s36", "s37", "s38", "s39", "s40", "s41", "s42", "s43", "s44", "s45", "s46", "s47", "s48", \
  "s49", "s50", "s51", "s52", "s53", "s54", "s55", "s56", "s57", "s58", "s59", "s60", "s61", "s62", "s63", \
  "s64", "s65", "s66", "s67", "s68", "s69", "s70", "s71", "s72", "s73", "s74", "s75", "s76", "s77", "s78", \
  "s79", "s80", "s81", "s82", "s83", "s84", "s85", "s86", "s87", "s88", "s89", "s90", "s91", "s92", "s93", \
  "s94", "s95", "scc", "memory"
#define LD_CELL 34560
#define LD_DEG 35584
#define LD_FLAG 35840
#define LD_CRD 35872
#define LD_SINK 35904
#define LD_CGT 36160
#define LD_WQT 36288
#define LD_TAG 37312
#define LD_PAY 37328
#define LD_ENT 100096
#define LD_TB 106816
#define LD_ROWS 108864
#define LD_SK 129344
#define LD_HIST 134464
#define LD_HB 136512
#define WS_EP 32768
#define WS_FB 33024
#define WS_GR 49408
#define WS_RECK 606464
#define WS_RECP 1917184
#define WS_END 7160064

namespace {

constexpr int C_ = 128;
constexpr int NMAX = 40;
constexpr int NB = 256;
constexpr int CH = 1280;
constexpr int MT = 512;
constexpr int MW = MT / 64;
constexpr int RP = 1056;
constexpr unsigned W3 = 0x00020000u;

__device__ __forceinline__ float bf2f(unsigned short v) { return __uint_as_float(((unsigned)v) << 16); }
__device__ __forceinline__ unsigned short f2bf(float f) {
  __hip_bfloat16 b = __float2bfloat16(f);
  return *reinterpret_cast<unsigned short*>(&b);
}
__device__ __forceinline__ unsigned pk2bf(float lo, float hi) {
  return __builtin_bit_cast(unsigned, __builtin_convertvector(f32x2{lo, hi}, bf16x2));
}

}

extern "C" __global__ __launch_bounds__(MT) void tfn_kernel(
    const int* __restrict__ dst, unsigned dn, unsigned dw, const int* __restrict__ src, unsigned sn, unsigned sw,
    const u32x2* __restrict__ Y, unsigned yn, unsigned yw, const u32x2* __restrict__ ef, unsigned fn, unsigned fw,
    char* ws, unsigned wn, unsigned ww, const unsigned short* __restrict__ x, unsigned xn, unsigned xw,
    u32x4* __restrict__ out, unsigned pt, int N, unsigned M2, unsigned cb, int nbk, unsigned npb,
    const unsigned short* __restrict__ wrad, unsigned rn, unsigned rw, const unsigned short* __restrict__ cg, unsigned cn,
    unsigned cw, const unsigned short* __restrict__ wmix, unsigned mn, unsigned mw) {
  __shared__ __attribute__((aligned(16))) char lds[LD_ROWS + NMAX * RP];
  TDECL;
  MSTAMP(0);
  unsigned bad;
  asm volatile(STREAM_ASM
               : [bad] "=&s"(bad)
               : [ka] "s"(__builtin_amdgcn_kernarg_segment_ptr()), [blk] "s"(blockIdx.x), [tid] "v"(threadIdx.x)
               : STREAM_CLOBBERS);
  const bool slow = bad != 0;
  MSTAMP(6);
  if (slow) {
    const int lane = threadIdx.x & 63, w = __builtin_amdgcn_readfirstlane(threadIdx.x >> 6), E = dn / 4;
    const int bucket = (blockIdx.x & 7) * (nbk / 8) + (blockIdx.x >> 3);
    int n01[2];
#pragma unroll
    for (int h = 0; h < 2; h++) {
      unsigned c = (unsigned)(((unsigned long long)(bucket + h) * npb) >> 20);
#pragma unroll
      for (int r = 0; r < 2; r++) c += __umulhi(c, M2) < (unsigned)(bucket + h) ? 1 : 0;
      n01[h] = min(c, (unsigned)N);
    }
    const int node0 = n01[0], nn = n01[1] - n01[0];
    const unsigned mxa = LD_ROWS + ((w >> 2) * 4 + ((lane & 15) >> 2)) * RP + (lane & 3) * 64 + (lane >> 4) * 16;
    const unsigned mxo = ((w >> 2) * 4 + (lane >> 4)) * 1024 + (w & 3) * 256 + (lane & 15) * 16;
    __syncthreads();
#pragma unroll 1
    for (int it = w; it < 2 * nn; it += MW) {
      const int dl = it >> 1, ch = (it & 1) * 64 + lane;
      float acc[4] = {0.f, 0.f, 0.f, 0.f};
#pragma unroll 1
      for (int eb = 0; eb < E; eb += 64) {
        unsigned long long m = __ballot(eb + lane < E && dst[min(eb + lane, E - 1)] == node0 + dl);
#pragma unroll 1
        for (; m; m &= m - 1) {
          const int e = eb + __builtin_ctzll(m);
          const u32x2 ry = Y[e], rf = ef[e];
          float yy[4] = {bf2f(ry[0] & 0xffff), bf2f(ry[0] >> 16), bf2f(ry[1] & 0xffff), bf2f(ry[1] >> 16)};
          float ee[4] = {bf2f(rf[0] & 0xffff), bf2f(rf[0] >> 16), bf2f(rf[1] & 0xffff), bf2f(rf[1] >> 16)};
          float z = 0.f;
#pragma unroll
          for (int k = 0; k < 4; k++) z += ee[k] * bf2f(wrad[k * C_ + ch]);
          const float R = z / (1.f + __expf(-z));
          const unsigned short* xr = x + (size_t)src[e] * 512 + ch * 4;
#pragma unroll 1
          for (int o = 0; o < 4; o++) {
            float tp = 0.f;
#pragma unroll 1
            for (int i = 0; i < 4; i++) {
              float A = 0.f;
#pragma unroll
              for (int f = 0; f < 4; f++) A += yy[f] * bf2f(cg[i * 16 + f * 4 + o]);
              tp += bf2f(xr[i]) * bf2f(f2bf(A));
            }
            const float v = R * tp;
            acc[0] += o == 0 ? v : 0.f;
            acc[1] += o == 1 ? v : 0.f;
            acc[2] += o == 2 ? v : 0.f;
            acc[3] += o == 3 ? v : 0.f;
          }
        }
      }
      unsigned short* ar = reinterpret_cast<unsigned short*>(lds + LD_ROWS) + dl * (RP / 2) + (ch >> 5) * 128 + (ch & 31);
#pragma unroll
      for (int o = 0; o < 4; o++) ar[o * 32] = f2bf(acc[o]);
    }
    __syncthreads();
    const auto ors = __builtin_amdgcn_make_buffer_rsrc((void*)(out + (size_t)node0 * 64), 0, nn * 1024, 0x00020000);
#pragma unroll 1
    for (int t = 0; t < NMAX / 8; t++) {
      f32x4 a = {0.f, 0.f, 0.f, 0.f}, b = {0.f, 0.f, 0.f, 0.f};
#pragma unroll 1
      for (int k = 0; k < 4; k++) {
        const s16x8 f = *reinterpret_cast<const s16x8*>(lds + mxa + t * 8 * RP + k * 256);
        const unsigned short* wp = wmix + (32 * k + 8 * (lane >> 4)) * C_ + 32 * (w & 3) + 2 * (lane & 15);
        s16x8 w0, w1;
#pragma unroll
        for (int j = 0; j < 8; j++) {
          w0[j] = wp[j * C_];
          w1[j] = wp[j * C_ + 1];
        }
        a = __builtin_amdgcn_mfma_f32_16x16x32_bf16(f, w0, a, 0, 0, 0);
        b = __builtin_amdgcn_mfma_f32_16x16x32_bf16(f, w1, b, 0, 0, 0);
      }
      __builtin_amdgcn_raw_buffer_store_b128(u32x4{pk2bf(a[0], a[1]), pk2bf(a[2], a[3]), pk2bf(b[0], b[1]), pk2bf(b[2], b[3])},
                                             ors, mxo + t * 8192, 0, 0);
    }
  }
  MFLUSH;
}

extern "C" void kernel_launch(void* const* d_in, const int* in_sizes, int n_in, void* d_out, int out_size,
                              void* d_ws, size_t ws_size, hipStream_t stream) {
  const int N = in_sizes[0] / 512;
  const int E = in_sizes[1];
  int nbk = NB;
  while ((N + nbk - 1) / nbk > NMAX || (N % nbk == 0 && N / nbk >= NMAX)) nbk += NB;
  const unsigned long long M2l = (((unsigned long long)nbk) << 32) / (unsigned long long)N;
  const unsigned M2 = M2l > 0xffffffffull ? 0xffffffffu : (unsigned)M2l;
  const unsigned long long cb = (16ull << 32) / (unsigned long long)N;
  const bool fast = nbk == NB && E <= NB * CH && ws_size >= WS_END;
  const unsigned e4 = E * 4u;
  tfn_kernel<<<nbk, MT, 0, stream>>>(
      (const int*)d_in[2], e4, W3, (const int*)d_in[1], e4, W3, (const u32x2*)d_in[3], 2 * e4, W3, (const u32x2*)d_in[4], 2 * e4, W3,
      (char*)d_ws, WS_END, W3, (const unsigned short*)d_in[0], N * 1024u, W3, (u32x4*)d_out, fast ? 448u : 0u, N, M2,
      cb > 0xffffffull ? 0xffffffu : (unsigned)cb, nbk, (unsigned)((1ull << 52) / M2), (const unsigned short*)d_in[5], 4 * C_ * 2,
      W3, (const unsigned short*)d_in[6], 128, W3, (const unsigned short*)d_in[7], C_ * C_ * 2, W3);
}
```

```cpp
#include <hip/hip_runtime.h>
#include <hip/hip_bf16.h>
#include <stdint.h>
#include <stddef.h>

typedef __attribute__((ext_vector_type(2))) float f32x2;
typedef __attribute__((ext_vector_type(4))) float f32x4;
typedef __attribute__((ext_vector_type(8))) short s16x8;
typedef __attribute__((ext_vector_type(2))) unsigned int u32x2;
typedef __attribute__((ext_vector_type(4))) unsigned int u32x4;
typedef __attribute__((ext_vector_type(16))) unsigned int u32x16;
typedef __attribute__((ext_vector_type(2))) __bf16 bf16x2;

#define TDECL
#define TSTAMP(i)
#define TFLUSH
#define MSTAMP(i) TSTAMP(i)
#define MFLUSH do { asm volatile("s_waitcnt vmcnt(0)" ::: "memory"); TSTAMP(15); TFLUSH; } while (0)
#undef MFLUSH
#define MFLUSH

#define STREAM_ASM \
  "s_load_dwordx16 s[8:23], %[ka], 0\n" \
  "s_load_dwordx16 s[24:39], %[ka], 64\n" \
  "s_memrealtime s[76:77]\n" \
  "s_getpc_b64 s[40:41]\n" \
  "Lpc%=:\n" \
  "v_lshlrev_b32 v50, 7, %[tid]\n" \
  "s_mov_b32 s42, Lout%=-Lpc%=\n" \
  "s_mov_b32 s43, 0x20000\n" \
  "buffer_load_dword v208, v50, s[40:43], 0 offen\n" \
  "v_lshlrev_b32 v52, 3, %[tid]\n" \
  "v_lshlrev_b32 v53, 2, %[tid]\n" \
  "v_mov_b32 v48, 0\n" \
  "v_readfirstlane_b32 s44, %[tid]\n" \
  "v_add_u32 v49, 134464, v53\n" \
  "ds_write_b32 v49, v48\n" \
  "s_mul_i32 s63, %[blk], 5120\n" \
  "s_add_u32 s61, s63, 3584\n" \
  "s_cmp_lt_u32 s44, 384\n" \
  "s_cselect_b32 s61, s61, 0x40000000\n" \
  "s_waitcnt lgkmcnt(0)\n" \
  "s_cmp_lt_u32 s44, s34\n" \
  "s_cbranch_scc0 Lh0_%=\n" \
  "buffer_load_dwordx2 v[100:101], v52, s[8:11], s63 offen nt\n" \
  "buffer_load_dword v102, v53, s[8:11], s61 offen nt\n" \
  "s_barrier\n" \
  "buffer_load_dwordx2 v[104:105], v52, s[12:15], s63 offen nt\n" \
  "buffer_load_dword v106, v53, s[12:15], s61 offen nt\n" \
  "v_lshlrev_b32 v49, 1, v52\n" \
  "s_lshl_b32 s93, s63, 1\n" \
  "s_lshl_b32 s62, s61, 1\n" \
  "buffer_load_dwordx4 v[108:111], v49, s[16:19], s93 offen nt\n" \
  "buffer_load_dwordx4 v[114:117], v49, s[20:23], s93 offen nt\n" \
  "buffer_load_dwordx2 v[112:113], v52, s[16:19], s62 offen nt\n" \
  "buffer_load_dwordx2 v[118:119], v52, s[20:23], s62 offen nt\n" \
  "s_sub_i32 s63, s10, s63\n" \
  "s_sub_i32 s61, s10, s61\n" \
  "v_cmp_gt_i32 s[80:81], s63, v52\n" \
  "s_sub_i32 s63, s63, 4\n" \
  "v_cmp_gt_i32 s[84:85], s61, v53\n" \
  "v_cmp_gt_i32 s[82:83], s63, v52\n" \
  "s_branch Lh1_%=\n" \
  "Lh0_%=:\n" \
  "s_mov_b32 s93, 32768\n" \
  "buffer_load_dwordx2 v[88:89], off, s[24:27], s93\n" \
  "s_lshl_b32 s93, %[blk], 7\n" \
  "v_mov_b32 v51, s76\n" \
  "s_add_u32 s93, s93, 49408\n" \
  "s_mov_b64 exec, 1\n" \
  "buffer_store_dword v51, off, s[24:27], s93 sc1\n" \
  "s_mov_b64 exec, -1\n" \
  "s_barrier\n" \
  "s_load_dwordx8 s[8:15], %[ka], 128\n" \
  "s_load_dwordx4 s[16:19], %[ka], 160\n" \
  "s_mov_b64 s[80:81], 0\n" \
  "s_mov_b64 s[82:83], 0\n" \
  "s_mov_b64 s[84:85], 0\n" \
  "s_cmp_eq_u32 s34, 0\n" \
  "s_cbranch_scc1 Lnf%=\n" \
  "Lh1_%=:\n" \
  "ds_write_b32 v53, v48 offset:34560\n" \
  "s_lshr_b32 s44, s44, 6\n" \
  "v_and_b32 v86, 63, %[tid]\n" \
  "s_lshl_b32 s72, s44, 3\n" \
  "s_lshl_b32 s63, 1, s72\n" \
  "v_mov_b32 v93, s63\n" \
  "s_and_b32 s45, s72, 24\n" \
  "s_lshl_b64 s[46:47], -1, s72\n" \
  "s_and_b32 s46, s46, 0x01010101\n" \
  "s_and_b32 s47, s47, 0x01010101\n" \
  "s_and_b32 s73, s44, 4\n" \
  "s_add_u32 s74, s73, 134464\n" \
  "s_lshl_b32 s75, s44, 9\n" \
  "s_add_u32 s75, s75, 136512\n" \
  "v_lshlrev_b32 v13, 4, v86\n" \
  "v_lshlrev_b32 v54, 2, v86\n" \
  "v_lshl_add_u32 v52, v86, 3, s75\n" \
  "v_lshlrev_b32 v50, 5, v86\n" \
  "v_add_u32 v50, 134464, v50\n" \
  "s_and_b32 s71, %[blk], 7\n" \
  "s_lshr_b32 s66, %[blk], 3\n" \
  "s_lshl_b32 s68, s71, 5\n" \
  "s_add_u32 s68, s68, s66\n" \
  "s_and_b32 s61, s68, 2\n" \
  "s_lshl_b32 s61, s61, 9\n" \
  "v_and_b32 v55, 0xf8, v54\n" \
  "s_and_b32 s63, s68, 0xfc\n" \
  "s_lshl2_add_u32 s61, s63, s61\n" \
  "v_add_u32 v55, s44, v55\n" \
  "s_and_b32 s63, s68, 1\n" \
  "s_lshl3_add_u32 s61, s63, s61\n" \
  "v_mul_u32_u24 v17, 2176, v55\n" \
  "s_add_u32 s61, s61, 82176\n" \
  "v_mov_b32 v94, 36096\n" \
  "s_mul_i32 s63, s71, s35\n" \
  "s_lshr_b32 s63, s63, 3\n" \
  "s_lshl3_add_u32 s63, s66, s63\n" \
  "s_add_u32 s66, s63, s44\n" \
  "s_lshl_b32 s66, s66, 10\n" \
  "v_lshlrev_b32 v20, 3, v13\n" \
  "v_cmp_gt_u32 vcc, 8, v86\n" \
  "v_bfrev_b32 v51, 1\n" \
  "s_mov_b32 s79, m0\n" \
  "s_mov_b32 m0, 36160\n" \
  "v_cndmask_b32 v20, v51, v20, vcc\n" \
  "s_mov_b32 s63, 0x01010101\n" \
  "s_waitcnt vmcnt(6)\n" \
  "v_mul_hi_u32 v96, v100, s36\n" \
  "v_mul_hi_u32 v97, v101, s36\n" \
  "v_mul_hi_u32 v98, v102, s36\n" \
  "s_mov_b64 exec, s[80:81]\n" \
  "v_lshl_add_u32 v120, v96, 3, s74\n" \
  "ds_add_rtn_u32 v124, v120, v93\n" \
  "s_mov_b64 exec, s[82:83]\n" \
  "v_lshl_add_u32 v121, v97, 3, s74\n" \
  "ds_add_rtn_u32 v125, v121, v93\n" \
  "s_mov_b64 exec, s[84:85]\n" \
  "v_lshl_add_u32 v122, v98, 3, s74\n" \
  "ds_add_rtn_u32 v126, v122, v93\n" \
  "s_mov_b64 exec, -1\n" \
  "v_lshl_add_u32 v96, v96, 1, s75\n" \
  "v_lshl_add_u32 v97, v97, 1, s75\n" \
  "v_lshl_add_u32 v98, v98, 1, s75\n" \
  "s_waitcnt lgkmcnt(0)\n" \
  "s_barrier\n" \
  "ds_read_b128 v[128:131], v50\n" \
  "ds_read_b128 v[132:135], v50 offset:16\n" \
  "v_bfe_u32 v124, v124, s45, 8\n" \
  "v_bfe_u32 v125, v125, s45, 8\n" \
  "v_bfe_u32 v126, v126, s45, 8\n" \
  "s_waitcnt lgkmcnt(0)\n" \
  "v_dot4_u32_u8 v136, v128, s63, 0\n" \
  "v_dot4_u32_u8 v136, v129, s63, v136\n" \
  "v_dot4_u32_u8 v137, v130, s63, v136\n" \
  "v_dot4_u32_u8 v137, v131, s63, v137\n" \
  "v_dot4_u32_u8 v138, v132, s63, v137\n" \
  "v_dot4_u32_u8 v138, v133, s63, v138\n" \
  "v_dot4_u32_u8 v139, v134, s63, v138\n" \
  "v_dot4_u32_u8 v139, v135, s63, v139\n" \
  "v_dot4_u32_u8 v140, v128, s46, 0\n" \
  "v_dot4_u32_u8 v141, v130, s46, 0\n" \
  "v_dot4_u32_u8 v142, v132, s46, 0\n" \
  "v_add_u32_dpp v87, v139, v139 row_shr:1 row_mask:0xf bank_mask:0xf bound_ctrl:1\n" \
  "v_dot4_u32_u8 v143, v134, s46, 0\n" \
  "v_dot4_u32_u8 v140, v129, s47, v140\n" \
  "v_add_u32_dpp v87, v87, v87 row_shr:2 row_mask:0xf bank_mask:0xf bound_ctrl:1\n" \
  "v_dot4_u32_u8 v141, v131, s47, v141\n" \
  "v_dot4_u32_u8 v142, v133, s47, v142\n" \
  "v_add_u32_dpp v87, v87, v87 row_shr:4 row_mask:0xf bank_mask:0xf bound_ctrl:1\n" \
  "v_dot4_u32_u8 v143, v135, s47, v143\n" \
  "s_mov_b32 s63, 0x10001\n" \
  "v_add_u32_dpp v87, v87, v87 row_shr:8 row_mask:0xf bank_mask:0xf bound_ctrl:1\n" \
  "v_sub_u32 v140, v136, v140\n" \
  "v_sub_u32 v141, v137, v141\n" \
  "v_add_u32_dpp v87, v87, v87 row_bcast:15 row_mask:0xa bank_mask:0xf\n" \
  "v_sub_u32 v142, v138, v142\n" \
  "v_sub_u32 v143, v139, v143\n" \
  "v_add_u32_dpp v87, v87, v87 row_bcast:31 row_mask:0xc bank_mask:0xf\n" \
  "v_lshl_or_b32 v140, v141, 16, v140\n" \
  "v_sub_u32 v87, v87, v139\n" \
  "v_lshl_or_b32 v141, v143, 16, v142\n" \
  "s_cmp_eq_u32 s44, 7\n" \
  "v_mad_u32_u24 v140, v87, s63, v140\n" \
  "v_mad_u32_u24 v141, v87, s63, v141\n" \
  "s_cbranch_scc1 Lh2_%=\n" \
  "ds_write_b64 v52, v[140:141]\n" \
  "ds_read_u16 v96, v96\n" \
  "ds_read_u16 v97, v97\n" \
  "ds_read_u16 v98, v98\n" \
  "s_mov_b32 s63, 129344\n" \
  "s_mov_b32 s93, 108864\n" \
  "s_lshl_b32 s74, s44, 10\n" \
  "v_add_u32 v50, s74, v13\n" \
  "v_add_u32 v51, s93, v50\n" \
  "s_waitcnt vmcnt(4)\n" \
  "v_lshl_or_b32 v104, v100, 24, v104\n" \
  "v_lshl_or_b32 v105, v101, 24, v105\n" \
  "v_lshl_or_b32 v106, v102, 24, v106\n" \
  "s_waitcnt lgkmcnt(0)\n" \
  "s_mov_b64 exec, s[80:81]\n" \
  "v_add_u32 v124, v124, v96\n" \
  "v_lshl_add_u32 v120, v124, 2, s63\n" \
  "v_lshl_add_u32 v124, v124, 4, s93\n" \
  "ds_write_b32 v120, v104\n" \
  "s_mov_b64 exec, s[82:83]\n" \
  "v_add_u32 v125, v125, v97\n" \
  "v_lshl_add_u32 v121, v125, 2, s63\n" \
  "v_lshl_add_u32 v125, v125, 4, s93\n" \
  "ds_write_b32 v121, v105\n" \
  "s_mov_b64 exec, s[84:85]\n" \
  "v_add_u32 v126, v126, v98\n" \
  "v_lshl_add_u32 v122, v126, 2, s63\n" \
  "v_lshl_add_u32 v126, v126, 4, s93\n" \
  "ds_write_b32 v122, v106\n" \
  "s_mul_i32 s72, %[blk], 20480\n" \
  "s_lshr_b32 s62, s72, 2\n" \
  "s_add_u32 s72, s72, 1949952\n" \
  "s_add_u32 s62, s62, 640256\n" \
  "s_add_u32 s74, s72, 7168\n" \
  "s_add_u32 s75, s72, 14336\n" \
  "s_sub_u32 s63, s62, 7168\n" \
  "s_cmp_eq_u32 s44, 6\n" \
  "s_cselect_b32 s75, s63, s75\n" \
  "s_waitcnt vmcnt(0)\n" \
  "ds_write2_b64 v126, v[112:113], v[118:119] offset1:1\n" \
  "s_mov_b64 exec, s[82:83]\n" \
  "ds_write2_b64 v125, v[110:111], v[116:117] offset1:1\n" \
  "s_mov_b64 exec, s[80:81]\n" \
  "ds_write2_b64 v124, v[108:109], v[114:115] offset1:1\n" \
  "s_mov_b64 exec, -1\n" \
  "s_cmp_gt_u32 s44, 3\n" \
  "s_waitcnt lgkmcnt(0)\n" \
  "s_barrier\n" \
  "ds_read_b128 v[216:219], v51 offset:0\n" \
  "ds_read_b128 v[220:223], v51 offset:7168\n" \
  "ds_read_b128 v[224:227], v51 offset:14336\n" \
  "ds_read_b128 v[228:231], v51 offset:21504\n" \
  "s_waitcnt lgkmcnt(3)\n" \
  "buffer_store_dwordx4 v[216:219], v50, s[24:27], s72 offen sc1\n" \
  "s_waitcnt lgkmcnt(2)\n" \
  "buffer_store_dwordx4 v[220:223], v50, s[24:27], s74 offen sc1\n" \
  "s_waitcnt lgkmcnt(1)\n" \
  "buffer_store_dwordx4 v[224:227], v50, s[24:27], s75 offen sc1\n" \
  "s_cbranch_scc1 Lc1_%=\n" \
  "s_waitcnt lgkmcnt(0)\n" \
  "buffer_store_dwordx4 v[228:231], v50, s[24:27], s62 offen sc1\n" \
  "Lc1_%=:\n" \
  "s_mov_b32 s63, 0x80008\n" \
  "s_mov_b32 s93, 0x80008000\n" \
  "v_mul_lo_u32 v52, %[tid], s63\n" \
  "v_lshlrev_b32 v53, 4, %[tid]\n" \
  "v_add_u32 v49, 0x10000, v53\n" \
  "v_cmp_gt_u32 s[90:91], 32, %[tid]\n" \
  "v_cmp_gt_u32 vcc, 368, %[tid]\n" \
  "v_add_u32 v104, 0xf810f80, v52\n" \
  "v_add_u32 v105, 0xf830f82, v52\n" \
  "v_add_u32 v106, 0xf850f84, v52\n" \
  "v_add_u32 v107, 0xf870f86, v52\n" \
  "v_or_b32 v108, s93, v104\n" \
  "v_or_b32 v109, s93, v105\n" \
  "v_or_b32 v110, s93, v106\n" \
  "v_or_b32 v111, s93, v107\n" \
  "ds_write_b128 v53, v[104:107] offset:8192\n" \
  "ds_write_b128 v49, v[108:111] offset:8192\n" \
  "v_add_u32 v112, 0x1f811f80, v52\n" \
  "v_add_u32 v113, 0x1f831f82, v52\n" \
  "v_add_u32 v114, 0x1f851f84, v52\n" \
  "v_add_u32 v115, 0x1f871f86, v52\n" \
  "v_or_b32 v116, s93, v112\n" \
  "v_or_b32 v117, s93, v113\n" \
  "v_or_b32 v118, s93, v114\n" \
  "v_or_b32 v119, s93, v115\n" \
  "ds_write_b128 v53, v[112:115] offset:16384\n" \
  "ds_write_b128 v49, v[116:119] offset:16384\n" \
  "s_mov_b64 exec, vcc\n" \
  "v_add_u32 v120, 0x2f812f80, v52\n" \
  "v_add_u32 v121, 0x2f832f82, v52\n" \
  "v_add_u32 v122, 0x2f852f84, v52\n" \
  "v_add_u32 v123, 0x2f872f86, v52\n" \
  "v_or_b32 v124, s93, v120\n" \
  "v_or_b32 v125, s93, v121\n" \
  "v_or_b32 v126, s93, v122\n" \
  "v_or_b32 v127, s93, v123\n" \
  "ds_write_b128 v53, v[120:123] offset:24576\n" \
  "ds_write_b128 v49, v[124:127] offset:24576\n" \
  "s_mov_b64 exec, -1\n" \
  "v_add_u32 v96, 0xff80ff80, v52\n" \
  "v_add_u32 v97, 0xff82ff82, v52\n" \
  "v_add_u32 v98, 0xff84ff84, v52\n" \
  "v_add_u32 v99, 0xff86ff86, v52\n" \
  "v_lshrrev_b32 v52, 1, v52\n" \
  "s_mov_b64 exec, s[90:91]\n" \
  "v_add_u32 v96, 0x0, v52\n" \
  "v_add_u32 v97, 0x20001, v52\n" \
  "v_add_u32 v98, 0x20002, v52\n" \
  "v_add_u32 v99, 0x40003, v52\n" \
  "s_mov_b64 exec, -1\n" \
  "v_or_b32 v100, s93, v96\n" \
  "v_or_b32 v101, s93, v97\n" \
  "v_or_b32 v102, s93, v98\n" \
  "v_or_b32 v103, s93, v99\n" \
  "ds_write_b128 v53, v[96:99] offset:0\n" \
  "ds_write_b128 v49, v[100:103] offset:0\n" \
  "buffer_load_dwordx2 v[44:45], v17, s[24:27], s61 offen sc1\n" \
  "s_mov_b64 s[88:89], 0\n" \
  "s_mov_b32 s92, 0\n" \
  "v_mov_b32 v92, 1\n" \
  "v_and_b32 v41, 4, v54\n" \
  "s_movk_i32 s63, 5120\n" \
  "v_mad_u32_u24 v40, v55, s63, v41\n" \
  "s_lshl_b32 s63, s44, 10\n" \
  "s_add_u32 s63, s63, 37584\n" \
  "v_add_u32 v41, s63, v41\n" \
  "s_mul_i32 s90, s68, s39\n" \
  "s_mul_hi_u32 s91, s68, s39\n" \
  "s_lshr_b64 s[90:91], s[90:91], 20\n" \
  "s_mul_hi_u32 s63, s90, s36\n" \
  "s_cmp_lt_u32 s63, s68\n" \
  "s_addc_u32 s90, s90, 0\n" \
  "s_mul_hi_u32 s63, s90, s36\n" \
  "s_cmp_lt_u32 s63, s68\n" \
  "s_addc_u32 s90, s90, 0\n" \
  "s_min_u32 s67, s90, s35\n" \
  "s_add_u32 s68, s68, 1\n" \
  "s_mul_i32 s90, s68, s39\n" \
  "s_mul_hi_u32 s91, s68, s39\n" \
  "s_lshr_b64 s[90:91], s[90:91], 20\n" \
  "s_mul_hi_u32 s63, s90, s36\n" \
  "s_cmp_lt_u32 s63, s68\n" \
  "s_addc_u32 s90, s90, 0\n" \
  "s_mul_hi_u32 s63, s90, s36\n" \
  "s_cmp_lt_u32 s63, s68\n" \
  "s_addc_u32 s90, s90, 0\n" \
  "s_min_u32 s68, s90, s35\n" \
  "s_sub_u32 s68, s68, s67\n" \
  "s_lshl_b32 s71, s71, 1\n" \
  "v_lshlrev_b32 v50, 5, v86\n" \
  "v_and_b32 v50, 0x60, v50\n" \
  "v_bfrev_b32 v51, 1\n" \
  "s_lshl_b32 s63, %[blk], 6\n" \
  "s_add_u32 s63, s63, 33024\n" \
  "s_cmp_lg_u32 s44, 7\n" \
  "s_waitcnt vmcnt(1) lgkmcnt(0)\n" \
  "s_barrier\n" \
  "s_cbranch_scc1 Ls1_%=\n" \
  "s_mov_b64 exec, 1\n" \
  "buffer_store_dwordx2 v[88:89], off, s[24:27], s63 sc1\n" \
  "s_mov_b64 exec, -1\n" \
  "Ls1_%=:\n" \
  "ds_read_b96 v[56:58], v94 offset:1472\n" \
  "ds_read_b128 v[24:27], v50 offset:36416\n" \
  "ds_read_b128 v[28:31], v50 offset:36432\n" \
  "ds_read_b64 v[32:33], v13 offset:36544\n" \
  "ds_read_b64 v[36:37], v13 offset:36552\n" \
  "s_waitcnt lgkmcnt(4)\n" \
  "v_readfirstlane_b32 s50, v56\n" \
  "v_readfirstlane_b32 s51, v57\n" \
  "v_readfirstlane_b32 s69, v58\n" \
  "s_lshl_b32 s64, s50, 22\n" \
  "s_lshr_b64 s[90:91], s[50:51], 10\n" \
  "s_mov_b32 s65, s90\n" \
  "s_cmp_lg_u32 s44, 7\n" \
  "s_waitcnt vmcnt(1)\n" \
  "s_cbranch_scc0 Lchk%=\n" \
  "s_waitcnt vmcnt(0)\n" \
  "s_branch Lchk%=\n" \
  "Lpoll%=:\n" \
  "buffer_load_dwordx2 v[44:45], v17, s[24:27], s61 offen sc1\n" \
  "s_waitcnt vmcnt(0)\n" \
  "Lchk%=:\n" \
  "v_and_b32 v50, 0xffc00000, v44\n" \
  "v_cmp_eq_u32 s[90:91], s65, v45\n" \
  "v_cmp_eq_u32 vcc, s64, v50\n" \
  "s_and_b64 vcc, vcc, s[90:91]\n" \
  "s_andn2_b64 vcc, vcc, s[88:89]\n" \
  "s_or_b64 s[88:89], s[88:89], vcc\n" \
  "v_cndmask_b32 v48, v48, v44, vcc\n" \
  "s_cmp_eq_u64 s[88:89], -1\n" \
  "v_cndmask_b32 v17, v17, v51, vcc\n" \
  "s_cbranch_scc1 Lseen%=\n" \
  "s_add_u32 s92, s92, 1\n" \
  "s_cmp_lt_u32 s92, 4096\n" \
  "s_cbranch_scc1 Lpoll%=\n" \
  "v_mov_b32 v48, 0\n" \
  "Lseen%=:\n" \
  "buffer_load_dword v20, s[28:31], s66 offen lds\n" \
  "s_add_u32 s66, s66, 0x40000\n" \
  "buffer_load_dword v20, s[28:31], s66 offen lds\n" \
  "s_add_u32 s66, s66, 0x40000\n" \
  "v_bfe_u32 v49, v48, 11, 11\n" \
  "v_and_b32 v50, 0x7ff, v48\n" \
  "v_bfe_u32 v52, v13, 4, 1\n" \
  "v_add_u32_dpp v87, v49, v49 row_shr:1 row_mask:0xf bank_mask:0xf bound_ctrl:1\n" \
  "v_lshl_add_u32 v50, v50, 2, v40\n" \
  "v_lshlrev_b32 v53, v52, v49\n" \
  "v_add_u32_dpp v87, v87, v87 row_shr:2 row_mask:0xf bank_mask:0xf bound_ctrl:1\n" \
  "v_sub_u32 v49, v49, v52\n" \
  "v_and_b32 v85, -8, v41\n" \
  "v_add_u32_dpp v87, v87, v87 row_shr:4 row_mask:0xf bank_mask:0xf bound_ctrl:1\n" \
  "v_add_u32 v85, v85, v54\n" \
  "s_nop 0\n" \
  "v_add_u32_dpp v87, v87, v87 row_shr:8 row_mask:0xf bank_mask:0xf bound_ctrl:1\n" \
  "s_nop 1\n" \
  "v_add_u32_dpp v87, v87, v87 row_bcast:15 row_mask:0xa bank_mask:0xf\n" \
  "s_nop 1\n" \
  "v_add_u32_dpp v87, v87, v87 row_bcast:31 row_mask:0xc bank_mask:0xf\n" \
  "v_sub_u32 v53, v87, v53\n" \
  "v_readlane_b32 s62, v87, 63\n" \
  "v_lshl_add_u32 v53, v53, 1, v41\n" \
  "v_lshl_add_u32 v49, v49, 2, v53\n" \
  "s_lshr_b32 s62, s62, 1\n" \
  "s_cmp_lt_u32 s62, 256\n" \
  "s_cbranch_scc1 Ll0_%=\n" \
  "s_mov_b32 s62, 0\n" \
  "ds_or_b32 v94, v92\n" \
  "s_branch Ll2_%=\n" \
  "Ll0_%=:\n" \
  "v_cmp_lt_i32 vcc, v53, v49\n" \
  "s_mov_b64 exec, vcc\n" \
  "Ll1_%=:\n" \
  "ds_write_b32 v53, v50\n" \
  "v_add_u32 v53, 8, v53\n" \
  "v_cmp_lt_i32 vcc, v53, v49\n" \
  "v_add_u32 v50, 8, v50\n" \
  "s_and_b64 exec, vcc, vcc\n" \
  "s_cbranch_scc1 Ll1_%=\n" \
  "s_mov_b64 exec, -1\n" \
  "Ll2_%=:\n" \
  "ds_read2st64_b32 v[56:57], v85 offset1:1\n" \
  "ds_read2st64_b32 v[58:59], v85 offset0:2 offset1:3\n" \
  "v_mov_b32 v50, s62\n" \
  "s_mov_b64 exec, 1\n" \
  "ds_add_u32 v94, v50 offset:4\n" \
  "s_mov_b64 exec, -1\n" \
  "v_cmp_gt_i32 s[80:81], s62, v86\n" \
  "s_sub_i32 s63, s62, 64\n" \
  "v_cmp_gt_i32 s[82:83], s63, v86\n" \
  "s_sub_i32 s63, s62, 128\n" \
  "v_cmp_gt_i32 s[84:85], s63, v86\n" \
  "s_sub_i32 s63, s62, 192\n" \
  "v_cmp_gt_i32 s[86:87], s63, v86\n" \
  "v_bfrev_b32 v50, 4\n" \
  "s_waitcnt lgkmcnt(1)\n" \
  "v_cndmask_b32 v56, v50, v56, s[80:81]\n" \
  "v_cndmask_b32 v57, v50, v57, s[82:83]\n" \
  "v_cndmask_b32 v58, v50, v58, s[84:85]\n" \
  "v_cndmask_b32 v59, v50, v59, s[86:87]\n" \
  "s_sleep 4\n" \
  "v_mul_u32_u24 v18, 64, v55\n" \
  "s_mov_b32 s93, 33024\n" \
  "s_mov_b64 s[88:89], 0\n" \
  "s_mov_b32 s63, 0xbfb8aa3b\n" \
  "v_lshlrev_b32 v100, 17, %[tid]\n" \
  "v_add_u32 v103, 0x3f810000, v100\n" \
  "v_add_u32 v102, 0x3f800000, v100\n" \
  "v_add_u32 v101, 0x3b810000, v100\n" \
  "v_add_u32 v100, 0x3b800000, v100\n" \
  "v_mul_f32 v104, s63, v100\n" \
  "v_mul_f32 v105, s63, v101\n" \
  "v_mul_f32 v106, s63, v102\n" \
  "v_mul_f32 v107, s63, v103\n" \
  "v_exp_f32 v104, v104\n" \
  "v_exp_f32 v105, v105\n" \
  "v_exp_f32 v106, v106\n" \
  "v_exp_f32 v107, v107\n" \
  "v_lshlrev_b32 v96, 2, %[tid]\n" \
  "v_add_f32 v108, 1.0, v104\n" \
  "v_add_f32 v109, 1.0, v105\n" \
  "v_add_f32 v110, 1.0, v106\n" \
  "v_add_f32 v111, 1.0, v107\n" \
  "v_rcp_f32 v108, v108\n" \
  "v_rcp_f32 v109, v109\n" \
  "v_rcp_f32 v110, v110\n" \
  "v_rcp_f32 v111, v111\n" \
  "v_add_u32 v97, 0x10000, v96\n" \
  "v_pk_mul_f32 v[100:101], v[100:101], v[108:109]\n" \
  "v_pk_mul_f32 v[102:103], v[102:103], v[110:111]\n" \
  "v_pk_mul_f32 v[104:105], v[104:105], v[100:101] neg_lo:[0,1] neg_hi:[0,1]\n" \
  "v_pk_mul_f32 v[106:107], v[106:107], v[102:103] neg_lo:[0,1] neg_hi:[0,1]\n" \
  "v_cvt_pk_bf16_f32 v100, v100, v101\n" \
  "v_cvt_pk_bf16_f32 v102, v102, v103\n" \
  "v_cvt_pk_bf16_f32 v104, v104, v105\n" \
  "v_cvt_pk_bf16_f32 v106, v106, v107\n" \
  "ds_write2st64_b32 v96, v100, v102 offset0:119 offset1:127\n" \
  "ds_write2st64_b32 v97, v104, v106 offset0:119 offset1:127\n" \
  "buffer_load_dwordx2 v[44:45], v18, s[24:27], s93 offen sc1\n" \
  "v_mov_b64 v[128:129], 0\n" \
  "v_mov_b64 v[130:131], 0\n" \
  "v_mov_b64 v[132:133], 0\n" \
  "v_mov_b64 v[134:135], 0\n" \
  "v_mov_b64 v[136:137], 0\n" \
  "v_mov_b64 v[138:139], 0\n" \
  "v_mov_b64 v[140:141], 0\n" \
  "v_mov_b64 v[142:143], 0\n" \
  "v_mov_b64 v[144:145], 0\n" \
  "v_mov_b64 v[146:147], 0\n" \
  "v_mov_b64 v[148:149], 0\n" \
  "v_mov_b64 v[150:151], 0\n" \
  "v_mov_b64 v[152:153], 0\n" \
  "v_mov_b64 v[154:155], 0\n" \
  "v_mov_b64 v[156:157], 0\n" \
  "v_mov_b64 v[158:159], 0\n" \
  "v_mov_b64 v[160:161], 0\n" \
  "v_mov_b64 v[162:163], 0\n" \
  "v_mov_b64 v[164:165], 0\n" \
  "v_mov_b64 v[166:167], 0\n" \
  "v_mov_b64 v[168:169], 0\n" \
  "v_mov_b64 v[170:171], 0\n" \
  "v_mov_b64 v[172:173], 0\n" \
  "v_mov_b64 v[174:175], 0\n" \
  "v_mov_b64 v[176:177], 0\n" \
  "v_mov_b64 v[178:179], 0\n" \
  "v_mov_b64 v[180:181], 0\n" \
  "v_mov_b64 v[182:183], 0\n" \
  "v_mov_b64 v[184:185], 0\n" \
  "v_mov_b64 v[186:187], 0\n" \
  "v_mov_b64 v[188:189], 0\n" \
  "v_mov_b64 v[190:191], 0\n" \
  "v_mov_b64 v[192:193], 0\n" \
  "v_mov_b64 v[194:195], 0\n" \
  "v_mov_b64 v[196:197], 0\n" \
  "v_mov_b64 v[198:199], 0\n" \
  "v_mov_b64 v[200:201], 0\n" \
  "v_mov_b64 v[202:203], 0\n" \
  "v_mov_b64 v[204:205], 0\n" \
  "v_mov_b64 v[206:207], 0\n" \
  "s_sleep 4\n" \
  "Ld1_%=:\n" \
  "buffer_load_dwordx2 v[46:47], v18, s[24:27], s93 offen sc1\n" \
  "s_waitcnt vmcnt(1)\n" \
  "v_cmp_eq_u32 s[90:91], s65, v45\n" \
  "v_cmp_eq_u32 vcc, s64, v44\n" \
  "s_mov_b32 s63, 639232\n" \
  "s_and_b64 vcc, vcc, s[90:91]\n" \
  "s_or_b64 s[88:89], s[88:89], vcc\n" \
  "s_cmp_eq_u64 s[88:89], -1\n" \
  "s_cbranch_scc1 Ld2_%=\n" \
  "v_cndmask_b32 v18, v18, v51, vcc\n" \
  "buffer_load_dwordx2 v[44:45], v18, s[24:27], s93 offen sc1\n" \
  "s_waitcnt vmcnt(1)\n" \
  "v_cmp_eq_u32 s[90:91], s65, v47\n" \
  "v_cmp_eq_u32 vcc, s64, v46\n" \
  "s_mov_b32 s63, 639232\n" \
  "s_and_b64 vcc, vcc, s[90:91]\n" \
  "s_or_b64 s[88:89], s[88:89], vcc\n" \
  "s_cmp_eq_u64 s[88:89], -1\n" \
  "s_cbranch_scc1 Ld2_%=\n" \
  "v_cndmask_b32 v18, v18, v51, vcc\n" \
  "s_add_u32 s92, s92, 1\n" \
  "s_cmp_lt_u32 s92, 4096\n" \
  "s_cbranch_scc1 Ld1_%=\n" \
  "s_mov_b64 s[80:81], 0\n" \
  "s_mov_b64 s[82:83], 0\n" \
  "s_mov_b64 s[84:85], 0\n" \
  "s_mov_b64 s[86:87], 0\n" \
  "Ld2_%=:\n" \
  "s_mov_b32 s93, 1949952\n" \
  "s_cmp_le_i32 s62, 192\n" \
  "s_cbranch_scc1 Lk537_%=\n" \
  "buffer_load_dword v63, v59, s[24:27], s63 offen sc1\n" \
  "v_lshlrev_b32 v59, 2, v59\n" \
  "buffer_load_dwordx4 v[228:231], v59, s[24:27], s93 offen sc1\n" \
  "Lk537_%=:\n" \
  "buffer_load_dword v60, v56, s[24:27], s63 offen sc1\n" \
  "buffer_load_dword v61, v57, s[24:27], s63 offen sc1\n" \
  "buffer_load_dword v62, v58, s[24:27], s63 offen sc1\n" \
  "v_lshlrev_b32 v56, 2, v56\n" \
  "v_lshlrev_b32 v57, 2, v57\n" \
  "v_lshlrev_b32 v58, 2, v58\n" \
  "s_lshl_b32 s75, s44, 8\n" \
  "s_add_u32 s75, s75, 106816\n" \
  "s_mov_b32 s52, s28\n" \
  "s_or_b32 s53, s29, 0x4000000\n" \
  "s_mov_b32 s54, s35\n" \
  "s_mov_b32 s55, 0x20000\n" \
  "s_lshl_b32 s63, s67, 10\n" \
  "s_add_u32 s56, s32, s63\n" \
  "s_addc_u32 s57, s33, 0\n" \
  "s_lshl_b32 s58, s68, 10\n" \
  "s_mov_b32 s59, 0x20000\n" \
  "s_lshl_b32 s67, s67, 24\n" \
  "s_mov_b32 s70, 32768\n" \
  "s_mul_i32 s63, s44, 3328\n" \
  "s_add_u32 s63, s63, 37584\n" \
  "v_add_u32 v21, s63, v13\n" \
  "v_and_b32 v15, 0xf0, v54\n" \
  "v_add_u32 v15, s63, v15\n" \
  "v_and_b32 v23, 0xf0, v13\n" \
  "v_add3_u32 v16, v23, s63, 8\n" \
  "s_mul_i32 s63, s44, 832\n" \
  "s_add_u32 s63, s63, 100096\n" \
  "v_mov_b32 v14, s63\n" \
  "v_and_b32 v50, 0x300, v13\n" \
  "v_and_or_b32 v19, v54, 60, v50\n" \
  "v_add_u32 v19, 108864, v19\n" \
  "v_lshl_add_u32 v23, v50, 2, v23\n" \
  "s_lshl_b32 s63, s73, 10\n" \
  "s_lshl_b32 s61, s45, 5\n" \
  "s_add_u32 s63, s63, s61\n" \
  "v_add_u32 v23, s63, v23\n" \
  "v_bfe_u32 v50, v86, 2, 2\n" \
  "v_mul_u32_u24 v22, 1056, v50\n" \
  "v_and_b32 v50, 48, v13\n" \
  "v_lshl_add_u32 v22, v50, 2, v22\n" \
  "v_and_b32 v50, 48, v86\n" \
  "s_mul_i32 s63, s73, 1056\n" \
  "s_add_u32 s63, s63, 108864\n" \
  "v_add3_u32 v22, v22, v50, s63\n" \
  "v_mul_u32_u24 v50, 26, v86\n" \
  "v_lshrrev_b32 v50, 8, v50\n" \
  "v_mad_i32_i24 v50, v50, -10, v86\n" \
  "v_lshlrev_b32 v42, 19, v50\n" \
  "s_lshl_b32 s63, s73, 28\n" \
  "s_sub_u32 s67, s67, s63\n" \
  "s_cmp_lt_u32 s92, 4096\n" \
  "s_cbranch_scc1 Lq2_%=\n" \
  "v_mov_b32 v53, 3\n" \
  "ds_or_b32 v94, v53\n" \
  "Lq2_%=:\n" \
  "v_mov_b32 v50, 2\n" \
  "s_waitcnt vmcnt(0)\n" \
  "v_subrev_u32 v60, s67, v60\n" \
  "s_mov_b64 exec, s[80:81]\n" \
  "v_lshlrev_b32_sdwa v64, v50, v60 dst_sel:DWORD dst_unused:UNUSED_PAD src0_sel:DWORD src1_sel:BYTE_3\n" \
  "ds_add_rtn_u32 v80, v64, v93 offset:35584\n" \
  "v_subrev_u32 v61, s67, v61\n" \
  "s_mov_b64 exec, s[82:83]\n" \
  "v_lshlrev_b32_sdwa v65, v50, v61 dst_sel:DWORD dst_unused:UNUSED_PAD src0_sel:DWORD src1_sel:BYTE_3\n" \
  "ds_add_rtn_u32 v81, v65, v93 offset:35584\n" \
  "v_subrev_u32 v62, s67, v62\n" \
  "s_mov_b64 exec, s[84:85]\n" \
  "v_lshlrev_b32_sdwa v66, v50, v62 dst_sel:DWORD dst_unused:UNUSED_PAD src0_sel:DWORD src1_sel:BYTE_3\n" \
  "ds_add_rtn_u32 v82, v66, v93 offset:35584\n" \
  "s_cmp_le_i32 s62, 192\n" \
  "s_cbranch_scc1 Lk613_%=\n" \
  "v_subrev_u32 v63, s67, v63\n" \
  "s_mov_b64 exec, s[86:87]\n" \
  "v_lshlrev_b32_sdwa v67, v50, v63 dst_sel:DWORD dst_unused:UNUSED_PAD src0_sel:DWORD src1_sel:BYTE_3\n" \
  "ds_add_rtn_u32 v83, v67, v93 offset:35584\n" \
  "Lk613_%=:\n" \
  "s_mov_b64 exec, -1\n" \
  "buffer_load_dwordx4 v[216:219], v56, s[24:27], s93 offen sc1\n" \
  "buffer_load_dwordx4 v[220:223], v57, s[24:27], s93 offen sc1\n" \
  "buffer_load_dwordx4 v[224:227], v58, s[24:27], s93 offen sc1\n" \
  "s_mov_b32 s63, 0x01010101\n" \
  "s_waitcnt lgkmcnt(0)\n" \
  "s_barrier\n" \
  "ds_read2st64_b32 v[44:45], v54 offset0:139 offset1:140\n" \
  "ds_read_b32 v86, v94 offset:4\n" \
  "ds_bpermute_b32 v76, v64, v42\n" \
  "ds_bpermute_b32 v77, v65, v42\n" \
  "ds_bpermute_b32 v78, v66, v42\n" \
  "ds_bpermute_b32 v79, v67, v42\n" \
  "buffer_load_dword v20, s[28:31], s66 offen lds\n" \
  "s_add_u32 s66, s66, 0x40000\n" \
  "buffer_load_dword v20, s[28:31], s66 offen lds\n" \
  "s_add_u32 s66, s66, 0x40000\n" \
  "v_bfe_u32 v80, v80, s45, 8\n" \
  "v_bfe_u32 v81, v81, s45, 8\n" \
  "v_bfe_u32 v82, v82, s45, 8\n" \
  "v_bfe_u32 v83, v83, s45, 8\n" \
  "s_waitcnt lgkmcnt(4)\n" \
  "v_dot4_u32_u8 v85, v44, s63, 0\n" \
  "v_dot4_u32_u8 v85, v45, s63, v85\n" \
  "v_dot4_u32_u8 v46, v44, s46, 0\n" \
  "v_dot4_u32_u8 v46, v45, s47, v46\n" \
  "v_add_u32 v86, 31, v86\n" \
  "v_add_u32_dpp v87, v85, v85 row_shr:1 row_mask:0xf bank_mask:0xf bound_ctrl:1\n" \
  "v_lshrrev_b32 v86, 5, v86\n" \
  "v_cvt_f32_u32 v86, v86\n" \
  "v_add_u32_dpp v87, v87, v87 row_shr:2 row_mask:0xf bank_mask:0xf bound_ctrl:1\n" \
  "v_rcp_f32 v86, v86\n" \
  "v_mul_hi_u32_u24 v68, s37, v60\n" \
  "v_add_u32_dpp v87, v87, v87 row_shr:4 row_mask:0xf bank_mask:0xf bound_ctrl:1\n" \
  "v_subrev_u32 v68, s71, v68\n" \
  "v_and_b32 v68, 15, v68\n" \
  "v_add_u32_dpp v87, v87, v87 row_shr:8 row_mask:0xf bank_mask:0xf bound_ctrl:1\n" \
  "v_lshl_add_u32 v72, v68, 1, s75\n" \
  "v_lshl_add_u32 v68, v68, 3, s73\n" \
  "v_add_u32_dpp v87, v87, v87 row_bcast:15 row_mask:0xa bank_mask:0xf\n" \
  "v_mul_hi_u32_u24 v69, s37, v61\n" \
  "v_subrev_u32 v69, s71, v69\n" \
  "v_add_u32_dpp v87, v87, v87 row_bcast:31 row_mask:0xc bank_mask:0xf\n" \
  "v_sub_u32 v46, v87, v46\n" \
  "ds_bpermute_b32 v88, v64, v46\n" \
  "ds_bpermute_b32 v89, v65, v46\n" \
  "ds_bpermute_b32 v90, v66, v46\n" \
  "ds_bpermute_b32 v91, v67, v46\n" \
  "v_mul_f32 v86, 0x48800000, v86\n" \
  "v_cvt_u32_f32 v86, v86\n" \
  "v_add_u32 v86, 1, v86\n" \
  "v_and_b32 v69, 15, v69\n" \
  "v_lshl_add_u32 v73, v69, 1, s75\n" \
  "v_lshl_add_u32 v69, v69, 3, s73\n" \
  "v_mul_hi_u32_u24 v70, s37, v62\n" \
  "v_subrev_u32 v70, s71, v70\n" \
  "v_and_b32 v70, 15, v70\n" \
  "v_lshl_add_u32 v74, v70, 1, s75\n" \
  "v_lshl_add_u32 v70, v70, 3, s73\n" \
  "v_mul_hi_u32_u24 v71, s37, v63\n" \
  "v_subrev_u32 v71, s71, v71\n" \
  "v_and_b32 v71, 15, v71\n" \
  "v_lshl_add_u32 v75, v71, 1, s75\n" \
  "v_lshl_add_u32 v71, v71, 3, s73\n" \
  "s_mov_b32 s63, 0xffff\n" \
  "s_waitcnt lgkmcnt(4)\n" \
  "v_and_or_b32 v76, v60, s63, v76\n" \
  "v_and_or_b32 v77, v61, s63, v77\n" \
  "v_and_or_b32 v78, v62, s63, v78\n" \
  "v_and_or_b32 v79, v63, s63, v79\n" \
  "s_waitcnt lgkmcnt(3)\n" \
  "v_add_u32 v80, v80, v88\n" \
  "v_mul_u32_u24 v80, v80, v86\n" \
  "s_waitcnt lgkmcnt(2)\n" \
  "v_add_u32 v81, v81, v89\n" \
  "v_mul_u32_u24 v81, v81, v86\n" \
  "s_waitcnt lgkmcnt(1)\n" \
  "v_add_u32 v82, v82, v90\n" \
  "v_mul_u32_u24 v82, v82, v86\n" \
  "s_waitcnt lgkmcnt(0)\n" \
  "v_add_u32 v83, v83, v91\n" \
  "v_mul_u32_u24 v83, v83, v86\n" \
  "v_lshrrev_b32 v60, 20, v80\n" \
  "s_mov_b64 exec, s[80:81]\n" \
  "v_lshl_add_u32 v68, v60, 7, v68\n" \
  "ds_add_rtn_u32 v80, v68, v93 offset:34560\n" \
  "v_lshrrev_b32 v61, 20, v81\n" \
  "s_mov_b64 exec, s[82:83]\n" \
  "v_lshl_add_u32 v69, v61, 7, v69\n" \
  "ds_add_rtn_u32 v81, v69, v93 offset:34560\n" \
  "v_lshrrev_b32 v62, 20, v82\n" \
  "s_mov_b64 exec, s[84:85]\n" \
  "v_lshl_add_u32 v70, v62, 7, v70\n" \
  "ds_add_rtn_u32 v82, v70, v93 offset:34560\n" \
  "s_cmp_le_i32 s62, 192\n" \
  "s_cbranch_scc1 Lk714_%=\n" \
  "v_lshrrev_b32 v63, 20, v83\n" \
  "s_mov_b64 exec, s[86:87]\n" \
  "v_lshl_add_u32 v71, v63, 7, v71\n" \
  "ds_add_rtn_u32 v83, v71, v93 offset:34560\n" \
  "Lk714_%=:\n" \
  "s_mov_b64 exec, -1\n" \
  "buffer_load_dword v20, s[28:31], s66 offen lds\n" \
  "s_add_u32 s66, s66, 0x40000\n" \
  "v_lshl_add_u32 v72, v60, 5, v72\n" \
  "v_lshl_add_u32 v73, v61, 5, v73\n" \
  "v_lshl_add_u32 v74, v62, 5, v74\n" \
  "v_lshl_add_u32 v75, v63, 5, v75\n" \
  "v_sub_u32 v85, v87, v85\n" \
  "v_add_u32 v87, -1, v87\n" \
  "v_max_i32 v87, v87, v85\n" \
  "v_mul_u32_u24 v84, v85, v86\n" \
  "v_mul_u32_u24 v43, v87, v86\n" \
  "v_lshrrev_b32 v50, 1, v13\n" \
  "v_lshrrev_b32 v51, 3, v13\n" \
  "v_add_u32 v51, s75, v51\n" \
  "v_lshrrev_b32 v52, 8, v13\n" \
  "v_mul_u32_u24 v52, 0xd000d0, v52\n" \
  "v_add_u32 v52, 0x3400000, v52\n" \
  "s_mov_b32 s63, 0x01010101\n" \
  "s_waitcnt lgkmcnt(0)\n" \
  "s_barrier\n" \
  "ds_read_b64 v[88:89], v50 offset:34560\n" \
  "ds_read_b64 v[90:91], v50 offset:35072\n" \
  "v_bfe_u32 v80, v80, s45, 8\n" \
  "v_bfe_u32 v81, v81, s45, 8\n" \
  "v_bfe_u32 v82, v82, s45, 8\n" \
  "v_bfe_u32 v83, v83, s45, 8\n" \
  "s_lshl_b32 s72, s44, 4\n" \
  "s_or_b32 s72, s72, 15\n" \
  "s_lshl_b32 s73, s73, 2\n" \
  "s_waitcnt lgkmcnt(0)\n" \
  "v_dot4_u32_u8 v44, v88, s63, 0\n" \
  "v_dot4_u32_u8 v45, v90, s63, 0\n" \
  "v_dot4_u32_u8 v44, v89, s63, v44\n" \
  "v_dot4_u32_u8 v45, v91, s63, v45\n" \
  "v_dot4_u32_u8 v46, v88, s46, 0\n" \
  "v_dot4_u32_u8 v47, v90, s46, 0\n" \
  "v_dot4_u32_u8 v46, v89, s47, v46\n" \
  "v_lshl_or_b32 v44, v45, 16, v44\n" \
  "v_dot4_u32_u8 v47, v91, s47, v47\n" \
  "s_nop 0\n" \
  "v_add_u32_dpp v87, v44, v44 row_shr:1 row_mask:0xf bank_mask:0xf bound_ctrl:1\n" \
  "s_nop 0\n" \
  "v_lshl_or_b32 v46, v47, 16, v46\n" \
  "v_add_u32_dpp v87, v87, v87 row_shr:2 row_mask:0xf bank_mask:0xf bound_ctrl:1\n" \
  "v_sub_u32 v52, v52, v46\n" \
  "s_nop 0\n" \
  "v_add_u32_dpp v87, v87, v87 row_shr:4 row_mask:0xf bank_mask:0xf bound_ctrl:1\n" \
  "s_nop 1\n" \
  "v_add_u32_dpp v87, v87, v87 row_shr:8 row_mask:0xf bank_mask:0xf bound_ctrl:1\n" \
  "v_add_u32 v52, v52, v87\n" \
  "ds_write_b16 v51, v52\n" \
  "ds_write_b16_d16_hi v51, v52 offset:128\n" \
  "ds_read_u16 v72, v72\n" \
  "ds_read_u16 v73, v73\n" \
  "ds_read_u16 v74, v74\n" \
  "ds_read_u16 v75, v75\n" \
  "v_readlane_b32 s60, v87, s72\n" \
  "s_movk_i32 s63, 200\n" \
  "v_cmp_lt_u32_sdwa s[90:91], s63, v87 src0_sel:DWORD src1_sel:WORD_0\n" \
  "v_cmp_lt_u32_sdwa vcc, s63, v87 src0_sel:DWORD src1_sel:WORD_1\n" \
  "s_lshr_b32 s72, s60, s73\n" \
  "s_and_b32 s72, s72, 0xffff\n" \
  "s_add_u32 s60, s72, 3\n" \
  "s_lshr_b32 s60, s60, 2\n" \
  "s_max_u32 s60, s60, 1\n" \
  "s_lshl2_add_u32 s73, s60, 8\n" \
  "s_sub_u32 s73, s73, s72\n" \
  "s_bfm_b64 s[88:89], s73, 0\n" \
  "s_or_b64 vcc, vcc, s[90:91]\n" \
  "s_cbranch_scc0 Lo1_%=\n" \
  "s_mov_b64 s[80:81], 0\n" \
  "s_mov_b64 s[82:83], 0\n" \
  "s_mov_b64 s[84:85], 0\n" \
  "s_mov_b64 s[86:87], 0\n" \
  "s_mov_b64 s[88:89], 0xfff\n" \
  "s_mov_b32 s72, 0\n" \
  "s_mov_b32 s60, 1\n" \
  "ds_or_b32 v94, v92\n" \
  "Lo1_%=:\n" \
  "v_lshrrev_b32 v50, 2, v13\n" \
  "s_lshl_b32 s72, s72, 2\n" \
  "v_add3_u32 v50, v50, s72, v14\n" \
  "v_mov_b32 v51, 0x50ffff\n" \
  "s_mov_b32 s63, 100096\n" \
  "s_mov_b64 exec, s[88:89]\n" \
  "ds_write_b32 v50, v51\n" \
  "s_waitcnt lgkmcnt(1)\n" \
  "s_mov_b64 exec, s[80:81]\n" \
  "v_add_u32 v80, v80, v72\n" \
  "v_lshl_add_u32 v72, v80, 2, s63\n" \
  "ds_write_b32 v72, v76\n" \
  "s_mov_b64 exec, s[82:83]\n" \
  "v_add_u32 v81, v81, v73\n" \
  "v_lshl_add_u32 v73, v81, 2, s63\n" \
  "ds_write_b32 v73, v77\n" \
  "s_mov_b64 exec, s[84:85]\n" \
  "v_add_u32 v82, v82, v74\n" \
  "v_lshl_add_u32 v74, v82, 2, s63\n" \
  "ds_write_b32 v74, v78\n" \
  "s_cmp_le_i32 s62, 192\n" \
  "s_cbranch_scc1 Lk821_%=\n" \
  "s_mov_b64 exec, s[86:87]\n" \
  "v_add_u32 v83, v83, v75\n" \
  "v_lshl_add_u32 v75, v83, 2, s63\n" \
  "ds_write_b32 v75, v79\n" \
  "Lk821_%=:\n" \
  "s_mov_b64 exec, -1\n" \
  "s_sub_u32 s60, s60, 1\n" \
  "s_lshl_b32 s63, s60, 4\n" \
  "v_sub_u32 v50, v14, v13\n" \
  "v_add_u32 v50, s63, v50\n" \
  "s_waitcnt lgkmcnt(0)\n" \
  "s_barrier\n" \
  "ds_read_b128 v[56:59], v14\n" \
  "ds_read_b128 v[60:63], v14 offset:16\n" \
  "ds_read_b128 v[64:67], v50\n" \
  "v_lshlrev_b32 v232, 4, v80\n" \
  "v_lshlrev_b32 v233, 4, v81\n" \
  "v_lshlrev_b32 v234, 4, v82\n" \
  "v_lshlrev_b32 v235, 4, v83\n" \
  "s_waitcnt vmcnt(3)\n" \
  "s_mov_b64 exec, s[80:81]\n" \
  "ds_write_b128 v232, v[216:219] offset:37584\n" \
  "s_mov_b64 exec, s[82:83]\n" \
  "ds_write_b128 v233, v[220:223] offset:37584\n" \
  "s_mov_b64 exec, s[84:85]\n" \
  "ds_write_b128 v234, v[224:227] offset:37584\n" \
  "s_cmp_le_i32 s62, 192\n" \
  "s_cbranch_scc1 Lk849_%=\n" \
  "s_mov_b64 exec, s[86:87]\n" \
  "ds_write_b128 v235, v[228:231] offset:37584\n" \
  "Lk849_%=:\n" \
  "s_mov_b64 exec, -1\n" \
  "s_waitcnt lgkmcnt(4)\n" \
  "v_and_b32 v12, 0xffff, v56\n" \
  "buffer_load_dwordx4 v[96:99], v[12:13], s[52:55], 0 idxen offen\n" \
  "v_and_b32 v12, 0xffff, v57\n" \
  "buffer_load_dwordx4 v[100:103], v[12:13], s[52:55], 0 idxen offen\n" \
  "v_and_b32 v12, 0xffff, v58\n" \
  "buffer_load_dwordx4 v[104:107], v[12:13], s[52:55], 0 idxen offen\n" \
  "v_and_b32 v12, 0xffff, v59\n" \
  "buffer_load_dwordx4 v[108:111], v[12:13], s[52:55], 0 idxen offen\n" \
  "v_lshrrev_b32 v51, 20, v84\n" \
  "v_lshrrev_b32 v52, 20, v43\n" \
  "v_min_u32 v51, 7, v51\n" \
  "v_min_u32 v52, 7, v52\n" \
  "s_bfm_b64 s[88:89], s68, 0\n" \
  "v_mov_b32 v18, v94\n" \
  "v_cmp_ge_u32 s[90:91], s44, v51\n" \
  "v_cmp_le_u32 vcc, s44, v52\n" \
  "s_and_b64 s[88:89], s[88:89], s[90:91]\n" \
  "s_and_b64 s[88:89], s[88:89], vcc\n" \
  "s_ff1_i32_b64 s72, s[88:89]\n" \
  "s_flbit_i32_b64 s74, s[88:89]\n" \
  "s_xor_b32 s74, s74, 63\n" \
  "s_mov_b32 s63, 0x0c0c0602\n" \
  "s_waitcnt lgkmcnt(0)\n" \
  "s_barrier\n" \
  "ds_read_b64 v[92:93], v16\n" \
  "ds_read_b64 v[94:95], v15\n" \
  "v_and_b32 v12, 0xffff, v60\n" \
  "buffer_load_dwordx4 v[112:115], v[12:13], s[52:55], 0 idxen offen\n" \
  "v_and_b32 v12, 0xffff, v61\n" \
  "buffer_load_dwordx4 v[116:119], v[12:13], s[52:55], 0 idxen offen\n" \
  "v_and_b32 v12, 0xffff, v62\n" \
  "buffer_load_dwordx4 v[120:123], v[12:13], s[52:55], 0 idxen offen\n" \
  "v_and_b32 v12, 0xffff, v63\n" \
  "buffer_load_dwordx4 v[124:127], v[12:13], s[52:55], 0 idxen offen\n" \
  "ds_read_u16 v12, v14 offset:32\n" \
  "v_perm_b32 v64, v65, v64, s63\n" \
  "v_perm_b32 v66, v67, v66, s63\n" \
  "v_readlane_b32 s93, v51, s72\n" \
  "v_readlane_b32 s75, v52, s74\n" \
  "v_lshl_or_b32 v17, v66, 16, v64\n" \
  "v_readlane_b32 s63, v51, s74\n" \
  "v_readlane_b32 s73, v42, s72\n" \
  "v_readlane_b32 s61, v17, s60\n" \
  "s_sub_u32 s74, s74, s72\n" \
  "s_sub_u32 s75, s75, s44\n" \
  "s_cmp_eq_u32 s63, s44\n" \
  "s_cselect_b32 s75, s75, 0\n" \
  "s_lshr_b32 s73, s73, 16\n" \
  "s_mul_i32 s72, s72, 1056\n" \
  "s_or_b32 s63, %[blk], s44\n" \
  "s_cbranch_scc0 Lep0_%=\n" \
  "Lgo%=:\n" \
  "s_waitcnt lgkmcnt(0)\n" \
  "v_mfma_f32_4x4x4_16b_bf16 v[56:59], v[92:93], v[32:33], 0 cbsz:4\n" \
  "v_mfma_f32_4x4x4_16b_bf16 v[60:63], v[92:93], v[36:37], 0 cbsz:4\n" \
  "v_mfma_f32_4x4x4_16b_bf16 v[64:67], v[92:93], v[32:33], 0 cbsz:4 abid:1\n" \
  "v_mfma_f32_4x4x4_16b_bf16 v[68:71], v[92:93], v[36:37], 0 cbsz:4 abid:1\n" \
  "v_mfma_f32_4x4x4_16b_bf16 v[84:87], v[94:95], v[24:25], 0\n" \
  "v_mfma_f32_4x4x4_16b_bf16 v[88:91], v[94:95], v[26:27], 0\n" \
  "v_mfma_f32_4x4x4_16b_bf16 v[48:51], v[94:95], v[28:29], 0\n" \
  "v_mfma_f32_4x4x4_16b_bf16 v[52:55], v[94:95], v[30:31], 0\n" \
  "s_nop 1\n" \
  "v_cvt_pk_bf16_f32 v57, v56, v57\n" \
  "v_lshlrev_b32_sdwa v56, 1, v57 dst_sel:DWORD dst_unused:UNUSED_PAD src0_sel:DWORD src1_sel:WORD_0\n" \
  "v_lshlrev_b32_sdwa v57, 1, v57 dst_sel:DWORD dst_unused:UNUSED_PAD src0_sel:DWORD src1_sel:WORD_1\n" \
  "ds_read_u16_d16_hi v56, v56\n" \
  "ds_read_u16_d16_hi v57, v57\n" \
  "v_cvt_pk_bf16_f32 v59, v58, v59\n" \
  "v_lshlrev_b32_sdwa v58, 1, v59 dst_sel:DWORD dst_unused:UNUSED_PAD src0_sel:DWORD src1_sel:WORD_0\n" \
  "v_lshlrev_b32_sdwa v59, 1, v59 dst_sel:DWORD dst_unused:UNUSED_PAD src0_sel:DWORD src1_sel:WORD_1\n" \
  "ds_read_u16_d16_hi v58, v58\n" \
  "ds_read_u16_d16_hi v59, v59\n" \
  "v_cvt_pk_bf16_f32 v61, v60, v61\n" \
  "v_lshlrev_b32_sdwa v60, 1, v61 dst_sel:DWORD dst_unused:UNUSED_PAD src0_sel:DWORD src1_sel:WORD_0\n" \
  "v_lshlrev_b32_sdwa v61, 1, v61 dst_sel:DWORD dst_unused:UNUSED_PAD src0_sel:DWORD src1_sel:WORD_1\n" \
  "ds_read_u16_d16_hi v60, v60\n" \
  "ds_read_u16_d16_hi v61, v61\n" \
  "v_cvt_pk_bf16_f32 v63, v62, v63\n" \
  "v_lshlrev_b32_sdwa v62, 1, v63 dst_sel:DWORD dst_unused:UNUSED_PAD src0_sel:DWORD src1_sel:WORD_0\n" \
  "v_lshlrev_b32_sdwa v63, 1, v63 dst_sel:DWORD dst_unused:UNUSED_PAD src0_sel:DWORD src1_sel:WORD_1\n" \
  "ds_read_u16_d16_hi v62, v62\n" \
  "ds_read_u16_d16_hi v63, v63\n" \
  "v_cvt_pk_bf16_f32 v76, v84, v88\n" \
  "v_cvt_pk_bf16_f32 v77, v48, v52\n" \
  "s_mov_b32 s63, 0x7fff7fff\n" \
  "v_and_b32 v41, s63, v218\n" \
  "v_and_b32 v42, s63, v219\n" \
  "v_pk_max_u16 v40, v41, v42\n" \
  "v_and_b32 v41, s63, v222\n" \
  "v_and_b32 v42, s63, v223\n" \
  "v_pk_max_u16 v41, v41, v40\n" \
  "v_pk_max_u16 v40, v41, v42\n" \
  "v_and_b32 v41, s63, v226\n" \
  "v_and_b32 v42, s63, v227\n" \
  "v_pk_max_u16 v41, v41, v40\n" \
  "v_pk_max_u16 v40, v41, v42\n" \
  "s_cmp_le_i32 s62, 192\n" \
  "s_cbranch_scc1 Lk952_%=\n" \
  "v_and_b32 v41, s63, v230\n" \
  "v_and_b32 v42, s63, v231\n" \
  "v_pk_max_u16 v41, v41, v40\n" \
  "v_pk_max_u16 v40, v41, v42\n" \
  "Lk952_%=:\n" \
  "v_max_u16_sdwa v40, v40, v40 dst_sel:DWORD dst_unused:UNUSED_PAD src0_sel:WORD_0 src1_sel:WORD_1\n" \
  "s_nop 0\n" \
  "v_cmp_lt_i32 vcc, s69, v40\n" \
  "s_cmp_eq_u64 vcc, 0\n" \
  "s_cselect_b32 s74, s74, -1\n" \
  "s_waitcnt vmcnt(7) lgkmcnt(0)\n" \
  "v_mfma_f32_4x4x4_16b_bf16 v[48:51], v[76:77], v[96:97], 0 cbsz:4\n" \
  "v_mfma_f32_4x4x4_16b_bf16 v[52:55], v[76:77], v[98:99], 0 cbsz:4\n" \
  "s_nop 4\n" \
  "Lbody%=:\n" \
  "s_set_gpr_idx_on s61, gpr_idx(SRC2,DST)\n" \
  "v_pk_fma_f32 v[128:129], v[56:57], v[48:49], v[128:129] op_sel:[0,0,0] op_sel_hi:[0,1,1]\n" \
  "v_pk_fma_f32 v[130:131], v[56:57], v[50:51], v[130:131] op_sel:[0,0,0] op_sel_hi:[0,1,1]\n" \
  "v_pk_fma_f32 v[132:133], v[60:61], v[52:53], v[132:133] op_sel:[0,0,0] op_sel_hi:[0,1,1]\n" \
  "v_pk_fma_f32 v[134:135], v[60:61], v[54:55], v[134:135] op_sel:[0,0,0] op_sel_hi:[0,1,1]\n" \
  "s_set_gpr_idx_off\n" \
  "s_waitcnt vmcnt(6) lgkmcnt(2)\n" \
  "buffer_load_dwordx4 v[96:99], v[12:13], s[52:55], 0 idxen offen\n" \
  "ds_read_u16 v12, v14 offset:36\n" \
  "s_lshr_b32 s61, s61, 8\n" \
  "v_mfma_f32_4x4x4_16b_bf16 v[48:51], v[76:77], v[100:101], 0 cbsz:4 abid:1\n" \
  "v_mfma_f32_4x4x4_16b_bf16 v[52:55], v[76:77], v[102:103], 0 cbsz:4 abid:1\n" \
  "v_cvt_pk_bf16_f32 v65, v64, v65\n" \
  "v_lshlrev_b32_sdwa v64, 1, v65 dst_sel:DWORD dst_unused:UNUSED_PAD src0_sel:DWORD src1_sel:WORD_0\n" \
  "v_lshlrev_b32_sdwa v65, 1, v65 dst_sel:DWORD dst_unused:UNUSED_PAD src0_sel:DWORD src1_sel:WORD_1\n" \
  "ds_read_u16_d16_hi v64, v64\n" \
  "ds_read_u16_d16_hi v65, v65\n" \
  "s_set_gpr_idx_on s61, gpr_idx(SRC2,DST)\n" \
  "v_pk_fma_f32 v[128:129], v[56:57], v[48:49], v[128:129] op_sel:[1,0,0] op_sel_hi:[1,1,1]\n" \
  "v_pk_fma_f32 v[130:131], v[56:57], v[50:51], v[130:131] op_sel:[1,0,0] op_sel_hi:[1,1,1]\n" \
  "v_pk_fma_f32 v[132:133], v[60:61], v[52:53], v[132:133] op_sel:[1,0,0] op_sel_hi:[1,1,1]\n" \
  "v_pk_fma_f32 v[134:135], v[60:61], v[54:55], v[134:135] op_sel:[1,0,0] op_sel_hi:[1,1,1]\n" \
  "s_set_gpr_idx_off\n" \
  "s_waitcnt vmcnt(6) lgkmcnt(2)\n" \
  "buffer_load_dwordx4 v[100:103], v[12:13], s[52:55], 0 idxen offen\n" \
  "ds_read_u16 v12, v14 offset:40\n" \
  "s_lshr_b32 s61, s61, 8\n" \
  "v_mfma_f32_4x4x4_16b_bf16 v[48:51], v[76:77], v[104:105], 0 cbsz:4 abid:2\n" \
  "v_mfma_f32_4x4x4_16b_bf16 v[52:55], v[76:77], v[106:107], 0 cbsz:4 abid:2\n" \
  "v_cvt_pk_bf16_f32 v69, v68, v69\n" \
  "v_lshlrev_b32_sdwa v68, 1, v69 dst_sel:DWORD dst_unused:UNUSED_PAD src0_sel:DWORD src1_sel:WORD_0\n" \
  "v_lshlrev_b32_sdwa v69, 1, v69 dst_sel:DWORD dst_unused:UNUSED_PAD src0_sel:DWORD src1_sel:WORD_1\n" \
  "ds_read_u16_d16_hi v68, v68\n" \
  "ds_read_u16_d16_hi v69, v69\n" \
  "s_set_gpr_idx_on s61, gpr_idx(SRC2,DST)\n" \
  "v_pk_fma_f32 v[128:129], v[58:59], v[48:49], v[128:129] op_sel:[0,0,0] op_sel_hi:[0,1,1]\n" \
  "v_pk_fma_f32 v[130:131], v[58:59], v[50:51], v[130:131] op_sel:[0,0,0] op_sel_hi:[0,1,1]\n" \
  "v_pk_fma_f32 v[132:133], v[62:63], v[52:53], v[132:133] op_sel:[0,0,0] op_sel_hi:[0,1,1]\n" \
  "v_pk_fma_f32 v[134:135], v[62:63], v[54:55], v[134:135] op_sel:[0,0,0] op_sel_hi:[0,1,1]\n" \
  "s_set_gpr_idx_off\n" \
  "s_waitcnt vmcnt(6) lgkmcnt(2)\n" \
  "buffer_load_dwordx4 v[104:107], v[12:13], s[52:55], 0 idxen offen\n" \
  "ds_read_u16 v12, v14 offset:44\n" \
  "s_lshr_b32 s61, s61, 8\n" \
  "v_mfma_f32_4x4x4_16b_bf16 v[48:51], v[76:77], v[108:109], 0 cbsz:4 abid:3\n" \
  "v_mfma_f32_4x4x4_16b_bf16 v[52:55], v[76:77], v[110:111], 0 cbsz:4 abid:3\n" \
  "v_cvt_pk_bf16_f32 v67, v66, v67\n" \
  "v_lshlrev_b32_sdwa v66, 1, v67 dst_sel:DWORD dst_unused:UNUSED_PAD src0_sel:DWORD src1_sel:WORD_0\n" \
  "v_lshlrev_b32_sdwa v67, 1, v67 dst_sel:DWORD dst_unused:UNUSED_PAD src0_sel:DWORD src1_sel:WORD_1\n" \
  "ds_read_u16_d16_hi v66, v66\n" \
  "ds_read_u16_d16_hi v67, v67\n" \
  "s_set_gpr_idx_on s61, gpr_idx(SRC2,DST)\n" \
  "v_pk_fma_f32 v[128:129], v[58:59], v[48:49], v[128:129] op_sel:[1,0,0] op_sel_hi:[1,1,1]\n" \
  "v_pk_fma_f32 v[130:131], v[58:59], v[50:51], v[130:131] op_sel:[1,0,0] op_sel_hi:[1,1,1]\n" \
  "v_pk_fma_f32 v[132:133], v[62:63], v[52:53], v[132:133] op_sel:[1,0,0] op_sel_hi:[1,1,1]\n" \
  "v_pk_fma_f32 v[134:135], v[62:63], v[54:55], v[134:135] op_sel:[1,0,0] op_sel_hi:[1,1,1]\n" \
  "s_set_gpr_idx_off\n" \
  "s_waitcnt vmcnt(6) lgkmcnt(2)\n" \
  "buffer_load_dwordx4 v[108:111], v[12:13], s[52:55], 0 idxen offen\n" \
  "ds_read_u16 v12, v14 offset:48\n" \
  "s_sub_u32 s60, s60, 1\n" \
  "s_cbranch_scc1 Lend%=\n" \
  "v_readlane_b32 s61, v17, s60\n" \
  "s_setprio 2\n" \
  "v_mfma_f32_4x4x4_16b_bf16 v[56:59], v[92:93], v[32:33], 0 cbsz:4 abid:2\n" \
  "v_mfma_f32_4x4x4_16b_bf16 v[60:63], v[92:93], v[36:37], 0 cbsz:4 abid:2\n" \
  "v_mfma_f32_4x4x4_16b_bf16 v[48:51], v[76:77], v[112:113], 0 cbsz:4 abid:4\n" \
  "v_mfma_f32_4x4x4_16b_bf16 v[52:55], v[76:77], v[114:115], 0 cbsz:4 abid:4\n" \
  "v_cvt_pk_bf16_f32 v71, v70, v71\n" \
  "v_lshlrev_b32_sdwa v70, 1, v71 dst_sel:DWORD dst_unused:UNUSED_PAD src0_sel:DWORD src1_sel:WORD_0\n" \
  "v_lshlrev_b32_sdwa v71, 1, v71 dst_sel:DWORD dst_unused:UNUSED_PAD src0_sel:DWORD src1_sel:WORD_1\n" \
  "ds_read_u16_d16_hi v70, v70\n" \
  "ds_read_u16_d16_hi v71, v71\n" \
  "v_add_u32 v16, 0x100, v16\n" \
  "s_set_gpr_idx_on s61, gpr_idx(SRC2,DST)\n" \
  "v_pk_fma_f32 v[128:129], v[64:65], v[48:49], v[128:129] op_sel:[0,0,0] op_sel_hi:[0,1,1]\n" \
  "v_pk_fma_f32 v[130:131], v[64:65], v[50:51], v[130:131] op_sel:[0,0,0] op_sel_hi:[0,1,1]\n" \
  "v_pk_fma_f32 v[132:133], v[68:69], v[52:53], v[132:133] op_sel:[0,0,0] op_sel_hi:[0,1,1]\n" \
  "v_pk_fma_f32 v[134:135], v[68:69], v[54:55], v[134:135] op_sel:[0,0,0] op_sel_hi:[0,1,1]\n" \
  "s_set_gpr_idx_off\n" \
  "s_waitcnt vmcnt(6) lgkmcnt(2)\n" \
  "buffer_load_dwordx4 v[112:115], v[12:13], s[52:55], 0 idxen offen\n" \
  "ds_read_u16 v12, v14 offset:52\n" \
  "ds_read_b64 v[94:95], v15 offset:256\n" \
  "s_lshr_b32 s61, s61, 8\n" \
  "v_mfma_f32_4x4x4_16b_bf16 v[48:51], v[76:77], v[116:117], 0 cbsz:4 abid:5\n" \
  "v_mfma_f32_4x4x4_16b_bf16 v[52:55], v[76:77], v[118:119], 0 cbsz:4 abid:5\n" \
  "v_cvt_pk_bf16_f32 v57, v56, v57\n" \
  "v_lshlrev_b32_sdwa v56, 1, v57 dst_sel:DWORD dst_unused:UNUSED_PAD src0_sel:DWORD src1_sel:WORD_0\n" \
  "v_lshlrev_b32_sdwa v57, 1, v57 dst_sel:DWORD dst_unused:UNUSED_PAD src0_sel:DWORD src1_sel:WORD_1\n" \
  "ds_read_u16_d16_hi v56, v56\n" \
  "ds_read_u16_d16_hi v57, v57\n" \
  "s_set_gpr_idx_on s61, gpr_idx(SRC2,DST)\n" \
  "v_pk_fma_f32 v[128:129], v[64:65], v[48:49], v[128:129] op_sel:[1,0,0] op_sel_hi:[1,1,1]\n" \
  "v_pk_fma_f32 v[130:131], v[64:65], v[50:51], v[130:131] op_sel:[1,0,0] op_sel_hi:[1,1,1]\n" \
  "v_pk_fma_f32 v[132:133], v[68:69], v[52:53], v[132:133] op_sel:[1,0,0] op_sel_hi:[1,1,1]\n" \
  "v_pk_fma_f32 v[134:135], v[68:69], v[54:55], v[134:135] op_sel:[1,0,0] op_sel_hi:[1,1,1]\n" \
  "s_set_gpr_idx_off\n" \
  "s_waitcnt vmcnt(6) lgkmcnt(2)\n" \
  "buffer_load_dwordx4 v[116:119], v[12:13], s[52:55], 0 idxen offen\n" \
  "ds_read_u16 v12, v14 offset:56\n" \
  "s_lshr_b32 s61, s61, 8\n" \
  "v_mfma_f32_4x4x4_16b_bf16 v[48:51], v[76:77], v[120:121], 0 cbsz:4 abid:6\n" \
  "v_mfma_f32_4x4x4_16b_bf16 v[52:55], v[76:77], v[122:123], 0 cbsz:4 abid:6\n" \
  "v_cvt_pk_bf16_f32 v61, v60, v61\n" \
  "v_lshlrev_b32_sdwa v60, 1, v61 dst_sel:DWORD dst_unused:UNUSED_PAD src0_sel:DWORD src1_sel:WORD_0\n" \
  "v_lshlrev_b32_sdwa v61, 1, v61 dst_sel:DWORD dst_unused:UNUSED_PAD src0_sel:DWORD src1_sel:WORD_1\n" \
  "ds_read_u16_d16_hi v60, v60\n" \
  "ds_read_u16_d16_hi v61, v61\n" \
  "s_set_gpr_idx_on s61, gpr_idx(SRC2,DST)\n" \
  "v_pk_fma_f32 v[128:129], v[66:67], v[48:49], v[128:129] op_sel:[0,0,0] op_sel_hi:[0,1,1]\n" \
  "v_pk_fma_f32 v[130:131], v[66:67], v[50:51], v[130:131] op_sel:[0,0,0] op_sel_hi:[0,1,1]\n" \
  "v_pk_fma_f32 v[132:133], v[70:71], v[52:53], v[132:133] op_sel:[0,0,0] op_sel_hi:[0,1,1]\n" \
  "v_pk_fma_f32 v[134:135], v[70:71], v[54:55], v[134:135] op_sel:[0,0,0] op_sel_hi:[0,1,1]\n" \
  "s_set_gpr_idx_off\n" \
  "s_waitcnt vmcnt(6) lgkmcnt(2)\n" \
  "buffer_load_dwordx4 v[120:123], v[12:13], s[52:55], 0 idxen offen\n" \
  "ds_read_u16 v12, v14 offset:60\n" \
  "s_lshr_b32 s61, s61, 8\n" \
  "v_mfma_f32_4x4x4_16b_bf16 v[84:87], v[94:95], v[24:25], 0\n" \
  "v_mfma_f32_4x4x4_16b_bf16 v[48:51], v[76:77], v[124:125], 0 cbsz:4 abid:7\n" \
  "v_mfma_f32_4x4x4_16b_bf16 v[52:55], v[76:77], v[126:127], 0 cbsz:4 abid:7\n" \
  "v_cvt_pk_bf16_f32 v59, v58, v59\n" \
  "v_lshlrev_b32_sdwa v58, 1, v59 dst_sel:DWORD dst_unused:UNUSED_PAD src0_sel:DWORD src1_sel:WORD_0\n" \
  "v_lshlrev_b32_sdwa v59, 1, v59 dst_sel:DWORD dst_unused:UNUSED_PAD src0_sel:DWORD src1_sel:WORD_1\n" \
  "ds_read_u16_d16_hi v58, v58\n" \
  "ds_read_u16_d16_hi v59, v59\n" \
  "s_set_gpr_idx_on s61, gpr_idx(SRC2,DST)\n" \
  "v_pk_fma_f32 v[128:129], v[66:67], v[48:49], v[128:129] op_sel:[1,0,0] op_sel_hi:[1,1,1]\n" \
  "v_pk_fma_f32 v[130:131], v[66:67], v[50:51], v[130:131] op_sel:[1,0,0] op_sel_hi:[1,1,1]\n" \
  "v_pk_fma_f32 v[132:133], v[70:71], v[52:53], v[132:133] op_sel:[1,0,0] op_sel_hi:[1,1,1]\n" \
  "v_pk_fma_f32 v[134:135], v[70:71], v[54:55], v[134:135] op_sel:[1,0,0] op_sel_hi:[1,1,1]\n" \
  "s_set_gpr_idx_off\n" \
  "s_waitcnt vmcnt(6) lgkmcnt(2)\n" \
  "buffer_load_dwordx4 v[124:127], v[12:13], s[52:55], 0 idxen offen\n" \
  "ds_read_u16 v12, v14 offset:64\n" \
  "s_sub_u32 s60, s60, 1\n" \
  "s_cbranch_scc1 Lend%=\n" \
  "v_readlane_b32 s61, v17, s60\n" \
  "s_setprio 1\n" \
  "v_mfma_f32_4x4x4_16b_bf16 v[64:67], v[92:93], v[32:33], 0 cbsz:4 abid:3\n" \
  "v_mfma_f32_4x4x4_16b_bf16 v[68:71], v[92:93], v[36:37], 0 cbsz:4 abid:3\n" \
  "v_mfma_f32_4x4x4_16b_bf16 v[48:51], v[76:77], v[96:97], 0 cbsz:4 abid:8\n" \
  "v_mfma_f32_4x4x4_16b_bf16 v[52:55], v[76:77], v[98:99], 0 cbsz:4 abid:8\n" \
  "v_cvt_pk_bf16_f32 v63, v62, v63\n" \
  "v_lshlrev_b32_sdwa v62, 1, v63 dst_sel:DWORD dst_unused:UNUSED_PAD src0_sel:DWORD src1_sel:WORD_0\n" \
  "v_lshlrev_b32_sdwa v63, 1, v63 dst_sel:DWORD dst_unused:UNUSED_PAD src0_sel:DWORD src1_sel:WORD_1\n" \
  "ds_read_u16_d16_hi v62, v62\n" \
  "ds_read_u16_d16_hi v63, v63\n" \
  "v_add_u32 v15, 0x100, v15\n" \
  "s_set_gpr_idx_on s61, gpr_idx(SRC2,DST)\n" \
  "v_pk_fma_f32 v[128:129], v[56:57], v[48:49], v[128:129] op_sel:[0,0,0] op_sel_hi:[0,1,1]\n" \
  "v_pk_fma_f32 v[130:131], v[56:57], v[50:51], v[130:131] op_sel:[0,0,0] op_sel_hi:[0,1,1]\n" \
  "v_pk_fma_f32 v[132:133], v[60:61], v[52:53], v[132:133] op_sel:[0,0,0] op_sel_hi:[0,1,1]\n" \
  "v_pk_fma_f32 v[134:135], v[60:61], v[54:55], v[134:135] op_sel:[0,0,0] op_sel_hi:[0,1,1]\n" \
  "s_set_gpr_idx_off\n" \
  "s_waitcnt vmcnt(6) lgkmcnt(2)\n" \
  "buffer_load_dwordx4 v[96:99], v[12:13], s[52:55], 0 idxen offen\n" \
  "ds_read_u16 v12, v14 offset:68\n" \
  "s_lshr_b32 s61, s61, 8\n" \
  "v_mfma_f32_4x4x4_16b_bf16 v[48:51], v[76:77], v[100:101], 0 cbsz:4 abid:9\n" \
  "v_mfma_f32_4x4x4_16b_bf16 v[52:55], v[76:77], v[102:103], 0 cbsz:4 abid:9\n" \
  "v_cvt_pk_bf16_f32 v65, v64, v65\n" \
  "v_lshlrev_b32_sdwa v64, 1, v65 dst_sel:DWORD dst_unused:UNUSED_PAD src0_sel:DWORD src1_sel:WORD_0\n" \
  "v_lshlrev_b32_sdwa v65, 1, v65 dst_sel:DWORD dst_unused:UNUSED_PAD src0_sel:DWORD src1_sel:WORD_1\n" \
  "ds_read_u16_d16_hi v64, v64\n" \
  "ds_read_u16_d16_hi v65, v65\n" \
  "s_set_gpr_idx_on s61, gpr_idx(SRC2,DST)\n" \
  "v_pk_fma_f32 v[128:129], v[56:57], v[48:49], v[128:129] op_sel:[1,0,0] op_sel_hi:[1,1,1]\n" \
  "v_pk_fma_f32 v[130:131], v[56:57], v[50:51], v[130:131] op_sel:[1,0,0] op_sel_hi:[1,1,1]\n" \
  "v_pk_fma_f32 v[132:133], v[60:61], v[52:53], v[132:133] op_sel:[1,0,0] op_sel_hi:[1,1,1]\n" \
  "v_pk_fma_f32 v[134:135], v[60:61], v[54:55], v[134:135] op_sel:[1,0,0] op_sel_hi:[1,1,1]\n" \
  "s_set_gpr_idx_off\n" \
  "s_waitcnt vmcnt(6) lgkmcnt(2)\n" \
  "buffer_load_dwordx4 v[100:103], v[12:13], s[52:55], 0 idxen offen\n" \
  "ds_read_u16 v12, v14 offset:72\n" \
  "s_lshr_b32 s61, s61, 8\n" \
  "v_mfma_f32_4x4x4_16b_bf16 v[48:51], v[76:77], v[104:105], 0 cbsz:4 abid:10\n" \
  "v_mfma_f32_4x4x4_16b_bf16 v[52:55], v[76:77], v[106:107], 0 cbsz:4 abid:10\n" \
  "v_cvt_pk_bf16_f32 v69, v68, v69\n" \
  "v_lshlrev_b32_sdwa v68, 1, v69 dst_sel:DWORD dst_unused:UNUSED_PAD src0_sel:DWORD src1_sel:WORD_0\n" \
  "v_lshlrev_b32_sdwa v69, 1, v69 dst_sel:DWORD dst_unused:UNUSED_PAD src0_sel:DWORD src1_sel:WORD_1\n" \
  "ds_read_u16_d16_hi v68, v68\n" \
  "ds_read_u16_d16_hi v69, v69\n" \
  "s_set_gpr_idx_on s61, gpr_idx(SRC2,DST)\n" \
  "v_pk_fma_f32 v[128:129], v[58:59], v[48:49], v[128:129] op_sel:[0,0,0] op_sel_hi:[0,1,1]\n" \
  "v_pk_fma_f32 v[130:131], v[58:59], v[50:51], v[130:131] op_sel:[0,0,0] op_sel_hi:[0,1,1]\n" \
  "v_pk_fma_f32 v[132:133], v[62:63], v[52:53], v[132:133] op_sel:[0,0,0] op_sel_hi:[0,1,1]\n" \
  "v_pk_fma_f32 v[134:135], v[62:63], v[54:55], v[134:135] op_sel:[0,0,0] op_sel_hi:[0,1,1]\n" \
  "s_set_gpr_idx_off\n" \
  "s_waitcnt vmcnt(6) lgkmcnt(2)\n" \
  "buffer_load_dwordx4 v[104:107], v[12:13], s[52:55], 0 idxen offen\n" \
  "ds_read_u16 v12, v14 offset:76\n" \
  "ds_read_b64 v[92:93], v16\n" \
  "s_lshr_b32 s61, s61, 8\n" \
  "v_mfma_f32_4x4x4_16b_bf16 v[88:91], v[94:95], v[26:27], 0\n" \
  "v_mfma_f32_4x4x4_16b_bf16 v[48:51], v[76:77], v[108:109], 0 cbsz:4 abid:11\n" \
  "v_mfma_f32_4x4x4_16b_bf16 v[52:55], v[76:77], v[110:111], 0 cbsz:4 abid:11\n" \
  "v_cvt_pk_bf16_f32 v67, v66, v67\n" \
  "v_lshlrev_b32_sdwa v66, 1, v67 dst_sel:DWORD dst_unused:UNUSED_PAD src0_sel:DWORD src1_sel:WORD_0\n" \
  "v_lshlrev_b32_sdwa v67, 1, v67 dst_sel:DWORD dst_unused:UNUSED_PAD src0_sel:DWORD src1_sel:WORD_1\n" \
  "ds_read_u16_d16_hi v66, v66\n" \
  "ds_read_u16_d16_hi v67, v67\n" \
  "s_set_gpr_idx_on s61, gpr_idx(SRC2,DST)\n" \
  "v_pk_fma_f32 v[128:129], v[58:59], v[48:49], v[128:129] op_sel:[1,0,0] op_sel_hi:[1,1,1]\n" \
  "v_pk_fma_f32 v[130:131], v[58:59], v[50:51], v[130:131] op_sel:[1,0,0] op_sel_hi:[1,1,1]\n" \
  "v_pk_fma_f32 v[132:133], v[62:63], v[52:53], v[132:133] op_sel:[1,0,0] op_sel_hi:[1,1,1]\n" \
  "v_pk_fma_f32 v[134:135], v[62:63], v[54:55], v[134:135] op_sel:[1,0,0] op_sel_hi:[1,1,1]\n" \
  "s_set_gpr_idx_off\n" \
  "s_waitcnt vmcnt(6) lgkmcnt(2)\n" \
  "buffer_load_dwordx4 v[108:111], v[12:13], s[52:55], 0 idxen offen\n" \
  "ds_read_u16 v12, v14 offset:80\n" \
  "s_sub_u32 s60, s60, 1\n" \
  "s_cbranch_scc1 Lend%=\n" \
  "v_readlane_b32 s61, v17, s60\n" \
  "s_setprio 0\n" \
  "v_mfma_f32_4x4x4_16b_bf16 v[56:59], v[92:93], v[32:33], 0 cbsz:4\n" \
  "v_mfma_f32_4x4x4_16b_bf16 v[60:63], v[92:93], v[36:37], 0 cbsz:4\n" \
  "v_mfma_f32_4x4x4_16b_bf16 v[48:51], v[76:77], v[112:113], 0 cbsz:4 abid:12\n" \
  "v_mfma_f32_4x4x4_16b_bf16 v[52:55], v[76:77], v[114:115], 0 cbsz:4 abid:12\n" \
  "v_cvt_pk_bf16_f32 v71, v70, v71\n" \
  "v_lshlrev_b32_sdwa v70, 1, v71 dst_sel:DWORD dst_unused:UNUSED_PAD src0_sel:DWORD src1_sel:WORD_0\n" \
  "v_lshlrev_b32_sdwa v71, 1, v71 dst_sel:DWORD dst_unused:UNUSED_PAD src0_sel:DWORD src1_sel:WORD_1\n" \
  "ds_read_u16_d16_hi v70, v70\n" \
  "ds_read_u16_d16_hi v71, v71\n" \
  "v_cvt_pk_bf16_f32 v20, v84, v88\n" \
  "s_set_gpr_idx_on s61, gpr_idx(SRC2,DST)\n" \
  "v_pk_fma_f32 v[128:129], v[64:65], v[48:49], v[128:129] op_sel:[0,0,0] op_sel_hi:[0,1,1]\n" \
  "v_pk_fma_f32 v[130:131], v[64:65], v[50:51], v[130:131] op_sel:[0,0,0] op_sel_hi:[0,1,1]\n" \
  "v_pk_fma_f32 v[132:133], v[68:69], v[52:53], v[132:133] op_sel:[0,0,0] op_sel_hi:[0,1,1]\n" \
  "v_pk_fma_f32 v[134:135], v[68:69], v[54:55], v[134:135] op_sel:[0,0,0] op_sel_hi:[0,1,1]\n" \
  "s_set_gpr_idx_off\n" \
  "s_waitcnt vmcnt(6) lgkmcnt(2)\n" \
  "buffer_load_dwordx4 v[112:115], v[12:13], s[52:55], 0 idxen offen\n" \
  "ds_read_u16 v12, v14 offset:84\n" \
  "s_lshr_b32 s61, s61, 8\n" \
  "v_mfma_f32_4x4x4_16b_bf16 v[48:51], v[76:77], v[116:117], 0 cbsz:4 abid:13\n" \
  "v_mfma_f32_4x4x4_16b_bf16 v[52:55], v[76:77], v[118:119], 0 cbsz:4 abid:13\n" \
  "v_cvt_pk_bf16_f32 v57, v56, v57\n" \
  "v_lshlrev_b32_sdwa v56, 1, v57 dst_sel:DWORD dst_unused:UNUSED_PAD src0_sel:DWORD src1_sel:WORD_0\n" \
  "v_lshlrev_b32_sdwa v57, 1, v57 dst_sel:DWORD dst_unused:UNUSED_PAD src0_sel:DWORD src1_sel:WORD_1\n" \
  "ds_read_u16_d16_hi v56, v56\n" \
  "ds_read_u16_d16_hi v57, v57\n" \
  "s_set_gpr_idx_on s61, gpr_idx(SRC2,DST)\n" \
  "v_pk_fma_f32 v[128:129], v[64:65], v[48:49], v[128:129] op_sel:[1,0,0] op_sel_hi:[1,1,1]\n" \
  "v_pk_fma_f32 v[130:131], v[64:65], v[50:51], v[130:131] op_sel:[1,0,0] op_sel_hi:[1,1,1]\n" \
  "v_pk_fma_f32 v[132:133], v[68:69], v[52:53], v[132:133] op_sel:[1,0,0] op_sel_hi:[1,1,1]\n" \
  "v_pk_fma_f32 v[134:135], v[68:69], v[54:55], v[134:135] op_sel:[1,0,0] op_sel_hi:[1,1,1]\n" \
  "s_set_gpr_idx_off\n" \
  "s_waitcnt vmcnt(6) lgkmcnt(2)\n" \
  "buffer_load_dwordx4 v[116:119], v[12:13], s[52:55], 0 idxen offen\n" \
  "ds_read_u16 v12, v14 offset:88\n" \
  "s_lshr_b32 s61, s61, 8\n" \
  "v_mfma_f32_4x4x4_16b_bf16 v[48:51], v[76:77], v[120:121], 0 cbsz:4 abid:14\n" \
  "v_mfma_f32_4x4x4_16b_bf16 v[52:55], v[76:77], v[122:123], 0 cbsz:4 abid:14\n" \
  "v_cvt_pk_bf16_f32 v61, v60, v61\n" \
  "v_lshlrev_b32_sdwa v60, 1, v61 dst_sel:DWORD dst_unused:UNUSED_PAD src0_sel:DWORD src1_sel:WORD_0\n" \
  "v_lshlrev_b32_sdwa v61, 1, v61 dst_sel:DWORD dst_unused:UNUSED_PAD src0_sel:DWORD src1_sel:WORD_1\n" \
  "ds_read_u16_d16_hi v60, v60\n" \
  "ds_read_u16_d16_hi v61, v61\n" \
  "s_set_gpr_idx_on s61, gpr_idx(SRC2,DST)\n" \
  "v_pk_fma_f32 v[128:129], v[66:67], v[48:49], v[128:129] op_sel:[0,0,0] op_sel_hi:[0,1,1]\n" \
  "v_pk_fma_f32 v[130:131], v[66:67], v[50:51], v[130:131] op_sel:[0,0,0] op_sel_hi:[0,1,1]\n" \
  "v_pk_fma_f32 v[132:133], v[70:71], v[52:53], v[132:133] op_sel:[0,0,0] op_sel_hi:[0,1,1]\n" \
  "v_pk_fma_f32 v[134:135], v[70:71], v[54:55], v[134:135] op_sel:[0,0,0] op_sel_hi:[0,1,1]\n" \
  "s_set_gpr_idx_off\n" \
  "s_waitcnt vmcnt(6) lgkmcnt(2)\n" \
  "buffer_load_dwordx4 v[120:123], v[12:13], s[52:55], 0 idxen offen\n" \
  "ds_read_u16 v12, v14 offset:92\n" \
  "s_lshr_b32 s61, s61, 8\n" \
  "v_mfma_f32_4x4x4_16b_bf16 v[84:87], v[94:95], v[28:29], 0\n" \
  "v_mfma_f32_4x4x4_16b_bf16 v[88:91], v[94:95], v[30:31], 0\n" \
  "v_mfma_f32_4x4x4_16b_bf16 v[48:51], v[76:77], v[124:125], 0 cbsz:4 abid:15\n" \
  "v_mfma_f32_4x4x4_16b_bf16 v[52:55], v[76:77], v[126:127], 0 cbsz:4 abid:15\n" \
  "v_cvt_pk_bf16_f32 v59, v58, v59\n" \
  "v_lshlrev_b32_sdwa v58, 1, v59 dst_sel:DWORD dst_unused:UNUSED_PAD src0_sel:DWORD src1_sel:WORD_0\n" \
  "v_lshlrev_b32_sdwa v59, 1, v59 dst_sel:DWORD dst_unused:UNUSED_PAD src0_sel:DWORD src1_sel:WORD_1\n" \
  "ds_read_u16_d16_hi v58, v58\n" \
  "ds_read_u16_d16_hi v59, v59\n" \
  "v_cvt_pk_bf16_f32 v77, v84, v88\n" \
  "v_mov_b32 v76, v20\n" \
  "s_set_gpr_idx_on s61, gpr_idx(SRC2,DST)\n" \
  "v_pk_fma_f32 v[128:129], v[66:67], v[48:49], v[128:129] op_sel:[1,0,0] op_sel_hi:[1,1,1]\n" \
  "v_pk_fma_f32 v[130:131], v[66:67], v[50:51], v[130:131] op_sel:[1,0,0] op_sel_hi:[1,1,1]\n" \
  "v_pk_fma_f32 v[132:133], v[70:71], v[52:53], v[132:133] op_sel:[1,0,0] op_sel_hi:[1,1,1]\n" \
  "v_pk_fma_f32 v[134:135], v[70:71], v[54:55], v[134:135] op_sel:[1,0,0] op_sel_hi:[1,1,1]\n" \
  "s_set_gpr_idx_off\n" \
  "s_waitcnt vmcnt(6) lgkmcnt(2)\n" \
  "buffer_load_dwordx4 v[124:127], v[12:13], s[52:55], 0 idxen offen\n" \
  "ds_read_u16 v12, v14 offset:96\n" \
  "s_sub_u32 s60, s60, 1\n" \
  "s_cbranch_scc1 Lend%=\n" \
  "v_readlane_b32 s61, v17, s60\n" \
  "s_setprio 3\n" \
  "v_mfma_f32_4x4x4_16b_bf16 v[64:67], v[92:93], v[32:33], 0 cbsz:4 abid:1\n" \
  "v_mfma_f32_4x4x4_16b_bf16 v[68:71], v[92:93], v[36:37], 0 cbsz:4 abid:1\n" \
  "v_mfma_f32_4x4x4_16b_bf16 v[48:51], v[76:77], v[96:97], 0 cbsz:4\n" \
  "v_mfma_f32_4x4x4_16b_bf16 v[52:55], v[76:77], v[98:99], 0 cbsz:4\n" \
  "v_cvt_pk_bf16_f32 v63, v62, v63\n" \
  "v_lshlrev_b32_sdwa v62, 1, v63 dst_sel:DWORD dst_unused:UNUSED_PAD src0_sel:DWORD src1_sel:WORD_0\n" \
  "v_lshlrev_b32_sdwa v63, 1, v63 dst_sel:DWORD dst_unused:UNUSED_PAD src0_sel:DWORD src1_sel:WORD_1\n" \
  "ds_read_u16_d16_hi v62, v62\n" \
  "ds_read_u16_d16_hi v63, v63\n" \
  "v_add_u32 v14, 64, v14\n" \
  "s_branch Lbody%=\n" \
  "Lep0_%=:\n" \
  "ds_read_b32 v50, v18\n" \
  "s_waitcnt lgkmcnt(0)\n" \
  "v_readfirstlane_b32 s63, v50\n" \
  "s_bitcmp1_b32 s63, 1\n" \
  "s_cbranch_scc1 Lgo%=\n" \
  "v_mov_b64 v[50:51], s[50:51]\n" \
  "s_mov_b64 exec, 1\n" \
  "buffer_store_dwordx2 v[50:51], off, s[24:27], s70 sc1\n" \
  "s_mov_b64 exec, -1\n" \
  "s_nop 3\n" \
  "s_branch Lgo%=\n" \
  "Lbad%=:\n" \
  "s_cmp_eq_u32 s74, 0xffffffc1\n" \
  "s_cbranch_scc1 Lf9_%=\n" \
  "v_mov_b32 v72, 1\n" \
  "v_mov_b32 v73, s62\n" \
  "ds_or_b32 v18, v72\n" \
  "ds_write_b32 v73, v73\n" \
  "s_branch Lf9_%=\n" \
  "Lnf%=:\n" \
  "s_mov_b32 %[bad], 3\n" \
  "s_mov_b32 s79, m0\n" \
  "Lbad2_%=:\n" \
  "s_waitcnt vmcnt(0) lgkmcnt(0)\n" \
  "s_bitcmp1_b32 %[bad], 1\n" \
  "s_cbranch_scc1 Lout%=\n" \
  "s_branch Lep%=\n" \
  "Lh2_%=:\n" \
  "s_waitcnt vmcnt(1)\n" \
  "s_and_b32 s62, %[blk], -8\n" \
  "s_lshl_b32 s62, s62, 7\n" \
  "s_add_u32 s62, s62, 49408\n" \
  "v_lshlrev_b32 v53, 7, v86\n" \
  "s_mov_b64 exec, 0xff\n" \
  "buffer_load_dword v52, v53, s[24:27], s62 offen sc1\n" \
  "s_mov_b64 exec, -1\n" \
  "v_add_co_u32 v90, vcc, 1, v88\n" \
  "s_mul_i32 s63, %[blk], 2176\n" \
  "s_add_u32 s63, s63, 82176\n" \
  "v_addc_co_u32 v91, vcc, 0, v89, vcc\n" \
  "v_lshlrev_b32 v88, 22, v90\n" \
  "v_alignbit_b32 v89, v91, v90, 10\n" \
  "v_add_u32 v87, v87, v88\n" \
  "v_lshl_add_u32 v128, v136, 11, v87\n" \
  "v_mov_b32 v129, v89\n" \
  "v_sub_u32 v51, v137, v136\n" \
  "v_lshl_add_u32 v51, v51, 11, v136\n" \
  "v_add_u32 v130, v51, v87\n" \
  "v_mov_b32 v131, v89\n" \
  "v_sub_u32 v51, v138, v137\n" \
  "v_lshl_add_u32 v51, v51, 11, v137\n" \
  "v_add_u32 v132, v51, v87\n" \
  "v_mov_b32 v133, v89\n" \
  "v_sub_u32 v51, v139, v138\n" \
  "v_lshl_add_u32 v51, v51, 11, v138\n" \
  "v_add_u32 v134, v51, v87\n" \
  "v_mov_b32 v135, v89\n" \
  "buffer_store_dwordx4 v[128:131], v13, s[24:27], s63 offen sc1\n" \
  "buffer_store_dwordx4 v[132:135], v13, s[24:27], s63 offen offset:1024 sc1\n" \
  "buffer_load_dword v100, v54, s[8:11], 0 offen offset:0\n" \
  "buffer_load_dword v101, v54, s[8:11], 0 offen offset:256\n" \
  "buffer_load_dword v102, v54, s[8:11], 0 offen offset:512\n" \
  "buffer_load_dword v103, v54, s[8:11], 0 offen offset:768\n" \
  "buffer_load_dword v104, v54, s[12:15], 0 offen\n" \
  "s_lshr_b32 s63, %[blk], 2\n" \
  "s_lshl_b32 s63, s63, 13\n" \
  "s_and_b32 s93, %[blk], 3\n" \
  "s_lshl_b32 s93, s93, 6\n" \
  "s_add_u32 s63, s63, s93\n" \
  "v_and_b32 v50, 0x300, v13\n" \
  "v_and_b32 v51, 60, v54\n" \
  "v_lshl_add_u32 v50, v50, 3, v51\n" \
  "s_cmp_ge_u32 %[blk], 16\n" \
  "s_cbranch_scc1 Lh4_%=\n" \
  "buffer_load_dword v108, v50, s[16:19], s63 offen offset:0\n" \
  "buffer_load_dword v109, v50, s[16:19], s63 offen offset:256\n" \
  "buffer_load_dword v110, v50, s[16:19], s63 offen offset:512\n" \
  "buffer_load_dword v111, v50, s[16:19], s63 offen offset:768\n" \
  "buffer_load_dword v112, v50, s[16:19], s63 offen offset:1024\n" \
  "buffer_load_dword v113, v50, s[16:19], s63 offen offset:1280\n" \
  "buffer_load_dword v114, v50, s[16:19], s63 offen offset:1536\n" \
  "buffer_load_dword v115, v50, s[16:19], s63 offen offset:1792\n" \
  "Lh4_%=:\n" \
  "s_mov_b64 exec, 0xff\n" \
  "s_cmp_ge_u32 %[blk], 16\n" \
  "s_waitcnt vmcnt(15)\n" \
  "s_cbranch_scc0 Lw3_%=\n" \
  "s_waitcnt vmcnt(7)\n" \
  "s_branch Lw3_%=\n" \
  "Lw2_%=:\n" \
  "buffer_load_dword v52, v53, s[24:27], s62 offen sc1\n" \
  "s_memrealtime s[90:91]\n" \
  "s_waitcnt vmcnt(0) lgkmcnt(0)\n" \
  "s_sub_u32 s90, s90, s76\n" \
  "s_cmp_gt_u32 s90, 220\n" \
  "s_cbranch_scc1 Lw4_%=\n" \
  "Lw3_%=:\n" \
  "v_subrev_u32 v52, s76, v52\n" \
  "v_add_u32 v52, 200, v52\n" \
  "v_cmp_lt_u32 vcc, 400, v52\n" \
  "s_cbranch_vccnz Lw2_%=\n" \
  "s_nop 1\n" \
  "v_max_u32_dpp v52, v52, v52 row_shr:1 row_mask:0xf bank_mask:0xf bound_ctrl:1\n" \
  "s_nop 1\n" \
  "v_max_u32_dpp v52, v52, v52 row_shr:2 row_mask:0xf bank_mask:0xf bound_ctrl:1\n" \
  "s_nop 1\n" \
  "v_max_u32_dpp v52, v52, v52 row_shr:4 row_mask:0xf bank_mask:0xf bound_ctrl:1\n" \
  "s_nop 0\n" \
  "v_readlane_b32 s63, v52, 7\n" \
  "s_sub_u32 s63, s63, 200\n" \
  "s_add_u32 s76, s76, s63\n" \
  "Lw4_%=:\n" \
  "s_mov_b64 exec, -1\n" \
  "Lw1_%=:\n" \
  "s_memrealtime s[90:91]\n" \
  "s_waitcnt lgkmcnt(0)\n" \
  "s_sub_u32 s90, s90, s76\n" \
  "s_cmp_lt_i32 s90, 150\n" \
  "s_cbranch_scc1 Lw1_%=\n" \
  "s_barrier\n" \
  "ds_write_b64 v94, v[90:91] offset:1472\n" \
  "s_mov_b32 s63, 0x05040100\n" \
  "s_mov_b32 s93, 0x07060302\n" \
  "s_cmp_ge_u32 %[blk], 16\n" \
  "s_waitcnt vmcnt(0)\n" \
  "s_cbranch_scc1 Lh5_%=\n" \
  "v_perm_b32 v216, v109, v108, s63\n" \
  "v_perm_b32 v217, v111, v110, s63\n" \
  "v_perm_b32 v218, v113, v112, s63\n" \
  "v_perm_b32 v219, v115, v114, s63\n" \
  "v_perm_b32 v220, v109, v108, s93\n" \
  "v_perm_b32 v221, v111, v110, s93\n" \
  "v_perm_b32 v222, v113, v112, s93\n" \
  "v_perm_b32 v223, v115, v114, s93\n" \
  "s_lshl_b32 s72, %[blk], 11\n" \
  "buffer_store_dwordx4 v[216:219], v13, s[24:27], s72 offen sc1\n" \
  "buffer_store_dwordx4 v[220:223], v13, s[24:27], s72 offen offset:1024 sc1\n" \
  "Lh5_%=:\n" \
  "v_perm_b32 v120, v101, v100, s63\n" \
  "v_perm_b32 v121, v103, v102, s63\n" \
  "v_perm_b32 v122, v101, v100, s93\n" \
  "v_perm_b32 v123, v103, v102, s93\n" \
  "ds_write_b128 v13, v[120:123] offset:36544\n" \
  "v_and_b32 v50, 30, v86\n" \
  "v_and_b32 v51, 16, v13\n" \
  "v_lshl_add_u32 v50, v51, 2, v50\n" \
  "s_mov_b64 exec, 0xffffffff\n" \
  "ds_write_b16 v50, v104 offset:36416\n" \
  "ds_write_b16_d16_hi v50, v104 offset:36448\n" \
  "s_mov_b64 exec, -1\n" \
  "v_and_b32 v100, 0x7fff7fff, v100\n" \
  "v_max_u16_sdwa v100, v100, v100 dst_sel:WORD_1 dst_unused:UNUSED_PAD src0_sel:WORD_0 src1_sel:WORD_1\n" \
  "v_and_b32 v101, 0x7fff7fff, v101\n" \
  "v_max_u16_sdwa v101, v101, v101 dst_sel:WORD_1 dst_unused:UNUSED_PAD src0_sel:WORD_0 src1_sel:WORD_1\n" \
  "v_and_b32 v102, 0x7fff7fff, v102\n" \
  "v_max_u16_sdwa v102, v102, v102 dst_sel:WORD_1 dst_unused:UNUSED_PAD src0_sel:WORD_0 src1_sel:WORD_1\n" \
  "v_and_b32 v103, 0x7fff7fff, v103\n" \
  "v_max_u16_sdwa v103, v103, v103 dst_sel:WORD_1 dst_unused:UNUSED_PAD src0_sel:WORD_0 src1_sel:WORD_1\n" \
  "v_add_f32 v100, v100, v101\n" \
  "v_add_f32 v102, v102, v103\n" \
  "v_add_f32 v100, v100, v102\n" \
  "v_and_b32 v100, 0x7fffffff, v100\n" \
  "s_nop 1\n" \
  "v_max_u32_dpp v100, v100, v100 row_shr:1 row_mask:0xf bank_mask:0xf bound_ctrl:1\n" \
  "s_nop 1\n" \
  "v_max_u32_dpp v100, v100, v100 row_shr:2 row_mask:0xf bank_mask:0xf bound_ctrl:1\n" \
  "s_nop 1\n" \
  "v_max_u32_dpp v100, v100, v100 row_shr:4 row_mask:0xf bank_mask:0xf bound_ctrl:1\n" \
  "s_nop 1\n" \
  "v_max_u32_dpp v100, v100, v100 row_shr:8 row_mask:0xf bank_mask:0xf bound_ctrl:1\n" \
  "s_nop 1\n" \
  "v_max_u32_dpp v100, v100, v100 row_bcast:15 row_mask:0xa bank_mask:0xf\n" \
  "s_nop 1\n" \
  "v_max_u32_dpp v100, v100, v100 row_bcast:31 row_mask:0xc bank_mask:0xf\n" \
  "v_max_u32 v101, 0x0d000000, v100\n" \
  "v_rcp_f32 v101, v101\n" \
  "v_cmp_gt_u32 vcc, 0x71000000, v100\n" \
  "v_mul_f32 v101, 0x437a0000, v101\n" \
  "v_lshrrev_b32 v101, 16, v101\n" \
  "v_cndmask_b32 v101, -1, v101, vcc\n" \
  "s_brev_b64 exec, 1\n" \
  "ds_write_b32 v94, v101 offset:1480\n" \
  "s_mov_b64 exec, -1\n" \
  "s_branch Lc1_%=\n" \
  "Lend%=:\n" \
  "s_and_b32 s63, s44, 3\n" \
  "s_lshl_b32 s63, s63, 11\n" \
  "buffer_load_dwordx4 v[96:99], v13, s[24:27], s63 offen offset:0 sc1\n" \
  "buffer_load_dwordx4 v[100:103], v13, s[24:27], s63 offen offset:1024 sc1\n" \
  "s_add_u32 s63, s63, 8192\n" \
  "buffer_load_dwordx4 v[104:107], v13, s[24:27], s63 offen offset:0 sc1\n" \
  "buffer_load_dwordx4 v[108:111], v13, s[24:27], s63 offen offset:1024 sc1\n" \
  "s_add_u32 s63, s63, 8192\n" \
  "buffer_load_dwordx4 v[112:115], v13, s[24:27], s63 offen offset:0 sc1\n" \
  "buffer_load_dwordx4 v[116:119], v13, s[24:27], s63 offen offset:1024 sc1\n" \
  "s_add_u32 s63, s63, 8192\n" \
  "buffer_load_dwordx4 v[120:123], v13, s[24:27], s63 offen offset:0 sc1\n" \
  "buffer_load_dwordx4 v[124:127], v13, s[24:27], s63 offen offset:1024 sc1\n" \
  "buffer_load_dword v20, off, s[24:27], s70 sc1\n" \
  "s_lshl2_add_u32 s62, s44, 36128\n" \
  "s_cmp_ge_u32 s74, 10\n" \
  "s_cbranch_scc1 Lbad%=\n" \
  "s_cmp_lt_u32 s93, s44\n" \
  "s_cbranch_scc0 Lf1_%=\n" \
  "s_set_gpr_idx_on s73, gpr_idx(SRC0)\n" \
  "v_mov_b32 v84, v128\n" \
  "v_mov_b32 v85, v129\n" \
  "v_mov_b32 v86, v130\n" \
  "v_mov_b32 v87, v131\n" \
  "v_mov_b32 v88, v132\n" \
  "v_mov_b32 v89, v133\n" \
  "v_mov_b32 v90, v134\n" \
  "v_mov_b32 v91, v135\n" \
  "s_set_gpr_idx_off\n" \
  "ds_write_b128 v21, v[84:87]\n" \
  "ds_write_b128 v21, v[88:91] offset:1024\n" \
  "v_mov_b32 v53, s62\n" \
  "ds_write_b32 v53, v53\n" \
  "s_add_u32 s73, s73, 8\n" \
  "s_cmp_eq_u32 s73, 80\n" \
  "s_cselect_b32 s73, 0, s73\n" \
  "s_add_u32 s72, s72, 1056\n" \
  "s_sub_u32 s74, s74, 1\n" \
  "s_cbranch_scc1 Lf9_%=\n" \
  "Lf1_%=:\n" \
  "s_min_u32 s63, s75, 1\n" \
  "s_sub_u32 s60, s74, s63\n" \
  "s_cbranch_scc1 Lf3_%=\n" \
  "Lf2_%=:\n" \
  "s_set_gpr_idx_on s73, gpr_idx(SRC0,SRC1)\n" \
  "v_cvt_pk_bf16_f32 v48, v128, v132\n" \
  "v_cvt_pk_bf16_f32 v49, v129, v133\n" \
  "v_cvt_pk_bf16_f32 v50, v130, v134\n" \
  "v_cvt_pk_bf16_f32 v51, v131, v135\n" \
  "s_set_gpr_idx_off\n" \
  "v_add_u32 v52, s72, v19\n" \
  "s_add_u32 s73, s73, 8\n" \
  "s_cmp_eq_u32 s73, 80\n" \
  "s_cselect_b32 s73, 0, s73\n" \
  "s_add_u32 s72, s72, 1056\n" \
  "ds_write2_b32 v52, v48, v49 offset1:16\n" \
  "ds_write2_b32 v52, v50, v51 offset0:32 offset1:48\n" \
  "s_sub_u32 s60, s60, 1\n" \
  "s_cbranch_scc0 Lf2_%=\n" \
  "Lf3_%=:\n" \
  "s_set_gpr_idx_on s73, gpr_idx(SRC0)\n" \
  "v_mov_b32 v40, v128\n" \
  "v_mov_b32 v41, v129\n" \
  "v_mov_b32 v42, v130\n" \
  "v_mov_b32 v43, v131\n" \
  "v_mov_b32 v44, v132\n" \
  "v_mov_b32 v45, v133\n" \
  "v_mov_b32 v46, v134\n" \
  "v_mov_b32 v47, v135\n" \
  "s_set_gpr_idx_off\n" \
  "s_cmp_eq_u32 s75, 0\n" \
  "s_cbranch_scc1 Lf9_%=\n" \
  "s_mov_b32 s60, s75\n" \
  "v_mov_b32 v72, s62\n" \
  "Lf4_%=:\n" \
  "ds_read_b32 v73, v72 offset:4\n" \
  "ds_read_b128 v[84:87], v21 offset:3328\n" \
  "ds_read_b128 v[88:91], v21 offset:4352\n" \
  "s_waitcnt lgkmcnt(0)\n" \
  "v_readfirstlane_b32 s63, v73\n" \
  "s_cmp_eq_u32 s63, 0\n" \
  "s_cbranch_scc0 Lf5_%=\n" \
  "s_sleep 1\n" \
  "s_branch Lf4_%=\n" \
  "Lf5_%=:\n" \
  "v_pk_add_f32 v[40:41], v[40:41], v[84:85]\n" \
  "v_pk_add_f32 v[42:43], v[42:43], v[86:87]\n" \
  "v_pk_add_f32 v[44:45], v[44:45], v[88:89]\n" \
  "v_pk_add_f32 v[46:47], v[46:47], v[90:91]\n" \
  "v_add_u32 v72, 4, v72\n" \
  "v_add_u32 v21, 3328, v21\n" \
  "s_sub_u32 s60, s60, 1\n" \
  "s_cmp_lg_u32 s60, 0\n" \
  "s_cbranch_scc1 Lf4_%=\n" \
  "v_cvt_pk_bf16_f32 v48, v40, v44\n" \
  "v_cvt_pk_bf16_f32 v49, v41, v45\n" \
  "v_cvt_pk_bf16_f32 v50, v42, v46\n" \
  "v_cvt_pk_bf16_f32 v51, v43, v47\n" \
  "v_add_u32 v52, s72, v19\n" \
  "ds_write2_b32 v52, v48, v49 offset1:16\n" \
  "ds_write2_b32 v52, v50, v51 offset0:32 offset1:48\n" \
  "Lf9_%=:\n" \
  "s_waitcnt lgkmcnt(0)\n" \
  "s_barrier\n" \
  "ds_read_b32 v18, v18\n" \
  "ds_read_b128 v[128:131], v22 offset:0\n" \
  "ds_read_b128 v[132:135], v22 offset:256\n" \
  "ds_read_b128 v[136:139], v22 offset:512\n" \
  "ds_read_b128 v[140:143], v22 offset:768\n" \
  "ds_read_b128 v[144:147], v22 offset:8448\n" \
  "ds_read_b128 v[148:151], v22 offset:8704\n" \
  "ds_read_b128 v[152:155], v22 offset:8960\n" \
  "ds_read_b128 v[156:159], v22 offset:9216\n" \
  "ds_read_b128 v[160:163], v22 offset:16896\n" \
  "ds_read_b128 v[164:167], v22 offset:17152\n" \
  "ds_read_b128 v[168:171], v22 offset:17408\n" \
  "ds_read_b128 v[172:175], v22 offset:17664\n" \
  "ds_read_b128 v[176:179], v22 offset:25344\n" \
  "ds_read_b128 v[180:183], v22 offset:25600\n" \
  "s_waitcnt lgkmcnt(14)\n" \
  "v_readfirstlane_b32 %[bad], v18\n" \
  "s_cmp_lg_u32 %[bad], 0\n" \
  "s_cbranch_scc1 Lbad2_%=\n" \
  "s_waitcnt vmcnt(7) lgkmcnt(13)\n" \
  "v_mfma_f32_16x16x32_bf16 v[40:43], v[128:131], v[96:99], 0\n" \
  "v_mfma_f32_16x16x32_bf16 v[44:47], v[128:131], v[100:103], 0\n" \
  "ds_read_b128 v[184:187], v22 offset:25856\n" \
  "s_waitcnt vmcnt(5) lgkmcnt(13)\n" \
  "v_mfma_f32_16x16x32_bf16 v[40:43], v[132:135], v[104:107], v[40:43]\n" \
  "v_mfma_f32_16x16x32_bf16 v[44:47], v[132:135], v[108:111], v[44:47]\n" \
  "ds_read_b128 v[188:191], v22 offset:26112\n" \
  "s_waitcnt vmcnt(3) lgkmcnt(13)\n" \
  "v_mfma_f32_16x16x32_bf16 v[40:43], v[136:139], v[112:115], v[40:43]\n" \
  "v_mfma_f32_16x16x32_bf16 v[44:47], v[136:139], v[116:119], v[44:47]\n" \
  "ds_read_b128 v[192:195], v22 offset:33792\n" \
  "s_waitcnt vmcnt(1) lgkmcnt(13)\n" \
  "v_mfma_f32_16x16x32_bf16 v[40:43], v[140:143], v[120:123], v[40:43]\n" \
  "v_mfma_f32_16x16x32_bf16 v[44:47], v[140:143], v[124:127], v[44:47]\n" \
  "ds_read_b128 v[196:199], v22 offset:34048\n" \
  "s_waitcnt lgkmcnt(13)\n" \
  "v_mfma_f32_16x16x32_bf16 v[48:51], v[144:147], v[96:99], 0\n" \
  "v_mfma_f32_16x16x32_bf16 v[52:55], v[144:147], v[100:103], 0\n" \
  "ds_read_b128 v[200:203], v22 offset:34304\n" \
  "s_waitcnt lgkmcnt(13)\n" \
  "v_mfma_f32_16x16x32_bf16 v[48:51], v[148:151], v[104:107], v[48:51]\n" \
  "v_mfma_f32_16x16x32_bf16 v[52:55], v[148:151], v[108:111], v[52:55]\n" \
  "ds_read_b128 v[204:207], v22 offset:34560\n" \
  "s_waitcnt lgkmcnt(13)\n" \
  "v_mfma_f32_16x16x32_bf16 v[48:51], v[152:155], v[112:115], v[48:51]\n" \
  "v_mfma_f32_16x16x32_bf16 v[52:55], v[152:155], v[116:119], v[52:55]\n" \
  "v_cvt_pk_bf16_f32 v40, v40, v41\n" \
  "v_cvt_pk_bf16_f32 v41, v42, v43\n" \
  "v_cvt_pk_bf16_f32 v42, v44, v45\n" \
  "v_cvt_pk_bf16_f32 v43, v46, v47\n" \
  "buffer_store_dwordx4 v[40:43], v23, s[56:59], 0 offen sc1\n" \
  "s_waitcnt lgkmcnt(12)\n" \
  "v_mfma_f32_16x16x32_bf16 v[48:51], v[156:159], v[120:123], v[48:51]\n" \
  "v_mfma_f32_16x16x32_bf16 v[52:55], v[156:159], v[124:127], v[52:55]\n" \
  "s_waitcnt lgkmcnt(11)\n" \
  "v_mfma_f32_16x16x32_bf16 v[56:59], v[160:163], v[96:99], 0\n" \
  "v_mfma_f32_16x16x32_bf16 v[60:63], v[160:163], v[100:103], 0\n" \
  "s_waitcnt lgkmcnt(10)\n" \
  "v_mfma_f32_16x16x32_bf16 v[56:59], v[164:167], v[104:107], v[56:59]\n" \
  "v_mfma_f32_16x16x32_bf16 v[60:63], v[164:167], v[108:111], v[60:63]\n" \
  "s_waitcnt lgkmcnt(9)\n" \
  "v_mfma_f32_16x16x32_bf16 v[56:59], v[168:171], v[112:115], v[56:59]\n" \
  "v_mfma_f32_16x16x32_bf16 v[60:63], v[168:171], v[116:119], v[60:63]\n" \
  "v_cvt_pk_bf16_f32 v48, v48, v49\n" \
  "v_cvt_pk_bf16_f32 v49, v50, v51\n" \
  "v_cvt_pk_bf16_f32 v50, v52, v53\n" \
  "v_cvt_pk_bf16_f32 v51, v54, v55\n" \
  "s_movk_i32 s63, 0x1000\n" \
  "s_lshl_b32 s63, s63, 1\n" \
  "buffer_store_dwordx4 v[48:51], v23, s[56:59], s63 offen sc1\n" \
  "s_waitcnt lgkmcnt(8)\n" \
  "v_mfma_f32_16x16x32_bf16 v[56:59], v[172:175], v[120:123], v[56:59]\n" \
  "v_mfma_f32_16x16x32_bf16 v[60:63], v[172:175], v[124:127], v[60:63]\n" \
  "s_waitcnt lgkmcnt(7)\n" \
  "v_mfma_f32_16x16x32_bf16 v[64:67], v[176:179], v[96:99], 0\n" \
  "v_mfma_f32_16x16x32_bf16 v[68:71], v[176:179], v[100:103], 0\n" \
  "s_waitcnt lgkmcnt(6)\n" \
  "v_mfma_f32_16x16x32_bf16 v[64:67], v[180:183], v[104:107], v[64:67]\n" \
  "v_mfma_f32_16x16x32_bf16 v[68:71], v[180:183], v[108:111], v[68:71]\n" \
  "s_waitcnt lgkmcnt(5)\n" \
  "v_mfma_f32_16x16x32_bf16 v[64:67], v[184:187], v[112:115], v[64:67]\n" \
  "v_mfma_f32_16x16x32_bf16 v[68:71], v[184:187], v[116:119], v[68:71]\n" \
  "v_cvt_pk_bf16_f32 v56, v56, v57\n" \
  "v_cvt_pk_bf16_f32 v57, v58, v59\n" \
  "v_cvt_pk_bf16_f32 v58, v60, v61\n" \
  "v_cvt_pk_bf16_f32 v59, v62, v63\n" \
  "s_movk_i32 s63, 0x2000\n" \
  "s_lshl_b32 s63, s63, 1\n" \
  "buffer_store_dwordx4 v[56:59], v23, s[56:59], s63 offen sc1\n" \
  "s_waitcnt lgkmcnt(4)\n" \
  "v_mfma_f32_16x16x32_bf16 v[64:67], v[188:191], v[120:123], v[64:67]\n" \
  "v_mfma_f32_16x16x32_bf16 v[68:71], v[188:191], v[124:127], v[68:71]\n" \
  "s_waitcnt lgkmcnt(3)\n" \
  "v_mfma_f32_16x16x32_bf16 v[72:75], v[192:195], v[96:99], 0\n" \
  "v_mfma_f32_16x16x32_bf16 v[76:79], v[192:195], v[100:103], 0\n" \
  "s_waitcnt lgkmcnt(2)\n" \
  "v_mfma_f32_16x16x32_bf16 v[72:75], v[196:199], v[104:107], v[72:75]\n" \
  "v_mfma_f32_16x16x32_bf16 v[76:79], v[196:199], v[108:111], v[76:79]\n" \
  "s_waitcnt lgkmcnt(1)\n" \
  "v_mfma_f32_16x16x32_bf16 v[72:75], v[200:203], v[112:115], v[72:75]\n" \
  "v_mfma_f32_16x16x32_bf16 v[76:79], v[200:203], v[116:119], v[76:79]\n" \
  "v_cvt_pk_bf16_f32 v64, v64, v65\n" \
  "v_cvt_pk_bf16_f32 v65, v66, v67\n" \
  "v_cvt_pk_bf16_f32 v66, v68, v69\n" \
  "v_cvt_pk_bf16_f32 v67, v70, v71\n" \
  "s_movk_i32 s63, 0x3000\n" \
  "s_lshl_b32 s63, s63, 1\n" \
  "buffer_store_dwordx4 v[64:67], v23, s[56:59], s63 offen sc1\n" \
  "s_waitcnt lgkmcnt(0)\n" \
  "v_mfma_f32_16x16x32_bf16 v[72:75], v[204:207], v[120:123], v[72:75]\n" \
  "v_mfma_f32_16x16x32_bf16 v[76:79], v[204:207], v[124:127], v[76:79]\n" \
  "s_nop 7\n" \
  "v_cvt_pk_bf16_f32 v72, v72, v73\n" \
  "v_cvt_pk_bf16_f32 v73, v74, v75\n" \
  "v_cvt_pk_bf16_f32 v74, v76, v77\n" \
  "v_cvt_pk_bf16_f32 v75, v78, v79\n" \
  "s_movk_i32 s63, 0x4000\n" \
  "s_lshl_b32 s63, s63, 1\n" \
  "buffer_store_dwordx4 v[72:75], v23, s[56:59], s63 offen sc1\n" \
  "s_waitcnt vmcnt(5)\n" \
  "v_readfirstlane_b32 s63, v20\n" \
  "s_cmp_eq_u32 s63, s50\n" \
  "s_cbranch_scc1 Lout%=\n" \
  "Lep%=:\n" \
  "s_cmp_lg_u32 s44, 0\n" \
  "s_cbranch_scc1 Lout%=\n" \
  "v_mov_b64 v[84:85], s[50:51]\n" \
  "s_mov_b64 exec, 1\n" \
  "buffer_store_dwordx2 v[84:85], off, s[24:27], s70 sc1\n" \
  "s_mov_b64 exec, -1\n" \
  "Lout%=:\n" \
  "s_mov_b32 m0, s79\n"
#define STREAM_CLOBBERS \
  "v12", "v13", "v14", "v15", "v16", "v17", "v18", "v19", "v20", "v21", "v22", "v23", "v24", "v25", "v26", \
  "v27", "v28", "v29", "v30", "v31", "v32", "v33", "v34", "v35", "v36", "v37", "v38", "v39", "v40", "v41", \
  "v42", "v43", "v44", "v45", "v46", "v47", "v48", "v49", "v50", "v51", "v52", "v53", "v54", "v55", "v56", \
  "v57", "v58", "v59", "v60", "v61", "v62", "v63", "v64", "v65", "v66", "v67", "v68", "v69", "v70", "v71", \
  "v72", "v73", "v74", "v75", "v76", "v77", "v78", "v79", "v80", "v81", "v82", "v83", "v84", "v85", "v86", \
  "v87", "v88", "v89", "v90", "v91", "v92", "v93", "v94", "v95", "v96", "v97", "v98", "v99", "v100", "v101", \
  "v102", "v103", "v104", "v105", "v106", "v107", "v108", "v109", "v110", "v111", "v112", "v113", "v114", \
  "v115", "v116", "v117", "v118", "v119", "v120", "v121", "v122", "v123", "v124", "v125", "v126", "v127", \
  "v128", "v129", "v130", "v131", "v132", "v133", "v134", "v135", "v136", "v137", "v138", "v139", "v140", \
  "v141", "v142", "v143", "v144", "v145", "v146", "v147", "v148", "v149", "v150", "v151", "v152", "v153", \
  "v154", "v155", "v156", "v157", "v158", "v159", "v160", "v161", "v162", "v163", "v164", "v165", "v166", \
  "v167", "v168", "v169", "v170", "v171", "v172", "v173", "v174", "v175", "v176", "v177", "v178", "v179", \
  "v180", "v181", "v182", "v183", "v184", "v185", "v186", "v187", "v188", "v189", "v190", "v191", "v192", \
  "v193", "v194", "v195", "v196", "v197", "v198", "v199", "v200", "v201", "v202", "v203", "v204", "v205", \
  "v206", "v207", "v208", "v209", "v210", "v211", "v212", "v213", "v214", "v215", "v216", "v217", "v218", \
  "v219", "v220", "v221", "v222", "v223", "v224", "v225", "v226", "v227", "v228", "v229", "v230", "v231", \
  "v232", "v233", "v234", "v235", "vcc", "s8", "s9", "s10", "s11", "s12", "s13", "s14", "s15", "s16", "s17", \
  "s18", "s19", "s20", "s21", "s22", "s23", "s24", "s25", "s26", "s27", "s28", "s29", "s30", "s31", "s33", \
  "s34", "s35", "s36", "s37", "s38", "s39", "s40", "s41", "s42", "s43", "s44", "s45", "s46", "s47", "s48", \
  "s49", "s50", "s51", "s52", "s53", "s54", "s55", "s56", "s57", "s58", "s59", "s60", "s61", "s62", "s63", \
  "s64", "s65", "s66", "s67", "s68", "s69", "s70", "s71", "s72", "s73", "s74", "s75", "s76", "s77", "s78", \
  "s79", "s80", "s81", "s82", "s83", "s84", "s85", "s86", "s87", "s88", "s89", "s90", "s91", "s92", "s93", \
  "s94", "s95", "scc", "memory"
#define LD_CELL 34560
#define LD_DEG 35584
#define LD_FLAG 36096
#define LD_CRD 36128
#define LD_SINK 36160
#define LD_CGT 36416
#define LD_WQT 36544
#define LD_TAG 37568
#define LD_PAY 37584
#define LD_ENT 100096
#define LD_TB 106816
#define LD_ROWS 108864
#define LD_SK 129344
#define LD_HIST 134464
#define LD_HB 136512
#define WS_EP 32768
#define WS_FB 33024
#define WS_KF 49408
#define WS_GR 82176
#define WS_RECK 639232
#define WS_RECP 1949952
#define WS_END 7192832

namespace {

constexpr int C_ = 128;
constexpr int NMAX = 40;
constexpr int NB = 256;
constexpr int CH = 1280;
constexpr int MT = 512;
constexpr int MW = MT / 64;
constexpr int RP = 1056;
constexpr unsigned W3 = 0x00020000u;

__device__ __forceinline__ float bf2f(unsigned short v) { return __uint_as_float(((unsigned)v) << 16); }
__device__ __forceinline__ unsigned short f2bf(float f) {
  __hip_bfloat16 b = __float2bfloat16(f);
  return *reinterpret_cast<unsigned short*>(&b);
}
__device__ __forceinline__ unsigned pk2bf(float lo, float hi) {
  return __builtin_bit_cast(unsigned, __builtin_convertvector(f32x2{lo, hi}, bf16x2));
}

}

extern "C" __global__ __launch_bounds__(MT) void tfn_kernel(
    const int* __restrict__ dst, unsigned dn, unsigned dw, const int* __restrict__ src, unsigned sn, unsigned sw,
    const u32x2* __restrict__ Y, unsigned yn, unsigned yw, const u32x2* __restrict__ ef, unsigned fn, unsigned fw,
    char* ws, unsigned wn, unsigned ww, const unsigned short* __restrict__ x, unsigned xn, unsigned xw,
    u32x4* __restrict__ out, unsigned pt, int N, unsigned M2, unsigned cb, int nbk, unsigned npb,
    const unsigned short* __restrict__ wrad, unsigned rn, unsigned rw, const unsigned short* __restrict__ cg, unsigned cn,
    unsigned cw, const unsigned short* __restrict__ wmix, unsigned mn, unsigned mw) {
  __shared__ __attribute__((aligned(16))) char lds[LD_ROWS + NMAX * RP];
  TDECL;
  MSTAMP(0);
  unsigned bad;
  asm volatile(STREAM_ASM
               : [bad] "=&s"(bad)
               : [ka] "s"(__builtin_amdgcn_kernarg_segment_ptr()), [blk] "s"(blockIdx.x), [tid] "v"(threadIdx.x)
               : STREAM_CLOBBERS);
  const bool slow = bad != 0;
  MSTAMP(6);
  if (slow) {
    const int lane = threadIdx.x & 63, w = __builtin_amdgcn_readfirstlane(threadIdx.x >> 6), E = dn / 4;
    const int bucket = (blockIdx.x & 7) * (nbk / 8) + (blockIdx.x >> 3);
    int n01[2];
#pragma unroll
    for (int h = 0; h < 2; h++) {
      unsigned c = (unsigned)(((unsigned long long)(bucket + h) * npb) >> 20);
#pragma unroll
      for (int r = 0; r < 2; r++) c += __umulhi(c, M2) < (unsigned)(bucket + h) ? 1 : 0;
      n01[h] = min(c, (unsigned)N);
    }
    const int node0 = n01[0], nn = n01[1] - n01[0];
    const unsigned mxa = LD_ROWS + ((w >> 2) * 4 + ((lane & 15) >> 2)) * RP + (lane & 3) * 64 + (lane >> 4) * 16;
    const unsigned mxo = ((w >> 2) * 4 + (lane >> 4)) * 1024 + (w & 3) * 256 + (lane & 15) * 16;
    __syncthreads();
#pragma unroll 1
    for (int it = w; it < 2 * nn; it += MW) {
      const int dl = it >> 1, ch = (it & 1) * 64 + lane;
      float acc[4] = {0.f, 0.f, 0.f, 0.f};
#pragma unroll 1
      for (int eb = 0; eb < E; eb += 64) {
        unsigned long long m = __ballot(eb + lane < E && dst[min(eb + lane, E - 1)] == node0 + dl);
#pragma unroll 1
        for (; m; m &= m - 1) {
          const int e = eb + __builtin_ctzll(m);
          const u32x2 ry = Y[e], rf = ef[e];
          float yy[4] = {bf2f(ry[0] & 0xffff), bf2f(ry[0] >> 16), bf2f(ry[1] & 0xffff), bf2f(ry[1] >> 16)};
          float ee[4] = {bf2f(rf[0] & 0xffff), bf2f(rf[0] >> 16), bf2f(rf[1] & 0xffff), bf2f(rf[1] >> 16)};
          float z = 0.f;
#pragma unroll
          for (int k = 0; k < 4; k++) z += ee[k] * bf2f(wrad[k * C_ + ch]);
          const float R = z / (1.f + __expf(-z));
          const unsigned short* xr = x + (size_t)src[e] * 512 + ch * 4;
#pragma unroll 1
          for (int o = 0; o < 4; o++) {
            float tp = 0.f;
#pragma unroll 1
            for (int i = 0; i < 4; i++) {
              float A = 0.f;
#pragma unroll
              for (int f = 0; f < 4; f++) A += yy[f] * bf2f(cg[i * 16 + f * 4 + o]);
              tp += bf2f(xr[i]) * bf2f(f2bf(A));
            }
            const float v = R * tp;
            acc[0] += o == 0 ? v : 0.f;
            acc[1] += o == 1 ? v : 0.f;
            acc[2] += o == 2 ? v : 0.f;
            acc[3] += o == 3 ? v : 0.f;
          }
        }
      }
      unsigned short* ar = reinterpret_cast<unsigned short*>(lds + LD_ROWS) + dl * (RP / 2) + (ch >> 5) * 128 + (ch & 31);
#pragma unroll
      for (int o = 0; o < 4; o++) ar[o * 32] = f2bf(acc[o]);
    }
    __syncthreads();
    const auto ors = __builtin_amdgcn_make_buffer_rsrc((void*)(out + (size_t)node0 * 64), 0, nn * 1024, 0x00020000);
#pragma unroll 1
    for (int t = 0; t < NMAX / 8; t++) {
      f32x4 a = {0.f, 0.f, 0.f, 0.f}, b = {0.f, 0.f, 0.f, 0.f};
#pragma unroll 1
      for (int k = 0; k < 4; k++) {
        const s16x8 f = *reinterpret_cast<const s16x8*>(lds + mxa + t * 8 * RP + k * 256);
        const unsigned short* wp = wmix + (32 * k + 8 * (lane >> 4)) * C_ + 32 * (w & 3) + 2 * (lane & 15);
        s16x8 w0, w1;
#pragma unroll
        for (int j = 0; j < 8; j++) {
          w0[j] = wp[j * C_];
          w1[j] = wp[j * C_ + 1];
        }
        a = __builtin_amdgcn_mfma_f32_16x16x32_bf16(f, w0, a, 0, 0, 0);
        b = __builtin_amdgcn_mfma_f32_16x16x32_bf16(f, w1, b, 0, 0, 0);
      }
      __builtin_amdgcn_raw_buffer_store_b128(u32x4{pk2bf(a[0], a[1]), pk2bf(a[2], a[3]), pk2bf(b[0], b[1]), pk2bf(b[2], b[3])},
                                             ors, mxo + t * 8192, 0, 0);
    }
  }
  MFLUSH;
}

extern "C" void kernel_launch(void* const* d_in, const int* in_sizes, int n_in, void* d_out, int out_size,
                              void* d_ws, size_t ws_size, hipStream_t stream) {
  const int N = in_sizes[0] / 512;
  const int E = in_sizes[1];
  int nbk = NB;
  while ((N + nbk - 1) / nbk > NMAX || (N % nbk == 0 && N / nbk >= NMAX)) nbk += NB;
  const unsigned long long M2l = (((unsigned long long)nbk) << 32) / (unsigned long long)N;
  const unsigned M2 = M2l > 0xffffffffull ? 0xffffffffu : (unsigned)M2l;
  const unsigned long long cb = (16ull << 32) / (unsigned long long)N;
  const bool fast = nbk == NB && E <= NB * CH && ws_size >= WS_END;
  const unsigned e4 = E * 4u;
  tfn_kernel<<<nbk, MT, 0, stream>>>(
      (const int*)d_in[2], e4, W3, (const int*)d_in[1], e4, W3, (const u32x2*)d_in[3], 2 * e4, W3, (const u32x2*)d_in[4], 2 * e4, W3,
      (char*)d_ws, WS_END, W3, (const unsigned short*)d_in[0], N * 1024u, W3, (u32x4*)d_out, fast ? 448u : 0u, N, M2,
      cb > 0xffffffull ? 0xffffffu : (unsigned)cb, nbk, (unsigned)((1ull << 52) / M2), (const unsigned short*)d_in[5], 4 * C_ * 2,
      W3, (const unsigned short*)d_in[6], 128, W3, (const unsigned short*)d_in[7], C_ * C_ * 2, W3);
}
```

```cpp
#include <hip/hip_runtime.h>
#include <hip/hip_bf16.h>
#include <stdint.h>
#include <stddef.h>

typedef __attribute__((ext_vector_type(2))) float f32x2;
typedef __attribute__((ext_vector_type(4))) float f32x4;
typedef __attribute__((ext_vector_type(8))) short s16x8;
typedef __attribute__((ext_vector_type(2))) unsigned int u32x2;
typedef __attribute__((ext_vector_type(4))) unsigned int u32x4;
typedef __attribute__((ext_vector_type(16))) unsigned int u32x16;
typedef __attribute__((ext_vector_type(2))) __bf16 bf16x2;

#define TDECL
#define TSTAMP(i)
#define TFLUSH
#define MSTAMP(i) TSTAMP(i)
#define MFLUSH do { asm volatile("s_waitcnt vmcnt(0)" ::: "memory"); TSTAMP(15); TFLUSH; } while (0)
#undef MFLUSH
#define MFLUSH

#define STREAM_ASM \
  "s_load_dwordx16 s[8:23], %[ka], 0\n" \
  "s_load_dwordx16 s[24:39], %[ka], 64\n" \
  "s_load_dwordx4 s[52:55], %[ka], 160\n" \
  "s_getpc_b64 s[40:41]\n" \
  "Lpc%=:\n" \
  "v_lshlrev_b32 v50, 7, %[tid]\n" \
  "s_mov_b32 s42, Lout%=-Lpc%=\n" \
  "s_mov_b32 s43, 0x20000\n" \
  "buffer_load_dword v208, v50, s[40:43], 0 offen\n" \
  "v_lshlrev_b32 v52, 3, %[tid]\n" \
  "v_lshlrev_b32 v53, 2, %[tid]\n" \
  "v_mov_b32 v48, 0\n" \
  "v_readfirstlane_b32 s44, %[tid]\n" \
  "v_add_u32 v49, 134464, v53\n" \
  "ds_write_b32 v49, v48\n" \
  "s_mul_i32 s63, %[blk], 5120\n" \
  "s_add_u32 s61, s63, 3584\n" \
  "s_cmp_lt_u32 s44, 384\n" \
  "s_cselect_b32 s61, s61, 0x40000000\n" \
  "s_waitcnt lgkmcnt(0)\n" \
  "s_cmp_lt_u32 s44, s34\n" \
  "s_cbranch_scc0 Lh0_%=\n" \
  "buffer_load_dwordx2 v[100:101], v52, s[8:11], s63 offen nt\n" \
  "buffer_load_dword v102, v53, s[8:11], s61 offen nt\n" \
  "s_barrier\n" \
  "buffer_load_dwordx2 v[104:105], v52, s[12:15], s63 offen nt\n" \
  "buffer_load_dword v106, v53, s[12:15], s61 offen nt\n" \
  "v_lshlrev_b32 v49, 1, v52\n" \
  "s_lshl_b32 s93, s63, 1\n" \
  "s_lshl_b32 s62, s61, 1\n" \
  "buffer_load_dwordx4 v[108:111], v49, s[16:19], s93 offen nt\n" \
  "buffer_load_dwordx4 v[114:117], v49, s[20:23], s93 offen nt\n" \
  "buffer_load_dwordx2 v[112:113], v52, s[16:19], s62 offen nt\n" \
  "buffer_load_dwordx2 v[118:119], v52, s[20:23], s62 offen nt\n" \
  "s_sub_i32 s63, s10, s63\n" \
  "s_sub_i32 s61, s10, s61\n" \
  "v_cmp_gt_i32 s[80:81], s63, v52\n" \
  "s_sub_i32 s63, s63, 4\n" \
  "v_cmp_gt_i32 s[84:85], s61, v53\n" \
  "v_cmp_gt_i32 s[82:83], s63, v52\n" \
  "s_branch Lh1_%=\n" \
  "Lh0_%=:\n" \
  "s_mov_b32 s93, 0\n" \
  "buffer_load_dwordx2 v[88:89], off, s[24:27], s93\n" \
  "s_barrier\n" \
  "s_load_dwordx8 s[8:15], %[ka], 128\n" \
  "s_mov_b64 s[80:81], 0\n" \
  "s_mov_b64 s[82:83], 0\n" \
  "s_mov_b64 s[84:85], 0\n" \
  "s_cmp_eq_u32 s34, 0\n" \
  "s_cbranch_scc1 Lnf%=\n" \
  "Lh1_%=:\n" \
  "ds_write_b32 v53, v48 offset:34560\n" \
  "s_lshr_b32 s44, s44, 6\n" \
  "v_and_b32 v86, 63, %[tid]\n" \
  "s_lshl_b32 s72, s44, 3\n" \
  "s_lshl_b32 s63, 1, s72\n" \
  "v_mov_b32 v93, s63\n" \
  "s_and_b32 s45, s72, 24\n" \
  "s_lshl_b64 s[46:47], -1, s72\n" \
  "s_and_b32 s46, s46, 0x01010101\n" \
  "s_and_b32 s47, s47, 0x01010101\n" \
  "s_and_b32 s73, s44, 4\n" \
  "s_add_u32 s74, s73, 134464\n" \
  "s_lshl_b32 s75, s44, 9\n" \
  "s_add_u32 s75, s75, 136512\n" \
  "v_lshlrev_b32 v13, 4, v86\n" \
  "v_lshlrev_b32 v54, 2, v86\n" \
  "v_lshl_add_u32 v52, v86, 3, s75\n" \
  "v_lshlrev_b32 v50, 5, v86\n" \
  "v_add_u32 v50, 134464, v50\n" \
  "s_and_b32 s71, %[blk], 7\n" \
  "s_lshr_b32 s66, %[blk], 3\n" \
  "s_lshl_b32 s68, s71, 5\n" \
  "s_add_u32 s68, s68, s66\n" \
  "s_and_b32 s61, s68, 2\n" \
  "s_lshl_b32 s61, s61, 9\n" \
  "v_and_b32 v55, 0xf8, v54\n" \
  "s_and_b32 s63, s68, 0xfc\n" \
  "s_lshl2_add_u32 s61, s63, s61\n" \
  "v_add_u32 v55, s44, v55\n" \
  "s_and_b32 s63, s68, 1\n" \
  "s_lshl3_add_u32 s61, s63, s61\n" \
  "v_mul_u32_u24 v17, 2176, v55\n" \
  "s_add_u32 s61, s61, 49408\n" \
  "v_mov_b32 v94, 36096\n" \
  "s_mul_i32 s63, s71, s35\n" \
  "s_lshr_b32 s63, s63, 3\n" \
  "s_lshl3_add_u32 s66, s66, s44\n" \
  "s_mul_i32 s66, s66, 1024\n" \
  "s_lshl_b32 s63, s63, 10\n" \
  "s_add_u32 s66, s66, s63\n" \
  "v_lshlrev_b32 v20, 3, v13\n" \
  "v_cmp_gt_u32 vcc, 8, v86\n" \
  "v_bfrev_b32 v51, 1\n" \
  "s_mov_b32 s79, m0\n" \
  "s_mov_b32 m0, 36160\n" \
  "v_cndmask_b32 v20, v51, v20, vcc\n" \
  "s_mov_b32 s63, 0x01010101\n" \
  "s_waitcnt vmcnt(6)\n" \
  "v_mul_hi_u32 v96, v100, s36\n" \
  "v_mul_hi_u32 v97, v101, s36\n" \
  "v_mul_hi_u32 v98, v102, s36\n" \
  "s_mov_b64 exec, s[80:81]\n" \
  "v_lshl_add_u32 v120, v96, 3, s74\n" \
  "ds_add_rtn_u32 v124, v120, v93\n" \
  "s_mov_b64 exec, s[82:83]\n" \
  "v_lshl_add_u32 v121, v97, 3, s74\n" \
  "ds_add_rtn_u32 v125, v121, v93\n" \
  "s_mov_b64 exec, s[84:85]\n" \
  "v_lshl_add_u32 v122, v98, 3, s74\n" \
  "ds_add_rtn_u32 v126, v122, v93\n" \
  "s_mov_b64 exec, -1\n" \
  "v_lshl_add_u32 v96, v96, 1, s75\n" \
  "v_lshl_add_u32 v97, v97, 1, s75\n" \
  "v_lshl_add_u32 v98, v98, 1, s75\n" \
  "s_waitcnt lgkmcnt(0)\n" \
  "s_barrier\n" \
  "ds_read_b128 v[128:131], v50\n" \
  "ds_read_b128 v[132:135], v50 offset:16\n" \
  "v_bfe_u32 v124, v124, s45, 8\n" \
  "v_bfe_u32 v125, v125, s45, 8\n" \
  "v_bfe_u32 v126, v126, s45, 8\n" \
  "s_waitcnt lgkmcnt(0)\n" \
  "v_dot4_u32_u8 v136, v128, s63, 0\n" \
  "v_dot4_u32_u8 v136, v129, s63, v136\n" \
  "v_dot4_u32_u8 v137, v130, s63, v136\n" \
  "v_dot4_u32_u8 v137, v131, s63, v137\n" \
  "v_dot4_u32_u8 v138, v132, s63, v137\n" \
  "v_dot4_u32_u8 v138, v133, s63, v138\n" \
  "v_dot4_u32_u8 v139, v134, s63, v138\n" \
  "v_dot4_u32_u8 v139, v135, s63, v139\n" \
  "v_dot4_u32_u8 v140, v128, s46, 0\n" \
  "v_dot4_u32_u8 v141, v130, s46, 0\n" \
  "v_dot4_u32_u8 v142, v132, s46, 0\n" \
  "v_add_u32_dpp v87, v139, v139 row_shr:1 row_mask:0xf bank_mask:0xf bound_ctrl:1\n" \
  "v_dot4_u32_u8 v143, v134, s46, 0\n" \
  "v_dot4_u32_u8 v140, v129, s47, v140\n" \
  "v_add_u32_dpp v87, v87, v87 row_shr:2 row_mask:0xf bank_mask:0xf bound_ctrl:1\n" \
  "v_dot4_u32_u8 v141, v131, s47, v141\n" \
  "v_dot4_u32_u8 v142, v133, s47, v142\n" \
  "v_add_u32_dpp v87, v87, v87 row_shr:4 row_mask:0xf bank_mask:0xf bound_ctrl:1\n" \
  "v_dot4_u32_u8 v143, v135, s47, v143\n" \
  "s_mov_b32 s63, 0x10001\n" \
  "v_add_u32_dpp v87, v87, v87 row_shr:8 row_mask:0xf bank_mask:0xf bound_ctrl:1\n" \
  "v_sub_u32 v140, v136, v140\n" \
  "v_sub_u32 v141, v137, v141\n" \
  "v_add_u32_dpp v87, v87, v87 row_bcast:15 row_mask:0xa bank_mask:0xf\n" \
  "v_sub_u32 v142, v138, v142\n" \
  "v_sub_u32 v143, v139, v143\n" \
  "v_add_u32_dpp v87, v87, v87 row_bcast:31 row_mask:0xc bank_mask:0xf\n" \
  "v_lshl_or_b32 v140, v141, 16, v140\n" \
  "v_sub_u32 v87, v87, v139\n" \
  "v_lshl_or_b32 v141, v143, 16, v142\n" \
  "s_cmp_eq_u32 s44, 7\n" \
  "v_mad_u32_u24 v140, v87, s63, v140\n" \
  "v_mad_u32_u24 v141, v87, s63, v141\n" \
  "s_cbranch_scc1 Lh2_%=\n" \
  "ds_write_b64 v52, v[140:141]\n" \
  "ds_read_u16 v96, v96\n" \
  "ds_read_u16 v97, v97\n" \
  "ds_read_u16 v98, v98\n" \
  "s_mov_b32 s63, 129344\n" \
  "s_mov_b32 s93, 108864\n" \
  "s_lshl_b32 s74, s44, 10\n" \
  "v_add_u32 v50, s74, v13\n" \
  "v_add_u32 v51, s93, v50\n" \
  "s_waitcnt vmcnt(4)\n" \
  "v_lshl_or_b32 v104, v100, 24, v104\n" \
  "v_lshl_or_b32 v105, v101, 24, v105\n" \
  "v_lshl_or_b32 v106, v102, 24, v106\n" \
  "s_waitcnt lgkmcnt(0)\n" \
  "s_mov_b64 exec, s[80:81]\n" \
  "v_add_u32 v124, v124, v96\n" \
  "v_lshl_add_u32 v120, v124, 2, s63\n" \
  "v_lshl_add_u32 v124, v124, 4, s93\n" \
  "ds_write_b32 v120, v104\n" \
  "s_mov_b64 exec, s[82:83]\n" \
  "v_add_u32 v125, v125, v97\n" \
  "v_lshl_add_u32 v121, v125, 2, s63\n" \
  "v_lshl_add_u32 v125, v125, 4, s93\n" \
  "ds_write_b32 v121, v105\n" \
  "s_mov_b64 exec, s[84:85]\n" \
  "v_add_u32 v126, v126, v98\n" \
  "v_lshl_add_u32 v122, v126, 2, s63\n" \
  "v_lshl_add_u32 v126, v126, 4, s93\n" \
  "ds_write_b32 v122, v106\n" \
  "s_mov_b64 exec, -1\n" \
  "s_mul_i32 s72, s45, 528\n" \
  "s_cmp_lt_u32 s44, 4\n" \
  "s_cselect_b32 s62, 45776, 0\n" \
  "s_cselect_b32 s62, s62, 140608\n" \
  "s_add_u32 m0, s72, s62\n" \
  "s_lshl_b32 s72, s44, 12\n" \
  "buffer_load_dwordx4 v13, s[52:55], s72 offen lds\n" \
  "buffer_load_dwordx4 v13, s[52:55], s72 offen offset:1024 lds\n" \
  "s_add_u32 m0, m0, 64\n" \
  "s_nop 0\n" \
  "buffer_load_dwordx4 v13, s[52:55], s72 offen offset:2048 lds\n" \
  "buffer_load_dwordx4 v13, s[52:55], s72 offen offset:3072 lds\n" \
  "s_mov_b32 m0, 36160\n" \
  "s_mul_i32 s72, %[blk], 20480\n" \
  "s_lshr_b32 s62, s72, 2\n" \
  "s_add_u32 s72, s72, 1917184\n" \
  "s_add_u32 s62, s62, 607488\n" \
  "s_add_u32 s74, s72, 7168\n" \
  "s_add_u32 s75, s72, 14336\n" \
  "s_sub_u32 s63, s62, 7168\n" \
  "s_cmp_eq_u32 s44, 6\n" \
  "s_cselect_b32 s75, s63, s75\n" \
  "s_waitcnt vmcnt(4)\n" \
  "s_mov_b64 exec, s[84:85]\n" \
  "ds_write2_b64 v126, v[112:113], v[118:119] offset1:1\n" \
  "s_mov_b64 exec, s[82:83]\n" \
  "ds_write2_b64 v125, v[110:111], v[116:117] offset1:1\n" \
  "s_mov_b64 exec, s[80:81]\n" \
  "ds_write2_b64 v124, v[108:109], v[114:115] offset1:1\n" \
  "s_mov_b64 exec, -1\n" \
  "s_cmp_gt_u32 s44, 3\n" \
  "s_waitcnt lgkmcnt(0)\n" \
  "s_barrier\n" \
  "ds_read_b128 v[192:195], v51 offset:0\n" \
  "ds_read_b128 v[196:199], v51 offset:7168\n" \
  "ds_read_b128 v[200:203], v51 offset:14336\n" \
  "ds_read_b128 v[204:207], v51 offset:21504\n" \
  "s_waitcnt lgkmcnt(3)\n" \
  "buffer_store_dwordx4 v[192:195], v50, s[24:27], s72 offen sc1\n" \
  "s_waitcnt lgkmcnt(2)\n" \
  "buffer_store_dwordx4 v[196:199], v50, s[24:27], s74 offen sc1\n" \
  "s_waitcnt lgkmcnt(1)\n" \
  "buffer_store_dwordx4 v[200:203], v50, s[24:27], s75 offen sc1\n" \
  "s_cbranch_scc1 Lc1_%=\n" \
  "s_waitcnt lgkmcnt(0)\n" \
  "buffer_store_dwordx4 v[204:207], v50, s[24:27], s62 offen sc1\n" \
  "Lc1_%=:\n" \
  "s_mov_b32 s63, 0x80008\n" \
  "s_mov_b32 s93, 0x80008000\n" \
  "v_mul_lo_u32 v52, %[tid], s63\n" \
  "v_lshlrev_b32 v53, 4, %[tid]\n" \
  "v_add_u32 v49, 0x10000, v53\n" \
  "v_cmp_gt_u32 s[90:91], 32, %[tid]\n" \
  "v_cmp_gt_u32 vcc, 368, %[tid]\n" \
  "v_add_u32 v104, 0xf810f80, v52\n" \
  "v_add_u32 v105, 0xf830f82, v52\n" \
  "v_add_u32 v106, 0xf850f84, v52\n" \
  "v_add_u32 v107, 0xf870f86, v52\n" \
  "v_or_b32 v108, s93, v104\n" \
  "v_or_b32 v109, s93, v105\n" \
  "v_or_b32 v110, s93, v106\n" \
  "v_or_b32 v111, s93, v107\n" \
  "ds_write_b128 v53, v[104:107] offset:8192\n" \
  "ds_write_b128 v49, v[108:111] offset:8192\n" \
  "v_add_u32 v112, 0x1f811f80, v52\n" \
  "v_add_u32 v113, 0x1f831f82, v52\n" \
  "v_add_u32 v114, 0x1f851f84, v52\n" \
  "v_add_u32 v115, 0x1f871f86, v52\n" \
  "v_or_b32 v116, s93, v112\n" \
  "v_or_b32 v117, s93, v113\n" \
  "v_or_b32 v118, s93, v114\n" \
  "v_or_b32 v119, s93, v115\n" \
  "ds_write_b128 v53, v[112:115] offset:16384\n" \
  "ds_write_b128 v49, v[116:119] offset:16384\n" \
  "s_mov_b64 exec, vcc\n" \
  "v_add_u32 v120, 0x2f812f80, v52\n" \
  "v_add_u32 v121, 0x2f832f82, v52\n" \
  "v_add_u32 v122, 0x2f852f84, v52\n" \
  "v_add_u32 v123, 0x2f872f86, v52\n" \
  "v_or_b32 v124, s93, v120\n" \
  "v_or_b32 v125, s93, v121\n" \
  "v_or_b32 v126, s93, v122\n" \
  "v_or_b32 v127, s93, v123\n" \
  "ds_write_b128 v53, v[120:123] offset:24576\n" \
  "ds_write_b128 v49, v[124:127] offset:24576\n" \
  "s_mov_b64 exec, -1\n" \
  "v_add_u32 v96, 0xff80ff80, v52\n" \
  "v_add_u32 v97, 0xff82ff82, v52\n" \
  "v_add_u32 v98, 0xff84ff84, v52\n" \
  "v_add_u32 v99, 0xff86ff86, v52\n" \
  "v_lshrrev_b32 v52, 1, v52\n" \
  "s_mov_b64 exec, s[90:91]\n" \
  "v_add_u32 v96, 0x0, v52\n" \
  "v_add_u32 v97, 0x20001, v52\n" \
  "v_add_u32 v98, 0x20002, v52\n" \
  "v_add_u32 v99, 0x40003, v52\n" \
  "s_mov_b64 exec, -1\n" \
  "v_or_b32 v100, s93, v96\n" \
  "v_or_b32 v101, s93, v97\n" \
  "v_or_b32 v102, s93, v98\n" \
  "v_or_b32 v103, s93, v99\n" \
  "ds_write_b128 v53, v[96:99] offset:0\n" \
  "ds_write_b128 v49, v[100:103] offset:0\n" \
  "buffer_load_dwordx2 v[44:45], v17, s[24:27], s61 offen sc1\n" \
  "s_mov_b64 s[88:89], 0\n" \
  "s_mov_b32 s92, 0\n" \
  "v_mov_b32 v92, 1\n" \
  "v_and_b32 v41, 4, v54\n" \
  "s_movk_i32 s63, 5120\n" \
  "v_mad_u32_u24 v40, v55, s63, v41\n" \
  "s_lshl_b32 s63, s44, 10\n" \
  "s_add_u32 s63, s63, 37584\n" \
  "v_add_u32 v41, s63, v41\n" \
  "s_mul_i32 s90, s68, s39\n" \
  "s_mul_hi_u32 s91, s68, s39\n" \
  "s_lshr_b64 s[90:91], s[90:91], 20\n" \
  "s_mul_hi_u32 s63, s90, s36\n" \
  "s_cmp_lt_u32 s63, s68\n" \
  "s_addc_u32 s90, s90, 0\n" \
  "s_mul_hi_u32 s63, s90, s36\n" \
  "s_cmp_lt_u32 s63, s68\n" \
  "s_addc_u32 s90, s90, 0\n" \
  "s_min_u32 s67, s90, s35\n" \
  "s_add_u32 s68, s68, 1\n" \
  "s_mul_i32 s90, s68, s39\n" \
  "s_mul_hi_u32 s91, s68, s39\n" \
  "s_lshr_b64 s[90:91], s[90:91], 20\n" \
  "s_mul_hi_u32 s63, s90, s36\n" \
  "s_cmp_lt_u32 s63, s68\n" \
  "s_addc_u32 s90, s90, 0\n" \
  "s_mul_hi_u32 s63, s90, s36\n" \
  "s_cmp_lt_u32 s63, s68\n" \
  "s_addc_u32 s90, s90, 0\n" \
  "s_min_u32 s68, s90, s35\n" \
  "s_sub_u32 s68, s68, s67\n" \
  "s_lshl_b32 s71, s71, 1\n" \
  "v_lshlrev_b32 v50, 5, v86\n" \
  "v_and_b32 v50, 0x60, v50\n" \
  "v_bfrev_b32 v51, 1\n" \
  "s_lshl_b32 s63, %[blk], 6\n" \
  "s_add_u32 s63, s63, 256\n" \
  "s_cmp_lg_u32 s44, 7\n" \
  "s_waitcnt vmcnt(1) lgkmcnt(0)\n" \
  "s_barrier\n" \
  "s_cbranch_scc1 Ls1_%=\n" \
  "s_mov_b64 exec, 1\n" \
  "buffer_store_dwordx2 v[88:89], off, s[24:27], s63 sc1\n" \
  "s_mov_b64 exec, -1\n" \
  "Ls1_%=:\n" \
  "ds_read_b96 v[56:58], v94 offset:1472\n" \
  "ds_read_b128 v[24:27], v50 offset:36416\n" \
  "ds_read_b128 v[28:31], v50 offset:36432\n" \
  "ds_read_b64 v[32:33], v13 offset:36544\n" \
  "ds_read_b64 v[36:37], v13 offset:36552\n" \
  "s_waitcnt lgkmcnt(4)\n" \
  "v_readfirstlane_b32 s50, v56\n" \
  "v_readfirstlane_b32 s51, v57\n" \
  "v_readfirstlane_b32 s69, v58\n" \
  "s_lshl_b32 s64, s50, 22\n" \
  "s_lshr_b64 s[90:91], s[50:51], 10\n" \
  "s_mov_b32 s65, s90\n" \
  "s_cmp_lg_u32 s44, 7\n" \
  "s_waitcnt vmcnt(1)\n" \
  "s_cbranch_scc0 Lchk%=\n" \
  "s_waitcnt vmcnt(0)\n" \
  "s_branch Lchk%=\n" \
  "Lpoll%=:\n" \
  "buffer_load_dwordx2 v[44:45], v17, s[24:27], s61 offen sc1\n" \
  "s_waitcnt vmcnt(0)\n" \
  "Lchk%=:\n" \
  "v_and_b32 v50, 0xffc00000, v44\n" \
  "v_cmp_eq_u32 s[90:91], s65, v45\n" \
  "v_cmp_eq_u32 vcc, s64, v50\n" \
  "s_and_b64 vcc, vcc, s[90:91]\n" \
  "s_andn2_b64 vcc, vcc, s[88:89]\n" \
  "s_or_b64 s[88:89], s[88:89], vcc\n" \
  "v_cndmask_b32 v48, v48, v44, vcc\n" \
  "s_cmp_eq_u64 s[88:89], -1\n" \
  "v_cndmask_b32 v17, v17, v51, vcc\n" \
  "s_cbranch_scc1 Lseen%=\n" \
  "s_add_u32 s92, s92, 1\n" \
  "s_cmp_lt_u32 s92, 4096\n" \
  "s_cbranch_scc1 Lpoll%=\n" \
  "v_mov_b32 v48, 0\n" \
  "Lseen%=:\n" \
  "buffer_load_dword v20, s[28:31], s66 offen lds\n" \
  "s_add_u32 s66, s66, 0x40000\n" \
  "buffer_load_dword v20, s[28:31], s66 offen lds\n" \
  "s_add_u32 s66, s66, 0x40000\n" \
  "v_bfe_u32 v49, v48, 11, 11\n" \
  "v_and_b32 v50, 0x7ff, v48\n" \
  "v_bfe_u32 v52, v13, 4, 1\n" \
  "v_add_u32_dpp v87, v49, v49 row_shr:1 row_mask:0xf bank_mask:0xf bound_ctrl:1\n" \
  "v_lshl_add_u32 v50, v50, 2, v40\n" \
  "v_lshlrev_b32 v53, v52, v49\n" \
  "v_add_u32_dpp v87, v87, v87 row_shr:2 row_mask:0xf bank_mask:0xf bound_ctrl:1\n" \
  "v_sub_u32 v49, v49, v52\n" \
  "v_and_b32 v85, -8, v41\n" \
  "v_add_u32_dpp v87, v87, v87 row_shr:4 row_mask:0xf bank_mask:0xf bound_ctrl:1\n" \
  "v_add_u32 v85, v85, v54\n" \
  "s_nop 0\n" \
  "v_add_u32_dpp v87, v87, v87 row_shr:8 row_mask:0xf bank_mask:0xf bound_ctrl:1\n" \
  "s_nop 1\n" \
  "v_add_u32_dpp v87, v87, v87 row_bcast:15 row_mask:0xa bank_mask:0xf\n" \
  "s_nop 1\n" \
  "v_add_u32_dpp v87, v87, v87 row_bcast:31 row_mask:0xc bank_mask:0xf\n" \
  "v_sub_u32 v53, v87, v53\n" \
  "v_readlane_b32 s62, v87, 63\n" \
  "v_lshl_add_u32 v53, v53, 1, v41\n" \
  "v_lshl_add_u32 v49, v49, 2, v53\n" \
  "s_lshr_b32 s62, s62, 1\n" \
  "s_cmp_lt_u32 s62, 256\n" \
  "s_cbranch_scc1 Ll0_%=\n" \
  "s_mov_b32 s62, 0\n" \
  "ds_or_b32 v94, v92\n" \
  "s_branch Ll2_%=\n" \
  "Ll0_%=:\n" \
  "v_cmp_lt_i32 vcc, v53, v49\n" \
  "s_mov_b64 exec, vcc\n" \
  "Ll1_%=:\n" \
  "ds_write_b32 v53, v50\n" \
  "v_add_u32 v53, 8, v53\n" \
  "v_cmp_lt_i32 vcc, v53, v49\n" \
  "v_add_u32 v50, 8, v50\n" \
  "s_and_b64 exec, vcc, vcc\n" \
  "s_cbranch_scc1 Ll1_%=\n" \
  "s_mov_b64 exec, -1\n" \
  "Ll2_%=:\n" \
  "ds_read2st64_b32 v[56:57], v85 offset1:1\n" \
  "ds_read2st64_b32 v[58:59], v85 offset0:2 offset1:3\n" \
  "v_mov_b32 v50, s62\n" \
  "s_mov_b64 exec, 1\n" \
  "ds_add_u32 v94, v50 offset:4\n" \
  "s_mov_b64 exec, -1\n" \
  "v_cmp_gt_i32 s[80:81], s62, v86\n" \
  "s_sub_i32 s63, s62, 64\n" \
  "v_cmp_gt_i32 s[82:83], s63, v86\n" \
  "s_sub_i32 s63, s62, 128\n" \
  "v_cmp_gt_i32 s[84:85], s63, v86\n" \
  "s_sub_i32 s63, s62, 192\n" \
  "v_cmp_gt_i32 s[86:87], s63, v86\n" \
  "v_bfrev_b32 v50, 4\n" \
  "s_waitcnt lgkmcnt(1)\n" \
  "v_cndmask_b32 v56, v50, v56, s[80:81]\n" \
  "v_cndmask_b32 v57, v50, v57, s[82:83]\n" \
  "v_cndmask_b32 v58, v50, v58, s[84:85]\n" \
  "v_cndmask_b32 v59, v50, v59, s[86:87]\n" \
  "s_sleep 4\n" \
  "v_mul_u32_u24 v18, 64, v55\n" \
  "s_mov_b32 s93, 256\n" \
  "s_mov_b64 s[88:89], 0\n" \
  "s_mov_b32 s63, 0xbfb8aa3b\n" \
  "v_lshlrev_b32 v100, 17, %[tid]\n" \
  "v_add_u32 v103, 0x3f810000, v100\n" \
  "v_add_u32 v102, 0x3f800000, v100\n" \
  "v_add_u32 v101, 0x3b810000, v100\n" \
  "v_add_u32 v100, 0x3b800000, v100\n" \
  "v_mul_f32 v104, s63, v100\n" \
  "v_mul_f32 v105, s63, v101\n" \
  "v_mul_f32 v106, s63, v102\n" \
  "v_mul_f32 v107, s63, v103\n" \
  "v_exp_f32 v104, v104\n" \
  "v_exp_f32 v105, v105\n" \
  "v_exp_f32 v106, v106\n" \
  "v_exp_f32 v107, v107\n" \
  "v_lshlrev_b32 v96, 2, %[tid]\n" \
  "v_add_f32 v108, 1.0, v104\n" \
  "v_add_f32 v109, 1.0, v105\n" \
  "v_add_f32 v110, 1.0, v106\n" \
  "v_add_f32 v111, 1.0, v107\n" \
  "v_rcp_f32 v108, v108\n" \
  "v_rcp_f32 v109, v109\n" \
  "v_rcp_f32 v110, v110\n" \
  "v_rcp_f32 v111, v111\n" \
  "v_add_u32 v97, 0x10000, v96\n" \
  "v_pk_mul_f32 v[100:101], v[100:101], v[108:109]\n" \
  "v_pk_mul_f32 v[102:103], v[102:103], v[110:111]\n" \
  "v_pk_mul_f32 v[104:105], v[104:105], v[100:101] neg_lo:[0,1] neg_hi:[0,1]\n" \
  "v_pk_mul_f32 v[106:107], v[106:107], v[102:103] neg_lo:[0,1] neg_hi:[0,1]\n" \
  "v_cvt_pk_bf16_f32 v100, v100, v101\n" \
  "v_cvt_pk_bf16_f32 v102, v102, v103\n" \
  "v_cvt_pk_bf16_f32 v104, v104, v105\n" \
  "v_cvt_pk_bf16_f32 v106, v106, v107\n" \
  "ds_write2st64_b32 v96, v100, v102 offset0:119 offset1:127\n" \
  "ds_write2st64_b32 v97, v104, v106 offset0:119 offset1:127\n" \
  "buffer_load_dwordx2 v[44:45], v18, s[24:27], s93 offen sc1\n" \
  "v_lshrrev_b32 v50, 8, v13\n" \
  "v_mul_u32_u24 v50, 2112, v50\n" \
  "v_and_or_b32 v50, v54, 60, v50\n" \
  "s_lshl3_add_u32 s63, s45, 45776\n" \
  "v_add_u32 v50, s63, v50\n" \
  "v_add_u32 v52, 94832, v50\n" \
  "ds_read2st64_b32 v[128:129], v50 offset0:0 offset1:1\n" \
  "ds_read2st64_b32 v[130:131], v50 offset0:2 offset1:3\n" \
  "ds_read2st64_b32 v[132:133], v50 offset0:4 offset1:5\n" \
  "ds_read2st64_b32 v[134:135], v50 offset0:6 offset1:7\n" \
  "ds_read2st64_b32 v[136:137], v50 offset0:33 offset1:34\n" \
  "ds_read2st64_b32 v[138:139], v50 offset0:35 offset1:36\n" \
  "ds_read2st64_b32 v[140:141], v50 offset0:37 offset1:38\n" \
  "ds_read2st64_b32 v[142:143], v50 offset0:39 offset1:40\n" \
  "ds_read2st64_b32 v[144:145], v52 offset0:0 offset1:1\n" \
  "ds_read2st64_b32 v[146:147], v52 offset0:2 offset1:3\n" \
  "ds_read2st64_b32 v[148:149], v52 offset0:4 offset1:5\n" \
  "ds_read2st64_b32 v[150:151], v52 offset0:6 offset1:7\n" \
  "s_waitcnt lgkmcnt(8)\n" \
  "ds_read2st64_b32 v[152:153], v52 offset0:33 offset1:34\n" \
  "ds_read2st64_b32 v[154:155], v52 offset0:35 offset1:36\n" \
  "ds_read2st64_b32 v[156:157], v52 offset0:37 offset1:38\n" \
  "ds_read2st64_b32 v[158:159], v52 offset0:39 offset1:40\n" \
  "v_mov_b64 v[160:161], 0\n" \
  "v_mov_b64 v[162:163], 0\n" \
  "v_mov_b64 v[164:165], 0\n" \
  "v_mov_b64 v[166:167], 0\n" \
  "v_mov_b64 v[168:169], 0\n" \
  "v_mov_b64 v[170:171], 0\n" \
  "v_mov_b64 v[172:173], 0\n" \
  "v_mov_b64 v[174:175], 0\n" \
  "v_mov_b64 v[176:177], 0\n" \
  "v_mov_b64 v[178:179], 0\n" \
  "v_mov_b64 v[180:181], 0\n" \
  "v_mov_b64 v[182:183], 0\n" \
  "v_mov_b64 v[184:185], 0\n" \
  "v_mov_b64 v[186:187], 0\n" \
  "v_mov_b64 v[188:189], 0\n" \
  "v_mov_b64 v[190:191], 0\n" \
  "s_mov_b32 s63, 0x05040100\n" \
  "s_mov_b32 s72, 0x07060302\n" \
  "s_waitcnt lgkmcnt(0)\n" \
  "v_perm_b32 v216, v129, v128, s63\n" \
  "v_perm_b32 v220, v129, v128, s72\n" \
  "v_perm_b32 v217, v131, v130, s63\n" \
  "v_perm_b32 v221, v131, v130, s72\n" \
  "v_perm_b32 v218, v133, v132, s63\n" \
  "v_perm_b32 v222, v133, v132, s72\n" \
  "v_perm_b32 v219, v135, v134, s63\n" \
  "v_perm_b32 v223, v135, v134, s72\n" \
  "v_perm_b32 v224, v137, v136, s63\n" \
  "v_perm_b32 v228, v137, v136, s72\n" \
  "v_perm_b32 v225, v139, v138, s63\n" \
  "v_perm_b32 v229, v139, v138, s72\n" \
  "v_perm_b32 v226, v141, v140, s63\n" \
  "v_perm_b32 v230, v141, v140, s72\n" \
  "v_perm_b32 v227, v143, v142, s63\n" \
  "v_perm_b32 v231, v143, v142, s72\n" \
  "v_perm_b32 v232, v145, v144, s63\n" \
  "v_perm_b32 v236, v145, v144, s72\n" \
  "v_perm_b32 v233, v147, v146, s63\n" \
  "v_perm_b32 v237, v147, v146, s72\n" \
  "v_perm_b32 v234, v149, v148, s63\n" \
  "v_perm_b32 v238, v149, v148, s72\n" \
  "v_perm_b32 v235, v151, v150, s63\n" \
  "v_perm_b32 v239, v151, v150, s72\n" \
  "v_perm_b32 v240, v153, v152, s63\n" \
  "v_perm_b32 v244, v153, v152, s72\n" \
  "v_perm_b32 v241, v155, v154, s63\n" \
  "v_perm_b32 v245, v155, v154, s72\n" \
  "v_perm_b32 v242, v157, v156, s63\n" \
  "v_perm_b32 v246, v157, v156, s72\n" \
  "v_perm_b32 v243, v159, v158, s63\n" \
  "v_perm_b32 v247, v159, v158, s72\n" \
  "v_mov_b64 v[128:129], 0\n" \
  "v_mov_b64 v[130:131], 0\n" \
  "v_mov_b64 v[132:133], 0\n" \
  "v_mov_b64 v[134:135], 0\n" \
  "v_mov_b64 v[136:137], 0\n" \
  "v_mov_b64 v[138:139], 0\n" \
  "v_mov_b64 v[140:141], 0\n" \
  "v_mov_b64 v[142:143], 0\n" \
  "v_mov_b64 v[144:145], 0\n" \
  "v_mov_b64 v[146:147], 0\n" \
  "v_mov_b64 v[148:149], 0\n" \
  "v_mov_b64 v[150:151], 0\n" \
  "v_mov_b64 v[152:153], 0\n" \
  "v_mov_b64 v[154:155], 0\n" \
  "v_mov_b64 v[156:157], 0\n" \
  "v_mov_b64 v[158:159], 0\n" \
  "Ld1_%=:\n" \
  "buffer_load_dwordx2 v[46:47], v18, s[24:27], s93 offen sc1\n" \
  "s_waitcnt vmcnt(1)\n" \
  "v_cmp_eq_u32 s[90:91], s65, v45\n" \
  "v_cmp_eq_u32 vcc, s64, v44\n" \
  "s_mov_b32 s63, 606464\n" \
  "s_and_b64 vcc, vcc, s[90:91]\n" \
  "s_or_b64 s[88:89], s[88:89], vcc\n" \
  "s_cmp_eq_u64 s[88:89], -1\n" \
  "s_cbranch_scc1 Ld2_%=\n" \
  "v_cndmask_b32 v18, v18, v51, vcc\n" \
  "buffer_load_dwordx2 v[44:45], v18, s[24:27], s93 offen sc1\n" \
  "s_waitcnt vmcnt(1)\n" \
  "v_cmp_eq_u32 s[90:91], s65, v47\n" \
  "v_cmp_eq_u32 vcc, s64, v46\n" \
  "s_mov_b32 s63, 606464\n" \
  "s_and_b64 vcc, vcc, s[90:91]\n" \
  "s_or_b64 s[88:89], s[88:89], vcc\n" \
  "s_cmp_eq_u64 s[88:89], -1\n" \
  "s_cbranch_scc1 Ld2_%=\n" \
  "v_cndmask_b32 v18, v18, v51, vcc\n" \
  "s_add_u32 s92, s92, 1\n" \
  "s_cmp_lt_u32 s92, 4096\n" \
  "s_cbranch_scc1 Ld1_%=\n" \
  "s_mov_b64 s[80:81], 0\n" \
  "s_mov_b64 s[82:83], 0\n" \
  "s_mov_b64 s[84:85], 0\n" \
  "s_mov_b64 s[86:87], 0\n" \
  "Ld2_%=:\n" \
  "s_mov_b32 s93, 1917184\n" \
  "s_cmp_le_i32 s62, 192\n" \
  "s_cbranch_scc1 Lk595_%=\n" \
  "buffer_load_dword v63, v59, s[24:27], s63 offen sc1\n" \
  "v_lshlrev_b32 v59, 2, v59\n" \
  "buffer_load_dwordx4 v[204:207], v59, s[24:27], s93 offen sc1\n" \
  "Lk595_%=:\n" \
  "buffer_load_dword v60, v56, s[24:27], s63 offen sc1\n" \
  "buffer_load_dword v61, v57, s[24:27], s63 offen sc1\n" \
  "buffer_load_dword v62, v58, s[24:27], s63 offen sc1\n" \
  "v_lshlrev_b32 v56, 2, v56\n" \
  "v_lshlrev_b32 v57, 2, v57\n" \
  "v_lshlrev_b32 v58, 2, v58\n" \
  "s_lshl_b32 s75, s44, 8\n" \
  "s_add_u32 s75, s75, 106816\n" \
  "s_mov_b32 s52, s28\n" \
  "s_or_b32 s53, s29, 0x4000000\n" \
  "s_mov_b32 s54, s35\n" \
  "s_mov_b32 s55, 0x20000\n" \
  "s_lshl_b32 s63, s67, 10\n" \
  "s_add_u32 s56, s32, s63\n" \
  "s_addc_u32 s57, s33, 0\n" \
  "s_lshl_b32 s58, s68, 10\n" \
  "s_mov_b32 s59, 0x20000\n" \
  "s_lshl_b32 s67, s67, 24\n" \
  "s_mov_b32 s70, 0\n" \
  "s_mul_i32 s63, s44, 3328\n" \
  "s_add_u32 s63, s63, 37584\n" \
  "v_add_u32 v21, s63, v13\n" \
  "v_and_b32 v15, 0xf0, v54\n" \
  "v_add_u32 v15, s63, v15\n" \
  "v_and_b32 v23, 0xf0, v13\n" \
  "v_add3_u32 v16, v23, s63, 8\n" \
  "s_mul_i32 s63, s44, 832\n" \
  "s_add_u32 s63, s63, 100096\n" \
  "v_mov_b32 v14, s63\n" \
  "v_and_b32 v50, 0x300, v13\n" \
  "v_and_or_b32 v19, v54, 60, v50\n" \
  "v_add_u32 v19, 108864, v19\n" \
  "v_lshl_add_u32 v23, v50, 2, v23\n" \
  "s_lshl_b32 s63, s73, 10\n" \
  "s_lshl_b32 s61, s45, 5\n" \
  "s_add_u32 s63, s63, s61\n" \
  "v_add_u32 v23, s63, v23\n" \
  "v_bfe_u32 v50, v86, 2, 2\n" \
  "v_mul_u32_u24 v22, 1056, v50\n" \
  "v_and_b32 v50, 48, v13\n" \
  "v_lshl_add_u32 v22, v50, 2, v22\n" \
  "v_and_b32 v50, 48, v86\n" \
  "s_mul_i32 s63, s73, 1056\n" \
  "s_add_u32 s63, s63, 108864\n" \
  "v_add3_u32 v22, v22, v50, s63\n" \
  "v_mul_u32_u24 v50, 26, v86\n" \
  "v_lshrrev_b32 v50, 8, v50\n" \
  "v_mad_i32_i24 v50, v50, -10, v86\n" \
  "v_lshlrev_b32 v42, 19, v50\n" \
  "s_lshl_b32 s63, s73, 28\n" \
  "s_sub_u32 s67, s67, s63\n" \
  "s_cmp_lt_u32 s92, 4096\n" \
  "s_cbranch_scc1 Lq2_%=\n" \
  "v_mov_b32 v53, 3\n" \
  "ds_or_b32 v94, v53\n" \
  "Lq2_%=:\n" \
  "v_mov_b32 v50, 2\n" \
  "s_waitcnt vmcnt(0)\n" \
  "v_subrev_u32 v60, s67, v60\n" \
  "s_mov_b64 exec, s[80:81]\n" \
  "v_lshlrev_b32_sdwa v64, v50, v60 dst_sel:DWORD dst_unused:UNUSED_PAD src0_sel:DWORD src1_sel:BYTE_3\n" \
  "ds_add_rtn_u32 v80, v64, v93 offset:35584\n" \
  "v_subrev_u32 v61, s67, v61\n" \
  "s_mov_b64 exec, s[82:83]\n" \
  "v_lshlrev_b32_sdwa v65, v50, v61 dst_sel:DWORD dst_unused:UNUSED_PAD src0_sel:DWORD src1_sel:BYTE_3\n" \
  "ds_add_rtn_u32 v81, v65, v93 offset:35584\n" \
  "v_subrev_u32 v62, s67, v62\n" \
  "s_mov_b64 exec, s[84:85]\n" \
  "v_lshlrev_b32_sdwa v66, v50, v62 dst_sel:DWORD dst_unused:UNUSED_PAD src0_sel:DWORD src1_sel:BYTE_3\n" \
  "ds_add_rtn_u32 v82, v66, v93 offset:35584\n" \
  "s_cmp_le_i32 s62, 192\n" \
  "s_cbranch_scc1 Lk671_%=\n" \
  "v_subrev_u32 v63, s67, v63\n" \
  "s_mov_b64 exec, s[86:87]\n" \
  "v_lshlrev_b32_sdwa v67, v50, v63 dst_sel:DWORD dst_unused:UNUSED_PAD src0_sel:DWORD src1_sel:BYTE_3\n" \
  "ds_add_rtn_u32 v83, v67, v93 offset:35584\n" \
  "Lk671_%=:\n" \
  "s_mov_b64 exec, -1\n" \
  "buffer_load_dwordx4 v[192:195], v56, s[24:27], s93 offen sc1\n" \
  "buffer_load_dwordx4 v[196:199], v57, s[24:27], s93 offen sc1\n" \
  "buffer_load_dwordx4 v[200:203], v58, s[24:27], s93 offen sc1\n" \
  "s_mov_b32 s63, 0x01010101\n" \
  "s_waitcnt lgkmcnt(0)\n" \
  "s_barrier\n" \
  "ds_read2st64_b32 v[44:45], v54 offset0:139 offset1:140\n" \
  "ds_read_b32 v86, v94 offset:4\n" \
  "ds_bpermute_b32 v76, v64, v42\n" \
  "ds_bpermute_b32 v77, v65, v42\n" \
  "ds_bpermute_b32 v78, v66, v42\n" \
  "ds_bpermute_b32 v79, v67, v42\n" \
  "buffer_load_dword v20, s[28:31], s66 offen lds\n" \
  "s_add_u32 s66, s66, 0x40000\n" \
  "buffer_load_dword v20, s[28:31], s66 offen lds\n" \
  "s_add_u32 s66, s66, 0x40000\n" \
  "v_bfe_u32 v80, v80, s45, 8\n" \
  "v_bfe_u32 v81, v81, s45, 8\n" \
  "v_bfe_u32 v82, v82, s45, 8\n" \
  "v_bfe_u32 v83, v83, s45, 8\n" \
  "s_waitcnt lgkmcnt(4)\n" \
  "v_dot4_u32_u8 v85, v44, s63, 0\n" \
  "v_dot4_u32_u8 v85, v45, s63, v85\n" \
  "v_dot4_u32_u8 v46, v44, s46, 0\n" \
  "v_dot4_u32_u8 v46, v45, s47, v46\n" \
  "v_add_u32 v86, 31, v86\n" \
  "v_add_u32_dpp v87, v85, v85 row_shr:1 row_mask:0xf bank_mask:0xf bound_ctrl:1\n" \
  "v_lshrrev_b32 v86, 5, v86\n" \
  "v_cvt_f32_u32 v86, v86\n" \
  "v_add_u32_dpp v87, v87, v87 row_shr:2 row_mask:0xf bank_mask:0xf bound_ctrl:1\n" \
  "v_rcp_f32 v86, v86\n" \
  "v_mul_hi_u32_u24 v68, s37, v60\n" \
  "v_add_u32_dpp v87, v87, v87 row_shr:4 row_mask:0xf bank_mask:0xf bound_ctrl:1\n" \
  "v_subrev_u32 v68, s71, v68\n" \
  "v_and_b32 v68, 15, v68\n" \
  "v_add_u32_dpp v87, v87, v87 row_shr:8 row_mask:0xf bank_mask:0xf bound_ctrl:1\n" \
  "v_lshl_add_u32 v72, v68, 1, s75\n" \
  "v_lshl_add_u32 v68, v68, 3, s73\n" \
  "v_add_u32_dpp v87, v87, v87 row_bcast:15 row_mask:0xa bank_mask:0xf\n" \
  "v_mul_hi_u32_u24 v69, s37, v61\n" \
  "v_subrev_u32 v69, s71, v69\n" \
  "v_add_u32_dpp v87, v87, v87 row_bcast:31 row_mask:0xc bank_mask:0xf\n" \
  "v_sub_u32 v46, v87, v46\n" \
  "ds_bpermute_b32 v88, v64, v46\n" \
  "ds_bpermute_b32 v89, v65, v46\n" \
  "ds_bpermute_b32 v90, v66, v46\n" \
  "ds_bpermute_b32 v91, v67, v46\n" \
  "v_mul_f32 v86, 0x48800000, v86\n" \
  "v_cvt_u32_f32 v86, v86\n" \
  "v_add_u32 v86, 1, v86\n" \
  "v_and_b32 v69, 15, v69\n" \
  "v_lshl_add_u32 v73, v69, 1, s75\n" \
  "v_lshl_add_u32 v69, v69, 3, s73\n" \
  "v_mul_hi_u32_u24 v70, s37, v62\n" \
  "v_subrev_u32 v70, s71, v70\n" \
  "v_and_b32 v70, 15, v70\n" \
  "v_lshl_add_u32 v74, v70, 1, s75\n" \
  "v_lshl_add_u32 v70, v70, 3, s73\n" \
  "v_mul_hi_u32_u24 v71, s37, v63\n" \
  "v_subrev_u32 v71, s71, v71\n" \
  "v_and_b32 v71, 15, v71\n" \
  "v_lshl_add_u32 v75, v71, 1, s75\n" \
  "v_lshl_add_u32 v71, v71, 3, s73\n" \
  "s_mov_b32 s63, 0xffff\n" \
  "s_waitcnt lgkmcnt(4)\n" \
  "v_and_or_b32 v76, v60, s63, v76\n" \
  "v_and_or_b32 v77, v61, s63, v77\n" \
  "v_and_or_b32 v78, v62, s63, v78\n" \
  "v_and_or_b32 v79, v63, s63, v79\n" \
  "s_waitcnt lgkmcnt(3)\n" \
  "v_add_u32 v80, v80, v88\n" \
  "v_mul_u32_u24 v80, v80, v86\n" \
  "s_waitcnt lgkmcnt(2)\n" \
  "v_add_u32 v81, v81, v89\n" \
  "v_mul_u32_u24 v81, v81, v86\n" \
  "s_waitcnt lgkmcnt(1)\n" \
  "v_add_u32 v82, v82, v90\n" \
  "v_mul_u32_u24 v82, v82, v86\n" \
  "s_waitcnt lgkmcnt(0)\n" \
  "v_add_u32 v83, v83, v91\n" \
  "v_mul_u32_u24 v83, v83, v86\n" \
  "v_lshrrev_b32 v60, 20, v80\n" \
  "s_mov_b64 exec, s[80:81]\n" \
  "v_lshl_add_u32 v68, v60, 7, v68\n" \
  "ds_add_rtn_u32 v80, v68, v93 offset:34560\n" \
  "v_lshrrev_b32 v61, 20, v81\n" \
  "s_mov_b64 exec, s[82:83]\n" \
  "v_lshl_add_u32 v69, v61, 7, v69\n" \
  "ds_add_rtn_u32 v81, v69, v93 offset:34560\n" \
  "v_lshrrev_b32 v62, 20, v82\n" \
  "s_mov_b64 exec, s[84:85]\n" \
  "v_lshl_add_u32 v70, v62, 7, v70\n" \
  "ds_add_rtn_u32 v82, v70, v93 offset:34560\n" \
  "s_cmp_le_i32 s62, 192\n" \
  "s_cbranch_scc1 Lk772_%=\n" \
  "v_lshrrev_b32 v63, 20, v83\n" \
  "s_mov_b64 exec, s[86:87]\n" \
  "v_lshl_add_u32 v71, v63, 7, v71\n" \
  "ds_add_rtn_u32 v83, v71, v93 offset:34560\n" \
  "Lk772_%=:\n" \
  "s_mov_b64 exec, -1\n" \
  "buffer_load_dword v20, s[28:31], s66 offen lds\n" \
  "s_add_u32 s66, s66, 0x40000\n" \
  "v_lshl_add_u32 v72, v60, 5, v72\n" \
  "v_lshl_add_u32 v73, v61, 5, v73\n" \
  "v_lshl_add_u32 v74, v62, 5, v74\n" \
  "v_lshl_add_u32 v75, v63, 5, v75\n" \
  "v_sub_u32 v85, v87, v85\n" \
  "v_add_u32 v87, -1, v87\n" \
  "v_max_i32 v87, v87, v85\n" \
  "v_mul_u32_u24 v84, v85, v86\n" \
  "v_mul_u32_u24 v43, v87, v86\n" \
  "v_lshrrev_b32 v50, 1, v13\n" \
  "v_lshrrev_b32 v51, 3, v13\n" \
  "v_add_u32 v51, s75, v51\n" \
  "v_lshrrev_b32 v52, 8, v13\n" \
  "v_mul_u32_u24 v52, 0xd000d0, v52\n" \
  "v_add_u32 v52, 0x3400000, v52\n" \
  "s_mov_b32 s63, 0x01010101\n" \
  "s_waitcnt lgkmcnt(0)\n" \
  "s_barrier\n" \
  "ds_read_b64 v[88:89], v50 offset:34560\n" \
  "ds_read_b64 v[90:91], v50 offset:35072\n" \
  "v_bfe_u32 v80, v80, s45, 8\n" \
  "v_bfe_u32 v81, v81, s45, 8\n" \
  "v_bfe_u32 v82, v82, s45, 8\n" \
  "v_bfe_u32 v83, v83, s45, 8\n" \
  "s_lshl_b32 s72, s44, 4\n" \
  "s_or_b32 s72, s72, 15\n" \
  "s_lshl_b32 s73, s73, 2\n" \
  "s_waitcnt lgkmcnt(0)\n" \
  "v_dot4_u32_u8 v44, v88, s63, 0\n" \
  "v_dot4_u32_u8 v45, v90, s63, 0\n" \
  "v_dot4_u32_u8 v44, v89, s63, v44\n" \
  "v_dot4_u32_u8 v45, v91, s63, v45\n" \
  "v_dot4_u32_u8 v46, v88, s46, 0\n" \
  "v_dot4_u32_u8 v47, v90, s46, 0\n" \
  "v_dot4_u32_u8 v46, v89, s47, v46\n" \
  "v_lshl_or_b32 v44, v45, 16, v44\n" \
  "v_dot4_u32_u8 v47, v91, s47, v47\n" \
  "s_nop 0\n" \
  "v_add_u32_dpp v87, v44, v44 row_shr:1 row_mask:0xf bank_mask:0xf bound_ctrl:1\n" \
  "s_nop 0\n" \
  "v_lshl_or_b32 v46, v47, 16, v46\n" \
  "v_add_u32_dpp v87, v87, v87 row_shr:2 row_mask:0xf bank_mask:0xf bound_ctrl:1\n" \
  "v_sub_u32 v52, v52, v46\n" \
  "s_nop 0\n" \
  "v_add_u32_dpp v87, v87, v87 row_shr:4 row_mask:0xf bank_mask:0xf bound_ctrl:1\n" \
  "s_nop 1\n" \
  "v_add_u32_dpp v87, v87, v87 row_shr:8 row_mask:0xf bank_mask:0xf bound_ctrl:1\n" \
  "v_add_u32 v52, v52, v87\n" \
  "ds_write_b16 v51, v52\n" \
  "ds_write_b16_d16_hi v51, v52 offset:128\n" \
  "ds_read_u16 v72, v72\n" \
  "ds_read_u16 v73, v73\n" \
  "ds_read_u16 v74, v74\n" \
  "ds_read_u16 v75, v75\n" \
  "v_readlane_b32 s60, v87, s72\n" \
  "s_movk_i32 s63, 200\n" \
  "v_cmp_lt_u32_sdwa s[90:91], s63, v87 src0_sel:DWORD src1_sel:WORD_0\n" \
  "v_cmp_lt_u32_sdwa vcc, s63, v87 src0_sel:DWORD src1_sel:WORD_1\n" \
  "s_lshr_b32 s72, s60, s73\n" \
  "s_and_b32 s72, s72, 0xffff\n" \
  "s_add_u32 s60, s72, 3\n" \
  "s_lshr_b32 s60, s60, 2\n" \
  "s_max_u32 s60, s60, 1\n" \
  "s_lshl2_add_u32 s73, s60, 8\n" \
  "s_sub_u32 s73, s73, s72\n" \
  "s_bfm_b64 s[88:89], s73, 0\n" \
  "s_or_b64 vcc, vcc, s[90:91]\n" \
  "s_cbranch_scc0 Lo1_%=\n" \
  "s_mov_b64 s[80:81], 0\n" \
  "s_mov_b64 s[82:83], 0\n" \
  "s_mov_b64 s[84:85], 0\n" \
  "s_mov_b64 s[86:87], 0\n" \
  "s_mov_b64 s[88:89], 0xfff\n" \
  "s_mov_b32 s72, 0\n" \
  "s_mov_b32 s60, 1\n" \
  "ds_or_b32 v94, v92\n" \
  "Lo1_%=:\n" \
  "v_lshrrev_b32 v50, 2, v13\n" \
  "s_lshl_b32 s72, s72, 2\n" \
  "v_add3_u32 v50, v50, s72, v14\n" \
  "v_mov_b32 v51, 0x50ffff\n" \
  "s_mov_b32 s63, 100096\n" \
  "s_mov_b64 exec, s[88:89]\n" \
  "ds_write_b32 v50, v51\n" \
  "s_waitcnt lgkmcnt(1)\n" \
  "s_mov_b64 exec, s[80:81]\n" \
  "v_add_u32 v80, v80, v72\n" \
  "v_lshl_add_u32 v72, v80, 2, s63\n" \
  "ds_write_b32 v72, v76\n" \
  "s_mov_b64 exec, s[82:83]\n" \
  "v_add_u32 v81, v81, v73\n" \
  "v_lshl_add_u32 v73, v81, 2, s63\n" \
  "ds_write_b32 v73, v77\n" \
  "s_mov_b64 exec, s[84:85]\n" \
  "v_add_u32 v82, v82, v74\n" \
  "v_lshl_add_u32 v74, v82, 2, s63\n" \
  "ds_write_b32 v74, v78\n" \
  "s_cmp_le_i32 s62, 192\n" \
  "s_cbranch_scc1 Lk879_%=\n" \
  "s_mov_b64 exec, s[86:87]\n" \
  "v_add_u32 v83, v83, v75\n" \
  "v_lshl_add_u32 v75, v83, 2, s63\n" \
  "ds_write_b32 v75, v79\n" \
  "Lk879_%=:\n" \
  "s_mov_b64 exec, -1\n" \
  "s_sub_u32 s60, s60, 1\n" \
  "s_lshl_b32 s63, s60, 4\n" \
  "v_sub_u32 v50, v14, v13\n" \
  "v_add_u32 v50, s63, v50\n" \
  "s_waitcnt lgkmcnt(0)\n" \
  "s_barrier\n" \
  "ds_read_b128 v[56:59], v14\n" \
  "ds_read_b128 v[60:63], v14 offset:16\n" \
  "ds_read_b128 v[64:67], v50\n" \
  "v_lshlrev_b32 v208, 4, v80\n" \
  "v_lshlrev_b32 v209, 4, v81\n" \
  "v_lshlrev_b32 v210, 4, v82\n" \
  "v_lshlrev_b32 v211, 4, v83\n" \
  "s_waitcnt vmcnt(3)\n" \
  "s_mov_b64 exec, s[80:81]\n" \
  "ds_write_b128 v208, v[192:195] offset:37584\n" \
  "s_mov_b64 exec, s[82:83]\n" \
  "ds_write_b128 v209, v[196:199] offset:37584\n" \
  "s_mov_b64 exec, s[84:85]\n" \
  "ds_write_b128 v210, v[200:203] offset:37584\n" \
  "s_cmp_le_i32 s62, 192\n" \
  "s_cbranch_scc1 Lk907_%=\n" \
  "s_mov_b64 exec, s[86:87]\n" \
  "ds_write_b128 v211, v[204:207] offset:37584\n" \
  "Lk907_%=:\n" \
  "s_mov_b64 exec, -1\n" \
  "s_waitcnt lgkmcnt(4)\n" \
  "v_and_b32 v12, 0xffff, v56\n" \
  "buffer_load_dwordx4 v[96:99], v[12:13], s[52:55], 0 idxen offen\n" \
  "v_and_b32 v12, 0xffff, v57\n" \
  "buffer_load_dwordx4 v[100:103], v[12:13], s[52:55], 0 idxen offen\n" \
  "v_and_b32 v12, 0xffff, v58\n" \
  "buffer_load_dwordx4 v[104:107], v[12:13], s[52:55], 0 idxen offen\n" \
  "v_and_b32 v12, 0xffff, v59\n" \
  "buffer_load_dwordx4 v[108:111], v[12:13], s[52:55], 0 idxen offen\n" \
  "v_lshrrev_b32 v51, 20, v84\n" \
  "v_lshrrev_b32 v52, 20, v43\n" \
  "v_min_u32 v51, 7, v51\n" \
  "v_min_u32 v52, 7, v52\n" \
  "s_bfm_b64 s[88:89], s68, 0\n" \
  "v_mov_b32 v18, v94\n" \
  "v_cmp_ge_u32 s[90:91], s44, v51\n" \
  "v_cmp_le_u32 vcc, s44, v52\n" \
  "s_and_b64 s[88:89], s[88:89], s[90:91]\n" \
  "s_and_b64 s[88:89], s[88:89], vcc\n" \
  "s_ff1_i32_b64 s72, s[88:89]\n" \
  "s_flbit_i32_b64 s74, s[88:89]\n" \
  "s_xor_b32 s74, s74, 63\n" \
  "s_mov_b32 s63, 0x0c0c0602\n" \
  "s_waitcnt lgkmcnt(0)\n" \
  "s_barrier\n" \
  "ds_read_b64 v[92:93], v16\n" \
  "ds_read_b64 v[94:95], v15\n" \
  "v_and_b32 v12, 0xffff, v60\n" \
  "buffer_load_dwordx4 v[112:115], v[12:13], s[52:55], 0 idxen offen\n" \
  "v_and_b32 v12, 0xffff, v61\n" \
  "buffer_load_dwordx4 v[116:119], v[12:13], s[52:55], 0 idxen offen\n" \
  "v_and_b32 v12, 0xffff, v62\n" \
  "buffer_load_dwordx4 v[120:123], v[12:13], s[52:55], 0 idxen offen\n" \
  "v_and_b32 v12, 0xffff, v63\n" \
  "buffer_load_dwordx4 v[124:127], v[12:13], s[52:55], 0 idxen offen\n" \
  "ds_read_u16 v12, v14 offset:32\n" \
  "v_perm_b32 v64, v65, v64, s63\n" \
  "v_perm_b32 v66, v67, v66, s63\n" \
  "v_readlane_b32 s93, v51, s72\n" \
  "v_readlane_b32 s75, v52, s74\n" \
  "v_lshl_or_b32 v17, v66, 16, v64\n" \
  "v_readlane_b32 s63, v51, s74\n" \
  "v_readlane_b32 s73, v42, s72\n" \
  "v_readlane_b32 s61, v17, s60\n" \
  "s_sub_u32 s74, s74, s72\n" \
  "s_sub_u32 s75, s75, s44\n" \
  "s_cmp_eq_u32 s63, s44\n" \
  "s_cselect_b32 s75, s75, 0\n" \
  "s_lshr_b32 s73, s73, 16\n" \
  "s_mul_i32 s72, s72, 1056\n" \
  "s_or_b32 s63, %[blk], s44\n" \
  "s_cbranch_scc0 Lep0_%=\n" \
  "Lgo%=:\n" \
  "s_waitcnt lgkmcnt(0)\n" \
  "v_mfma_f32_4x4x4_16b_bf16 v[56:59], v[92:93], v[32:33], 0 cbsz:4\n" \
  "v_mfma_f32_4x4x4_16b_bf16 v[60:63], v[92:93], v[36:37], 0 cbsz:4\n" \
  "v_mfma_f32_4x4x4_16b_bf16 v[64:67], v[92:93], v[32:33], 0 cbsz:4 abid:1\n" \
  "v_mfma_f32_4x4x4_16b_bf16 v[68:71], v[92:93], v[36:37], 0 cbsz:4 abid:1\n" \
  "v_mfma_f32_4x4x4_16b_bf16 v[84:87], v[94:95], v[24:25], 0\n" \
  "v_mfma_f32_4x4x4_16b_bf16 v[88:91], v[94:95], v[26:27], 0\n" \
  "v_mfma_f32_4x4x4_16b_bf16 v[48:51], v[94:95], v[28:29], 0\n" \
  "v_mfma_f32_4x4x4_16b_bf16 v[52:55], v[94:95], v[30:31], 0\n" \
  "s_nop 1\n" \
  "v_cvt_pk_bf16_f32 v57, v56, v57\n" \
  "v_lshlrev_b32_sdwa v56, 1, v57 dst_sel:DWORD dst_unused:UNUSED_PAD src0_sel:DWORD src1_sel:WORD_0\n" \
  "v_lshlrev_b32_sdwa v57, 1, v57 dst_sel:DWORD dst_unused:UNUSED_PAD src0_sel:DWORD src1_sel:WORD_1\n" \
  "ds_read_u16_d16_hi v56, v56\n" \
  "ds_read_u16_d16_hi v57, v57\n" \
  "v_cvt_pk_bf16_f32 v59, v58, v59\n" \
  "v_lshlrev_b32_sdwa v58, 1, v59 dst_sel:DWORD dst_unused:UNUSED_PAD src0_sel:DWORD src1_sel:WORD_0\n" \
  "v_lshlrev_b32_sdwa v59, 1, v59 dst_sel:DWORD dst_unused:UNUSED_PAD src0_sel:DWORD src1_sel:WORD_1\n" \
  "ds_read_u16_d16_hi v58, v58\n" \
  "ds_read_u16_d16_hi v59, v59\n" \
  "v_cvt_pk_bf16_f32 v61, v60, v61\n" \
  "v_lshlrev_b32_sdwa v60, 1, v61 dst_sel:DWORD dst_unused:UNUSED_PAD src0_sel:DWORD src1_sel:WORD_0\n" \
  "v_lshlrev_b32_sdwa v61, 1, v61 dst_sel:DWORD dst_unused:UNUSED_PAD src0_sel:DWORD src1_sel:WORD_1\n" \
  "ds_read_u16_d16_hi v60, v60\n" \
  "ds_read_u16_d16_hi v61, v61\n" \
  "v_cvt_pk_bf16_f32 v63, v62, v63\n" \
  "v_lshlrev_b32_sdwa v62, 1, v63 dst_sel:DWORD dst_unused:UNUSED_PAD src0_sel:DWORD src1_sel:WORD_0\n" \
  "v_lshlrev_b32_sdwa v63, 1, v63 dst_sel:DWORD dst_unused:UNUSED_PAD src0_sel:DWORD src1_sel:WORD_1\n" \
  "ds_read_u16_d16_hi v62, v62\n" \
  "ds_read_u16_d16_hi v63, v63\n" \
  "v_cvt_pk_bf16_f32 v76, v84, v88\n" \
  "v_cvt_pk_bf16_f32 v77, v48, v52\n" \
  "s_mov_b32 s63, 0x7fff7fff\n" \
  "v_and_b32 v41, s63, v194\n" \
  "v_and_b32 v42, s63, v195\n" \
  "v_pk_max_u16 v40, v41, v42\n" \
  "v_and_b32 v41, s63, v198\n" \
  "v_and_b32 v42, s63, v199\n" \
  "v_pk_max_u16 v41, v41, v40\n" \
  "v_pk_max_u16 v40, v41, v42\n" \
  "v_and_b32 v41, s63, v202\n" \
  "v_and_b32 v42, s63, v203\n" \
  "v_pk_max_u16 v41, v41, v40\n" \
  "v_pk_max_u16 v40, v41, v42\n" \
  "s_cmp_le_i32 s62, 192\n" \
  "s_cbranch_scc1 Lk1010_%=\n" \
  "v_and_b32 v41, s63, v206\n" \
  "v_and_b32 v42, s63, v207\n" \
  "v_pk_max_u16 v41, v41, v40\n" \
  "v_pk_max_u16 v40, v41, v42\n" \
  "Lk1010_%=:\n" \
  "v_max_u16_sdwa v40, v40, v40 dst_sel:DWORD dst_unused:UNUSED_PAD src0_sel:WORD_0 src1_sel:WORD_1\n" \
  "s_nop 0\n" \
  "v_cmp_lt_i32 vcc, s69, v40\n" \
  "s_cmp_eq_u64 vcc, 0\n" \
  "s_cselect_b32 s74, s74, -1\n" \
  "v_mov_b64 v[192:193], 0\n" \
  "v_mov_b64 v[194:195], 0\n" \
  "v_mov_b64 v[196:197], 0\n" \
  "v_mov_b64 v[198:199], 0\n" \
  "v_mov_b64 v[200:201], 0\n" \
  "v_mov_b64 v[202:203], 0\n" \
  "v_mov_b64 v[204:205], 0\n" \
  "v_mov_b64 v[206:207], 0\n" \
  "s_waitcnt vmcnt(7) lgkmcnt(0)\n" \
  "v_mfma_f32_4x4x4_16b_bf16 v[48:51], v[76:77], v[96:97], 0 cbsz:4\n" \
  "v_mfma_f32_4x4x4_16b_bf16 v[52:55], v[76:77], v[98:99], 0 cbsz:4\n" \
  "s_nop 4\n" \
  "Lbody%=:\n" \
  "s_set_gpr_idx_on s61, gpr_idx(SRC2,DST)\n" \
  "v_pk_fma_f32 v[128:129], v[56:57], v[48:49], v[128:129] op_sel:[0,0,0] op_sel_hi:[0,1,1]\n" \
  "v_pk_fma_f32 v[130:131], v[56:57], v[50:51], v[130:131] op_sel:[0,0,0] op_sel_hi:[0,1,1]\n" \
  "v_pk_fma_f32 v[132:133], v[60:61], v[52:53], v[132:133] op_sel:[0,0,0] op_sel_hi:[0,1,1]\n" \
  "v_pk_fma_f32 v[134:135], v[60:61], v[54:55], v[134:135] op_sel:[0,0,0] op_sel_hi:[0,1,1]\n" \
  "s_set_gpr_idx_off\n" \
  "s_waitcnt vmcnt(6) lgkmcnt(2)\n" \
  "buffer_load_dwordx4 v[96:99], v[12:13], s[52:55], 0 idxen offen\n" \
  "ds_read_u16 v12, v14 offset:36\n" \
  "s_lshr_b32 s61, s61, 8\n" \
  "v_mfma_f32_4x4x4_16b_bf16 v[48:51], v[76:77], v[100:101], 0 cbsz:4 abid:1\n" \
  "v_mfma_f32_4x4x4_16b_bf16 v[52:55], v[76:77], v[102:103], 0 cbsz:4 abid:1\n" \
  "v_cvt_pk_bf16_f32 v65, v64, v65\n" \
  "v_lshlrev_b32_sdwa v64, 1, v65 dst_sel:DWORD dst_unused:UNUSED_PAD src0_sel:DWORD src1_sel:WORD_0\n" \
  "v_lshlrev_b32_sdwa v65, 1, v65 dst_sel:DWORD dst_unused:UNUSED_PAD src0_sel:DWORD src1_sel:WORD_1\n" \
  "ds_read_u16_d16_hi v64, v64\n" \
  "ds_read_u16_d16_hi v65, v65\n" \
  "s_set_gpr_idx_on s61, gpr_idx(SRC2,DST)\n" \
  "v_pk_fma_f32 v[128:129], v[56:57], v[48:49], v[128:129] op_sel:[1,0,0] op_sel_hi:[1,1,1]\n" \
  "v_pk_fma_f32 v[130:131], v[56:57], v[50:51], v[130:131] op_sel:[1,0,0] op_sel_hi:[1,1,1]\n" \
  "v_pk_fma_f32 v[132:133], v[60:61], v[52:53], v[132:133] op_sel:[1,0,0] op_sel_hi:[1,1,1]\n" \
  "v_pk_fma_f32 v[134:135], v[60:61], v[54:55], v[134:135] op_sel:[1,0,0] op_sel_hi:[1,1,1]\n" \
  "s_set_gpr_idx_off\n" \
  "s_waitcnt vmcnt(6) lgkmcnt(2)\n" \
  "buffer_load_dwordx4 v[100:103], v[12:13], s[52:55], 0 idxen offen\n" \
  "ds_read_u16 v12, v14 offset:40\n" \
  "s_lshr_b32 s61, s61, 8\n" \
  "v_mfma_f32_4x4x4_16b_bf16 v[48:51], v[76:77], v[104:105], 0 cbsz:4 abid:2\n" \
  "v_mfma_f32_4x4x4_16b_bf16 v[52:55], v[76:77], v[106:107], 0 cbsz:4 abid:2\n" \
  "v_cvt_pk_bf16_f32 v69, v68, v69\n" \
  "v_lshlrev_b32_sdwa v68, 1, v69 dst_sel:DWORD dst_unused:UNUSED_PAD src0_sel:DWORD src1_sel:WORD_0\n" \
  "v_lshlrev_b32_sdwa v69, 1, v69 dst_sel:DWORD dst_unused:UNUSED_PAD src0_sel:DWORD src1_sel:WORD_1\n" \
  "ds_read_u16_d16_hi v68, v68\n" \
  "ds_read_u16_d16_hi v69, v69\n" \
  "s_set_gpr_idx_on s61, gpr_idx(SRC2,DST)\n" \
  "v_pk_fma_f32 v[128:129], v[58:59], v[48:49], v[128:129] op_sel:[0,0,0] op_sel_hi:[0,1,1]\n" \
  "v_pk_fma_f32 v[130:131], v[58:59], v[50:51], v[130:131] op_sel:[0,0,0] op_sel_hi:[0,1,1]\n" \
  "v_pk_fma_f32 v[132:133], v[62:63], v[52:53], v[132:133] op_sel:[0,0,0] op_sel_hi:[0,1,1]\n" \
  "v_pk_fma_f32 v[134:135], v[62:63], v[54:55], v[134:135] op_sel:[0,0,0] op_sel_hi:[0,1,1]\n" \
  "s_set_gpr_idx_off\n" \
  "s_waitcnt vmcnt(6) lgkmcnt(2)\n" \
  "buffer_load_dwordx4 v[104:107], v[12:13], s[52:55], 0 idxen offen\n" \
  "ds_read_u16 v12, v14 offset:44\n" \
  "s_lshr_b32 s61, s61, 8\n" \
  "v_mfma_f32_4x4x4_16b_bf16 v[48:51], v[76:77], v[108:109], 0 cbsz:4 abid:3\n" \
  "v_mfma_f32_4x4x4_16b_bf16 v[52:55], v[76:77], v[110:111], 0 cbsz:4 abid:3\n" \
  "v_cvt_pk_bf16_f32 v67, v66, v67\n" \
  "v_lshlrev_b32_sdwa v66, 1, v67 dst_sel:DWORD dst_unused:UNUSED_PAD src0_sel:DWORD src1_sel:WORD_0\n" \
  "v_lshlrev_b32_sdwa v67, 1, v67 dst_sel:DWORD dst_unused:UNUSED_PAD src0_sel:DWORD src1_sel:WORD_1\n" \
  "ds_read_u16_d16_hi v66, v66\n" \
  "ds_read_u16_d16_hi v67, v67\n" \
  "s_set_gpr_idx_on s61, gpr_idx(SRC2,DST)\n" \
  "v_pk_fma_f32 v[128:129], v[58:59], v[48:49], v[128:129] op_sel:[1,0,0] op_sel_hi:[1,1,1]\n" \
  "v_pk_fma_f32 v[130:131], v[58:59], v[50:51], v[130:131] op_sel:[1,0,0] op_sel_hi:[1,1,1]\n" \
  "v_pk_fma_f32 v[132:133], v[62:63], v[52:53], v[132:133] op_sel:[1,0,0] op_sel_hi:[1,1,1]\n" \
  "v_pk_fma_f32 v[134:135], v[62:63], v[54:55], v[134:135] op_sel:[1,0,0] op_sel_hi:[1,1,1]\n" \
  "s_set_gpr_idx_off\n" \
  "s_waitcnt vmcnt(6) lgkmcnt(2)\n" \
  "buffer_load_dwordx4 v[108:111], v[12:13], s[52:55], 0 idxen offen\n" \
  "ds_read_u16 v12, v14 offset:48\n" \
  "s_sub_u32 s60, s60, 1\n" \
  "s_cbranch_scc1 Lend%=\n" \
  "v_readlane_b32 s61, v17, s60\n" \
  "s_setprio 2\n" \
  "v_mfma_f32_4x4x4_16b_bf16 v[56:59], v[92:93], v[32:33], 0 cbsz:4 abid:2\n" \
  "v_mfma_f32_4x4x4_16b_bf16 v[60:63], v[92:93], v[36:37], 0 cbsz:4 abid:2\n" \
  "v_mfma_f32_4x4x4_16b_bf16 v[48:51], v[76:77], v[112:113], 0 cbsz:4 abid:4\n" \
  "v_mfma_f32_4x4x4_16b_bf16 v[52:55], v[76:77], v[114:115], 0 cbsz:4 abid:4\n" \
  "v_cvt_pk_bf16_f32 v71, v70, v71\n" \
  "v_lshlrev_b32_sdwa v70, 1, v71 dst_sel:DWORD dst_unused:UNUSED_PAD src0_sel:DWORD src1_sel:WORD_0\n" \
  "v_lshlrev_b32_sdwa v71, 1, v71 dst_sel:DWORD dst_unused:UNUSED_PAD src0_sel:DWORD src1_sel:WORD_1\n" \
  "ds_read_u16_d16_hi v70, v70\n" \
  "ds_read_u16_d16_hi v71, v71\n" \
  "v_add_u32 v16, 0x100, v16\n" \
  "s_set_gpr_idx_on s61, gpr_idx(SRC2,DST)\n" \
  "v_pk_fma_f32 v[128:129], v[64:65], v[48:49], v[128:129] op_sel:[0,0,0] op_sel_hi:[0,1,1]\n" \
  "v_pk_fma_f32 v[130:131], v[64:65], v[50:51], v[130:131] op_sel:[0,0,0] op_sel_hi:[0,1,1]\n" \
  "v_pk_fma_f32 v[132:133], v[68:69], v[52:53], v[132:133] op_sel:[0,0,0] op_sel_hi:[0,1,1]\n" \
  "v_pk_fma_f32 v[134:135], v[68:69], v[54:55], v[134:135] op_sel:[0,0,0] op_sel_hi:[0,1,1]\n" \
  "s_set_gpr_idx_off\n" \
  "s_waitcnt vmcnt(6) lgkmcnt(2)\n" \
  "buffer_load_dwordx4 v[112:115], v[12:13], s[52:55], 0 idxen offen\n" \
  "ds_read_u16 v12, v14 offset:52\n" \
  "ds_read_b64 v[94:95], v15 offset:256\n" \
  "s_lshr_b32 s61, s61, 8\n" \
  "v_mfma_f32_4x4x4_16b_bf16 v[48:51], v[76:77], v[116:117], 0 cbsz:4 abid:5\n" \
  "v_mfma_f32_4x4x4_16b_bf16 v[52:55], v[76:77], v[118:119], 0 cbsz:4 abid:5\n" \
  "v_cvt_pk_bf16_f32 v57, v56, v57\n" \
  "v_lshlrev_b32_sdwa v56, 1, v57 dst_sel:DWORD dst_unused:UNUSED_PAD src0_sel:DWORD src1_sel:WORD_0\n" \
  "v_lshlrev_b32_sdwa v57, 1, v57 dst_sel:DWORD dst_unused:UNUSED_PAD src0_sel:DWORD src1_sel:WORD_1\n" \
  "ds_read_u16_d16_hi v56, v56\n" \
  "ds_read_u16_d16_hi v57, v57\n" \
  "s_set_gpr_idx_on s61, gpr_idx(SRC2,DST)\n" \
  "v_pk_fma_f32 v[128:129], v[64:65], v[48:49], v[128:129] op_sel:[1,0,0] op_sel_hi:[1,1,1]\n" \
  "v_pk_fma_f32 v[130:131], v[64:65], v[50:51], v[130:131] op_sel:[1,0,0] op_sel_hi:[1,1,1]\n" \
  "v_pk_fma_f32 v[132:133], v[68:69], v[52:53], v[132:133] op_sel:[1,0,0] op_sel_hi:[1,1,1]\n" \
  "v_pk_fma_f32 v[134:135], v[68:69], v[54:55], v[134:135] op_sel:[1,0,0] op_sel_hi:[1,1,1]\n" \
  "s_set_gpr_idx_off\n" \
  "s_waitcnt vmcnt(6) lgkmcnt(2)\n" \
  "buffer_load_dwordx4 v[116:119], v[12:13], s[52:55], 0 idxen offen\n" \
  "ds_read_u16 v12, v14 offset:56\n" \
  "s_lshr_b32 s61, s61, 8\n" \
  "v_mfma_f32_4x4x4_16b_bf16 v[48:51], v[76:77], v[120:121], 0 cbsz:4 abid:6\n" \
  "v_mfma_f32_4x4x4_16b_bf16 v[52:55], v[76:77], v[122:123], 0 cbsz:4 abid:6\n" \
  "v_cvt_pk_bf16_f32 v61, v60, v61\n" \
  "v_lshlrev_b32_sdwa v60, 1, v61 dst_sel:DWORD dst_unused:UNUSED_PAD src0_sel:DWORD src1_sel:WORD_0\n" \
  "v_lshlrev_b32_sdwa v61, 1, v61 dst_sel:DWORD dst_unused:UNUSED_PAD src0_sel:DWORD src1_sel:WORD_1\n" \
  "ds_read_u16_d16_hi v60, v60\n" \
  "ds_read_u16_d16_hi v61, v61\n" \
  "s_set_gpr_idx_on s61, gpr_idx(SRC2,DST)\n" \
  "v_pk_fma_f32 v[128:129], v[66:67], v[48:49], v[128:129] op_sel:[0,0,0] op_sel_hi:[0,1,1]\n" \
  "v_pk_fma_f32 v[130:131], v[66:67], v[50:51], v[130:131] op_sel:[0,0,0] op_sel_hi:[0,1,1]\n" \
  "v_pk_fma_f32 v[132:133], v[70:71], v[52:53], v[132:133] op_sel:[0,0,0] op_sel_hi:[0,1,1]\n" \
  "v_pk_fma_f32 v[134:135], v[70:71], v[54:55], v[134:135] op_sel:[0,0,0] op_sel_hi:[0,1,1]\n" \
  "s_set_gpr_idx_off\n" \
  "s_waitcnt vmcnt(6) lgkmcnt(2)\n" \
  "buffer_load_dwordx4 v[120:123], v[12:13], s[52:55], 0 idxen offen\n" \
  "ds_read_u16 v12, v14 offset:60\n" \
  "s_lshr_b32 s61, s61, 8\n" \
  "v_mfma_f32_4x4x4_16b_bf16 v[84:87], v[94:95], v[24:25], 0\n" \
  "v_mfma_f32_4x4x4_16b_bf16 v[48:51], v[76:77], v[124:125], 0 cbsz:4 abid:7\n" \
  "v_mfma_f32_4x4x4_16b_bf16 v[52:55], v[76:77], v[126:127], 0 cbsz:4 abid:7\n" \
  "v_cvt_pk_bf16_f32 v59, v58, v59\n" \
  "v_lshlrev_b32_sdwa v58, 1, v59 dst_sel:DWORD dst_unused:UNUSED_PAD src0_sel:DWORD src1_sel:WORD_0\n" \
  "v_lshlrev_b32_sdwa v59, 1, v59 dst_sel:DWORD dst_unused:UNUSED_PAD src0_sel:DWORD src1_sel:WORD_1\n" \
  "ds_read_u16_d16_hi v58, v58\n" \
  "ds_read_u16_d16_hi v59, v59\n" \
  "s_set_gpr_idx_on s61, gpr_idx(SRC2,DST)\n" \
  "v_pk_fma_f32 v[128:129], v[66:67], v[48:49], v[128:129] op_sel:[1,0,0] op_sel_hi:[1,1,1]\n" \
  "v_pk_fma_f32 v[130:131], v[66:67], v[50:51], v[130:131] op_sel:[1,0,0] op_sel_hi:[1,1,1]\n" \
  "v_pk_fma_f32 v[132:133], v[70:71], v[52:53], v[132:133] op_sel:[1,0,0] op_sel_hi:[1,1,1]\n" \
  "v_pk_fma_f32 v[134:135], v[70:71], v[54:55], v[134:135] op_sel:[1,0,0] op_sel_hi:[1,1,1]\n" \
  "s_set_gpr_idx_off\n" \
  "s_waitcnt vmcnt(6) lgkmcnt(2)\n" \
  "buffer_load_dwordx4 v[124:127], v[12:13], s[52:55], 0 idxen offen\n" \
  "ds_read_u16 v12, v14 offset:64\n" \
  "s_sub_u32 s60, s60, 1\n" \
  "s_cbranch_scc1 Lend%=\n" \
  "v_readlane_b32 s61, v17, s60\n" \
  "s_setprio 1\n" \
  "v_mfma_f32_4x4x4_16b_bf16 v[64:67], v[92:93], v[32:33], 0 cbsz:4 abid:3\n" \
  "v_mfma_f32_4x4x4_16b_bf16 v[68:71], v[92:93], v[36:37], 0 cbsz:4 abid:3\n" \
  "v_mfma_f32_4x4x4_16b_bf16 v[48:51], v[76:77], v[96:97], 0 cbsz:4 abid:8\n" \
  "v_mfma_f32_4x4x4_16b_bf16 v[52:55], v[76:77], v[98:99], 0 cbsz:4 abid:8\n" \
  "v_cvt_pk_bf16_f32 v63, v62, v63\n" \
  "v_lshlrev_b32_sdwa v62, 1, v63 dst_sel:DWORD dst_unused:UNUSED_PAD src0_sel:DWORD src1_sel:WORD_0\n" \
  "v_lshlrev_b32_sdwa v63, 1, v63 dst_sel:DWORD dst_unused:UNUSED_PAD src0_sel:DWORD src1_sel:WORD_1\n" \
  "ds_read_u16_d16_hi v62, v62\n" \
  "ds_read_u16_d16_hi v63, v63\n" \
  "v_add_u32 v15, 0x100, v15\n" \
  "s_set_gpr_idx_on s61, gpr_idx(SRC2,DST)\n" \
  "v_pk_fma_f32 v[128:129], v[56:57], v[48:49], v[128:129] op_sel:[0,0,0] op_sel_hi:[0,1,1]\n" \
  "v_pk_fma_f32 v[130:131], v[56:57], v[50:51], v[130:131] op_sel:[0,0,0] op_sel_hi:[0,1,1]\n" \
  "v_pk_fma_f32 v[132:133], v[60:61], v[52:53], v[132:133] op_sel:[0,0,0] op_sel_hi:[0,1,1]\n" \
  "v_pk_fma_f32 v[134:135], v[60:61], v[54:55], v[134:135] op_sel:[0,0,0] op_sel_hi:[0,1,1]\n" \
  "s_set_gpr_idx_off\n" \
  "s_waitcnt vmcnt(6) lgkmcnt(2)\n" \
  "buffer_load_dwordx4 v[96:99], v[12:13], s[52:55], 0 idxen offen\n" \
  "ds_read_u16 v12, v14 offset:68\n" \
  "s_lshr_b32 s61, s61, 8\n" \
  "v_mfma_f32_4x4x4_16b_bf16 v[48:51], v[76:77], v[100:101], 0 cbsz:4 abid:9\n" \
  "v_mfma_f32_4x4x4_16b_bf16 v[52:55], v[76:77], v[102:103], 0 cbsz:4 abid:9\n" \
  "v_cvt_pk_bf16_f32 v65, v64, v65\n" \
  "v_lshlrev_b32_sdwa v64, 1, v65 dst_sel:DWORD dst_unused:UNUSED_PAD src0_sel:DWORD src1_sel:WORD_0\n" \
  "v_lshlrev_b32_sdwa v65, 1, v65 dst_sel:DWORD dst_unused:UNUSED_PAD src0_sel:DWORD src1_sel:WORD_1\n" \
  "ds_read_u16_d16_hi v64, v64\n" \
  "ds_read_u16_d16_hi v65, v65\n" \
  "s_set_gpr_idx_on s61, gpr_idx(SRC2,DST)\n" \
  "v_pk_fma_f32 v[128:129], v[56:57], v[48:49], v[128:129] op_sel:[1,0,0] op_sel_hi:[1,1,1]\n" \
  "v_pk_fma_f32 v[130:131], v[56:57], v[50:51], v[130:131] op_sel:[1,0,0] op_sel_hi:[1,1,1]\n" \
  "v_pk_fma_f32 v[132:133], v[60:61], v[52:53], v[132:133] op_sel:[1,0,0] op_sel_hi:[1,1,1]\n" \
  "v_pk_fma_f32 v[134:135], v[60:61], v[54:55], v[134:135] op_sel:[1,0,0] op_sel_hi:[1,1,1]\n" \
  "s_set_gpr_idx_off\n" \
  "s_waitcnt vmcnt(6) lgkmcnt(2)\n" \
  "buffer_load_dwordx4 v[100:103], v[12:13], s[52:55], 0 idxen offen\n" \
  "ds_read_u16 v12, v14 offset:72\n" \
  "s_lshr_b32 s61, s61, 8\n" \
  "v_mfma_f32_4x4x4_16b_bf16 v[48:51], v[76:77], v[104:105], 0 cbsz:4 abid:10\n" \
  "v_mfma_f32_4x4x4_16b_bf16 v[52:55], v[76:77], v[106:107], 0 cbsz:4 abid:10\n" \
  "v_cvt_pk_bf16_f32 v69, v68, v69\n" \
  "v_lshlrev_b32_sdwa v68, 1, v69 dst_sel:DWORD dst_unused:UNUSED_PAD src0_sel:DWORD src1_sel:WORD_0\n" \
  "v_lshlrev_b32_sdwa v69, 1, v69 dst_sel:DWORD dst_unused:UNUSED_PAD src0_sel:DWORD src1_sel:WORD_1\n" \
  "ds_read_u16_d16_hi v68, v68\n" \
  "ds_read_u16_d16_hi v69, v69\n" \
  "s_set_gpr_idx_on s61, gpr_idx(SRC2,DST)\n" \
  "v_pk_fma_f32 v[128:129], v[58:59], v[48:49], v[128:129] op_sel:[0,0,0] op_sel_hi:[0,1,1]\n" \
  "v_pk_fma_f32 v[130:131], v[58:59], v[50:51], v[130:131] op_sel:[0,0,0] op_sel_hi:[0,1,1]\n" \
  "v_pk_fma_f32 v[132:133], v[62:63], v[52:53], v[132:133] op_sel:[0,0,0] op_sel_hi:[0,1,1]\n" \
  "v_pk_fma_f32 v[134:135], v[62:63], v[54:55], v[134:135] op_sel:[0,0,0] op_sel_hi:[0,1,1]\n" \
  "s_set_gpr_idx_off\n" \
  "s_waitcnt vmcnt(6) lgkmcnt(2)\n" \
  "buffer_load_dwordx4 v[104:107], v[12:13], s[52:55], 0 idxen offen\n" \
  "ds_read_u16 v12, v14 offset:76\n" \
  "ds_read_b64 v[92:93], v16\n" \
  "s_lshr_b32 s61, s61, 8\n" \
  "v_mfma_f32_4x4x4_16b_bf16 v[88:91], v[94:95], v[26:27], 0\n" \
  "v_mfma_f32_4x4x4_16b_bf16 v[48:51], v[76:77], v[108:109], 0 cbsz:4 abid:11\n" \
  "v_mfma_f32_4x4x4_16b_bf16 v[52:55], v[76:77], v[110:111], 0 cbsz:4 abid:11\n" \
  "v_cvt_pk_bf16_f32 v67, v66, v67\n" \
  "v_lshlrev_b32_sdwa v66, 1, v67 dst_sel:DWORD dst_unused:UNUSED_PAD src0_sel:DWORD src1_sel:WORD_0\n" \
  "v_lshlrev_b32_sdwa v67, 1, v67 dst_sel:DWORD dst_unused:UNUSED_PAD src0_sel:DWORD src1_sel:WORD_1\n" \
  "ds_read_u16_d16_hi v66, v66\n" \
  "ds_read_u16_d16_hi v67, v67\n" \
  "s_set_gpr_idx_on s61, gpr_idx(SRC2,DST)\n" \
  "v_pk_fma_f32 v[128:129], v[58:59], v[48:49], v[128:129] op_sel:[1,0,0] op_sel_hi:[1,1,1]\n" \
  "v_pk_fma_f32 v[130:131], v[58:59], v[50:51], v[130:131] op_sel:[1,0,0] op_sel_hi:[1,1,1]\n" \
  "v_pk_fma_f32 v[132:133], v[62:63], v[52:53], v[132:133] op_sel:[1,0,0] op_sel_hi:[1,1,1]\n" \
  "v_pk_fma_f32 v[134:135], v[62:63], v[54:55], v[134:135] op_sel:[1,0,0] op_sel_hi:[1,1,1]\n" \
  "s_set_gpr_idx_off\n" \
  "s_waitcnt vmcnt(6) lgkmcnt(2)\n" \
  "buffer_load_dwordx4 v[108:111], v[12:13], s[52:55], 0 idxen offen\n" \
  "ds_read_u16 v12, v14 offset:80\n" \
  "s_sub_u32 s60, s60, 1\n" \
  "s_cbranch_scc1 Lend%=\n" \
  "v_readlane_b32 s61, v17, s60\n" \
  "s_setprio 0\n" \
  "v_mfma_f32_4x4x4_16b_bf16 v[56:59], v[92:93], v[32:33], 0 cbsz:4\n" \
  "v_mfma_f32_4x4x4_16b_bf16 v[60:63], v[92:93], v[36:37], 0 cbsz:4\n" \
  "v_mfma_f32_4x4x4_16b_bf16 v[48:51], v[76:77], v[112:113], 0 cbsz:4 abid:12\n" \
  "v_mfma_f32_4x4x4_16b_bf16 v[52:55], v[76:77], v[114:115], 0 cbsz:4 abid:12\n" \
  "v_cvt_pk_bf16_f32 v71, v70, v71\n" \
  "v_lshlrev_b32_sdwa v70, 1, v71 dst_sel:DWORD dst_unused:UNUSED_PAD src0_sel:DWORD src1_sel:WORD_0\n" \
  "v_lshlrev_b32_sdwa v71, 1, v71 dst_sel:DWORD dst_unused:UNUSED_PAD src0_sel:DWORD src1_sel:WORD_1\n" \
  "ds_read_u16_d16_hi v70, v70\n" \
  "ds_read_u16_d16_hi v71, v71\n" \
  "v_cvt_pk_bf16_f32 v20, v84, v88\n" \
  "s_set_gpr_idx_on s61, gpr_idx(SRC2,DST)\n" \
  "v_pk_fma_f32 v[128:129], v[64:65], v[48:49], v[128:129] op_sel:[0,0,0] op_sel_hi:[0,1,1]\n" \
  "v_pk_fma_f32 v[130:131], v[64:65], v[50:51], v[130:131] op_sel:[0,0,0] op_sel_hi:[0,1,1]\n" \
  "v_pk_fma_f32 v[132:133], v[68:69], v[52:53], v[132:133] op_sel:[0,0,0] op_sel_hi:[0,1,1]\n" \
  "v_pk_fma_f32 v[134:135], v[68:69], v[54:55], v[134:135] op_sel:[0,0,0] op_sel_hi:[0,1,1]\n" \
  "s_set_gpr_idx_off\n" \
  "s_waitcnt vmcnt(6) lgkmcnt(2)\n" \
  "buffer_load_dwordx4 v[112:115], v[12:13], s[52:55], 0 idxen offen\n" \
  "ds_read_u16 v12, v14 offset:84\n" \
  "s_lshr_b32 s61, s61, 8\n" \
  "v_mfma_f32_4x4x4_16b_bf16 v[48:51], v[76:77], v[116:117], 0 cbsz:4 abid:13\n" \
  "v_mfma_f32_4x4x4_16b_bf16 v[52:55], v[76:77], v[118:119], 0 cbsz:4 abid:13\n" \
  "v_cvt_pk_bf16_f32 v57, v56, v57\n" \
  "v_lshlrev_b32_sdwa v56, 1, v57 dst_sel:DWORD dst_unused:UNUSED_PAD src0_sel:DWORD src1_sel:WORD_0\n" \
  "v_lshlrev_b32_sdwa v57, 1, v57 dst_sel:DWORD dst_unused:UNUSED_PAD src0_sel:DWORD src1_sel:WORD_1\n" \
  "ds_read_u16_d16_hi v56, v56\n" \
  "ds_read_u16_d16_hi v57, v57\n" \
  "s_set_gpr_idx_on s61, gpr_idx(SRC2,DST)\n" \
  "v_pk_fma_f32 v[128:129], v[64:65], v[48:49], v[128:129] op_sel:[1,0,0] op_sel_hi:[1,1,1]\n" \
  "v_pk_fma_f32 v[130:131], v[64:65], v[50:51], v[130:131] op_sel:[1,0,0] op_sel_hi:[1,1,1]\n" \
  "v_pk_fma_f32 v[132:133], v[68:69], v[52:53], v[132:133] op_sel:[1,0,0] op_sel_hi:[1,1,1]\n" \
  "v_pk_fma_f32 v[134:135], v[68:69], v[54:55], v[134:135] op_sel:[1,0,0] op_sel_hi:[1,1,1]\n" \
  "s_set_gpr_idx_off\n" \
  "s_waitcnt vmcnt(6) lgkmcnt(2)\n" \
  "buffer_load_dwordx4 v[116:119], v[12:13], s[52:55], 0 idxen offen\n" \
  "ds_read_u16 v12, v14 offset:88\n" \
  "s_lshr_b32 s61, s61, 8\n" \
  "v_mfma_f32_4x4x4_16b_bf16 v[48:51], v[76:77], v[120:121], 0 cbsz:4 abid:14\n" \
  "v_mfma_f32_4x4x4_16b_bf16 v[52:55], v[76:77], v[122:123], 0 cbsz:4 abid:14\n" \
  "v_cvt_pk_bf16_f32 v61, v60, v61\n" \
  "v_lshlrev_b32_sdwa v60, 1, v61 dst_sel:DWORD dst_unused:UNUSED_PAD src0_sel:DWORD src1_sel:WORD_0\n" \
  "v_lshlrev_b32_sdwa v61, 1, v61 dst_sel:DWORD dst_unused:UNUSED_PAD src0_sel:DWORD src1_sel:WORD_1\n" \
  "ds_read_u16_d16_hi v60, v60\n" \
  "ds_read_u16_d16_hi v61, v61\n" \
  "s_set_gpr_idx_on s61, gpr_idx(SRC2,DST)\n" \
  "v_pk_fma_f32 v[128:129], v[66:67], v[48:49], v[128:129] op_sel:[0,0,0] op_sel_hi:[0,1,1]\n" \
  "v_pk_fma_f32 v[130:131], v[66:67], v[50:51], v[130:131] op_sel:[0,0,0] op_sel_hi:[0,1,1]\n" \
  "v_pk_fma_f32 v[132:133], v[70:71], v[52:53], v[132:133] op_sel:[0,0,0] op_sel_hi:[0,1,1]\n" \
  "v_pk_fma_f32 v[134:135], v[70:71], v[54:55], v[134:135] op_sel:[0,0,0] op_sel_hi:[0,1,1]\n" \
  "s_set_gpr_idx_off\n" \
  "s_waitcnt vmcnt(6) lgkmcnt(2)\n" \
  "buffer_load_dwordx4 v[120:123], v[12:13], s[52:55], 0 idxen offen\n" \
  "ds_read_u16 v12, v14 offset:92\n" \
  "s_lshr_b32 s61, s61, 8\n" \
  "v_mfma_f32_4x4x4_16b_bf16 v[84:87], v[94:95], v[28:29], 0\n" \
  "v_mfma_f32_4x4x4_16b_bf16 v[88:91], v[94:95], v[30:31], 0\n" \
  "v_mfma_f32_4x4x4_16b_bf16 v[48:51], v[76:77], v[124:125], 0 cbsz:4 abid:15\n" \
  "v_mfma_f32_4x4x4_16b_bf16 v[52:55], v[76:77], v[126:127], 0 cbsz:4 abid:15\n" \
  "v_cvt_pk_bf16_f32 v59, v58, v59\n" \
  "v_lshlrev_b32_sdwa v58, 1, v59 dst_sel:DWORD dst_unused:UNUSED_PAD src0_sel:DWORD src1_sel:WORD_0\n" \
  "v_lshlrev_b32_sdwa v59, 1, v59 dst_sel:DWORD dst_unused:UNUSED_PAD src0_sel:DWORD src1_sel:WORD_1\n" \
  "ds_read_u16_d16_hi v58, v58\n" \
  "ds_read_u16_d16_hi v59, v59\n" \
  "v_cvt_pk_bf16_f32 v77, v84, v88\n" \
  "v_mov_b32 v76, v20\n" \
  "s_set_gpr_idx_on s61, gpr_idx(SRC2,DST)\n" \
  "v_pk_fma_f32 v[128:129], v[66:67], v[48:49], v[128:129] op_sel:[1,0,0] op_sel_hi:[1,1,1]\n" \
  "v_pk_fma_f32 v[130:131], v[66:67], v[50:51], v[130:131] op_sel:[1,0,0] op_sel_hi:[1,1,1]\n" \
  "v_pk_fma_f32 v[132:133], v[70:71], v[52:53], v[132:133] op_sel:[1,0,0] op_sel_hi:[1,1,1]\n" \
  "v_pk_fma_f32 v[134:135], v[70:71], v[54:55], v[134:135] op_sel:[1,0,0] op_sel_hi:[1,1,1]\n" \
  "s_set_gpr_idx_off\n" \
  "s_waitcnt vmcnt(6) lgkmcnt(2)\n" \
  "buffer_load_dwordx4 v[124:127], v[12:13], s[52:55], 0 idxen offen\n" \
  "ds_read_u16 v12, v14 offset:96\n" \
  "s_sub_u32 s60, s60, 1\n" \
  "s_cbranch_scc1 Lend%=\n" \
  "v_readlane_b32 s61, v17, s60\n" \
  "s_setprio 3\n" \
  "v_mfma_f32_4x4x4_16b_bf16 v[64:67], v[92:93], v[32:33], 0 cbsz:4 abid:1\n" \
  "v_mfma_f32_4x4x4_16b_bf16 v[68:71], v[92:93], v[36:37], 0 cbsz:4 abid:1\n" \
  "v_mfma_f32_4x4x4_16b_bf16 v[48:51], v[76:77], v[96:97], 0 cbsz:4\n" \
  "v_mfma_f32_4x4x4_16b_bf16 v[52:55], v[76:77], v[98:99], 0 cbsz:4\n" \
  "v_cvt_pk_bf16_f32 v63, v62, v63\n" \
  "v_lshlrev_b32_sdwa v62, 1, v63 dst_sel:DWORD dst_unused:UNUSED_PAD src0_sel:DWORD src1_sel:WORD_0\n" \
  "v_lshlrev_b32_sdwa v63, 1, v63 dst_sel:DWORD dst_unused:UNUSED_PAD src0_sel:DWORD src1_sel:WORD_1\n" \
  "ds_read_u16_d16_hi v62, v62\n" \
  "ds_read_u16_d16_hi v63, v63\n" \
  "v_add_u32 v14, 64, v14\n" \
  "s_branch Lbody%=\n" \
  "Lep0_%=:\n" \
  "ds_read_b32 v50, v18\n" \
  "s_waitcnt lgkmcnt(0)\n" \
  "v_readfirstlane_b32 s63, v50\n" \
  "s_bitcmp1_b32 s63, 1\n" \
  "s_cbranch_scc1 Lgo%=\n" \
  "v_mov_b64 v[50:51], s[50:51]\n" \
  "s_mov_b64 exec, 1\n" \
  "buffer_store_dwordx2 v[50:51], off, s[24:27], s70 sc1\n" \
  "s_mov_b64 exec, -1\n" \
  "s_nop 3\n" \
  "s_branch Lgo%=\n" \
  "Lbad%=:\n" \
  "s_cmp_eq_u32 s74, 0xffffffc1\n" \
  "s_cbranch_scc1 Lf9_%=\n" \
  "v_mov_b32 v72, 1\n" \
  "v_mov_b32 v73, s62\n" \
  "ds_or_b32 v18, v72\n" \
  "ds_write_b32 v73, v73\n" \
  "s_branch Lf9_%=\n" \
  "Lnf%=:\n" \
  "s_mov_b32 %[bad], 3\n" \
  "s_mov_b32 s79, m0\n" \
  "Lbad2_%=:\n" \
  "s_waitcnt vmcnt(0) lgkmcnt(0)\n" \
  "s_bitcmp1_b32 %[bad], 1\n" \
  "s_cbranch_scc1 Lout%=\n" \
  "s_branch Lep%=\n" \
  "Lh2_%=:\n" \
  "s_waitcnt vmcnt(0)\n" \
  "v_add_co_u32 v90, vcc, 1, v88\n" \
  "s_mul_i32 s63, %[blk], 2176\n" \
  "s_add_u32 s63, s63, 49408\n" \
  "v_addc_co_u32 v91, vcc, 0, v89, vcc\n" \
  "v_lshlrev_b32 v88, 22, v90\n" \
  "v_alignbit_b32 v89, v91, v90, 10\n" \
  "v_add_u32 v87, v87, v88\n" \
  "v_lshl_add_u32 v128, v136, 11, v87\n" \
  "v_mov_b32 v129, v89\n" \
  "v_sub_u32 v51, v137, v136\n" \
  "v_lshl_add_u32 v51, v51, 11, v136\n" \
  "v_add_u32 v130, v51, v87\n" \
  "v_mov_b32 v131, v89\n" \
  "v_sub_u32 v51, v138, v137\n" \
  "v_lshl_add_u32 v51, v51, 11, v137\n" \
  "v_add_u32 v132, v51, v87\n" \
  "v_mov_b32 v133, v89\n" \
  "v_sub_u32 v51, v139, v138\n" \
  "v_lshl_add_u32 v51, v51, 11, v138\n" \
  "v_add_u32 v134, v51, v87\n" \
  "v_mov_b32 v135, v89\n" \
  "buffer_store_dwordx4 v[128:131], v13, s[24:27], s63 offen sc1\n" \
  "buffer_store_dwordx4 v[132:135], v13, s[24:27], s63 offen offset:1024 sc1\n" \
  "buffer_load_dword v100, v54, s[8:11], 0 offen offset:0\n" \
  "buffer_load_dword v101, v54, s[8:11], 0 offen offset:256\n" \
  "buffer_load_dword v102, v54, s[8:11], 0 offen offset:512\n" \
  "buffer_load_dword v103, v54, s[8:11], 0 offen offset:768\n" \
  "buffer_load_dword v104, v54, s[12:15], 0 offen\n" \
  "s_mul_i32 s72, s45, 528\n" \
  "s_cmp_lt_u32 s44, 4\n" \
  "s_cselect_b32 s62, 45776, 0\n" \
  "s_cselect_b32 s62, s62, 140608\n" \
  "s_add_u32 m0, s72, s62\n" \
  "s_lshl_b32 s72, s44, 12\n" \
  "buffer_load_dwordx4 v13, s[52:55], s72 offen lds\n" \
  "buffer_load_dwordx4 v13, s[52:55], s72 offen offset:1024 lds\n" \
  "s_add_u32 m0, m0, 64\n" \
  "s_nop 0\n" \
  "buffer_load_dwordx4 v13, s[52:55], s72 offen offset:2048 lds\n" \
  "buffer_load_dwordx4 v13, s[52:55], s72 offen offset:3072 lds\n" \
  "s_mov_b32 m0, 36160\n" \
  "s_barrier\n" \
  "ds_write_b64 v94, v[90:91] offset:1472\n" \
  "s_mov_b32 s63, 0x05040100\n" \
  "s_mov_b32 s93, 0x07060302\n" \
  "s_waitcnt vmcnt(4)\n" \
  "v_perm_b32 v120, v101, v100, s63\n" \
  "v_perm_b32 v121, v103, v102, s63\n" \
  "v_perm_b32 v122, v101, v100, s93\n" \
  "v_perm_b32 v123, v103, v102, s93\n" \
  "ds_write_b128 v13, v[120:123] offset:36544\n" \
  "v_and_b32 v50, 30, v86\n" \
  "v_and_b32 v51, 16, v13\n" \
  "v_lshl_add_u32 v50, v51, 2, v50\n" \
  "s_mov_b64 exec, 0xffffffff\n" \
  "ds_write_b16 v50, v104 offset:36416\n" \
  "ds_write_b16_d16_hi v50, v104 offset:36448\n" \
  "s_mov_b64 exec, -1\n" \
  "v_and_b32 v100, 0x7fff7fff, v100\n" \
  "v_max_u16_sdwa v100, v100, v100 dst_sel:WORD_1 dst_unused:UNUSED_PAD src0_sel:WORD_0 src1_sel:WORD_1\n" \
  "v_and_b32 v101, 0x7fff7fff, v101\n" \
  "v_max_u16_sdwa v101, v101, v101 dst_sel:WORD_1 dst_unused:UNUSED_PAD src0_sel:WORD_0 src1_sel:WORD_1\n" \
  "v_and_b32 v102, 0x7fff7fff, v102\n" \
  "v_max_u16_sdwa v102, v102, v102 dst_sel:WORD_1 dst_unused:UNUSED_PAD src0_sel:WORD_0 src1_sel:WORD_1\n" \
  "v_and_b32 v103, 0x7fff7fff, v103\n" \
  "v_max_u16_sdwa v103, v103, v103 dst_sel:WORD_1 dst_unused:UNUSED_PAD src0_sel:WORD_0 src1_sel:WORD_1\n" \
  "v_add_f32 v100, v100, v101\n" \
  "v_add_f32 v102, v102, v103\n" \
  "v_add_f32 v100, v100, v102\n" \
  "v_and_b32 v100, 0x7fffffff, v100\n" \
  "s_nop 1\n" \
  "v_max_u32_dpp v100, v100, v100 row_shr:1 row_mask:0xf bank_mask:0xf bound_ctrl:1\n" \
  "s_nop 1\n" \
  "v_max_u32_dpp v100, v100, v100 row_shr:2 row_mask:0xf bank_mask:0xf bound_ctrl:1\n" \
  "s_nop 1\n" \
  "v_max_u32_dpp v100, v100, v100 row_shr:4 row_mask:0xf bank_mask:0xf bound_ctrl:1\n" \
  "s_nop 1\n" \
  "v_max_u32_dpp v100, v100, v100 row_shr:8 row_mask:0xf bank_mask:0xf bound_ctrl:1\n" \
  "s_nop 1\n" \
  "v_max_u32_dpp v100, v100, v100 row_bcast:15 row_mask:0xa bank_mask:0xf\n" \
  "s_nop 1\n" \
  "v_max_u32_dpp v100, v100, v100 row_bcast:31 row_mask:0xc bank_mask:0xf\n" \
  "v_max_u32 v101, 0x0d000000, v100\n" \
  "v_rcp_f32 v101, v101\n" \
  "v_cmp_gt_u32 vcc, 0x71000000, v100\n" \
  "v_mul_f32 v101, 0x437a0000, v101\n" \
  "v_lshrrev_b32 v101, 16, v101\n" \
  "v_cndmask_b32 v101, -1, v101, vcc\n" \
  "s_brev_b64 exec, 1\n" \
  "ds_write_b32 v94, v101 offset:1480\n" \
  "s_mov_b64 exec, -1\n" \
  "s_branch Lc1_%=\n" \
  "Lend%=:\n" \
  "s_lshl2_add_u32 s62, s44, 36128\n" \
  "v_add_u32 v19, s72, v19\n" \
  "s_cmp_ge_u32 s74, 10\n" \
  "s_cbranch_scc1 Lbad%=\n" \
  "v_mov_b32 v72, s62\n" \
  "s_cmp_lt_u32 s93, s44\n" \
  "s_cbranch_scc0 Lf1_%=\n" \
  "s_set_gpr_idx_on s73, gpr_idx(SRC0)\n" \
  "v_mov_b64 v[84:85], v[128:129]\n" \
  "v_mov_b64 v[86:87], v[130:131]\n" \
  "v_mov_b64 v[88:89], v[132:133]\n" \
  "v_mov_b64 v[90:91], v[134:135]\n" \
  "s_set_gpr_idx_off\n" \
  "ds_write_b128 v21, v[84:87]\n" \
  "ds_write_b128 v21, v[88:91] offset:1024\n" \
  "ds_write_b32 v72, v72\n" \
  "s_add_u32 s73, s73, 8\n" \
  "s_cmp_eq_u32 s73, 80\n" \
  "s_cselect_b32 s73, 0, s73\n" \
  "v_add_u32 v19, 1056, v19\n" \
  "s_sub_u32 s74, s74, 1\n" \
  "s_cbranch_scc1 Lf9_%=\n" \
  "Lf1_%=:\n" \
  "s_min_u32 s63, s75, 1\n" \
  "s_sub_u32 s60, s74, s63\n" \
  "s_cbranch_scc1 Lf4_%=\n" \
  "Lf2_%=:\n" \
  "s_set_gpr_idx_on s73, gpr_idx(SRC0,SRC1)\n" \
  "v_cvt_pk_bf16_f32 v48, v128, v132\n" \
  "v_cvt_pk_bf16_f32 v49, v129, v133\n" \
  "v_cvt_pk_bf16_f32 v50, v130, v134\n" \
  "v_cvt_pk_bf16_f32 v51, v131, v135\n" \
  "s_set_gpr_idx_off\n" \
  "s_add_u32 s73, s73, 8\n" \
  "s_cmp_eq_u32 s73, 80\n" \
  "s_cselect_b32 s73, 0, s73\n" \
  "ds_write2_b32 v19, v48, v49 offset1:16\n" \
  "ds_write2_b32 v19, v50, v51 offset0:32 offset1:48\n" \
  "s_sub_u32 s60, s60, 1\n" \
  "v_add_u32 v19, 1056, v19\n" \
  "s_cbranch_scc0 Lf2_%=\n" \
  "s_cmp_eq_u32 s75, 0\n" \
  "s_cbranch_scc1 Lf9_%=\n" \
  "Lf4_%=:\n" \
  "ds_read_b32 v73, v72 offset:4\n" \
  "ds_read_b128 v[84:87], v21 offset:3328\n" \
  "ds_read_b128 v[88:91], v21 offset:4352\n" \
  "s_waitcnt lgkmcnt(0)\n" \
  "v_cmp_eq_u32 vcc, 0, v73\n" \
  "s_cbranch_vccz Lf5_%=\n" \
  "s_setprio 0\n" \
  "s_sleep 1\n" \
  "s_branch Lf4_%=\n" \
  "Lf5_%=:\n" \
  "s_set_gpr_idx_on s73, gpr_idx(SRC0,DST)\n" \
  "v_pk_add_f32 v[128:129], v[128:129], v[84:85]\n" \
  "v_pk_add_f32 v[130:131], v[130:131], v[86:87]\n" \
  "v_pk_add_f32 v[132:133], v[132:133], v[88:89]\n" \
  "v_pk_add_f32 v[134:135], v[134:135], v[90:91]\n" \
  "s_set_gpr_idx_off\n" \
  "s_mov_b32 s60, 0\n" \
  "s_sub_u32 s75, s75, 1\n" \
  "s_cmp_eq_u32 s75, 0\n" \
  "s_cbranch_scc1 Lf2_%=\n" \
  "v_add_u32 v72, 4, v72\n" \
  "v_add_u32 v21, 3328, v21\n" \
  "s_branch Lf4_%=\n" \
  "Lf9_%=:\n" \
  "buffer_load_dword v20, off, s[24:27], s70 sc1\n" \
  "s_waitcnt lgkmcnt(0)\n" \
  "s_barrier\n" \
  "ds_read_b32 v18, v18\n" \
  "ds_read_b128 v[128:131], v22 offset:0\n" \
  "ds_read_b128 v[132:135], v22 offset:256\n" \
  "ds_read_b128 v[136:139], v22 offset:512\n" \
  "ds_read_b128 v[140:143], v22 offset:768\n" \
  "ds_read_b128 v[144:147], v22 offset:8448\n" \
  "ds_read_b128 v[148:151], v22 offset:8704\n" \
  "ds_read_b128 v[152:155], v22 offset:8960\n" \
  "ds_read_b128 v[156:159], v22 offset:9216\n" \
  "ds_read_b128 v[160:163], v22 offset:16896\n" \
  "ds_read_b128 v[164:167], v22 offset:17152\n" \
  "ds_read_b128 v[168:171], v22 offset:17408\n" \
  "ds_read_b128 v[172:175], v22 offset:17664\n" \
  "ds_read_b128 v[176:179], v22 offset:25344\n" \
  "ds_read_b128 v[180:183], v22 offset:25600\n" \
  "s_waitcnt lgkmcnt(14)\n" \
  "v_readfirstlane_b32 %[bad], v18\n" \
  "s_cmp_lg_u32 %[bad], 0\n" \
  "s_cbranch_scc1 Lbad2_%=\n" \
  "s_waitcnt lgkmcnt(13)\n" \
  "v_mfma_f32_16x16x32_bf16 v[40:43], v[128:131], v[216:219], 0\n" \
  "v_mfma_f32_16x16x32_bf16 v[44:47], v[128:131], v[220:223], 0\n" \
  "ds_read_b128 v[184:187], v22 offset:25856\n" \
  "s_waitcnt lgkmcnt(13)\n" \
  "v_mfma_f32_16x16x32_bf16 v[40:43], v[132:135], v[224:227], v[40:43]\n" \
  "v_mfma_f32_16x16x32_bf16 v[44:47], v[132:135], v[228:231], v[44:47]\n" \
  "ds_read_b128 v[188:191], v22 offset:26112\n" \
  "s_waitcnt lgkmcnt(13)\n" \
  "v_mfma_f32_16x16x32_bf16 v[40:43], v[136:139], v[232:235], v[40:43]\n" \
  "v_mfma_f32_16x16x32_bf16 v[44:47], v[136:139], v[236:239], v[44:47]\n" \
  "ds_read_b128 v[192:195], v22 offset:33792\n" \
  "s_waitcnt lgkmcnt(13)\n" \
  "v_mfma_f32_16x16x32_bf16 v[40:43], v[140:143], v[240:243], v[40:43]\n" \
  "v_mfma_f32_16x16x32_bf16 v[44:47], v[140:143], v[244:247], v[44:47]\n" \
  "ds_read_b128 v[196:199], v22 offset:34048\n" \
  "s_waitcnt lgkmcnt(13)\n" \
  "v_mfma_f32_16x16x32_bf16 v[48:51], v[144:147], v[216:219], 0\n" \
  "v_mfma_f32_16x16x32_bf16 v[52:55], v[144:147], v[220:223], 0\n" \
  "ds_read_b128 v[200:203], v22 offset:34304\n" \
  "s_waitcnt lgkmcnt(13)\n" \
  "v_mfma_f32_16x16x32_bf16 v[48:51], v[148:151], v[224:227], v[48:51]\n" \
  "v_mfma_f32_16x16x32_bf16 v[52:55], v[148:151], v[228:231], v[52:55]\n" \
  "ds_read_b128 v[204:207], v22 offset:34560\n" \
  "v_cvt_pk_bf16_f32 v40, v40, v41\n" \
  "v_cvt_pk_bf16_f32 v41, v42, v43\n" \
  "v_cvt_pk_bf16_f32 v42, v44, v45\n" \
  "v_cvt_pk_bf16_f32 v43, v46, v47\n" \
  "buffer_store_dwordx4 v[40:43], v23, s[56:59], 0 offen sc1\n" \
  "s_waitcnt lgkmcnt(13)\n" \
  "v_mfma_f32_16x16x32_bf16 v[48:51], v[152:155], v[232:235], v[48:51]\n" \
  "v_mfma_f32_16x16x32_bf16 v[52:55], v[152:155], v[236:239], v[52:55]\n" \
  "s_waitcnt lgkmcnt(12)\n" \
  "v_mfma_f32_16x16x32_bf16 v[48:51], v[156:159], v[240:243], v[48:51]\n" \
  "v_mfma_f32_16x16x32_bf16 v[52:55], v[156:159], v[244:247], v[52:55]\n" \
  "s_waitcnt lgkmcnt(11)\n" \
  "v_mfma_f32_16x16x32_bf16 v[56:59], v[160:163], v[216:219], 0\n" \
  "v_mfma_f32_16x16x32_bf16 v[60:63], v[160:163], v[220:223], 0\n" \
  "s_waitcnt lgkmcnt(10)\n" \
  "v_mfma_f32_16x16x32_bf16 v[56:59], v[164:167], v[224:227], v[56:59]\n" \
  "v_mfma_f32_16x16x32_bf16 v[60:63], v[164:167], v[228:231], v[60:63]\n" \
  "s_nop 1\n" \
  "v_cvt_pk_bf16_f32 v48, v48, v49\n" \
  "v_cvt_pk_bf16_f32 v49, v50, v51\n" \
  "v_cvt_pk_bf16_f32 v50, v52, v53\n" \
  "v_cvt_pk_bf16_f32 v51, v54, v55\n" \
  "s_mov_b32 s63, 0x2000\n" \
  "buffer_store_dwordx4 v[48:51], v23, s[56:59], s63 offen sc1\n" \
  "s_waitcnt lgkmcnt(9)\n" \
  "v_mfma_f32_16x16x32_bf16 v[56:59], v[168:171], v[232:235], v[56:59]\n" \
  "v_mfma_f32_16x16x32_bf16 v[60:63], v[168:171], v[236:239], v[60:63]\n" \
  "s_waitcnt lgkmcnt(8)\n" \
  "v_mfma_f32_16x16x32_bf16 v[56:59], v[172:175], v[240:243], v[56:59]\n" \
  "v_mfma_f32_16x16x32_bf16 v[60:63], v[172:175], v[244:247], v[60:63]\n" \
  "s_waitcnt lgkmcnt(7)\n" \
  "v_mfma_f32_16x16x32_bf16 v[64:67], v[176:179], v[216:219], 0\n" \
  "v_mfma_f32_16x16x32_bf16 v[68:71], v[176:179], v[220:223], 0\n" \
  "s_waitcnt lgkmcnt(6)\n" \
  "v_mfma_f32_16x16x32_bf16 v[64:67], v[180:183], v[224:227], v[64:67]\n" \
  "v_mfma_f32_16x16x32_bf16 v[68:71], v[180:183], v[228:231], v[68:71]\n" \
  "s_nop 1\n" \
  "v_cvt_pk_bf16_f32 v56, v56, v57\n" \
  "v_cvt_pk_bf16_f32 v57, v58, v59\n" \
  "v_cvt_pk_bf16_f32 v58, v60, v61\n" \
  "v_cvt_pk_bf16_f32 v59, v62, v63\n" \
  "s_mov_b32 s63, 0x4000\n" \
  "buffer_store_dwordx4 v[56:59], v23, s[56:59], s63 offen sc1\n" \
  "s_waitcnt lgkmcnt(5)\n" \
  "v_mfma_f32_16x16x32_bf16 v[64:67], v[184:187], v[232:235], v[64:67]\n" \
  "v_mfma_f32_16x16x32_bf16 v[68:71], v[184:187], v[236:239], v[68:71]\n" \
  "s_waitcnt lgkmcnt(4)\n" \
  "v_mfma_f32_16x16x32_bf16 v[64:67], v[188:191], v[240:243], v[64:67]\n" \
  "v_mfma_f32_16x16x32_bf16 v[68:71], v[188:191], v[244:247], v[68:71]\n" \
  "s_waitcnt lgkmcnt(3)\n" \
  "v_mfma_f32_16x16x32_bf16 v[72:75], v[192:195], v[216:219], 0\n" \
  "v_mfma_f32_16x16x32_bf16 v[76:79], v[192:195], v[220:223], 0\n" \
  "s_waitcnt lgkmcnt(2)\n" \
  "v_mfma_f32_16x16x32_bf16 v[72:75], v[196:199], v[224:227], v[72:75]\n" \
  "v_mfma_f32_16x16x32_bf16 v[76:79], v[196:199], v[228:231], v[76:79]\n" \
  "s_nop 1\n" \
  "v_cvt_pk_bf16_f32 v64, v64, v65\n" \
  "v_cvt_pk_bf16_f32 v65, v66, v67\n" \
  "v_cvt_pk_bf16_f32 v66, v68, v69\n" \
  "v_cvt_pk_bf16_f32 v67, v70, v71\n" \
  "s_mov_b32 s63, 0x6000\n" \
  "buffer_store_dwordx4 v[64:67], v23, s[56:59], s63 offen sc1\n" \
  "s_waitcnt lgkmcnt(1)\n" \
  "v_mfma_f32_16x16x32_bf16 v[72:75], v[200:203], v[232:235], v[72:75]\n" \
  "v_mfma_f32_16x16x32_bf16 v[76:79], v[200:203], v[236:239], v[76:79]\n" \
  "s_waitcnt lgkmcnt(0)\n" \
  "v_mfma_f32_16x16x32_bf16 v[72:75], v[204:207], v[240:243], v[72:75]\n" \
  "v_mfma_f32_16x16x32_bf16 v[76:79], v[204:207], v[244:247], v[76:79]\n" \
  "s_nop 7\n" \
  "v_cvt_pk_bf16_f32 v72, v72, v73\n" \
  "v_cvt_pk_bf16_f32 v73, v74, v75\n" \
  "v_cvt_pk_bf16_f32 v74, v76, v77\n" \
  "v_cvt_pk_bf16_f32 v75, v78, v79\n" \
  "s_mov_b32 s63, 0x8000\n" \
  "buffer_store_dwordx4 v[72:75], v23, s[56:59], s63 offen sc1\n" \
  "s_waitcnt vmcnt(5)\n" \
  "v_readfirstlane_b32 s63, v20\n" \
  "s_cmp_eq_u32 s63, s50\n" \
  "s_cbranch_scc1 Lout%=\n" \
  "Lep%=:\n" \
  "s_cmp_lg_u32 s44, 0\n" \
  "s_cbranch_scc1 Lout%=\n" \
  "v_mov_b64 v[84:85], s[50:51]\n" \
  "s_mov_b64 exec, 1\n" \
  "buffer_store_dwordx2 v[84:85], off, s[24:27], s70 sc1\n" \
  "s_mov_b64 exec, -1\n" \
  "Lout%=:\n" \
  "s_mov_b32 m0, s79\n"
#define STREAM_CLOBBERS \
  "v12", "v13", "v14", "v15", "v16", "v17", "v18", "v19", "v20", "v21", "v22", "v23", "v24", "v25", "v26", \
  "v27", "v28", "v29", "v30", "v31", "v32", "v33", "v34", "v35", "v36", "v37", "v38", "v39", "v40", "v41", \
  "v42", "v43", "v44", "v45", "v46", "v47", "v48", "v49", "v50", "v51", "v52", "v53", "v54", "v55", "v56", \
  "v57", "v58", "v59", "v60", "v61", "v62", "v63", "v64", "v65", "v66", "v67", "v68", "v69", "v70", "v71", \
  "v72", "v73", "v74", "v75", "v76", "v77", "v78", "v79", "v80", "v81", "v82", "v83", "v84", "v85", "v86", \
  "v87", "v88", "v89", "v90", "v91", "v92", "v93", "v94", "v95", "v96", "v97", "v98", "v99", "v100", "v101", \
  "v102", "v103", "v104", "v105", "v106", "v107", "v108", "v109", "v110", "v111", "v112", "v113", "v114", \
  "v115", "v116", "v117", "v118", "v119", "v120", "v121", "v122", "v123", "v124", "v125", "v126", "v127", \
  "v128", "v129", "v130", "v131", "v132", "v133", "v134", "v135", "v136", "v137", "v138", "v139", "v140", \
  "v141", "v142", "v143", "v144", "v145", "v146", "v147", "v148", "v149", "v150", "v151", "v152", "v153", \
  "v154", "v155", "v156", "v157", "v158", "v159", "v160", "v161", "v162", "v163", "v164", "v165", "v166", \
  "v167", "v168", "v169", "v170", "v171", "v172", "v173", "v174", "v175", "v176", "v177", "v178", "v179", \
  "v180", "v181", "v182", "v183", "v184", "v185", "v186", "v187", "v188", "v189", "v190", "v191", "v192", \
  "v193", "v194", "v195", "v196", "v197", "v198", "v199", "v200", "v201", "v202", "v203", "v204", "v205", \
  "v206", "v207", "v208", "v209", "v210", "v211", "v212", "v213", "v214", "v215", "v216", "v217", "v218", \
  "v219", "v220", "v221", "v222", "v223", "v224", "v225", "v226", "v227", "v228", "v229", "v230", "v231", \
  "v232", "v233", "v234", "v235", "v236", "v237", "v238", "v239", "v240", "v241", "v242", "v243", "v244", \
  "v245", "v246", "v247", "vcc", "s8", "s9", "s10", "s11", "s12", "s13", "s14", "s15", "s16", "s17", "s18", \
  "s19", "s20", "s21", "s22", "s23", "s24", "s25", "s26", "s27", "s28", "s29", "s30", "s31", "s33", "s34", \
  "s35", "s36", "s37", "s38", "s39", "s40", "s41", "s42", "s43", "s44", "s45", "s46", "s47", "s48", "s49", \
  "s50", "s51", "s52", "s53", "s54", "s55", "s56", "s57", "s58", "s59", "s60", "s61", "s62", "s63", "s64", \
  "s65", "s66", "s67", "s68", "s69", "s70", "s71", "s72", "s73", "s74", "s75", "s76", "s77", "s78", "s79", \
  "s80", "s81", "s82", "s83", "s84", "s85", "s86", "s87", "s88", "s89", "s90", "s91", "s92", "s93", "s94", \
  "s95", "scc", "memory"
#define LD_CELL 34560
#define LD_DEG 35584
#define LD_FLAG 36096
#define LD_CRD 36128
#define LD_SINK 36160
#define LD_CGT 36416
#define LD_WQT 36544
#define LD_TAG 37568
#define LD_PAY 37584
#define LD_ENT 100096
#define LD_TB 106816
#define LD_ROWS 108864
#define LD_SK 129344
#define LD_HIST 134464
#define LD_HB 136512
#define LD_W0 45776
#define LD_W1 140608
#define LD_END 157504
#define WS_EP 0
#define WS_FB 256
#define WS_KF 16640
#define WS_GR 49408
#define WS_RECK 606464
#define WS_RECP 1917184
#define WS_END 7160064

namespace {

constexpr int C_ = 128;
constexpr int NMAX = 40;
constexpr int NB = 256;
constexpr int CH = 1280;
constexpr int MT = 512;
constexpr int MW = MT / 64;
constexpr int RP = 1056;
constexpr unsigned W3 = 0x00020000u;

__device__ __forceinline__ float bf2f(unsigned short v) { return __uint_as_float(((unsigned)v) << 16); }
__device__ __forceinline__ unsigned short f2bf(float f) {
  __hip_bfloat16 b = __float2bfloat16(f);
  return *reinterpret_cast<unsigned short*>(&b);
}
__device__ __forceinline__ unsigned pk2bf(float lo, float hi) {
  return __builtin_bit_cast(unsigned, __builtin_convertvector(f32x2{lo, hi}, bf16x2));
}

}

extern "C" __global__ __launch_bounds__(MT) void tfn_kernel(
    const int* __restrict__ dst, unsigned dn, unsigned dw, const int* __restrict__ src, unsigned sn, unsigned sw,
    const u32x2* __restrict__ Y, unsigned yn, unsigned yw, const u32x2* __restrict__ ef, unsigned fn, unsigned fw,
    char* ws, unsigned wn, unsigned ww, const unsigned short* __restrict__ x, unsigned xn, unsigned xw,
    u32x4* __restrict__ out, unsigned pt, int N, unsigned M2, unsigned cb, int nbk, unsigned npb,
    const unsigned short* __restrict__ wrad, unsigned rn, unsigned rw, const unsigned short* __restrict__ cg, unsigned cn,
    unsigned cw, const unsigned short* __restrict__ wmix, unsigned mn, unsigned mw) {
  __shared__ __attribute__((aligned(16))) char lds[LD_END];
  static_assert(LD_END >= LD_ROWS + NMAX * RP && LD_END <= 163840, "LDS");
  TDECL;
  MSTAMP(0);
  unsigned bad;
  asm volatile(STREAM_ASM
               : [bad] "=&s"(bad)
               : [ka] "s"(__builtin_amdgcn_kernarg_segment_ptr()), [blk] "s"(blockIdx.x), [tid] "v"(threadIdx.x)
               : STREAM_CLOBBERS);
  const bool slow = bad != 0;
  MSTAMP(6);
  if (slow) {
    const int lane = threadIdx.x & 63, w = __builtin_amdgcn_readfirstlane(threadIdx.x >> 6), E = dn / 4;
    const int bucket = (blockIdx.x & 7) * (nbk / 8) + (blockIdx.x >> 3);
    int n01[2];
#pragma unroll
    for (int h = 0; h < 2; h++) {
      unsigned c = (unsigned)(((unsigned long long)(bucket + h) * npb) >> 20);
#pragma unroll
      for (int r = 0; r < 2; r++) c += __umulhi(c, M2) < (unsigned)(bucket + h) ? 1 : 0;
      n01[h] = min(c, (unsigned)N);
    }
    const int node0 = n01[0], nn = n01[1] - n01[0];
    const unsigned mxa = LD_ROWS + ((w >> 2) * 4 + ((lane & 15) >> 2)) * RP + (lane & 3) * 64 + (lane >> 4) * 16;
    const unsigned mxo = ((w >> 2) * 4 + (lane >> 4)) * 1024 + (w & 3) * 256 + (lane & 15) * 16;
    __syncthreads();
#pragma unroll 1
    for (int it = w; it < 2 * nn; it += MW) {
      const int dl = it >> 1, ch = (it & 1) * 64 + lane;
      float acc[4] = {0.f, 0.f, 0.f, 0.f};
#pragma unroll 1
      for (int eb = 0; eb < E; eb += 64) {
        unsigned long long m = __ballot(eb + lane < E && dst[min(eb + lane, E - 1)] == node0 + dl);
#pragma unroll 1
        for (; m; m &= m - 1) {
          const int e = eb + __builtin_ctzll(m);
          const u32x2 ry = Y[e], rf = ef[e];
          float yy[4] = {bf2f(ry[0] & 0xffff), bf2f(ry[0] >> 16), bf2f(ry[1] & 0xffff), bf2f(ry[1] >> 16)};
          float ee[4] = {bf2f(rf[0] & 0xffff), bf2f(rf[0] >> 16), bf2f(rf[1] & 0xffff), bf2f(rf[1] >> 16)};
          float z = 0.f;
#pragma unroll
          for (int k = 0; k < 4; k++) z += ee[k] * bf2f(wrad[k * C_ + ch]);
          const float R = z / (1.f + __expf(-z));
          const unsigned short* xr = x + (size_t)src[e] * 512 + ch * 4;
#pragma unroll 1
          for (int o = 0; o < 4; o++) {
            float tp = 0.f;
#pragma unroll 1
            for (int i = 0; i < 4; i++) {
              float A = 0.f;
#pragma unroll
              for (int f = 0; f < 4; f++) A += yy[f] * bf2f(cg[i * 16 + f * 4 + o]);
              tp += bf2f(xr[i]) * bf2f(f2bf(A));
            }
            const float v = R * tp;
            acc[0] += o == 0 ? v : 0.f;
            acc[1] += o == 1 ? v : 0.f;
            acc[2] += o == 2 ? v : 0.f;
            acc[3] += o == 3 ? v : 0.f;
          }
        }
      }
      unsigned short* ar = reinterpret_cast<unsigned short*>(lds + LD_ROWS) + dl * (RP / 2) + (ch >> 5) * 128 + (ch & 31);
#pragma unroll
      for (int o = 0; o < 4; o++) ar[o * 32] = f2bf(acc[o]);
    }
    __syncthreads();
    const auto ors = __builtin_amdgcn_make_buffer_rsrc((void*)(out + (size_t)node0 * 64), 0, nn * 1024, 0x00020000);
#pragma unroll 1
    for (int t = 0; t < NMAX / 8; t++) {
      f32x4 a = {0.f, 0.f, 0.f, 0.f}, b = {0.f, 0.f, 0.f, 0.f};
#pragma unroll 1
      for (int k = 0; k < 4; k++) {
        const s16x8 f = *reinterpret_cast<const s16x8*>(lds + mxa + t * 8 * RP + k * 256);
        const unsigned short* wp = wmix + (32 * k + 8 * (lane >> 4)) * C_ + 32 * (w & 3) + 2 * (lane & 15);
        s16x8 w0, w1;
#pragma unroll
        for (int j = 0; j < 8; j++) {
          w0[j] = wp[j * C_];
          w1[j] = wp[j * C_ + 1];
        }
        a = __builtin_amdgcn_mfma_f32_16x16x32_bf16(f, w0, a, 0, 0, 0);
        b = __builtin_amdgcn_mfma_f32_16x16x32_bf16(f, w1, b, 0, 0, 0);
      }
      __builtin_amdgcn_raw_buffer_store_b128(u32x4{pk2bf(a[0], a[1]), pk2bf(a[2], a[3]), pk2bf(b[0], b[1]), pk2bf(b[2], b[3])},
                                             ors, mxo + t * 8192, 0, 0);
    }
  }
  MFLUSH;
}

extern "C" void kernel_launch(void* const* d_in, const int* in_sizes, int n_in, void* d_out, int out_size,
                              void* d_ws, size_t ws_size, hipStream_t stream) {
  const int N = in_sizes[0] / 512;
  const int E = in_sizes[1];
  int nbk = NB;
  while ((N + nbk - 1) / nbk > NMAX || (N % nbk == 0 && N / nbk >= NMAX)) nbk += NB;
  const unsigned long long M2l = (((unsigned long long)nbk) << 32) / (unsigned long long)N;
  const unsigned M2 = M2l > 0xffffffffull ? 0xffffffffu : (unsigned)M2l;
  const unsigned long long cb = (16ull << 32) / (unsigned long long)N;
  const bool fast = nbk == NB && E <= NB * CH && ws_size >= WS_END;
  const unsigned e4 = E * 4u;
  tfn_kernel<<<nbk, MT, 0, stream>>>(
      (const int*)d_in[2], e4, W3, (const int*)d_in[1], e4, W3, (const u32x2*)d_in[3], 2 * e4, W3, (const u32x2*)d_in[4], 2 * e4, W3,
      (char*)d_ws, WS_END, W3, (const unsigned short*)d_in[0], N * 1024u, W3, (u32x4*)d_out, fast ? 448u : 0u, N, M2,
      cb > 0xffffffull ? 0xffffffu : (unsigned)cb, nbk, (unsigned)((1ull << 52) / M2), (const unsigned short*)d_in[5], 4 * C_ * 2,
      W3, (const unsigned short*)d_in[6], 128, W3, (const unsigned short*)d_in[7], C_ * C_ * 2, W3);
}
```

```cpp
#include <hip/hip_runtime.h>
#include <hip/hip_bf16.h>
#include <stdint.h>
#include <stddef.h>

typedef __attribute__((ext_vector_type(2))) float f32x2;
typedef __attribute__((ext_vector_type(4))) float f32x4;
typedef __attribute__((ext_vector_type(8))) short s16x8;
typedef __attribute__((ext_vector_type(2))) unsigned int u32x2;
typedef __attribute__((ext_vector_type(4))) unsigned int u32x4;
typedef __attribute__((ext_vector_type(16))) unsigned int u32x16;
typedef __attribute__((ext_vector_type(2))) __bf16 bf16x2;

#define TDECL
#define TSTAMP(i)
#define TFLUSH
#define MSTAMP(i) TSTAMP(i)
#define MFLUSH do { asm volatile("s_waitcnt vmcnt(0)" ::: "memory"); TSTAMP(15); TFLUSH; } while (0)
#undef MFLUSH
#define MFLUSH

#define STREAM_ASM \
  "s_load_dwordx16 s[8:23], %[ka], 0\n" \
  "s_load_dwordx16 s[24:39], %[ka], 64\n" \
  "s_load_dwordx4 s[52:55], %[ka], 160\n" \
  "s_memrealtime s[76:77]\n" \
  "s_getpc_b64 s[40:41]\n" \
  "Lpc%=:\n" \
  "v_lshlrev_b32 v50, 7, %[tid]\n" \
  "s_mov_b32 s42, Lout%=-Lpc%=\n" \
  "s_mov_b32 s43, 0x20000\n" \
  "buffer_load_dword v208, v50, s[40:43], 0 offen\n" \
  "v_lshlrev_b32 v52, 3, %[tid]\n" \
  "v_lshlrev_b32 v53, 2, %[tid]\n" \
  "v_mov_b32 v48, 0\n" \
  "v_readfirstlane_b32 s44, %[tid]\n" \
  "v_add_u32 v49, 134464, v53\n" \
  "ds_write_b32 v49, v48\n" \
  "s_mul_i32 s63, %[blk], 5120\n" \
  "s_add_u32 s61, s63, 3584\n" \
  "s_cmp_lt_u32 s44, 384\n" \
  "s_cselect_b32 s61, s61, 0x40000000\n" \
  "s_waitcnt lgkmcnt(0)\n" \
  "s_cmp_lt_u32 s44, s34\n" \
  "s_cbranch_scc0 Lh0_%=\n" \
  "buffer_load_dwordx2 v[100:101], v52, s[8:11], s63 offen nt\n" \
  "buffer_load_dword v102, v53, s[8:11], s61 offen nt\n" \
  "s_barrier\n" \
  "buffer_load_dwordx2 v[104:105], v52, s[12:15], s63 offen nt\n" \
  "buffer_load_dword v106, v53, s[12:15], s61 offen nt\n" \
  "v_lshlrev_b32 v49, 1, v52\n" \
  "s_lshl_b32 s93, s63, 1\n" \
  "s_lshl_b32 s62, s61, 1\n" \
  "buffer_load_dwordx4 v[108:111], v49, s[16:19], s93 offen nt\n" \
  "buffer_load_dwordx4 v[114:117], v49, s[20:23], s93 offen nt\n" \
  "buffer_load_dwordx2 v[112:113], v52, s[16:19], s62 offen nt\n" \
  "buffer_load_dwordx2 v[118:119], v52, s[20:23], s62 offen nt\n" \
  "s_sub_i32 s63, s10, s63\n" \
  "s_sub_i32 s61, s10, s61\n" \
  "v_cmp_gt_i32 s[80:81], s63, v52\n" \
  "s_sub_i32 s63, s63, 4\n" \
  "v_cmp_gt_i32 s[84:85], s61, v53\n" \
  "v_cmp_gt_i32 s[82:83], s63, v52\n" \
  "s_branch Lh1_%=\n" \
  "Lh0_%=:\n" \
  "s_mov_b32 s93, 0\n" \
  "buffer_load_dwordx2 v[88:89], off, s[24:27], s93\n" \
  "s_barrier\n" \
  "s_load_dwordx8 s[8:15], %[ka], 128\n" \
  "s_mov_b64 s[80:81], 0\n" \
  "s_mov_b64 s[82:83], 0\n" \
  "s_mov_b64 s[84:85], 0\n" \
  "s_cmp_eq_u32 s34, 0\n" \
  "s_cbranch_scc1 Lnf%=\n" \
  "Lh1_%=:\n" \
  "ds_write_b32 v53, v48 offset:34560\n" \
  "s_lshr_b32 s44, s44, 6\n" \
  "v_and_b32 v86, 63, %[tid]\n" \
  "s_lshl_b32 s72, s44, 3\n" \
  "s_lshl_b32 s63, 1, s72\n" \
  "v_mov_b32 v93, s63\n" \
  "s_and_b32 s45, s72, 24\n" \
  "s_lshl_b64 s[46:47], -1, s72\n" \
  "s_and_b32 s46, s46, 0x01010101\n" \
  "s_and_b32 s47, s47, 0x01010101\n" \
  "s_and_b32 s73, s44, 4\n" \
  "s_add_u32 s74, s73, 134464\n" \
  "s_lshl_b32 s75, s44, 9\n" \
  "s_add_u32 s75, s75, 136512\n" \
  "v_lshlrev_b32 v13, 4, v86\n" \
  "v_lshlrev_b32 v54, 2, v86\n" \
  "v_lshl_add_u32 v52, v86, 3, s75\n" \
  "v_lshlrev_b32 v50, 5, v86\n" \
  "v_add_u32 v50, 134464, v50\n" \
  "s_and_b32 s71, %[blk], 7\n" \
  "s_lshr_b32 s66, %[blk], 3\n" \
  "s_lshl_b32 s68, s71, 5\n" \
  "s_add_u32 s68, s68, s66\n" \
  "s_and_b32 s61, s68, 2\n" \
  "s_lshl_b32 s61, s61, 9\n" \
  "v_and_b32 v55, 0xf8, v54\n" \
  "s_and_b32 s63, s68, 0xfc\n" \
  "s_lshl2_add_u32 s61, s63, s61\n" \
  "v_add_u32 v55, s44, v55\n" \
  "s_and_b32 s63, s68, 1\n" \
  "s_lshl3_add_u32 s61, s63, s61\n" \
  "v_mul_u32_u24 v17, 2176, v55\n" \
  "s_add_u32 s61, s61, 49408\n" \
  "v_mov_b32 v94, 36096\n" \
  "s_mul_i32 s63, s71, s35\n" \
  "s_lshr_b32 s63, s63, 3\n" \
  "s_lshl3_add_u32 s66, s66, s44\n" \
  "s_mul_i32 s66, s66, 1024\n" \
  "s_lshl_b32 s63, s63, 10\n" \
  "s_add_u32 s66, s66, s63\n" \
  "v_lshlrev_b32 v20, 3, v13\n" \
  "v_cmp_gt_u32 vcc, 8, v86\n" \
  "v_bfrev_b32 v51, 1\n" \
  "s_mov_b32 s79, m0\n" \
  "s_mov_b32 m0, 36160\n" \
  "v_cndmask_b32 v20, v51, v20, vcc\n" \
  "s_mov_b32 s63, 0x01010101\n" \
  "s_waitcnt vmcnt(6)\n" \
  "v_mul_hi_u32 v96, v100, s36\n" \
  "v_mul_hi_u32 v97, v101, s36\n" \
  "v_mul_hi_u32 v98, v102, s36\n" \
  "s_mov_b64 exec, s[80:81]\n" \
  "v_lshl_add_u32 v120, v96, 3, s74\n" \
  "ds_add_rtn_u32 v124, v120, v93\n" \
  "s_mov_b64 exec, s[82:83]\n" \
  "v_lshl_add_u32 v121, v97, 3, s74\n" \
  "ds_add_rtn_u32 v125, v121, v93\n" \
  "s_mov_b64 exec, s[84:85]\n" \
  "v_lshl_add_u32 v122, v98, 3, s74\n" \
  "ds_add_rtn_u32 v126, v122, v93\n" \
  "s_mov_b64 exec, -1\n" \
  "v_lshl_add_u32 v96, v96, 1, s75\n" \
  "v_lshl_add_u32 v97, v97, 1, s75\n" \
  "v_lshl_add_u32 v98, v98, 1, s75\n" \
  "s_waitcnt lgkmcnt(0)\n" \
  "s_barrier\n" \
  "ds_read_b128 v[128:131], v50\n" \
  "ds_read_b128 v[132:135], v50 offset:16\n" \
  "v_bfe_u32 v124, v124, s45, 8\n" \
  "v_bfe_u32 v125, v125, s45, 8\n" \
  "v_bfe_u32 v126, v126, s45, 8\n" \
  "s_waitcnt lgkmcnt(0)\n" \
  "v_dot4_u32_u8 v136, v128, s63, 0\n" \
  "v_dot4_u32_u8 v136, v129, s63, v136\n" \
  "v_dot4_u32_u8 v137, v130, s63, v136\n" \
  "v_dot4_u32_u8 v137, v131, s63, v137\n" \
  "v_dot4_u32_u8 v138, v132, s63, v137\n" \
  "v_dot4_u32_u8 v138, v133, s63, v138\n" \
  "v_dot4_u32_u8 v139, v134, s63, v138\n" \
  "v_dot4_u32_u8 v139, v135, s63, v139\n" \
  "v_dot4_u32_u8 v140, v128, s46, 0\n" \
  "v_dot4_u32_u8 v141, v130, s46, 0\n" \
  "v_dot4_u32_u8 v142, v132, s46, 0\n" \
  "v_add_u32_dpp v87, v139, v139 row_shr:1 row_mask:0xf bank_mask:0xf bound_ctrl:1\n" \
  "v_dot4_u32_u8 v143, v134, s46, 0\n" \
  "v_dot4_u32_u8 v140, v129, s47, v140\n" \
  "v_add_u32_dpp v87, v87, v87 row_shr:2 row_mask:0xf bank_mask:0xf bound_ctrl:1\n" \
  "v_dot4_u32_u8 v141, v131, s47, v141\n" \
  "v_dot4_u32_u8 v142, v133, s47, v142\n" \
  "v_add_u32_dpp v87, v87, v87 row_shr:4 row_mask:0xf bank_mask:0xf bound_ctrl:1\n" \
  "v_dot4_u32_u8 v143, v135, s47, v143\n" \
  "s_mov_b32 s63, 0x10001\n" \
  "v_add_u32_dpp v87, v87, v87 row_shr:8 row_mask:0xf bank_mask:0xf bound_ctrl:1\n" \
  "v_sub_u32 v140, v136, v140\n" \
  "v_sub_u32 v141, v137, v141\n" \
  "v_add_u32_dpp v87, v87, v87 row_bcast:15 row_mask:0xa bank_mask:0xf\n" \
  "v_sub_u32 v142, v138, v142\n" \
  "v_sub_u32 v143, v139, v143\n" \
  "v_add_u32_dpp v87, v87, v87 row_bcast:31 row_mask:0xc bank_mask:0xf\n" \
  "v_lshl_or_b32 v140, v141, 16, v140\n" \
  "v_sub_u32 v87, v87, v139\n" \
  "v_lshl_or_b32 v141, v143, 16, v142\n" \
  "s_cmp_eq_u32 s44, 7\n" \
  "v_mad_u32_u24 v140, v87, s63, v140\n" \
  "v_mad_u32_u24 v141, v87, s63, v141\n" \
  "s_cbranch_scc1 Lh2_%=\n" \
  "ds_write_b64 v52, v[140:141]\n" \
  "ds_read_u16 v96, v96\n" \
  "ds_read_u16 v97, v97\n" \
  "ds_read_u16 v98, v98\n" \
  "s_mov_b32 s63, 129344\n" \
  "s_mov_b32 s93, 108864\n" \
  "s_lshl_b32 s74, s44, 10\n" \
  "v_add_u32 v50, s74, v13\n" \
  "v_add_u32 v51, s93, v50\n" \
  "s_waitcnt vmcnt(4)\n" \
  "v_lshl_or_b32 v104, v100, 24, v104\n" \
  "v_lshl_or_b32 v105, v101, 24, v105\n" \
  "v_lshl_or_b32 v106, v102, 24, v106\n" \
  "s_waitcnt lgkmcnt(0)\n" \
  "s_mov_b64 exec, s[80:81]\n" \
  "v_add_u32 v124, v124, v96\n" \
  "v_lshl_add_u32 v120, v124, 2, s63\n" \
  "v_lshl_add_u32 v124, v124, 4, s93\n" \
  "ds_write_b32 v120, v104\n" \
  "s_mov_b64 exec, s[82:83]\n" \
  "v_add_u32 v125, v125, v97\n" \
  "v_lshl_add_u32 v121, v125, 2, s63\n" \
  "v_lshl_add_u32 v125, v125, 4, s93\n" \
  "ds_write_b32 v121, v105\n" \
  "s_mov_b64 exec, s[84:85]\n" \
  "v_add_u32 v126, v126, v98\n" \
  "v_lshl_add_u32 v122, v126, 2, s63\n" \
  "v_lshl_add_u32 v126, v126, 4, s93\n" \
  "ds_write_b32 v122, v106\n" \
  "s_waitcnt vmcnt(0)\n" \
  "s_mov_b64 exec, s[84:85]\n" \
  "ds_write2_b64 v126, v[112:113], v[118:119] offset1:1\n" \
  "s_mov_b64 exec, s[82:83]\n" \
  "ds_write2_b64 v125, v[110:111], v[116:117] offset1:1\n" \
  "s_mov_b64 exec, s[80:81]\n" \
  "ds_write2_b64 v124, v[108:109], v[114:115] offset1:1\n" \
  "s_mov_b64 exec, -1\n" \
  "s_mul_i32 s72, s45, 528\n" \
  "s_cmp_lt_u32 s44, 4\n" \
  "s_cselect_b32 s62, 45776, 0\n" \
  "s_cselect_b32 s62, s62, 140608\n" \
  "s_add_u32 m0, s72, s62\n" \
  "s_lshl_b32 s72, s44, 12\n" \
  "buffer_load_dwordx4 v13, s[52:55], s72 offen lds\n" \
  "buffer_load_dwordx4 v13, s[52:55], s72 offen offset:1024 lds\n" \
  "s_add_u32 m0, m0, 64\n" \
  "s_nop 0\n" \
  "buffer_load_dwordx4 v13, s[52:55], s72 offen offset:2048 lds\n" \
  "buffer_load_dwordx4 v13, s[52:55], s72 offen offset:3072 lds\n" \
  "s_mov_b32 m0, 36160\n" \
  "s_mul_i32 s72, %[blk], 20480\n" \
  "s_lshr_b32 s62, s72, 2\n" \
  "s_add_u32 s72, s72, 1917184\n" \
  "s_add_u32 s62, s62, 607488\n" \
  "s_add_u32 s74, s72, 7168\n" \
  "s_add_u32 s75, s72, 14336\n" \
  "s_sub_u32 s63, s62, 7168\n" \
  "s_cmp_eq_u32 s44, 6\n" \
  "s_cselect_b32 s75, s63, s75\n" \
  "s_cmp_gt_u32 s44, 3\n" \
  "s_waitcnt lgkmcnt(0)\n" \
  "s_barrier\n" \
  "ds_read_b128 v[192:195], v51 offset:0\n" \
  "ds_read_b128 v[196:199], v51 offset:7168\n" \
  "ds_read_b128 v[200:203], v51 offset:14336\n" \
  "ds_read_b128 v[204:207], v51 offset:21504\n" \
  "s_waitcnt lgkmcnt(3)\n" \
  "buffer_store_dwordx4 v[192:195], v50, s[24:27], s72 offen sc1\n" \
  "s_waitcnt lgkmcnt(2)\n" \
  "buffer_store_dwordx4 v[196:199], v50, s[24:27], s74 offen sc1\n" \
  "s_waitcnt lgkmcnt(1)\n" \
  "buffer_store_dwordx4 v[200:203], v50, s[24:27], s75 offen sc1\n" \
  "s_cbranch_scc1 Lc1_%=\n" \
  "s_waitcnt lgkmcnt(0)\n" \
  "buffer_store_dwordx4 v[204:207], v50, s[24:27], s62 offen sc1\n" \
  "Lc1_%=:\n" \
  "s_mov_b32 s63, 0x80008\n" \
  "s_mov_b32 s93, 0x80008000\n" \
  "v_mul_lo_u32 v52, %[tid], s63\n" \
  "v_lshlrev_b32 v53, 4, %[tid]\n" \
  "v_add_u32 v49, 0x10000, v53\n" \
  "v_cmp_gt_u32 s[90:91], 32, %[tid]\n" \
  "v_cmp_gt_u32 vcc, 368, %[tid]\n" \
  "v_add_u32 v104, 0xf810f80, v52\n" \
  "v_add_u32 v105, 0xf830f82, v52\n" \
  "v_add_u32 v106, 0xf850f84, v52\n" \
  "v_add_u32 v107, 0xf870f86, v52\n" \
  "v_or_b32 v108, s93, v104\n" \
  "v_or_b32 v109, s93, v105\n" \
  "v_or_b32 v110, s93, v106\n" \
  "v_or_b32 v111, s93, v107\n" \
  "ds_write_b128 v53, v[104:107] offset:8192\n" \
  "ds_write_b128 v49, v[108:111] offset:8192\n" \
  "v_add_u32 v112, 0x1f811f80, v52\n" \
  "v_add_u32 v113, 0x1f831f82, v52\n" \
  "v_add_u32 v114, 0x1f851f84, v52\n" \
  "v_add_u32 v115, 0x1f871f86, v52\n" \
  "v_or_b32 v116, s93, v112\n" \
  "v_or_b32 v117, s93, v113\n" \
  "v_or_b32 v118, s93, v114\n" \
  "v_or_b32 v119, s93, v115\n" \
  "ds_write_b128 v53, v[112:115] offset:16384\n" \
  "ds_write_b128 v49, v[116:119] offset:16384\n" \
  "s_mov_b64 exec, vcc\n" \
  "v_add_u32 v120, 0x2f812f80, v52\n" \
  "v_add_u32 v121, 0x2f832f82, v52\n" \
  "v_add_u32 v122, 0x2f852f84, v52\n" \
  "v_add_u32 v123, 0x2f872f86, v52\n" \
  "v_or_b32 v124, s93, v120\n" \
  "v_or_b32 v125, s93, v121\n" \
  "v_or_b32 v126, s93, v122\n" \
  "v_or_b32 v127, s93, v123\n" \
  "ds_write_b128 v53, v[120:123] offset:24576\n" \
  "ds_write_b128 v49, v[124:127] offset:24576\n" \
  "s_mov_b64 exec, -1\n" \
  "v_add_u32 v96, 0xff80ff80, v52\n" \
  "v_add_u32 v97, 0xff82ff82, v52\n" \
  "v_add_u32 v98, 0xff84ff84, v52\n" \
  "v_add_u32 v99, 0xff86ff86, v52\n" \
  "v_lshrrev_b32 v52, 1, v52\n" \
  "s_mov_b64 exec, s[90:91]\n" \
  "v_add_u32 v96, 0x0, v52\n" \
  "v_add_u32 v97, 0x20001, v52\n" \
  "v_add_u32 v98, 0x20002, v52\n" \
  "v_add_u32 v99, 0x40003, v52\n" \
  "s_mov_b64 exec, -1\n" \
  "v_or_b32 v100, s93, v96\n" \
  "v_or_b32 v101, s93, v97\n" \
  "v_or_b32 v102, s93, v98\n" \
  "v_or_b32 v103, s93, v99\n" \
  "ds_write_b128 v53, v[96:99] offset:0\n" \
  "ds_write_b128 v49, v[100:103] offset:0\n" \
  "buffer_load_dwordx2 v[44:45], v17, s[24:27], s61 offen sc1\n" \
  "s_mov_b64 s[88:89], 0\n" \
  "s_mov_b32 s92, 0\n" \
  "v_mov_b32 v92, 1\n" \
  "v_and_b32 v41, 4, v54\n" \
  "s_movk_i32 s63, 5120\n" \
  "v_mad_u32_u24 v40, v55, s63, v41\n" \
  "s_lshl_b32 s63, s44, 10\n" \
  "s_add_u32 s63, s63, 37584\n" \
  "v_add_u32 v41, s63, v41\n" \
  "s_mul_i32 s90, s68, s39\n" \
  "s_mul_hi_u32 s91, s68, s39\n" \
  "s_lshr_b64 s[90:91], s[90:91], 20\n" \
  "s_mul_hi_u32 s63, s90, s36\n" \
  "s_cmp_lt_u32 s63, s68\n" \
  "s_addc_u32 s90, s90, 0\n" \
  "s_mul_hi_u32 s63, s90, s36\n" \
  "s_cmp_lt_u32 s63, s68\n" \
  "s_addc_u32 s90, s90, 0\n" \
  "s_min_u32 s67, s90, s35\n" \
  "s_add_u32 s68, s68, 1\n" \
  "s_mul_i32 s90, s68, s39\n" \
  "s_mul_hi_u32 s91, s68, s39\n" \
  "s_lshr_b64 s[90:91], s[90:91], 20\n" \
  "s_mul_hi_u32 s63, s90, s36\n" \
  "s_cmp_lt_u32 s63, s68\n" \
  "s_addc_u32 s90, s90, 0\n" \
  "s_mul_hi_u32 s63, s90, s36\n" \
  "s_cmp_lt_u32 s63, s68\n" \
  "s_addc_u32 s90, s90, 0\n" \
  "s_min_u32 s68, s90, s35\n" \
  "s_sub_u32 s68, s68, s67\n" \
  "s_lshl_b32 s71, s71, 1\n" \
  "v_lshlrev_b32 v50, 5, v86\n" \
  "v_and_b32 v50, 0x60, v50\n" \
  "v_bfrev_b32 v51, 1\n" \
  "s_lshl_b32 s63, %[blk], 6\n" \
  "s_add_u32 s63, s63, 256\n" \
  "s_cmp_lg_u32 s44, 7\n" \
  "s_waitcnt vmcnt(1) lgkmcnt(0)\n" \
  "s_barrier\n" \
  "s_cbranch_scc1 Ls1_%=\n" \
  "s_mov_b64 exec, 1\n" \
  "buffer_store_dwordx2 v[88:89], off, s[24:27], s63 sc1\n" \
  "s_mov_b64 exec, -1\n" \
  "Ls1_%=:\n" \
  "ds_read_b96 v[56:58], v94 offset:1472\n" \
  "ds_read_b128 v[24:27], v50 offset:36416\n" \
  "ds_read_b128 v[28:31], v50 offset:36432\n" \
  "ds_read_b64 v[32:33], v13 offset:36544\n" \
  "ds_read_b64 v[36:37], v13 offset:36552\n" \
  "s_waitcnt lgkmcnt(4)\n" \
  "v_readfirstlane_b32 s50, v56\n" \
  "v_readfirstlane_b32 s51, v57\n" \
  "v_readfirstlane_b32 s69, v58\n" \
  "s_lshl_b32 s64, s50, 22\n" \
  "s_lshr_b64 s[90:91], s[50:51], 10\n" \
  "s_mov_b32 s65, s90\n" \
  "s_cmp_lg_u32 s44, 7\n" \
  "s_waitcnt vmcnt(1)\n" \
  "s_cbranch_scc0 Lchk%=\n" \
  "s_waitcnt vmcnt(0)\n" \
  "s_branch Lchk%=\n" \
  "Lpoll%=:\n" \
  "buffer_load_dwordx2 v[44:45], v17, s[24:27], s61 offen sc1\n" \
  "s_waitcnt vmcnt(0)\n" \
  "Lchk%=:\n" \
  "v_and_b32 v50, 0xffc00000, v44\n" \
  "v_cmp_eq_u32 s[90:91], s65, v45\n" \
  "v_cmp_eq_u32 vcc, s64, v50\n" \
  "s_and_b64 vcc, vcc, s[90:91]\n" \
  "s_andn2_b64 vcc, vcc, s[88:89]\n" \
  "s_or_b64 s[88:89], s[88:89], vcc\n" \
  "v_cndmask_b32 v48, v48, v44, vcc\n" \
  "s_cmp_eq_u64 s[88:89], -1\n" \
  "v_cndmask_b32 v17, v17, v51, vcc\n" \
  "s_cbranch_scc1 Lseen%=\n" \
  "s_add_u32 s92, s92, 1\n" \
  "s_cmp_lt_u32 s92, 4096\n" \
  "s_cbranch_scc1 Lpoll%=\n" \
  "v_mov_b32 v48, 0\n" \
  "Lseen%=:\n" \
  "buffer_load_dword v20, s[28:31], s66 offen lds\n" \
  "s_add_u32 s66, s66, 0x40000\n" \
  "buffer_load_dword v20, s[28:31], s66 offen lds\n" \
  "s_add_u32 s66, s66, 0x40000\n" \
  "v_bfe_u32 v49, v48, 11, 11\n" \
  "v_and_b32 v50, 0x7ff, v48\n" \
  "v_bfe_u32 v52, v13, 4, 1\n" \
  "v_add_u32_dpp v87, v49, v49 row_shr:1 row_mask:0xf bank_mask:0xf bound_ctrl:1\n" \
  "v_lshl_add_u32 v50, v50, 2, v40\n" \
  "v_lshlrev_b32 v53, v52, v49\n" \
  "v_add_u32_dpp v87, v87, v87 row_shr:2 row_mask:0xf bank_mask:0xf bound_ctrl:1\n" \
  "v_sub_u32 v49, v49, v52\n" \
  "v_and_b32 v85, -8, v41\n" \
  "v_add_u32_dpp v87, v87, v87 row_shr:4 row_mask:0xf bank_mask:0xf bound_ctrl:1\n" \
  "v_add_u32 v85, v85, v54\n" \
  "s_nop 0\n" \
  "v_add_u32_dpp v87, v87, v87 row_shr:8 row_mask:0xf bank_mask:0xf bound_ctrl:1\n" \
  "s_nop 1\n" \
  "v_add_u32_dpp v87, v87, v87 row_bcast:15 row_mask:0xa bank_mask:0xf\n" \
  "s_nop 1\n" \
  "v_add_u32_dpp v87, v87, v87 row_bcast:31 row_mask:0xc bank_mask:0xf\n" \
  "v_sub_u32 v53, v87, v53\n" \
  "v_readlane_b32 s62, v87, 63\n" \
  "v_lshl_add_u32 v53, v53, 1, v41\n" \
  "v_lshl_add_u32 v49, v49, 2, v53\n" \
  "s_lshr_b32 s62, s62, 1\n" \
  "s_cmp_lt_u32 s62, 256\n" \
  "s_cbranch_scc1 Ll0_%=\n" \
  "s_mov_b32 s62, 0\n" \
  "ds_or_b32 v94, v92\n" \
  "s_branch Ll2_%=\n" \
  "Ll0_%=:\n" \
  "v_cmp_lt_i32 vcc, v53, v49\n" \
  "s_mov_b64 exec, vcc\n" \
  "Ll1_%=:\n" \
  "ds_write_b32 v53, v50\n" \
  "v_add_u32 v53, 8, v53\n" \
  "v_cmp_lt_i32 vcc, v53, v49\n" \
  "v_add_u32 v50, 8, v50\n" \
  "s_and_b64 exec, vcc, vcc\n" \
  "s_cbranch_scc1 Ll1_%=\n" \
  "s_mov_b64 exec, -1\n" \
  "Ll2_%=:\n" \
  "ds_read2st64_b32 v[56:57], v85 offset1:1\n" \
  "ds_read2st64_b32 v[58:59], v85 offset0:2 offset1:3\n" \
  "v_mov_b32 v50, s62\n" \
  "s_mov_b64 exec, 1\n" \
  "ds_add_u32 v94, v50 offset:4\n" \
  "s_mov_b64 exec, -1\n" \
  "v_cmp_gt_i32 s[80:81], s62, v86\n" \
  "s_sub_i32 s63, s62, 64\n" \
  "v_cmp_gt_i32 s[82:83], s63, v86\n" \
  "s_sub_i32 s63, s62, 128\n" \
  "v_cmp_gt_i32 s[84:85], s63, v86\n" \
  "s_sub_i32 s63, s62, 192\n" \
  "v_cmp_gt_i32 s[86:87], s63, v86\n" \
  "v_bfrev_b32 v50, 4\n" \
  "s_waitcnt lgkmcnt(1)\n" \
  "v_cndmask_b32 v56, v50, v56, s[80:81]\n" \
  "v_cndmask_b32 v57, v50, v57, s[82:83]\n" \
  "v_cndmask_b32 v58, v50, v58, s[84:85]\n" \
  "v_cndmask_b32 v59, v50, v59, s[86:87]\n" \
  "s_sleep 4\n" \
  "v_mul_u32_u24 v18, 64, v55\n" \
  "s_mov_b32 s93, 256\n" \
  "s_mov_b64 s[88:89], 0\n" \
  "s_mov_b32 s63, 0xbfb8aa3b\n" \
  "v_lshlrev_b32 v100, 17, %[tid]\n" \
  "v_add_u32 v103, 0x3f810000, v100\n" \
  "v_add_u32 v102, 0x3f800000, v100\n" \
  "v_add_u32 v101, 0x3b810000, v100\n" \
  "v_add_u32 v100, 0x3b800000, v100\n" \
  "v_mul_f32 v104, s63, v100\n" \
  "v_mul_f32 v105, s63, v101\n" \
  "v_mul_f32 v106, s63, v102\n" \
  "v_mul_f32 v107, s63, v103\n" \
  "v_exp_f32 v104, v104\n" \
  "v_exp_f32 v105, v105\n" \
  "v_exp_f32 v106, v106\n" \
  "v_exp_f32 v107, v107\n" \
  "v_lshlrev_b32 v96, 2, %[tid]\n" \
  "v_add_f32 v108, 1.0, v104\n" \
  "v_add_f32 v109, 1.0, v105\n" \
  "v_add_f32 v110, 1.0, v106\n" \
  "v_add_f32 v111, 1.0, v107\n" \
  "v_rcp_f32 v108, v108\n" \
  "v_rcp_f32 v109, v109\n" \
  "v_rcp_f32 v110, v110\n" \
  "v_rcp_f32 v111, v111\n" \
  "v_add_u32 v97, 0x10000, v96\n" \
  "v_pk_mul_f32 v[100:101], v[100:101], v[108:109]\n" \
  "v_pk_mul_f32 v[102:103], v[102:103], v[110:111]\n" \
  "v_pk_mul_f32 v[104:105], v[104:105], v[100:101] neg_lo:[0,1] neg_hi:[0,1]\n" \
  "v_pk_mul_f32 v[106:107], v[106:107], v[102:103] neg_lo:[0,1] neg_hi:[0,1]\n" \
  "v_cvt_pk_bf16_f32 v100, v100, v101\n" \
  "v_cvt_pk_bf16_f32 v102, v102, v103\n" \
  "v_cvt_pk_bf16_f32 v104, v104, v105\n" \
  "v_cvt_pk_bf16_f32 v106, v106, v107\n" \
  "ds_write2st64_b32 v96, v100, v102 offset0:119 offset1:127\n" \
  "ds_write2st64_b32 v97, v104, v106 offset0:119 offset1:127\n" \
  "buffer_load_dwordx2 v[44:45], v18, s[24:27], s93 offen sc1\n" \
  "v_lshrrev_b32 v50, 8, v13\n" \
  "v_mul_u32_u24 v50, 2112, v50\n" \
  "v_and_or_b32 v50, v54, 60, v50\n" \
  "s_lshl3_add_u32 s63, s45, 45776\n" \
  "v_add_u32 v50, s63, v50\n" \
  "v_add_u32 v52, 94832, v50\n" \
  "ds_read2st64_b32 v[128:129], v50 offset0:0 offset1:1\n" \
  "ds_read2st64_b32 v[130:131], v50 offset0:2 offset1:3\n" \
  "ds_read2st64_b32 v[132:133], v50 offset0:4 offset1:5\n" \
  "ds_read2st64_b32 v[134:135], v50 offset0:6 offset1:7\n" \
  "ds_read2st64_b32 v[136:137], v50 offset0:33 offset1:34\n" \
  "ds_read2st64_b32 v[138:139], v50 offset0:35 offset1:36\n" \
  "ds_read2st64_b32 v[140:141], v50 offset0:37 offset1:38\n" \
  "ds_read2st64_b32 v[142:143], v50 offset0:39 offset1:40\n" \
  "ds_read2st64_b32 v[144:145], v52 offset0:0 offset1:1\n" \
  "ds_read2st64_b32 v[146:147], v52 offset0:2 offset1:3\n" \
  "ds_read2st64_b32 v[148:149], v52 offset0:4 offset1:5\n" \
  "ds_read2st64_b32 v[150:151], v52 offset0:6 offset1:7\n" \
  "s_waitcnt lgkmcnt(8)\n" \
  "ds_read2st64_b32 v[152:153], v52 offset0:33 offset1:34\n" \
  "ds_read2st64_b32 v[154:155], v52 offset0:35 offset1:36\n" \
  "ds_read2st64_b32 v[156:157], v52 offset0:37 offset1:38\n" \
  "ds_read2st64_b32 v[158:159], v52 offset0:39 offset1:40\n" \
  "v_mov_b64 v[160:161], 0\n" \
  "v_mov_b64 v[162:163], 0\n" \
  "v_mov_b64 v[164:165], 0\n" \
  "v_mov_b64 v[166:167], 0\n" \
  "v_mov_b64 v[168:169], 0\n" \
  "v_mov_b64 v[170:171], 0\n" \
  "v_mov_b64 v[172:173], 0\n" \
  "v_mov_b64 v[174:175], 0\n" \
  "v_mov_b64 v[176:177], 0\n" \
  "v_mov_b64 v[178:179], 0\n" \
  "v_mov_b64 v[180:181], 0\n" \
  "v_mov_b64 v[182:183], 0\n" \
  "v_mov_b64 v[184:185], 0\n" \
  "v_mov_b64 v[186:187], 0\n" \
  "v_mov_b64 v[188:189], 0\n" \
  "v_mov_b64 v[190:191], 0\n" \
  "s_mov_b32 s63, 0x05040100\n" \
  "s_mov_b32 s72, 0x07060302\n" \
  "s_waitcnt lgkmcnt(0)\n" \
  "v_perm_b32 v216, v129, v128, s63\n" \
  "v_perm_b32 v220, v129, v128, s72\n" \
  "v_perm_b32 v217, v131, v130, s63\n" \
  "v_perm_b32 v221, v131, v130, s72\n" \
  "v_perm_b32 v218, v133, v132, s63\n" \
  "v_perm_b32 v222, v133, v132, s72\n" \
  "v_perm_b32 v219, v135, v134, s63\n" \
  "v_perm_b32 v223, v135, v134, s72\n" \
  "v_perm_b32 v224, v137, v136, s63\n" \
  "v_perm_b32 v228, v137, v136, s72\n" \
  "v_perm_b32 v225, v139, v138, s63\n" \
  "v_perm_b32 v229, v139, v138, s72\n" \
  "v_perm_b32 v226, v141, v140, s63\n" \
  "v_perm_b32 v230, v141, v140, s72\n" \
  "v_perm_b32 v227, v143, v142, s63\n" \
  "v_perm_b32 v231, v143, v142, s72\n" \
  "v_perm_b32 v232, v145, v144, s63\n" \
  "v_perm_b32 v236, v145, v144, s72\n" \
  "v_perm_b32 v233, v147, v146, s63\n" \
  "v_perm_b32 v237, v147, v146, s72\n" \
  "v_perm_b32 v234, v149, v148, s63\n" \
  "v_perm_b32 v238, v149, v148, s72\n" \
  "v_perm_b32 v235, v151, v150, s63\n" \
  "v_perm_b32 v239, v151, v150, s72\n" \
  "v_perm_b32 v240, v153, v152, s63\n" \
  "v_perm_b32 v244, v153, v152, s72\n" \
  "v_perm_b32 v241, v155, v154, s63\n" \
  "v_perm_b32 v245, v155, v154, s72\n" \
  "v_perm_b32 v242, v157, v156, s63\n" \
  "v_perm_b32 v246, v157, v156, s72\n" \
  "v_perm_b32 v243, v159, v158, s63\n" \
  "v_perm_b32 v247, v159, v158, s72\n" \
  "v_mov_b64 v[128:129], 0\n" \
  "v_mov_b64 v[130:131], 0\n" \
  "v_mov_b64 v[132:133], 0\n" \
  "v_mov_b64 v[134:135], 0\n" \
  "v_mov_b64 v[136:137], 0\n" \
  "v_mov_b64 v[138:139], 0\n" \
  "v_mov_b64 v[140:141], 0\n" \
  "v_mov_b64 v[142:143], 0\n" \
  "v_mov_b64 v[144:145], 0\n" \
  "v_mov_b64 v[146:147], 0\n" \
  "v_mov_b64 v[148:149], 0\n" \
  "v_mov_b64 v[150:151], 0\n" \
  "v_mov_b64 v[152:153], 0\n" \
  "v_mov_b64 v[154:155], 0\n" \
  "v_mov_b64 v[156:157], 0\n" \
  "v_mov_b64 v[158:159], 0\n" \
  "Ld1_%=:\n" \
  "buffer_load_dwordx2 v[46:47], v18, s[24:27], s93 offen sc1\n" \
  "s_waitcnt vmcnt(1)\n" \
  "v_cmp_eq_u32 s[90:91], s65, v45\n" \
  "v_cmp_eq_u32 vcc, s64, v44\n" \
  "s_mov_b32 s63, 606464\n" \
  "s_and_b64 vcc, vcc, s[90:91]\n" \
  "s_or_b64 s[88:89], s[88:89], vcc\n" \
  "s_cmp_eq_u64 s[88:89], -1\n" \
  "s_cbranch_scc1 Ld2_%=\n" \
  "v_cndmask_b32 v18, v18, v51, vcc\n" \
  "buffer_load_dwordx2 v[44:45], v18, s[24:27], s93 offen sc1\n" \
  "s_waitcnt vmcnt(1)\n" \
  "v_cmp_eq_u32 s[90:91], s65, v47\n" \
  "v_cmp_eq_u32 vcc, s64, v46\n" \
  "s_mov_b32 s63, 606464\n" \
  "s_and_b64 vcc, vcc, s[90:91]\n" \
  "s_or_b64 s[88:89], s[88:89], vcc\n" \
  "s_cmp_eq_u64 s[88:89], -1\n" \
  "s_cbranch_scc1 Ld2_%=\n" \
  "v_cndmask_b32 v18, v18, v51, vcc\n" \
  "s_add_u32 s92, s92, 1\n" \
  "s_cmp_lt_u32 s92, 4096\n" \
  "s_cbranch_scc1 Ld1_%=\n" \
  "s_mov_b64 s[80:81], 0\n" \
  "s_mov_b64 s[82:83], 0\n" \
  "s_mov_b64 s[84:85], 0\n" \
  "s_mov_b64 s[86:87], 0\n" \
  "Ld2_%=:\n" \
  "s_mov_b32 s93, 1917184\n" \
  "s_cmp_le_i32 s62, 192\n" \
  "s_cbranch_scc1 Lk595_%=\n" \
  "buffer_load_dword v63, v59, s[24:27], s63 offen sc1\n" \
  "v_lshlrev_b32 v59, 2, v59\n" \
  "buffer_load_dwordx4 v[204:207], v59, s[24:27], s93 offen sc1\n" \
  "Lk595_%=:\n" \
  "buffer_load_dword v60, v56, s[24:27], s63 offen sc1\n" \
  "buffer_load_dword v61, v57, s[24:27], s63 offen sc1\n" \
  "buffer_load_dword v62, v58, s[24:27], s63 offen sc1\n" \
  "v_lshlrev_b32 v56, 2, v56\n" \
  "v_lshlrev_b32 v57, 2, v57\n" \
  "v_lshlrev_b32 v58, 2, v58\n" \
  "s_lshl_b32 s75, s44, 8\n" \
  "s_add_u32 s75, s75, 106816\n" \
  "s_mov_b32 s52, s28\n" \
  "s_or_b32 s53, s29, 0x4000000\n" \
  "s_mov_b32 s54, s35\n" \
  "s_mov_b32 s55, 0x20000\n" \
  "s_lshl_b32 s63, s67, 10\n" \
  "s_add_u32 s56, s32, s63\n" \
  "s_addc_u32 s57, s33, 0\n" \
  "s_lshl_b32 s58, s68, 10\n" \
  "s_mov_b32 s59, 0x20000\n" \
  "s_lshl_b32 s67, s67, 24\n" \
  "s_mov_b32 s70, 0\n" \
  "s_mul_i32 s63, s44, 3328\n" \
  "s_add_u32 s63, s63, 37584\n" \
  "v_add_u32 v21, s63, v13\n" \
  "v_and_b32 v15, 0xf0, v54\n" \
  "v_add_u32 v15, s63, v15\n" \
  "v_and_b32 v23, 0xf0, v13\n" \
  "v_add3_u32 v16, v23, s63, 8\n" \
  "s_mul_i32 s63, s44, 832\n" \
  "s_add_u32 s63, s63, 100096\n" \
  "v_mov_b32 v14, s63\n" \
  "v_and_b32 v50, 0x300, v13\n" \
  "v_and_or_b32 v19, v54, 60, v50\n" \
  "v_add_u32 v19, 108864, v19\n" \
  "v_lshl_add_u32 v23, v50, 2, v23\n" \
  "s_lshl_b32 s63, s45, 5\n" \
  "v_add_u32 v23, s63, v23\n" \
  "v_bfe_u32 v50, v86, 2, 2\n" \
  "v_mul_u32_u24 v22, 1056, v50\n" \
  "v_and_b32 v50, 48, v13\n" \
  "v_lshl_add_u32 v22, v50, 2, v22\n" \
  "v_and_b32 v50, 48, v86\n" \
  "s_mov_b32 s63, 108864\n" \
  "v_add3_u32 v22, v22, v50, s63\n" \
  "v_mul_u32_u24 v50, 26, v86\n" \
  "v_lshrrev_b32 v50, 8, v50\n" \
  "v_mad_i32_i24 v50, v50, -10, v86\n" \
  "v_lshlrev_b32 v42, 19, v50\n" \
  "s_lshl_b32 s63, s73, 28\n" \
  "s_sub_u32 s67, s67, s63\n" \
  "s_cmp_lt_u32 s92, 4096\n" \
  "s_cbranch_scc1 Lq2_%=\n" \
  "v_mov_b32 v53, 3\n" \
  "ds_or_b32 v94, v53\n" \
  "Lq2_%=:\n" \
  "v_mov_b32 v50, 2\n" \
  "s_waitcnt vmcnt(0)\n" \
  "v_subrev_u32 v60, s67, v60\n" \
  "s_mov_b64 exec, s[80:81]\n" \
  "v_lshlrev_b32_sdwa v64, v50, v60 dst_sel:DWORD dst_unused:UNUSED_PAD src0_sel:DWORD src1_sel:BYTE_3\n" \
  "ds_add_rtn_u32 v80, v64, v93 offset:35584\n" \
  "v_subrev_u32 v61, s67, v61\n" \
  "s_mov_b64 exec, s[82:83]\n" \
  "v_lshlrev_b32_sdwa v65, v50, v61 dst_sel:DWORD dst_unused:UNUSED_PAD src0_sel:DWORD src1_sel:BYTE_3\n" \
  "ds_add_rtn_u32 v81, v65, v93 offset:35584\n" \
  "v_subrev_u32 v62, s67, v62\n" \
  "s_mov_b64 exec, s[84:85]\n" \
  "v_lshlrev_b32_sdwa v66, v50, v62 dst_sel:DWORD dst_unused:UNUSED_PAD src0_sel:DWORD src1_sel:BYTE_3\n" \
  "ds_add_rtn_u32 v82, v66, v93 offset:35584\n" \
  "s_cmp_le_i32 s62, 192\n" \
  "s_cbranch_scc1 Lk668_%=\n" \
  "v_subrev_u32 v63, s67, v63\n" \
  "s_mov_b64 exec, s[86:87]\n" \
  "v_lshlrev_b32_sdwa v67, v50, v63 dst_sel:DWORD dst_unused:UNUSED_PAD src0_sel:DWORD src1_sel:BYTE_3\n" \
  "ds_add_rtn_u32 v83, v67, v93 offset:35584\n" \
  "Lk668_%=:\n" \
  "s_mov_b64 exec, -1\n" \
  "buffer_load_dwordx4 v[192:195], v56, s[24:27], s93 offen sc1\n" \
  "buffer_load_dwordx4 v[196:199], v57, s[24:27], s93 offen sc1\n" \
  "buffer_load_dwordx4 v[200:203], v58, s[24:27], s93 offen sc1\n" \
  "s_mov_b32 s63, 0x01010101\n" \
  "s_waitcnt lgkmcnt(0)\n" \
  "s_barrier\n" \
  "ds_read2st64_b32 v[44:45], v54 offset0:139 offset1:140\n" \
  "ds_read_b32 v86, v94 offset:4\n" \
  "ds_bpermute_b32 v76, v64, v42\n" \
  "ds_bpermute_b32 v77, v65, v42\n" \
  "ds_bpermute_b32 v78, v66, v42\n" \
  "ds_bpermute_b32 v79, v67, v42\n" \
  "buffer_load_dword v20, s[28:31], s66 offen lds\n" \
  "s_add_u32 s66, s66, 0x40000\n" \
  "buffer_load_dword v20, s[28:31], s66 offen lds\n" \
  "s_add_u32 s66, s66, 0x40000\n" \
  "v_bfe_u32 v80, v80, s45, 8\n" \
  "v_bfe_u32 v81, v81, s45, 8\n" \
  "v_bfe_u32 v82, v82, s45, 8\n" \
  "v_bfe_u32 v83, v83, s45, 8\n" \
  "s_waitcnt lgkmcnt(4)\n" \
  "v_dot4_u32_u8 v85, v44, s63, 0\n" \
  "v_dot4_u32_u8 v85, v45, s63, v85\n" \
  "v_dot4_u32_u8 v46, v44, s46, 0\n" \
  "v_dot4_u32_u8 v46, v45, s47, v46\n" \
  "v_add_u32 v86, 31, v86\n" \
  "v_add_u32_dpp v87, v85, v85 row_shr:1 row_mask:0xf bank_mask:0xf bound_ctrl:1\n" \
  "v_lshrrev_b32 v86, 5, v86\n" \
  "v_cvt_f32_u32 v86, v86\n" \
  "v_add_u32_dpp v87, v87, v87 row_shr:2 row_mask:0xf bank_mask:0xf bound_ctrl:1\n" \
  "v_rcp_f32 v86, v86\n" \
  "v_mul_hi_u32_u24 v68, s37, v60\n" \
  "v_add_u32_dpp v87, v87, v87 row_shr:4 row_mask:0xf bank_mask:0xf bound_ctrl:1\n" \
  "v_subrev_u32 v68, s71, v68\n" \
  "v_and_b32 v68, 15, v68\n" \
  "v_add_u32_dpp v87, v87, v87 row_shr:8 row_mask:0xf bank_mask:0xf bound_ctrl:1\n" \
  "v_lshl_add_u32 v72, v68, 1, s75\n" \
  "v_lshl_add_u32 v68, v68, 3, s73\n" \
  "v_add_u32_dpp v87, v87, v87 row_bcast:15 row_mask:0xa bank_mask:0xf\n" \
  "v_mul_hi_u32_u24 v69, s37, v61\n" \
  "v_subrev_u32 v69, s71, v69\n" \
  "v_add_u32_dpp v87, v87, v87 row_bcast:31 row_mask:0xc bank_mask:0xf\n" \
  "v_sub_u32 v46, v87, v46\n" \
  "ds_bpermute_b32 v88, v64, v46\n" \
  "ds_bpermute_b32 v89, v65, v46\n" \
  "ds_bpermute_b32 v90, v66, v46\n" \
  "ds_bpermute_b32 v91, v67, v46\n" \
  "v_mul_f32 v86, 0x48800000, v86\n" \
  "v_cvt_u32_f32 v86, v86\n" \
  "v_add_u32 v86, 1, v86\n" \
  "v_and_b32 v69, 15, v69\n" \
  "v_lshl_add_u32 v73, v69, 1, s75\n" \
  "v_lshl_add_u32 v69, v69, 3, s73\n" \
  "v_mul_hi_u32_u24 v70, s37, v62\n" \
  "v_subrev_u32 v70, s71, v70\n" \
  "v_and_b32 v70, 15, v70\n" \
  "v_lshl_add_u32 v74, v70, 1, s75\n" \
  "v_lshl_add_u32 v70, v70, 3, s73\n" \
  "v_mul_hi_u32_u24 v71, s37, v63\n" \
  "v_subrev_u32 v71, s71, v71\n" \
  "v_and_b32 v71, 15, v71\n" \
  "v_lshl_add_u32 v75, v71, 1, s75\n" \
  "v_lshl_add_u32 v71, v71, 3, s73\n" \
  "s_mov_b32 s63, 0xffff\n" \
  "s_waitcnt lgkmcnt(4)\n" \
  "v_and_or_b32 v76, v60, s63, v76\n" \
  "v_and_or_b32 v77, v61, s63, v77\n" \
  "v_and_or_b32 v78, v62, s63, v78\n" \
  "v_and_or_b32 v79, v63, s63, v79\n" \
  "s_waitcnt lgkmcnt(3)\n" \
  "v_add_u32 v80, v80, v88\n" \
  "v_mul_u32_u24 v80, v80, v86\n" \
  "s_waitcnt lgkmcnt(2)\n" \
  "v_add_u32 v81, v81, v89\n" \
  "v_mul_u32_u24 v81, v81, v86\n" \
  "s_waitcnt lgkmcnt(1)\n" \
  "v_add_u32 v82, v82, v90\n" \
  "v_mul_u32_u24 v82, v82, v86\n" \
  "s_waitcnt lgkmcnt(0)\n" \
  "v_add_u32 v83, v83, v91\n" \
  "v_mul_u32_u24 v83, v83, v86\n" \
  "v_lshrrev_b32 v60, 20, v80\n" \
  "s_mov_b64 exec, s[80:81]\n" \
  "v_lshl_add_u32 v68, v60, 7, v68\n" \
  "ds_add_rtn_u32 v80, v68, v93 offset:34560\n" \
  "v_lshrrev_b32 v61, 20, v81\n" \
  "s_mov_b64 exec, s[82:83]\n" \
  "v_lshl_add_u32 v69, v61, 7, v69\n" \
  "ds_add_rtn_u32 v81, v69, v93 offset:34560\n" \
  "v_lshrrev_b32 v62, 20, v82\n" \
  "s_mov_b64 exec, s[84:85]\n" \
  "v_lshl_add_u32 v70, v62, 7, v70\n" \
  "ds_add_rtn_u32 v82, v70, v93 offset:34560\n" \
  "s_cmp_le_i32 s62, 192\n" \
  "s_cbranch_scc1 Lk769_%=\n" \
  "v_lshrrev_b32 v63, 20, v83\n" \
  "s_mov_b64 exec, s[86:87]\n" \
  "v_lshl_add_u32 v71, v63, 7, v71\n" \
  "ds_add_rtn_u32 v83, v71, v93 offset:34560\n" \
  "Lk769_%=:\n" \
  "s_mov_b64 exec, -1\n" \
  "buffer_load_dword v20, s[28:31], s66 offen lds\n" \
  "s_add_u32 s66, s66, 0x40000\n" \
  "v_lshl_add_u32 v72, v60, 5, v72\n" \
  "v_lshl_add_u32 v73, v61, 5, v73\n" \
  "v_lshl_add_u32 v74, v62, 5, v74\n" \
  "v_lshl_add_u32 v75, v63, 5, v75\n" \
  "v_sub_u32 v85, v87, v85\n" \
  "v_add_u32 v87, -1, v87\n" \
  "v_max_i32 v87, v87, v85\n" \
  "v_mul_u32_u24 v84, v85, v86\n" \
  "v_mul_u32_u24 v43, v87, v86\n" \
  "v_lshrrev_b32 v50, 1, v13\n" \
  "v_lshrrev_b32 v51, 3, v13\n" \
  "v_add_u32 v51, s75, v51\n" \
  "v_lshrrev_b32 v52, 8, v13\n" \
  "v_mul_u32_u24 v52, 0xd000d0, v52\n" \
  "v_add_u32 v52, 0x3400000, v52\n" \
  "s_mov_b32 s63, 0x01010101\n" \
  "s_waitcnt lgkmcnt(0)\n" \
  "s_barrier\n" \
  "ds_read_b64 v[88:89], v50 offset:34560\n" \
  "ds_read_b64 v[90:91], v50 offset:35072\n" \
  "v_bfe_u32 v80, v80, s45, 8\n" \
  "v_bfe_u32 v81, v81, s45, 8\n" \
  "v_bfe_u32 v82, v82, s45, 8\n" \
  "v_bfe_u32 v83, v83, s45, 8\n" \
  "s_lshl_b32 s72, s44, 4\n" \
  "s_or_b32 s72, s72, 15\n" \
  "s_lshl_b32 s73, s73, 2\n" \
  "s_waitcnt lgkmcnt(0)\n" \
  "v_dot4_u32_u8 v44, v88, s63, 0\n" \
  "v_dot4_u32_u8 v45, v90, s63, 0\n" \
  "v_dot4_u32_u8 v44, v89, s63, v44\n" \
  "v_dot4_u32_u8 v45, v91, s63, v45\n" \
  "v_dot4_u32_u8 v46, v88, s46, 0\n" \
  "v_dot4_u32_u8 v47, v90, s46, 0\n" \
  "v_dot4_u32_u8 v46, v89, s47, v46\n" \
  "v_lshl_or_b32 v44, v45, 16, v44\n" \
  "v_dot4_u32_u8 v47, v91, s47, v47\n" \
  "s_nop 0\n" \
  "v_add_u32_dpp v87, v44, v44 row_shr:1 row_mask:0xf bank_mask:0xf bound_ctrl:1\n" \
  "s_nop 0\n" \
  "v_lshl_or_b32 v46, v47, 16, v46\n" \
  "v_add_u32_dpp v87, v87, v87 row_shr:2 row_mask:0xf bank_mask:0xf bound_ctrl:1\n" \
  "v_sub_u32 v52, v52, v46\n" \
  "s_nop 0\n" \
  "v_add_u32_dpp v87, v87, v87 row_shr:4 row_mask:0xf bank_mask:0xf bound_ctrl:1\n" \
  "s_nop 1\n" \
  "v_add_u32_dpp v87, v87, v87 row_shr:8 row_mask:0xf bank_mask:0xf bound_ctrl:1\n" \
  "v_add_u32 v52, v52, v87\n" \
  "ds_write_b16 v51, v52\n" \
  "ds_write_b16_d16_hi v51, v52 offset:128\n" \
  "ds_read_u16 v72, v72\n" \
  "ds_read_u16 v73, v73\n" \
  "ds_read_u16 v74, v74\n" \
  "ds_read_u16 v75, v75\n" \
  "v_readlane_b32 s60, v87, s72\n" \
  "s_movk_i32 s63, 200\n" \
  "v_cmp_lt_u32_sdwa s[90:91], s63, v87 src0_sel:DWORD src1_sel:WORD_0\n" \
  "v_cmp_lt_u32_sdwa vcc, s63, v87 src0_sel:DWORD src1_sel:WORD_1\n" \
  "s_lshr_b32 s72, s60, s73\n" \
  "s_and_b32 s72, s72, 0xffff\n" \
  "s_add_u32 s60, s72, 3\n" \
  "s_lshr_b32 s60, s60, 2\n" \
  "s_max_u32 s60, s60, 1\n" \
  "s_lshl2_add_u32 s73, s60, 8\n" \
  "s_sub_u32 s73, s73, s72\n" \
  "s_bfm_b64 s[88:89], s73, 0\n" \
  "s_or_b64 vcc, vcc, s[90:91]\n" \
  "s_cbranch_scc0 Lo1_%=\n" \
  "s_mov_b64 s[80:81], 0\n" \
  "s_mov_b64 s[82:83], 0\n" \
  "s_mov_b64 s[84:85], 0\n" \
  "s_mov_b64 s[86:87], 0\n" \
  "s_mov_b64 s[88:89], 0xfff\n" \
  "s_mov_b32 s72, 0\n" \
  "s_mov_b32 s60, 1\n" \
  "ds_or_b32 v94, v92\n" \
  "Lo1_%=:\n" \
  "v_lshrrev_b32 v50, 2, v13\n" \
  "s_lshl_b32 s72, s72, 2\n" \
  "v_add3_u32 v50, v50, s72, v14\n" \
  "v_mov_b32 v51, 0x50ffff\n" \
  "s_mov_b32 s63, 100096\n" \
  "s_mov_b64 exec, s[88:89]\n" \
  "ds_write_b32 v50, v51\n" \
  "s_waitcnt lgkmcnt(1)\n" \
  "s_mov_b64 exec, s[80:81]\n" \
  "v_add_u32 v80, v80, v72\n" \
  "v_lshl_add_u32 v72, v80, 2, s63\n" \
  "ds_write_b32 v72, v76\n" \
  "s_mov_b64 exec, s[82:83]\n" \
  "v_add_u32 v81, v81, v73\n" \
  "v_lshl_add_u32 v73, v81, 2, s63\n" \
  "ds_write_b32 v73, v77\n" \
  "s_mov_b64 exec, s[84:85]\n" \
  "v_add_u32 v82, v82, v74\n" \
  "v_lshl_add_u32 v74, v82, 2, s63\n" \
  "ds_write_b32 v74, v78\n" \
  "s_cmp_le_i32 s62, 192\n" \
  "s_cbranch_scc1 Lk876_%=\n" \
  "s_mov_b64 exec, s[86:87]\n" \
  "v_add_u32 v83, v83, v75\n" \
  "v_lshl_add_u32 v75, v83, 2, s63\n" \
  "ds_write_b32 v75, v79\n" \
  "Lk876_%=:\n" \
  "s_mov_b64 exec, -1\n" \
  "s_sub_u32 s60, s60, 1\n" \
  "s_lshl_b32 s63, s60, 4\n" \
  "v_sub_u32 v50, v14, v13\n" \
  "v_add_u32 v50, s63, v50\n" \
  "s_waitcnt lgkmcnt(0)\n" \
  "s_barrier\n" \
  "ds_read_b128 v[56:59], v14\n" \
  "ds_read_b128 v[60:63], v14 offset:16\n" \
  "ds_read_b128 v[64:67], v50\n" \
  "v_lshlrev_b32 v208, 4, v80\n" \
  "v_lshlrev_b32 v209, 4, v81\n" \
  "v_lshlrev_b32 v210, 4, v82\n" \
  "v_lshlrev_b32 v211, 4, v83\n" \
  "s_waitcnt vmcnt(3)\n" \
  "s_mov_b64 exec, s[80:81]\n" \
  "ds_write_b128 v208, v[192:195] offset:37584\n" \
  "s_mov_b64 exec, s[82:83]\n" \
  "ds_write_b128 v209, v[196:199] offset:37584\n" \
  "s_mov_b64 exec, s[84:85]\n" \
  "ds_write_b128 v210, v[200:203] offset:37584\n" \
  "s_cmp_le_i32 s62, 192\n" \
  "s_cbranch_scc1 Lk904_%=\n" \
  "s_mov_b64 exec, s[86:87]\n" \
  "ds_write_b128 v211, v[204:207] offset:37584\n" \
  "Lk904_%=:\n" \
  "s_mov_b64 exec, -1\n" \
  "s_waitcnt lgkmcnt(4)\n" \
  "v_and_b32 v12, 0xffff, v56\n" \
  "buffer_load_dwordx4 v[96:99], v[12:13], s[52:55], 0 idxen offen\n" \
  "v_and_b32 v12, 0xffff, v57\n" \
  "buffer_load_dwordx4 v[100:103], v[12:13], s[52:55], 0 idxen offen\n" \
  "v_and_b32 v12, 0xffff, v58\n" \
  "buffer_load_dwordx4 v[104:107], v[12:13], s[52:55], 0 idxen offen\n" \
  "v_and_b32 v12, 0xffff, v59\n" \
  "buffer_load_dwordx4 v[108:111], v[12:13], s[52:55], 0 idxen offen\n" \
  "v_lshrrev_b32 v51, 20, v84\n" \
  "v_lshrrev_b32 v52, 20, v43\n" \
  "v_min_u32 v51, 7, v51\n" \
  "v_min_u32 v52, 7, v52\n" \
  "s_bfm_b64 s[88:89], s68, 0\n" \
  "v_mov_b32 v18, v94\n" \
  "v_cmp_ge_u32 s[90:91], s44, v51\n" \
  "v_cmp_le_u32 vcc, s44, v52\n" \
  "s_and_b64 s[88:89], s[88:89], s[90:91]\n" \
  "s_and_b64 s[88:89], s[88:89], vcc\n" \
  "s_ff1_i32_b64 s72, s[88:89]\n" \
  "s_flbit_i32_b64 s74, s[88:89]\n" \
  "s_xor_b32 s74, s74, 63\n" \
  "v_cmp_le_u32 vcc, 4, v52\n" \
  "s_add_u32 s77, s68, 3\n" \
  "s_lshr_b32 s77, s77, 2\n" \
  "s_ff1_i32_b64 s76, vcc\n" \
  "s_lshr_b32 s76, s76, 2\n" \
  "s_min_u32 s76, s76, s77\n" \
  "s_sub_u32 s78, s77, s76\n" \
  "s_min_u32 s78, s78, 2\n" \
  "s_add_u32 s63, s76, s78\n" \
  "s_cmp_lt_u32 s44, 4\n" \
  "s_cselect_b32 s77, s76, s77\n" \
  "s_cselect_b32 s76, 0, s63\n" \
  "s_cselect_b32 s78, s78, 0\n" \
  "s_sub_u32 s77, s77, s76\n" \
  "s_mov_b32 s63, 0x0c0c0602\n" \
  "s_waitcnt lgkmcnt(0)\n" \
  "s_barrier\n" \
  "ds_read_b64 v[92:93], v16\n" \
  "ds_read_b64 v[94:95], v15\n" \
  "v_and_b32 v12, 0xffff, v60\n" \
  "buffer_load_dwordx4 v[112:115], v[12:13], s[52:55], 0 idxen offen\n" \
  "v_and_b32 v12, 0xffff, v61\n" \
  "buffer_load_dwordx4 v[116:119], v[12:13], s[52:55], 0 idxen offen\n" \
  "v_and_b32 v12, 0xffff, v62\n" \
  "buffer_load_dwordx4 v[120:123], v[12:13], s[52:55], 0 idxen offen\n" \
  "v_and_b32 v12, 0xffff, v63\n" \
  "buffer_load_dwordx4 v[124:127], v[12:13], s[52:55], 0 idxen offen\n" \
  "ds_read_u16 v12, v14 offset:32\n" \
  "v_perm_b32 v64, v65, v64, s63\n" \
  "v_perm_b32 v66, v67, v66, s63\n" \
  "v_readlane_b32 s93, v52, s74\n" \
  "v_readlane_b32 s75, v51, s72\n" \
  "v_lshl_or_b32 v17, v66, 16, v64\n" \
  "v_readlane_b32 s63, v52, s72\n" \
  "v_readlane_b32 s73, v42, s74\n" \
  "v_readlane_b32 s61, v17, s60\n" \
  "s_sub_u32 s74, s74, s72\n" \
  "s_add_u32 s72, s72, s74\n" \
  "s_sub_u32 s75, s44, s75\n" \
  "s_cmp_eq_u32 s63, s44\n" \
  "s_cselect_b32 s75, s75, 0\n" \
  "s_lshr_b32 s73, s73, 16\n" \
  "s_mul_i32 s72, s72, 1056\n" \
  "s_or_b32 s63, %[blk], s44\n" \
  "s_cbranch_scc0 Lep0_%=\n" \
  "Lgo%=:\n" \
  "s_waitcnt lgkmcnt(0)\n" \
  "v_mfma_f32_4x4x4_16b_bf16 v[56:59], v[92:93], v[32:33], 0 cbsz:4\n" \
  "v_mfma_f32_4x4x4_16b_bf16 v[60:63], v[92:93], v[36:37], 0 cbsz:4\n" \
  "v_mfma_f32_4x4x4_16b_bf16 v[64:67], v[92:93], v[32:33], 0 cbsz:4 abid:1\n" \
  "v_mfma_f32_4x4x4_16b_bf16 v[68:71], v[92:93], v[36:37], 0 cbsz:4 abid:1\n" \
  "v_mfma_f32_4x4x4_16b_bf16 v[84:87], v[94:95], v[24:25], 0\n" \
  "v_mfma_f32_4x4x4_16b_bf16 v[88:91], v[94:95], v[26:27], 0\n" \
  "v_mfma_f32_4x4x4_16b_bf16 v[48:51], v[94:95], v[28:29], 0\n" \
  "v_mfma_f32_4x4x4_16b_bf16 v[52:55], v[94:95], v[30:31], 0\n" \
  "s_nop 1\n" \
  "v_cvt_pk_bf16_f32 v57, v56, v57\n" \
  "v_lshlrev_b32_sdwa v56, 1, v57 dst_sel:DWORD dst_unused:UNUSED_PAD src0_sel:DWORD src1_sel:WORD_0\n" \
  "v_lshlrev_b32_sdwa v57, 1, v57 dst_sel:DWORD dst_unused:UNUSED_PAD src0_sel:DWORD src1_sel:WORD_1\n" \
  "ds_read_u16_d16_hi v56, v56\n" \
  "ds_read_u16_d16_hi v57, v57\n" \
  "v_cvt_pk_bf16_f32 v59, v58, v59\n" \
  "v_lshlrev_b32_sdwa v58, 1, v59 dst_sel:DWORD dst_unused:UNUSED_PAD src0_sel:DWORD src1_sel:WORD_0\n" \
  "v_lshlrev_b32_sdwa v59, 1, v59 dst_sel:DWORD dst_unused:UNUSED_PAD src0_sel:DWORD src1_sel:WORD_1\n" \
  "ds_read_u16_d16_hi v58, v58\n" \
  "ds_read_u16_d16_hi v59, v59\n" \
  "v_cvt_pk_bf16_f32 v61, v60, v61\n" \
  "v_lshlrev_b32_sdwa v60, 1, v61 dst_sel:DWORD dst_unused:UNUSED_PAD src0_sel:DWORD src1_sel:WORD_0\n" \
  "v_lshlrev_b32_sdwa v61, 1, v61 dst_sel:DWORD dst_unused:UNUSED_PAD src0_sel:DWORD src1_sel:WORD_1\n" \
  "ds_read_u16_d16_hi v60, v60\n" \
  "ds_read_u16_d16_hi v61, v61\n" \
  "v_cvt_pk_bf16_f32 v63, v62, v63\n" \
  "v_lshlrev_b32_sdwa v62, 1, v63 dst_sel:DWORD dst_unused:UNUSED_PAD src0_sel:DWORD src1_sel:WORD_0\n" \
  "v_lshlrev_b32_sdwa v63, 1, v63 dst_sel:DWORD dst_unused:UNUSED_PAD src0_sel:DWORD src1_sel:WORD_1\n" \
  "ds_read_u16_d16_hi v62, v62\n" \
  "ds_read_u16_d16_hi v63, v63\n" \
  "v_cvt_pk_bf16_f32 v76, v84, v88\n" \
  "v_cvt_pk_bf16_f32 v77, v48, v52\n" \
  "s_mov_b32 s63, 0x7fff7fff\n" \
  "v_and_b32 v41, s63, v194\n" \
  "v_and_b32 v42, s63, v195\n" \
  "v_pk_max_u16 v40, v41, v42\n" \
  "v_and_b32 v41, s63, v198\n" \
  "v_and_b32 v42, s63, v199\n" \
  "v_pk_max_u16 v41, v41, v40\n" \
  "v_pk_max_u16 v40, v41, v42\n" \
  "v_and_b32 v41, s63, v202\n" \
  "v_and_b32 v42, s63, v203\n" \
  "v_pk_max_u16 v41, v41, v40\n" \
  "v_pk_max_u16 v40, v41, v42\n" \
  "s_cmp_le_i32 s62, 192\n" \
  "s_cbranch_scc1 Lk1022_%=\n" \
  "v_and_b32 v41, s63, v206\n" \
  "v_and_b32 v42, s63, v207\n" \
  "v_pk_max_u16 v41, v41, v40\n" \
  "v_pk_max_u16 v40, v41, v42\n" \
  "Lk1022_%=:\n" \
  "v_max_u16_sdwa v40, v40, v40 dst_sel:DWORD dst_unused:UNUSED_PAD src0_sel:WORD_0 src1_sel:WORD_1\n" \
  "s_nop 0\n" \
  "v_cmp_lt_i32 vcc, s69, v40\n" \
  "s_cmp_eq_u64 vcc, 0\n" \
  "s_cselect_b32 s74, s74, -1\n" \
  "v_mov_b64 v[192:193], 0\n" \
  "v_mov_b64 v[194:195], 0\n" \
  "v_mov_b64 v[196:197], 0\n" \
  "v_mov_b64 v[198:199], 0\n" \
  "v_mov_b64 v[200:201], 0\n" \
  "v_mov_b64 v[202:203], 0\n" \
  "v_mov_b64 v[204:205], 0\n" \
  "v_mov_b64 v[206:207], 0\n" \
  "s_waitcnt vmcnt(7) lgkmcnt(0)\n" \
  "v_mfma_f32_4x4x4_16b_bf16 v[48:51], v[76:77], v[96:97], 0 cbsz:4\n" \
  "v_mfma_f32_4x4x4_16b_bf16 v[52:55], v[76:77], v[98:99], 0 cbsz:4\n" \
  "s_nop 4\n" \
  "Lbody%=:\n" \
  "s_set_gpr_idx_on s61, gpr_idx(SRC2,DST)\n" \
  "v_pk_fma_f32 v[128:129], v[56:57], v[48:49], v[128:129] op_sel:[0,0,0] op_sel_hi:[0,1,1]\n" \
  "v_pk_fma_f32 v[130:131], v[56:57], v[50:51], v[130:131] op_sel:[0,0,0] op_sel_hi:[0,1,1]\n" \
  "v_pk_fma_f32 v[132:133], v[60:61], v[52:53], v[132:133] op_sel:[0,0,0] op_sel_hi:[0,1,1]\n" \
  "v_pk_fma_f32 v[134:135], v[60:61], v[54:55], v[134:135] op_sel:[0,0,0] op_sel_hi:[0,1,1]\n" \
  "s_set_gpr_idx_off\n" \
  "s_waitcnt vmcnt(6) lgkmcnt(2)\n" \
  "buffer_load_dwordx4 v[96:99], v[12:13], s[52:55], 0 idxen offen\n" \
  "ds_read_u16 v12, v14 offset:36\n" \
  "s_lshr_b32 s61, s61, 8\n" \
  "v_mfma_f32_4x4x4_16b_bf16 v[48:51], v[76:77], v[100:101], 0 cbsz:4 abid:1\n" \
  "v_mfma_f32_4x4x4_16b_bf16 v[52:55], v[76:77], v[102:103], 0 cbsz:4 abid:1\n" \
  "v_cvt_pk_bf16_f32 v65, v64, v65\n" \
  "v_lshlrev_b32_sdwa v64, 1, v65 dst_sel:DWORD dst_unused:UNUSED_PAD src0_sel:DWORD src1_sel:WORD_0\n" \
  "v_lshlrev_b32_sdwa v65, 1, v65 dst_sel:DWORD dst_unused:UNUSED_PAD src0_sel:DWORD src1_sel:WORD_1\n" \
  "ds_read_u16_d16_hi v64, v64\n" \
  "ds_read_u16_d16_hi v65, v65\n" \
  "s_set_gpr_idx_on s61, gpr_idx(SRC2,DST)\n" \
  "v_pk_fma_f32 v[128:129], v[56:57], v[48:49], v[128:129] op_sel:[1,0,0] op_sel_hi:[1,1,1]\n" \
  "v_pk_fma_f32 v[130:131], v[56:57], v[50:51], v[130:131] op_sel:[1,0,0] op_sel_hi:[1,1,1]\n" \
  "v_pk_fma_f32 v[132:133], v[60:61], v[52:53], v[132:133] op_sel:[1,0,0] op_sel_hi:[1,1,1]\n" \
  "v_pk_fma_f32 v[134:135], v[60:61], v[54:55], v[134:135] op_sel:[1,0,0] op_sel_hi:[1,1,1]\n" \
  "s_set_gpr_idx_off\n" \
  "s_waitcnt vmcnt(6) lgkmcnt(2)\n" \
  "buffer_load_dwordx4 v[100:103], v[12:13], s[52:55], 0 idxen offen\n" \
  "ds_read_u16 v12, v14 offset:40\n" \
  "s_lshr_b32 s61, s61, 8\n" \
  "v_mfma_f32_4x4x4_16b_bf16 v[48:51], v[76:77], v[104:105], 0 cbsz:4 abid:2\n" \
  "v_mfma_f32_4x4x4_16b_bf16 v[52:55], v[76:77], v[106:107], 0 cbsz:4 abid:2\n" \
  "v_cvt_pk_bf16_f32 v69, v68, v69\n" \
  "v_lshlrev_b32_sdwa v68, 1, v69 dst_sel:DWORD dst_unused:UNUSED_PAD src0_sel:DWORD src1_sel:WORD_0\n" \
  "v_lshlrev_b32_sdwa v69, 1, v69 dst_sel:DWORD dst_unused:UNUSED_PAD src0_sel:DWORD src1_sel:WORD_1\n" \
  "ds_read_u16_d16_hi v68, v68\n" \
  "ds_read_u16_d16_hi v69, v69\n" \
  "s_set_gpr_idx_on s61, gpr_idx(SRC2,DST)\n" \
  "v_pk_fma_f32 v[128:129], v[58:59], v[48:49], v[128:129] op_sel:[0,0,0] op_sel_hi:[0,1,1]\n" \
  "v_pk_fma_f32 v[130:131], v[58:59], v[50:51], v[130:131] op_sel:[0,0,0] op_sel_hi:[0,1,1]\n" \
  "v_pk_fma_f32 v[132:133], v[62:63], v[52:53], v[132:133] op_sel:[0,0,0] op_sel_hi:[0,1,1]\n" \
  "v_pk_fma_f32 v[134:135], v[62:63], v[54:55], v[134:135] op_sel:[0,0,0] op_sel_hi:[0,1,1]\n" \
  "s_set_gpr_idx_off\n" \
  "s_waitcnt vmcnt(6) lgkmcnt(2)\n" \
  "buffer_load_dwordx4 v[104:107], v[12:13], s[52:55], 0 idxen offen\n" \
  "ds_read_u16 v12, v14 offset:44\n" \
  "s_lshr_b32 s61, s61, 8\n" \
  "v_mfma_f32_4x4x4_16b_bf16 v[48:51], v[76:77], v[108:109], 0 cbsz:4 abid:3\n" \
  "v_mfma_f32_4x4x4_16b_bf16 v[52:55], v[76:77], v[110:111], 0 cbsz:4 abid:3\n" \
  "v_cvt_pk_bf16_f32 v67, v66, v67\n" \
  "v_lshlrev_b32_sdwa v66, 1, v67 dst_sel:DWORD dst_unused:UNUSED_PAD src0_sel:DWORD src1_sel:WORD_0\n" \
  "v_lshlrev_b32_sdwa v67, 1, v67 dst_sel:DWORD dst_unused:UNUSED_PAD src0_sel:DWORD src1_sel:WORD_1\n" \
  "ds_read_u16_d16_hi v66, v66\n" \
  "ds_read_u16_d16_hi v67, v67\n" \
  "s_set_gpr_idx_on s61, gpr_idx(SRC2,DST)\n" \
  "v_pk_fma_f32 v[128:129], v[58:59], v[48:49], v[128:129] op_sel:[1,0,0] op_sel_hi:[1,1,1]\n" \
  "v_pk_fma_f32 v[130:131], v[58:59], v[50:51], v[130:131] op_sel:[1,0,0] op_sel_hi:[1,1,1]\n" \
  "v_pk_fma_f32 v[132:133], v[62:63], v[52:53], v[132:133] op_sel:[1,0,0] op_sel_hi:[1,1,1]\n" \
  "v_pk_fma_f32 v[134:135], v[62:63], v[54:55], v[134:135] op_sel:[1,0,0] op_sel_hi:[1,1,1]\n" \
  "s_set_gpr_idx_off\n" \
  "s_waitcnt vmcnt(6) lgkmcnt(2)\n" \
  "buffer_load_dwordx4 v[108:111], v[12:13], s[52:55], 0 idxen offen\n" \
  "ds_read_u16 v12, v14 offset:48\n" \
  "s_sub_u32 s60, s60, 1\n" \
  "s_cbranch_scc1 Lend%=\n" \
  "v_readlane_b32 s61, v17, s60\n" \
  "s_setprio 2\n" \
  "v_mfma_f32_4x4x4_16b_bf16 v[56:59], v[92:93], v[32:33], 0 cbsz:4 abid:2\n" \
  "v_mfma_f32_4x4x4_16b_bf16 v[60:63], v[92:93], v[36:37], 0 cbsz:4 abid:2\n" \
  "v_mfma_f32_4x4x4_16b_bf16 v[48:51], v[76:77], v[112:113], 0 cbsz:4 abid:4\n" \
  "v_mfma_f32_4x4x4_16b_bf16 v[52:55], v[76:77], v[114:115], 0 cbsz:4 abid:4\n" \
  "v_cvt_pk_bf16_f32 v71, v70, v71\n" \
  "v_lshlrev_b32_sdwa v70, 1, v71 dst_sel:DWORD dst_unused:UNUSED_PAD src0_sel:DWORD src1_sel:WORD_0\n" \
  "v_lshlrev_b32_sdwa v71, 1, v71 dst_sel:DWORD dst_unused:UNUSED_PAD src0_sel:DWORD src1_sel:WORD_1\n" \
  "ds_read_u16_d16_hi v70, v70\n" \
  "ds_read_u16_d16_hi v71, v71\n" \
  "v_add_u32 v16, 0x100, v16\n" \
  "s_set_gpr_idx_on s61, gpr_idx(SRC2,DST)\n" \
  "v_pk_fma_f32 v[128:129], v[64:65], v[48:49], v[128:129] op_sel:[0,0,0] op_sel_hi:[0,1,1]\n" \
  "v_pk_fma_f32 v[130:131], v[64:65], v[50:51], v[130:131] op_sel:[0,0,0] op_sel_hi:[0,1,1]\n" \
  "v_pk_fma_f32 v[132:133], v[68:69], v[52:53], v[132:133] op_sel:[0,0,0] op_sel_hi:[0,1,1]\n" \
  "v_pk_fma_f32 v[134:135], v[68:69], v[54:55], v[134:135] op_sel:[0,0,0] op_sel_hi:[0,1,1]\n" \
  "s_set_gpr_idx_off\n" \
  "s_waitcnt vmcnt(6) lgkmcnt(2)\n" \
  "buffer_load_dwordx4 v[112:115], v[12:13], s[52:55], 0 idxen offen\n" \
  "ds_read_u16 v12, v14 offset:52\n" \
  "ds_read_b64 v[94:95], v15 offset:256\n" \
  "s_lshr_b32 s61, s61, 8\n" \
  "v_mfma_f32_4x4x4_16b_bf16 v[48:51], v[76:77], v[116:117], 0 cbsz:4 abid:5\n" \
  "v_mfma_f32_4x4x4_16b_bf16 v[52:55], v[76:77], v[118:119], 0 cbsz:4 abid:5\n" \
  "v_cvt_pk_bf16_f32 v57, v56, v57\n" \
  "v_lshlrev_b32_sdwa v56, 1, v57 dst_sel:DWORD dst_unused:UNUSED_PAD src0_sel:DWORD src1_sel:WORD_0\n" \
  "v_lshlrev_b32_sdwa v57, 1, v57 dst_sel:DWORD dst_unused:UNUSED_PAD src0_sel:DWORD src1_sel:WORD_1\n" \
  "ds_read_u16_d16_hi v56, v56\n" \
  "ds_read_u16_d16_hi v57, v57\n" \
  "s_set_gpr_idx_on s61, gpr_idx(SRC2,DST)\n" \
  "v_pk_fma_f32 v[128:129], v[64:65], v[48:49], v[128:129] op_sel:[1,0,0] op_sel_hi:[1,1,1]\n" \
  "v_pk_fma_f32 v[130:131], v[64:65], v[50:51], v[130:131] op_sel:[1,0,0] op_sel_hi:[1,1,1]\n" \
  "v_pk_fma_f32 v[132:133], v[68:69], v[52:53], v[132:133] op_sel:[1,0,0] op_sel_hi:[1,1,1]\n" \
  "v_pk_fma_f32 v[134:135], v[68:69], v[54:55], v[134:135] op_sel:[1,0,0] op_sel_hi:[1,1,1]\n" \
  "s_set_gpr_idx_off\n" \
  "s_waitcnt vmcnt(6) lgkmcnt(2)\n" \
  "buffer_load_dwordx4 v[116:119], v[12:13], s[52:55], 0 idxen offen\n" \
  "ds_read_u16 v12, v14 offset:56\n" \
  "s_lshr_b32 s61, s61, 8\n" \
  "v_mfma_f32_4x4x4_16b_bf16 v[48:51], v[76:77], v[120:121], 0 cbsz:4 abid:6\n" \
  "v_mfma_f32_4x4x4_16b_bf16 v[52:55], v[76:77], v[122:123], 0 cbsz:4 abid:6\n" \
  "v_cvt_pk_bf16_f32 v61, v60, v61\n" \
  "v_lshlrev_b32_sdwa v60, 1, v61 dst_sel:DWORD dst_unused:UNUSED_PAD src0_sel:DWORD src1_sel:WORD_0\n" \
  "v_lshlrev_b32_sdwa v61, 1, v61 dst_sel:DWORD dst_unused:UNUSED_PAD src0_sel:DWORD src1_sel:WORD_1\n" \
  "ds_read_u16_d16_hi v60, v60\n" \
  "ds_read_u16_d16_hi v61, v61\n" \
  "s_set_gpr_idx_on s61, gpr_idx(SRC2,DST)\n" \
  "v_pk_fma_f32 v[128:129], v[66:67], v[48:49], v[128:129] op_sel:[0,0,0] op_sel_hi:[0,1,1]\n" \
  "v_pk_fma_f32 v[130:131], v[66:67], v[50:51], v[130:131] op_sel:[0,0,0] op_sel_hi:[0,1,1]\n" \
  "v_pk_fma_f32 v[132:133], v[70:71], v[52:53], v[132:133] op_sel:[0,0,0] op_sel_hi:[0,1,1]\n" \
  "v_pk_fma_f32 v[134:135], v[70:71], v[54:55], v[134:135] op_sel:[0,0,0] op_sel_hi:[0,1,1]\n" \
  "s_set_gpr_idx_off\n" \
  "s_waitcnt vmcnt(6) lgkmcnt(2)\n" \
  "buffer_load_dwordx4 v[120:123], v[12:13], s[52:55], 0 idxen offen\n" \
  "ds_read_u16 v12, v14 offset:60\n" \
  "s_lshr_b32 s61, s61, 8\n" \
  "v_mfma_f32_4x4x4_16b_bf16 v[84:87], v[94:95], v[24:25], 0\n" \
  "v_mfma_f32_4x4x4_16b_bf16 v[48:51], v[76:77], v[124:125], 0 cbsz:4 abid:7\n" \
  "v_mfma_f32_4x4x4_16b_bf16 v[52:55], v[76:77], v[126:127], 0 cbsz:4 abid:7\n" \
  "v_cvt_pk_bf16_f32 v59, v58, v59\n" \
  "v_lshlrev_b32_sdwa v58, 1, v59 dst_sel:DWORD dst_unused:UNUSED_PAD src0_sel:DWORD src1_sel:WORD_0\n" \
  "v_lshlrev_b32_sdwa v59, 1, v59 dst_sel:DWORD dst_unused:UNUSED_PAD src0_sel:DWORD src1_sel:WORD_1\n" \
  "ds_read_u16_d16_hi v58, v58\n" \
  "ds_read_u16_d16_hi v59, v59\n" \
  "s_set_gpr_idx_on s61, gpr_idx(SRC2,DST)\n" \
  "v_pk_fma_f32 v[128:129], v[66:67], v[48:49], v[128:129] op_sel:[1,0,0] op_sel_hi:[1,1,1]\n" \
  "v_pk_fma_f32 v[130:131], v[66:67], v[50:51], v[130:131] op_sel:[1,0,0] op_sel_hi:[1,1,1]\n" \
  "v_pk_fma_f32 v[132:133], v[70:71], v[52:53], v[132:133] op_sel:[1,0,0] op_sel_hi:[1,1,1]\n" \
  "v_pk_fma_f32 v[134:135], v[70:71], v[54:55], v[134:135] op_sel:[1,0,0] op_sel_hi:[1,1,1]\n" \
  "s_set_gpr_idx_off\n" \
  "s_waitcnt vmcnt(6) lgkmcnt(2)\n" \
  "buffer_load_dwordx4 v[124:127], v[12:13], s[52:55], 0 idxen offen\n" \
  "ds_read_u16 v12, v14 offset:64\n" \
  "s_sub_u32 s60, s60, 1\n" \
  "s_cbranch_scc1 Lend%=\n" \
  "v_readlane_b32 s61, v17, s60\n" \
  "s_setprio 1\n" \
  "v_mfma_f32_4x4x4_16b_bf16 v[64:67], v[92:93], v[32:33], 0 cbsz:4 abid:3\n" \
  "v_mfma_f32_4x4x4_16b_bf16 v[68:71], v[92:93], v[36:37], 0 cbsz:4 abid:3\n" \
  "v_mfma_f32_4x4x4_16b_bf16 v[48:51], v[76:77], v[96:97], 0 cbsz:4 abid:8\n" \
  "v_mfma_f32_4x4x4_16b_bf16 v[52:55], v[76:77], v[98:99], 0 cbsz:4 abid:8\n" \
  "v_cvt_pk_bf16_f32 v63, v62, v63\n" \
  "v_lshlrev_b32_sdwa v62, 1, v63 dst_sel:DWORD dst_unused:UNUSED_PAD src0_sel:DWORD src1_sel:WORD_0\n" \
  "v_lshlrev_b32_sdwa v63, 1, v63 dst_sel:DWORD dst_unused:UNUSED_PAD src0_sel:DWORD src1_sel:WORD_1\n" \
  "ds_read_u16_d16_hi v62, v62\n" \
  "ds_read_u16_d16_hi v63, v63\n" \
  "v_add_u32 v15, 0x100, v15\n" \
  "s_set_gpr_idx_on s61, gpr_idx(SRC2,DST)\n" \
  "v_pk_fma_f32 v[128:129], v[56:57], v[48:49], v[128:129] op_sel:[0,0,0] op_sel_hi:[0,1,1]\n" \
  "v_pk_fma_f32 v[130:131], v[56:57], v[50:51], v[130:131] op_sel:[0,0,0] op_sel_hi:[0,1,1]\n" \
  "v_pk_fma_f32 v[132:133], v[60:61], v[52:53], v[132:133] op_sel:[0,0,0] op_sel_hi:[0,1,1]\n" \
  "v_pk_fma_f32 v[134:135], v[60:61], v[54:55], v[134:135] op_sel:[0,0,0] op_sel_hi:[0,1,1]\n" \
  "s_set_gpr_idx_off\n" \
  "s_waitcnt vmcnt(6) lgkmcnt(2)\n" \
  "buffer_load_dwordx4 v[96:99], v[12:13], s[52:55], 0 idxen offen\n" \
  "ds_read_u16 v12, v14 offset:68\n" \
  "s_lshr_b32 s61, s61, 8\n" \
  "v_mfma_f32_4x4x4_16b_bf16 v[48:51], v[76:77], v[100:101], 0 cbsz:4 abid:9\n" \
  "v_mfma_f32_4x4x4_16b_bf16 v[52:55], v[76:77], v[102:103], 0 cbsz:4 abid:9\n" \
  "v_cvt_pk_bf16_f32 v65, v64, v65\n" \
  "v_lshlrev_b32_sdwa v64, 1, v65 dst_sel:DWORD dst_unused:UNUSED_PAD src0_sel:DWORD src1_sel:WORD_0\n" \
  "v_lshlrev_b32_sdwa v65, 1, v65 dst_sel:DWORD dst_unused:UNUSED_PAD src0_sel:DWORD src1_sel:WORD_1\n" \
  "ds_read_u16_d16_hi v64, v64\n" \
  "ds_read_u16_d16_hi v65, v65\n" \
  "s_set_gpr_idx_on s61, gpr_idx(SRC2,DST)\n" \
  "v_pk_fma_f32 v[128:129], v[56:57], v[48:49], v[128:129] op_sel:[1,0,0] op_sel_hi:[1,1,1]\n" \
  "v_pk_fma_f32 v[130:131], v[56:57], v[50:51], v[130:131] op_sel:[1,0,0] op_sel_hi:[1,1,1]\n" \
  "v_pk_fma_f32 v[132:133], v[60:61], v[52:53], v[132:133] op_sel:[1,0,0] op_sel_hi:[1,1,1]\n" \
  "v_pk_fma_f32 v[134:135], v[60:61], v[54:55], v[134:135] op_sel:[1,0,0] op_sel_hi:[1,1,1]\n" \
  "s_set_gpr_idx_off\n" \
  "s_waitcnt vmcnt(6) lgkmcnt(2)\n" \
  "buffer_load_dwordx4 v[100:103], v[12:13], s[52:55], 0 idxen offen\n" \
  "ds_read_u16 v12, v14 offset:72\n" \
  "s_lshr_b32 s61, s61, 8\n" \
  "v_mfma_f32_4x4x4_16b_bf16 v[48:51], v[76:77], v[104:105], 0 cbsz:4 abid:10\n" \
  "v_mfma_f32_4x4x4_16b_bf16 v[52:55], v[76:77], v[106:107], 0 cbsz:4 abid:10\n" \
  "v_cvt_pk_bf16_f32 v69, v68, v69\n" \
  "v_lshlrev_b32_sdwa v68, 1, v69 dst_sel:DWORD dst_unused:UNUSED_PAD src0_sel:DWORD src1_sel:WORD_0\n" \
  "v_lshlrev_b32_sdwa v69, 1, v69 dst_sel:DWORD dst_unused:UNUSED_PAD src0_sel:DWORD src1_sel:WORD_1\n" \
  "ds_read_u16_d16_hi v68, v68\n" \
  "ds_read_u16_d16_hi v69, v69\n" \
  "s_set_gpr_idx_on s61, gpr_idx(SRC2,DST)\n" \
  "v_pk_fma_f32 v[128:129], v[58:59], v[48:49], v[128:129] op_sel:[0,0,0] op_sel_hi:[0,1,1]\n" \
  "v_pk_fma_f32 v[130:131], v[58:59], v[50:51], v[130:131] op_sel:[0,0,0] op_sel_hi:[0,1,1]\n" \
  "v_pk_fma_f32 v[132:133], v[62:63], v[52:53], v[132:133] op_sel:[0,0,0] op_sel_hi:[0,1,1]\n" \
  "v_pk_fma_f32 v[134:135], v[62:63], v[54:55], v[134:135] op_sel:[0,0,0] op_sel_hi:[0,1,1]\n" \
  "s_set_gpr_idx_off\n" \
  "s_waitcnt vmcnt(6) lgkmcnt(2)\n" \
  "buffer_load_dwordx4 v[104:107], v[12:13], s[52:55], 0 idxen offen\n" \
  "ds_read_u16 v12, v14 offset:76\n" \
  "ds_read_b64 v[92:93], v16\n" \
  "s_lshr_b32 s61, s61, 8\n" \
  "v_mfma_f32_4x4x4_16b_bf16 v[88:91], v[94:95], v[26:27], 0\n" \
  "v_mfma_f32_4x4x4_16b_bf16 v[48:51], v[76:77], v[108:109], 0 cbsz:4 abid:11\n" \
  "v_mfma_f32_4x4x4_16b_bf16 v[52:55], v[76:77], v[110:111], 0 cbsz:4 abid:11\n" \
  "v_cvt_pk_bf16_f32 v67, v66, v67\n" \
  "v_lshlrev_b32_sdwa v66, 1, v67 dst_sel:DWORD dst_unused:UNUSED_PAD src0_sel:DWORD src1_sel:WORD_0\n" \
  "v_lshlrev_b32_sdwa v67, 1, v67 dst_sel:DWORD dst_unused:UNUSED_PAD src0_sel:DWORD src1_sel:WORD_1\n" \
  "ds_read_u16_d16_hi v66, v66\n" \
  "ds_read_u16_d16_hi v67, v67\n" \
  "s_set_gpr_idx_on s61, gpr_idx(SRC2,DST)\n" \
  "v_pk_fma_f32 v[128:129], v[58:59], v[48:49], v[128:129] op_sel:[1,0,0] op_sel_hi:[1,1,1]\n" \
  "v_pk_fma_f32 v[130:131], v[58:59], v[50:51], v[130:131] op_sel:[1,0,0] op_sel_hi:[1,1,1]\n" \
  "v_pk_fma_f32 v[132:133], v[62:63], v[52:53], v[132:133] op_sel:[1,0,0] op_sel_hi:[1,1,1]\n" \
  "v_pk_fma_f32 v[134:135], v[62:63], v[54:55], v[134:135] op_sel:[1,0,0] op_sel_hi:[1,1,1]\n" \
  "s_set_gpr_idx_off\n" \
  "s_waitcnt vmcnt(6) lgkmcnt(2)\n" \
  "buffer_load_dwordx4 v[108:111], v[12:13], s[52:55], 0 idxen offen\n" \
  "ds_read_u16 v12, v14 offset:80\n" \
  "s_sub_u32 s60, s60, 1\n" \
  "s_cbranch_scc1 Lend%=\n" \
  "v_readlane_b32 s61, v17, s60\n" \
  "s_setprio 0\n" \
  "v_mfma_f32_4x4x4_16b_bf16 v[56:59], v[92:93], v[32:33], 0 cbsz:4\n" \
  "v_mfma_f32_4x4x4_16b_bf16 v[60:63], v[92:93], v[36:37], 0 cbsz:4\n" \
  "v_mfma_f32_4x4x4_16b_bf16 v[48:51], v[76:77], v[112:113], 0 cbsz:4 abid:12\n" \
  "v_mfma_f32_4x4x4_16b_bf16 v[52:55], v[76:77], v[114:115], 0 cbsz:4 abid:12\n" \
  "v_cvt_pk_bf16_f32 v71, v70, v71\n" \
  "v_lshlrev_b32_sdwa v70, 1, v71 dst_sel:DWORD dst_unused:UNUSED_PAD src0_sel:DWORD src1_sel:WORD_0\n" \
  "v_lshlrev_b32_sdwa v71, 1, v71 dst_sel:DWORD dst_unused:UNUSED_PAD src0_sel:DWORD src1_sel:WORD_1\n" \
  "ds_read_u16_d16_hi v70, v70\n" \
  "ds_read_u16_d16_hi v71, v71\n" \
  "v_cvt_pk_bf16_f32 v20, v84, v88\n" \
  "s_set_gpr_idx_on s61, gpr_idx(SRC2,DST)\n" \
  "v_pk_fma_f32 v[128:129], v[64:65], v[48:49], v[128:129] op_sel:[0,0,0] op_sel_hi:[0,1,1]\n" \
  "v_pk_fma_f32 v[130:131], v[64:65], v[50:51], v[130:131] op_sel:[0,0,0] op_sel_hi:[0,1,1]\n" \
  "v_pk_fma_f32 v[132:133], v[68:69], v[52:53], v[132:133] op_sel:[0,0,0] op_sel_hi:[0,1,1]\n" \
  "v_pk_fma_f32 v[134:135], v[68:69], v[54:55], v[134:135] op_sel:[0,0,0] op_sel_hi:[0,1,1]\n" \
  "s_set_gpr_idx_off\n" \
  "s_waitcnt vmcnt(6) lgkmcnt(2)\n" \
  "buffer_load_dwordx4 v[112:115], v[12:13], s[52:55], 0 idxen offen\n" \
  "ds_read_u16 v12, v14 offset:84\n" \
  "s_lshr_b32 s61, s61, 8\n" \
  "v_mfma_f32_4x4x4_16b_bf16 v[48:51], v[76:77], v[116:117], 0 cbsz:4 abid:13\n" \
  "v_mfma_f32_4x4x4_16b_bf16 v[52:55], v[76:77], v[118:119], 0 cbsz:4 abid:13\n" \
  "v_cvt_pk_bf16_f32 v57, v56, v57\n" \
  "v_lshlrev_b32_sdwa v56, 1, v57 dst_sel:DWORD dst_unused:UNUSED_PAD src0_sel:DWORD src1_sel:WORD_0\n" \
  "v_lshlrev_b32_sdwa v57, 1, v57 dst_sel:DWORD dst_unused:UNUSED_PAD src0_sel:DWORD src1_sel:WORD_1\n" \
  "ds_read_u16_d16_hi v56, v56\n" \
  "ds_read_u16_d16_hi v57, v57\n" \
  "s_set_gpr_idx_on s61, gpr_idx(SRC2,DST)\n" \
  "v_pk_fma_f32 v[128:129], v[64:65], v[48:49], v[128:129] op_sel:[1,0,0] op_sel_hi:[1,1,1]\n" \
  "v_pk_fma_f32 v[130:131], v[64:65], v[50:51], v[130:131] op_sel:[1,0,0] op_sel_hi:[1,1,1]\n" \
  "v_pk_fma_f32 v[132:133], v[68:69], v[52:53], v[132:133] op_sel:[1,0,0] op_sel_hi:[1,1,1]\n" \
  "v_pk_fma_f32 v[134:135], v[68:69], v[54:55], v[134:135] op_sel:[1,0,0] op_sel_hi:[1,1,1]\n" \
  "s_set_gpr_idx_off\n" \
  "s_waitcnt vmcnt(6) lgkmcnt(2)\n" \
  "buffer_load_dwordx4 v[116:119], v[12:13], s[52:55], 0 idxen offen\n" \
  "ds_read_u16 v12, v14 offset:88\n" \
  "s_lshr_b32 s61, s61, 8\n" \
  "v_mfma_f32_4x4x4_16b_bf16 v[48:51], v[76:77], v[120:121], 0 cbsz:4 abid:14\n" \
  "v_mfma_f32_4x4x4_16b_bf16 v[52:55], v[76:77], v[122:123], 0 cbsz:4 abid:14\n" \
  "v_cvt_pk_bf16_f32 v61, v60, v61\n" \
  "v_lshlrev_b32_sdwa v60, 1, v61 dst_sel:DWORD dst_unused:UNUSED_PAD src0_sel:DWORD src1_sel:WORD_0\n" \
  "v_lshlrev_b32_sdwa v61, 1, v61 dst_sel:DWORD dst_unused:UNUSED_PAD src0_sel:DWORD src1_sel:WORD_1\n" \
  "ds_read_u16_d16_hi v60, v60\n" \
  "ds_read_u16_d16_hi v61, v61\n" \
  "s_set_gpr_idx_on s61, gpr_idx(SRC2,DST)\n" \
  "v_pk_fma_f32 v[128:129], v[66:67], v[48:49], v[128:129] op_sel:[0,0,0] op_sel_hi:[0,1,1]\n" \
  "v_pk_fma_f32 v[130:131], v[66:67], v[50:51], v[130:131] op_sel:[0,0,0] op_sel_hi:[0,1,1]\n" \
  "v_pk_fma_f32 v[132:133], v[70:71], v[52:53], v[132:133] op_sel:[0,0,0] op_sel_hi:[0,1,1]\n" \
  "v_pk_fma_f32 v[134:135], v[70:71], v[54:55], v[134:135] op_sel:[0,0,0] op_sel_hi:[0,1,1]\n" \
  "s_set_gpr_idx_off\n" \
  "s_waitcnt vmcnt(6) lgkmcnt(2)\n" \
  "buffer_load_dwordx4 v[120:123], v[12:13], s[52:55], 0 idxen offen\n" \
  "ds_read_u16 v12, v14 offset:92\n" \
  "s_lshr_b32 s61, s61, 8\n" \
  "v_mfma_f32_4x4x4_16b_bf16 v[84:87], v[94:95], v[28:29], 0\n" \
  "v_mfma_f32_4x4x4_16b_bf16 v[88:91], v[94:95], v[30:31], 0\n" \
  "v_mfma_f32_4x4x4_16b_bf16 v[48:51], v[76:77], v[124:125], 0 cbsz:4 abid:15\n" \
  "v_mfma_f32_4x4x4_16b_bf16 v[52:55], v[76:77], v[126:127], 0 cbsz:4 abid:15\n" \
  "v_cvt_pk_bf16_f32 v59, v58, v59\n" \
  "v_lshlrev_b32_sdwa v58, 1, v59 dst_sel:DWORD dst_unused:UNUSED_PAD src0_sel:DWORD src1_sel:WORD_0\n" \
  "v_lshlrev_b32_sdwa v59, 1, v59 dst_sel:DWORD dst_unused:UNUSED_PAD src0_sel:DWORD src1_sel:WORD_1\n" \
  "ds_read_u16_d16_hi v58, v58\n" \
  "ds_read_u16_d16_hi v59, v59\n" \
  "v_cvt_pk_bf16_f32 v77, v84, v88\n" \
  "v_mov_b32 v76, v20\n" \
  "s_set_gpr_idx_on s61, gpr_idx(SRC2,DST)\n" \
  "v_pk_fma_f32 v[128:129], v[66:67], v[48:49], v[128:129] op_sel:[1,0,0] op_sel_hi:[1,1,1]\n" \
  "v_pk_fma_f32 v[130:131], v[66:67], v[50:51], v[130:131] op_sel:[1,0,0] op_sel_hi:[1,1,1]\n" \
  "v_pk_fma_f32 v[132:133], v[70:71], v[52:53], v[132:133] op_sel:[1,0,0] op_sel_hi:[1,1,1]\n" \
  "v_pk_fma_f32 v[134:135], v[70:71], v[54:55], v[134:135] op_sel:[1,0,0] op_sel_hi:[1,1,1]\n" \
  "s_set_gpr_idx_off\n" \
  "s_waitcnt vmcnt(6) lgkmcnt(2)\n" \
  "buffer_load_dwordx4 v[124:127], v[12:13], s[52:55], 0 idxen offen\n" \
  "ds_read_u16 v12, v14 offset:96\n" \
  "s_sub_u32 s60, s60, 1\n" \
  "s_cbranch_scc1 Lend%=\n" \
  "v_readlane_b32 s61, v17, s60\n" \
  "s_setprio 3\n" \
  "v_mfma_f32_4x4x4_16b_bf16 v[64:67], v[92:93], v[32:33], 0 cbsz:4 abid:1\n" \
  "v_mfma_f32_4x4x4_16b_bf16 v[68:71], v[92:93], v[36:37], 0 cbsz:4 abid:1\n" \
  "v_mfma_f32_4x4x4_16b_bf16 v[48:51], v[76:77], v[96:97], 0 cbsz:4\n" \
  "v_mfma_f32_4x4x4_16b_bf16 v[52:55], v[76:77], v[98:99], 0 cbsz:4\n" \
  "v_cvt_pk_bf16_f32 v63, v62, v63\n" \
  "v_lshlrev_b32_sdwa v62, 1, v63 dst_sel:DWORD dst_unused:UNUSED_PAD src0_sel:DWORD src1_sel:WORD_0\n" \
  "v_lshlrev_b32_sdwa v63, 1, v63 dst_sel:DWORD dst_unused:UNUSED_PAD src0_sel:DWORD src1_sel:WORD_1\n" \
  "ds_read_u16_d16_hi v62, v62\n" \
  "ds_read_u16_d16_hi v63, v63\n" \
  "v_add_u32 v14, 64, v14\n" \
  "s_branch Lbody%=\n" \
  "Lep0_%=:\n" \
  "ds_read_b32 v50, v18\n" \
  "s_waitcnt lgkmcnt(0)\n" \
  "v_readfirstlane_b32 s63, v50\n" \
  "s_bitcmp1_b32 s63, 1\n" \
  "s_cbranch_scc1 Lgo%=\n" \
  "v_mov_b64 v[50:51], s[50:51]\n" \
  "s_mov_b64 exec, 1\n" \
  "buffer_store_dwordx2 v[50:51], off, s[24:27], s70 sc1\n" \
  "s_mov_b64 exec, -1\n" \
  "s_nop 3\n" \
  "s_branch Lgo%=\n" \
  "Lbad%=:\n" \
  "s_cmp_eq_u32 s74, 0xffffffc1\n" \
  "s_cbranch_scc1 Lf9_%=\n" \
  "v_mov_b32 v72, 1\n" \
  "v_mov_b32 v73, s62\n" \
  "ds_or_b32 v18, v72\n" \
  "ds_write_b32 v73, v73\n" \
  "s_branch Lf9_%=\n" \
  "Lnf%=:\n" \
  "s_mov_b32 %[bad], 3\n" \
  "s_mov_b32 s79, m0\n" \
  "Lbad2_%=:\n" \
  "s_waitcnt vmcnt(0) lgkmcnt(0)\n" \
  "s_bitcmp1_b32 %[bad], 1\n" \
  "s_cbranch_scc1 Lout%=\n" \
  "s_branch Lep%=\n" \
  "Lh2_%=:\n" \
  "s_waitcnt vmcnt(0)\n" \
  "v_add_co_u32 v90, vcc, 1, v88\n" \
  "s_mul_i32 s63, %[blk], 2176\n" \
  "s_add_u32 s63, s63, 49408\n" \
  "v_addc_co_u32 v91, vcc, 0, v89, vcc\n" \
  "v_lshlrev_b32 v88, 22, v90\n" \
  "v_alignbit_b32 v89, v91, v90, 10\n" \
  "v_add_u32 v87, v87, v88\n" \
  "v_lshl_add_u32 v128, v136, 11, v87\n" \
  "v_mov_b32 v129, v89\n" \
  "v_sub_u32 v51, v137, v136\n" \
  "v_lshl_add_u32 v51, v51, 11, v136\n" \
  "v_add_u32 v130, v51, v87\n" \
  "v_mov_b32 v131, v89\n" \
  "v_sub_u32 v51, v138, v137\n" \
  "v_lshl_add_u32 v51, v51, 11, v137\n" \
  "v_add_u32 v132, v51, v87\n" \
  "v_mov_b32 v133, v89\n" \
  "v_sub_u32 v51, v139, v138\n" \
  "v_lshl_add_u32 v51, v51, 11, v138\n" \
  "v_add_u32 v134, v51, v87\n" \
  "v_mov_b32 v135, v89\n" \
  "buffer_store_dwordx4 v[128:131], v13, s[24:27], s63 offen sc1\n" \
  "buffer_store_dwordx4 v[132:135], v13, s[24:27], s63 offen offset:1024 sc1\n" \
  "buffer_load_dword v100, v54, s[8:11], 0 offen offset:0\n" \
  "buffer_load_dword v101, v54, s[8:11], 0 offen offset:256\n" \
  "buffer_load_dword v102, v54, s[8:11], 0 offen offset:512\n" \
  "buffer_load_dword v103, v54, s[8:11], 0 offen offset:768\n" \
  "buffer_load_dword v104, v54, s[12:15], 0 offen\n" \
  "s_mul_i32 s72, s45, 528\n" \
  "s_cmp_lt_u32 s44, 4\n" \
  "s_cselect_b32 s62, 45776, 0\n" \
  "s_cselect_b32 s62, s62, 140608\n" \
  "s_add_u32 m0, s72, s62\n" \
  "s_lshl_b32 s72, s44, 12\n" \
  "buffer_load_dwordx4 v13, s[52:55], s72 offen lds\n" \
  "buffer_load_dwordx4 v13, s[52:55], s72 offen offset:1024 lds\n" \
  "s_add_u32 m0, m0, 64\n" \
  "s_nop 0\n" \
  "buffer_load_dwordx4 v13, s[52:55], s72 offen offset:2048 lds\n" \
  "buffer_load_dwordx4 v13, s[52:55], s72 offen offset:3072 lds\n" \
  "s_mov_b32 m0, 36160\n" \
  "Lw1_%=:\n" \
  "s_memrealtime s[90:91]\n" \
  "s_waitcnt lgkmcnt(0)\n" \
  "s_sub_u32 s90, s90, s76\n" \
  "s_cmp_lt_i32 s90, 160\n" \
  "s_cbranch_scc1 Lw1_%=\n" \
  "s_barrier\n" \
  "ds_write_b64 v94, v[90:91] offset:1472\n" \
  "s_mov_b32 s63, 0x05040100\n" \
  "s_mov_b32 s93, 0x07060302\n" \
  "s_waitcnt vmcnt(4)\n" \
  "v_perm_b32 v120, v101, v100, s63\n" \
  "v_perm_b32 v121, v103, v102, s63\n" \
  "v_perm_b32 v122, v101, v100, s93\n" \
  "v_perm_b32 v123, v103, v102, s93\n" \
  "ds_write_b128 v13, v[120:123] offset:36544\n" \
  "v_and_b32 v50, 30, v86\n" \
  "v_and_b32 v51, 16, v13\n" \
  "v_lshl_add_u32 v50, v51, 2, v50\n" \
  "s_mov_b64 exec, 0xffffffff\n" \
  "ds_write_b16 v50, v104 offset:36416\n" \
  "ds_write_b16_d16_hi v50, v104 offset:36448\n" \
  "s_mov_b64 exec, -1\n" \
  "v_and_b32 v100, 0x7fff7fff, v100\n" \
  "v_max_u16_sdwa v100, v100, v100 dst_sel:WORD_1 dst_unused:UNUSED_PAD src0_sel:WORD_0 src1_sel:WORD_1\n" \
  "v_and_b32 v101, 0x7fff7fff, v101\n" \
  "v_max_u16_sdwa v101, v101, v101 dst_sel:WORD_1 dst_unused:UNUSED_PAD src0_sel:WORD_0 src1_sel:WORD_1\n" \
  "v_and_b32 v102, 0x7fff7fff, v102\n" \
  "v_max_u16_sdwa v102, v102, v102 dst_sel:WORD_1 dst_unused:UNUSED_PAD src0_sel:WORD_0 src1_sel:WORD_1\n" \
  "v_and_b32 v103, 0x7fff7fff, v103\n" \
  "v_max_u16_sdwa v103, v103, v103 dst_sel:WORD_1 dst_unused:UNUSED_PAD src0_sel:WORD_0 src1_sel:WORD_1\n" \
  "v_add_f32 v100, v100, v101\n" \
  "v_add_f32 v102, v102, v103\n" \
  "v_add_f32 v100, v100, v102\n" \
  "v_and_b32 v100, 0x7fffffff, v100\n" \
  "s_nop 1\n" \
  "v_max_u32_dpp v100, v100, v100 row_shr:1 row_mask:0xf bank_mask:0xf bound_ctrl:1\n" \
  "s_nop 1\n" \
  "v_max_u32_dpp v100, v100, v100 row_shr:2 row_mask:0xf bank_mask:0xf bound_ctrl:1\n" \
  "s_nop 1\n" \
  "v_max_u32_dpp v100, v100, v100 row_shr:4 row_mask:0xf bank_mask:0xf bound_ctrl:1\n" \
  "s_nop 1\n" \
  "v_max_u32_dpp v100, v100, v100 row_shr:8 row_mask:0xf bank_mask:0xf bound_ctrl:1\n" \
  "s_nop 1\n" \
  "v_max_u32_dpp v100, v100, v100 row_bcast:15 row_mask:0xa bank_mask:0xf\n" \
  "s_nop 1\n" \
  "v_max_u32_dpp v100, v100, v100 row_bcast:31 row_mask:0xc bank_mask:0xf\n" \
  "v_max_u32 v101, 0x0d000000, v100\n" \
  "v_rcp_f32 v101, v101\n" \
  "v_cmp_gt_u32 vcc, 0x71000000, v100\n" \
  "v_mul_f32 v101, 0x437a0000, v101\n" \
  "v_lshrrev_b32 v101, 16, v101\n" \
  "v_cndmask_b32 v101, -1, v101, vcc\n" \
  "s_brev_b64 exec, 1\n" \
  "ds_write_b32 v94, v101 offset:1480\n" \
  "s_mov_b64 exec, -1\n" \
  "s_branch Lc1_%=\n" \
  "Lend%=:\n" \
  "s_lshl2_add_u32 s62, s44, 36128\n" \
  "v_add_u32 v19, s72, v19\n" \
  "s_cmp_ge_u32 s74, 10\n" \
  "s_cbranch_scc1 Lbad%=\n" \
  "v_mov_b32 v72, s62\n" \
  "s_cmp_gt_u32 s93, s44\n" \
  "s_cbranch_scc0 Lf1_%=\n" \
  "s_set_gpr_idx_on s73, gpr_idx(SRC0)\n" \
  "v_mov_b64 v[84:85], v[128:129]\n" \
  "v_mov_b64 v[86:87], v[130:131]\n" \
  "v_mov_b64 v[88:89], v[132:133]\n" \
  "v_mov_b64 v[90:91], v[134:135]\n" \
  "s_set_gpr_idx_off\n" \
  "ds_write_b128 v21, v[84:87]\n" \
  "ds_write_b128 v21, v[88:91] offset:1024\n" \
  "ds_write_b32 v72, v72\n" \
  "s_sub_u32 s73, s73, 8\n" \
  "s_cselect_b32 s73, 72, s73\n" \
  "v_subrev_u32 v19, 1056, v19\n" \
  "s_sub_u32 s74, s74, 1\n" \
  "s_cbranch_scc1 Lf9_%=\n" \
  "Lf1_%=:\n" \
  "s_min_u32 s63, s75, 1\n" \
  "s_sub_u32 s60, s74, s63\n" \
  "s_cbranch_scc1 Lf3_%=\n" \
  "Lf2_%=:\n" \
  "s_set_gpr_idx_on s73, gpr_idx(SRC0,SRC1)\n" \
  "v_cvt_pk_bf16_f32 v48, v128, v132\n" \
  "v_cvt_pk_bf16_f32 v49, v129, v133\n" \
  "v_cvt_pk_bf16_f32 v50, v130, v134\n" \
  "v_cvt_pk_bf16_f32 v51, v131, v135\n" \
  "s_set_gpr_idx_off\n" \
  "s_sub_u32 s73, s73, 8\n" \
  "s_cselect_b32 s73, 72, s73\n" \
  "ds_write2_b32 v19, v48, v49 offset1:16\n" \
  "ds_write2_b32 v19, v50, v51 offset0:32 offset1:48\n" \
  "s_sub_u32 s60, s60, 1\n" \
  "v_subrev_u32 v19, 1056, v19\n" \
  "s_cbranch_scc0 Lf2_%=\n" \
  "s_cmp_eq_u32 s75, 0\n" \
  "s_cbranch_scc1 Lf9_%=\n" \
  "Lf3_%=:\n" \
  "v_subrev_u32 v72, 4, v72\n" \
  "v_subrev_u32 v21, 3328, v21\n" \
  "Lf4_%=:\n" \
  "ds_read_b32 v73, v72\n" \
  "ds_read_b128 v[84:87], v21\n" \
  "ds_read_b128 v[88:91], v21 offset:1024\n" \
  "s_waitcnt lgkmcnt(0)\n" \
  "v_cmp_eq_u32 vcc, 0, v73\n" \
  "s_cbranch_vccz Lf5_%=\n" \
  "s_setprio 0\n" \
  "s_sleep 1\n" \
  "s_branch Lf4_%=\n" \
  "Lf5_%=:\n" \
  "s_set_gpr_idx_on s73, gpr_idx(SRC0,DST)\n" \
  "v_pk_add_f32 v[128:129], v[128:129], v[84:85]\n" \
  "v_pk_add_f32 v[130:131], v[130:131], v[86:87]\n" \
  "v_pk_add_f32 v[132:133], v[132:133], v[88:89]\n" \
  "v_pk_add_f32 v[134:135], v[134:135], v[90:91]\n" \
  "s_set_gpr_idx_off\n" \
  "s_mov_b32 s60, 0\n" \
  "s_sub_u32 s75, s75, 1\n" \
  "s_cmp_eq_u32 s75, 0\n" \
  "s_cbranch_scc1 Lf2_%=\n" \
  "s_branch Lf3_%=\n" \
  "Lf9_%=:\n" \
  "s_cmp_lt_u32 s44, 4\n" \
  "s_cselect_b32 s63, 1, 0x100\n" \
  "s_cselect_b32 s93, 4, 0x404\n" \
  "v_mov_b32 v72, s63\n" \
  "s_mov_b64 exec, 1\n" \
  "ds_add_u32 v18, v72 offset:8\n" \
  "s_mov_b64 exec, -1\n" \
  "s_setprio 0\n" \
  "buffer_load_dword v20, off, s[24:27], s70 sc1\n" \
  "s_mul_i32 s63, s76, 4224\n" \
  "v_add_u32 v22, s63, v22\n" \
  "s_lshl_b32 s72, s76, 12\n" \
  "Lm0_%=:\n" \
  "ds_read_b32 v72, v18 offset:8\n" \
  "ds_read_b128 v[128:131], v22 offset:0\n" \
  "ds_read_b128 v[132:135], v22 offset:256\n" \
  "ds_read_b128 v[136:139], v22 offset:512\n" \
  "ds_read_b128 v[140:143], v22 offset:768\n" \
  "ds_read_b128 v[144:147], v22 offset:4224\n" \
  "ds_read_b128 v[148:151], v22 offset:4480\n" \
  "ds_read_b128 v[152:155], v22 offset:4736\n" \
  "ds_read_b128 v[156:159], v22 offset:4992\n" \
  "s_waitcnt lgkmcnt(8)\n" \
  "v_readfirstlane_b32 s63, v72\n" \
  "s_and_b32 s63, s63, s93\n" \
  "s_cmp_eq_u32 s63, s93\n" \
  "s_cbranch_scc1 Lm1_%=\n" \
  "Lm4_%=:\n" \
  "s_sleep 1\n" \
  "ds_read_b32 v72, v18 offset:8\n" \
  "s_waitcnt lgkmcnt(0)\n" \
  "v_readfirstlane_b32 s63, v72\n" \
  "s_and_b32 s63, s63, s93\n" \
  "s_cmp_eq_u32 s63, s93\n" \
  "s_cbranch_scc0 Lm4_%=\n" \
  "ds_read_b128 v[128:131], v22 offset:0\n" \
  "ds_read_b128 v[132:135], v22 offset:256\n" \
  "ds_read_b128 v[136:139], v22 offset:512\n" \
  "ds_read_b128 v[140:143], v22 offset:768\n" \
  "ds_read_b128 v[144:147], v22 offset:4224\n" \
  "ds_read_b128 v[148:151], v22 offset:4480\n" \
  "ds_read_b128 v[152:155], v22 offset:4736\n" \
  "ds_read_b128 v[156:159], v22 offset:4992\n" \
  "Lm1_%=:\n" \
  "s_sub_u32 s77, s77, 1\n" \
  "s_cbranch_scc1 Lm8_%=\n" \
  "ds_read_b128 v[160:163], v22 offset:8448\n" \
  "ds_read_b128 v[164:167], v22 offset:8704\n" \
  "ds_read_b128 v[168:171], v22 offset:8960\n" \
  "ds_read_b128 v[172:175], v22 offset:9216\n" \
  "s_waitcnt lgkmcnt(11)\n" \
  "v_mfma_f32_16x16x32_bf16 v[40:43], v[128:131], v[216:219], 0\n" \
  "v_mfma_f32_16x16x32_bf16 v[44:47], v[128:131], v[220:223], 0\n" \
  "s_waitcnt lgkmcnt(10)\n" \
  "v_mfma_f32_16x16x32_bf16 v[40:43], v[132:135], v[224:227], v[40:43]\n" \
  "v_mfma_f32_16x16x32_bf16 v[44:47], v[132:135], v[228:231], v[44:47]\n" \
  "s_waitcnt lgkmcnt(9)\n" \
  "v_mfma_f32_16x16x32_bf16 v[40:43], v[136:139], v[232:235], v[40:43]\n" \
  "v_mfma_f32_16x16x32_bf16 v[44:47], v[136:139], v[236:239], v[44:47]\n" \
  "s_waitcnt lgkmcnt(8)\n" \
  "v_mfma_f32_16x16x32_bf16 v[40:43], v[140:143], v[240:243], v[40:43]\n" \
  "v_mfma_f32_16x16x32_bf16 v[44:47], v[140:143], v[244:247], v[44:47]\n" \
  "Lm2_%=:\n" \
  "s_sub_u32 s77, s77, 1\n" \
  "s_cbranch_scc1 Lm5_%=\n" \
  "v_add_u32 v22, 4224, v22\n" \
  "s_nop 0\n" \
  "ds_read_b128 v[128:131], v22 offset:8448\n" \
  "ds_read_b128 v[132:135], v22 offset:8704\n" \
  "ds_read_b128 v[136:139], v22 offset:8960\n" \
  "ds_read_b128 v[140:143], v22 offset:9216\n" \
  "s_waitcnt lgkmcnt(11)\n" \
  "v_mfma_f32_16x16x32_bf16 v[48:51], v[144:147], v[216:219], 0\n" \
  "v_mfma_f32_16x16x32_bf16 v[52:55], v[144:147], v[220:223], 0\n" \
  "s_waitcnt lgkmcnt(10)\n" \
  "v_mfma_f32_16x16x32_bf16 v[48:51], v[148:151], v[224:227], v[48:51]\n" \
  "v_mfma_f32_16x16x32_bf16 v[52:55], v[148:151], v[228:231], v[52:55]\n" \
  "v_cvt_pk_bf16_f32 v40, v40, v41\n" \
  "v_cvt_pk_bf16_f32 v41, v42, v43\n" \
  "v_cvt_pk_bf16_f32 v42, v44, v45\n" \
  "v_cvt_pk_bf16_f32 v43, v46, v47\n" \
  "buffer_store_dwordx4 v[40:43], v23, s[56:59], s72 offen sc1\n" \
  "s_add_u32 s72, s72, 0x1000\n" \
  "s_waitcnt lgkmcnt(9)\n" \
  "v_mfma_f32_16x16x32_bf16 v[48:51], v[152:155], v[232:235], v[48:51]\n" \
  "v_mfma_f32_16x16x32_bf16 v[52:55], v[152:155], v[236:239], v[52:55]\n" \
  "s_waitcnt lgkmcnt(8)\n" \
  "v_mfma_f32_16x16x32_bf16 v[48:51], v[156:159], v[240:243], v[48:51]\n" \
  "v_mfma_f32_16x16x32_bf16 v[52:55], v[156:159], v[244:247], v[52:55]\n" \
  "s_sub_u32 s77, s77, 1\n" \
  "s_cbranch_scc1 Lm6_%=\n" \
  "v_add_u32 v22, 4224, v22\n" \
  "s_nop 0\n" \
  "ds_read_b128 v[144:147], v22 offset:8448\n" \
  "ds_read_b128 v[148:151], v22 offset:8704\n" \
  "ds_read_b128 v[152:155], v22 offset:8960\n" \
  "ds_read_b128 v[156:159], v22 offset:9216\n" \
  "s_waitcnt lgkmcnt(11)\n" \
  "v_mfma_f32_16x16x32_bf16 v[56:59], v[160:163], v[216:219], 0\n" \
  "v_mfma_f32_16x16x32_bf16 v[60:63], v[160:163], v[220:223], 0\n" \
  "s_waitcnt lgkmcnt(10)\n" \
  "v_mfma_f32_16x16x32_bf16 v[56:59], v[164:167], v[224:227], v[56:59]\n" \
  "v_mfma_f32_16x16x32_bf16 v[60:63], v[164:167], v[228:231], v[60:63]\n" \
  "v_cvt_pk_bf16_f32 v48, v48, v49\n" \
  "v_cvt_pk_bf16_f32 v49, v50, v51\n" \
  "v_cvt_pk_bf16_f32 v50, v52, v53\n" \
  "v_cvt_pk_bf16_f32 v51, v54, v55\n" \
  "buffer_store_dwordx4 v[48:51], v23, s[56:59], s72 offen sc1\n" \
  "s_add_u32 s72, s72, 0x1000\n" \
  "s_waitcnt lgkmcnt(9)\n" \
  "v_mfma_f32_16x16x32_bf16 v[56:59], v[168:171], v[232:235], v[56:59]\n" \
  "v_mfma_f32_16x16x32_bf16 v[60:63], v[168:171], v[236:239], v[60:63]\n" \
  "s_waitcnt lgkmcnt(8)\n" \
  "v_mfma_f32_16x16x32_bf16 v[56:59], v[172:175], v[240:243], v[56:59]\n" \
  "v_mfma_f32_16x16x32_bf16 v[60:63], v[172:175], v[244:247], v[60:63]\n" \
  "s_sub_u32 s77, s77, 1\n" \
  "s_cbranch_scc1 Lm7_%=\n" \
  "v_add_u32 v22, 4224, v22\n" \
  "s_nop 0\n" \
  "ds_read_b128 v[160:163], v22 offset:8448\n" \
  "ds_read_b128 v[164:167], v22 offset:8704\n" \
  "ds_read_b128 v[168:171], v22 offset:8960\n" \
  "ds_read_b128 v[172:175], v22 offset:9216\n" \
  "s_waitcnt lgkmcnt(11)\n" \
  "v_mfma_f32_16x16x32_bf16 v[40:43], v[128:131], v[216:219], 0\n" \
  "v_mfma_f32_16x16x32_bf16 v[44:47], v[128:131], v[220:223], 0\n" \
  "s_waitcnt lgkmcnt(10)\n" \
  "v_mfma_f32_16x16x32_bf16 v[40:43], v[132:135], v[224:227], v[40:43]\n" \
  "v_mfma_f32_16x16x32_bf16 v[44:47], v[132:135], v[228:231], v[44:47]\n" \
  "v_cvt_pk_bf16_f32 v56, v56, v57\n" \
  "v_cvt_pk_bf16_f32 v57, v58, v59\n" \
  "v_cvt_pk_bf16_f32 v58, v60, v61\n" \
  "v_cvt_pk_bf16_f32 v59, v62, v63\n" \
  "buffer_store_dwordx4 v[56:59], v23, s[56:59], s72 offen sc1\n" \
  "s_add_u32 s72, s72, 0x1000\n" \
  "s_waitcnt lgkmcnt(9)\n" \
  "v_mfma_f32_16x16x32_bf16 v[40:43], v[136:139], v[232:235], v[40:43]\n" \
  "v_mfma_f32_16x16x32_bf16 v[44:47], v[136:139], v[236:239], v[44:47]\n" \
  "s_waitcnt lgkmcnt(8)\n" \
  "v_mfma_f32_16x16x32_bf16 v[40:43], v[140:143], v[240:243], v[40:43]\n" \
  "v_mfma_f32_16x16x32_bf16 v[44:47], v[140:143], v[244:247], v[44:47]\n" \
  "s_branch Lm2_%=\n" \
  "Lm5_%=:\n" \
  "s_nop 5\n" \
  "v_cvt_pk_bf16_f32 v40, v40, v41\n" \
  "v_cvt_pk_bf16_f32 v41, v42, v43\n" \
  "v_cvt_pk_bf16_f32 v42, v44, v45\n" \
  "v_cvt_pk_bf16_f32 v43, v46, v47\n" \
  "buffer_store_dwordx4 v[40:43], v23, s[56:59], s72 offen sc1\n" \
  "s_add_u32 s72, s72, 0x1000\n" \
  "s_branch Lm9_%=\n" \
  "Lm6_%=:\n" \
  "s_nop 5\n" \
  "v_cvt_pk_bf16_f32 v48, v48, v49\n" \
  "v_cvt_pk_bf16_f32 v49, v50, v51\n" \
  "v_cvt_pk_bf16_f32 v50, v52, v53\n" \
  "v_cvt_pk_bf16_f32 v51, v54, v55\n" \
  "buffer_store_dwordx4 v[48:51], v23, s[56:59], s72 offen sc1\n" \
  "s_add_u32 s72, s72, 0x1000\n" \
  "s_branch Lm9_%=\n" \
  "Lm7_%=:\n" \
  "s_nop 5\n" \
  "v_cvt_pk_bf16_f32 v56, v56, v57\n" \
  "v_cvt_pk_bf16_f32 v57, v58, v59\n" \
  "v_cvt_pk_bf16_f32 v58, v60, v61\n" \
  "v_cvt_pk_bf16_f32 v59, v62, v63\n" \
  "buffer_store_dwordx4 v[56:59], v23, s[56:59], s72 offen sc1\n" \
  "s_add_u32 s72, s72, 0x1000\n" \
  "Lm9_%=:\n" \
  "v_add_u32 v22, 4224, v22\n" \
  "Lm8_%=:\n" \
  "s_waitcnt lgkmcnt(0)\n" \
  "s_mov_b32 s77, s78\n" \
  "s_mov_b32 s78, 0\n" \
  "s_movk_i32 s93, 0x404\n" \
  "s_cmp_lg_u32 s77, 0\n" \
  "s_cbranch_scc1 Lm0_%=\n" \
  "s_barrier\n" \
  "ds_read_b32 v18, v18\n" \
  "s_waitcnt lgkmcnt(0)\n" \
  "v_readfirstlane_b32 %[bad], v18\n" \
  "s_cmp_lg_u32 %[bad], 0\n" \
  "s_cbranch_scc1 Lbad2_%=\n" \
  "s_cmp_lg_u32 s44, 0\n" \
  "s_cbranch_scc1 Lout%=\n" \
  "s_waitcnt vmcnt(0)\n" \
  "v_readfirstlane_b32 s63, v20\n" \
  "s_cmp_eq_u32 s63, s50\n" \
  "s_cbranch_scc1 Lout%=\n" \
  "Lep%=:\n" \
  "s_cmp_lg_u32 s44, 0\n" \
  "s_cbranch_scc1 Lout%=\n" \
  "v_mov_b64 v[84:85], s[50:51]\n" \
  "s_mov_b64 exec, 1\n" \
  "buffer_store_dwordx2 v[84:85], off, s[24:27], s70 sc1\n" \
  "s_mov_b64 exec, -1\n" \
  "Lout%=:\n" \
  "s_mov_b32 m0, s79\n"
#define STREAM_CLOBBERS \
  "v12", "v13", "v14", "v15", "v16", "v17", "v18", "v19", "v20", "v21", "v22", "v23", "v24", "v25", "v26", \
  "v27", "v28", "v29", "v30", "v31", "v32", "v33", "v34", "v35", "v36", "v37", "v38", "v39", "v40", "v41", \
  "v42", "v43", "v44", "v45", "v46", "v47", "v48", "v49", "v50", "v51", "v52", "v53", "v54", "v55", "v56", \
  "v57", "v58", "v59", "v60", "v61", "v62", "v63", "v64", "v65", "v66", "v67", "v68", "v69", "v70", "v71", \
  "v72", "v73", "v74", "v75", "v76", "v77", "v78", "v79", "v80", "v81", "v82", "v83", "v84", "v85", "v86", \
  "v87", "v88", "v89", "v90", "v91", "v92", "v93", "v94", "v95", "v96", "v97", "v98", "v99", "v100", "v101", \
  "v102", "v103", "v104", "v105", "v106", "v107", "v108", "v109", "v110", "v111", "v112", "v113", "v114", \
  "v115", "v116", "v117", "v118", "v119", "v120", "v121", "v122", "v123", "v124", "v125", "v126", "v127", \
  "v128", "v129", "v130", "v131", "v132", "v133", "v134", "v135", "v136", "v137", "v138", "v139", "v140", \
  "v141", "v142", "v143", "v144", "v145", "v146", "v147", "v148", "v149", "v150", "v151", "v152", "v153", \
  "v154", "v155", "v156", "v157", "v158", "v159", "v160", "v161", "v162", "v163", "v164", "v165", "v166", \
  "v167", "v168", "v169", "v170", "v171", "v172", "v173", "v174", "v175", "v176", "v177", "v178", "v179", \
  "v180", "v181", "v182", "v183", "v184", "v185", "v186", "v187", "v188", "v189", "v190", "v191", "v192", \
  "v193", "v194", "v195", "v196", "v197", "v198", "v199", "v200", "v201", "v202", "v203", "v204", "v205", \
  "v206", "v207", "v208", "v209", "v210", "v211", "v212", "v213", "v214", "v215", "v216", "v217", "v218", \
  "v219", "v220", "v221", "v222", "v223", "v224", "v225", "v226", "v227", "v228", "v229", "v230", "v231", \
  "v232", "v233", "v234", "v235", "v236", "v237", "v238", "v239", "v240", "v241", "v242", "v243", "v244", \
  "v245", "v246", "v247", "vcc", "s8", "s9", "s10", "s11", "s12", "s13", "s14", "s15", "s16", "s17", "s18", \
  "s19", "s20", "s21", "s22", "s23", "s24", "s25", "s26", "s27", "s28", "s29", "s30", "s31", "s33", "s34", \
  "s35", "s36", "s37", "s38", "s39", "s40", "s41", "s42", "s43", "s44", "s45", "s46", "s47", "s48", "s49", \
  "s50", "s51", "s52", "s53", "s54", "s55", "s56", "s57", "s58", "s59", "s60", "s61", "s62", "s63", "s64", \
  "s65", "s66", "s67", "s68", "s69", "s70", "s71", "s72", "s73", "s74", "s75", "s76", "s77", "s78", "s79", \
  "s80", "s81", "s82", "s83", "s84", "s85", "s86", "s87", "s88", "s89", "s90", "s91", "s92", "s93", "s94", \
  "s95", "scc", "memory"
#define LD_CELL 34560
#define LD_DEG 35584
#define LD_FLAG 36096
#define LD_CRD 36128
#define LD_SINK 36160
#define LD_CGT 36416
#define LD_WQT 36544
#define LD_TAG 37568
#define LD_PAY 37584
#define LD_ENT 100096
#define LD_TB 106816
#define LD_ROWS 108864
#define LD_SK 129344
#define LD_HIST 134464
#define LD_HB 136512
#define LD_W0 45776
#define LD_W1 140608
#define LD_END 157504
#define WS_EP 0
#define WS_FB 256
#define WS_KF 16640
#define WS_GR 49408
#define WS_RECK 606464
#define WS_RECP 1917184
#define WS_END 7160064

namespace {

constexpr int C_ = 128;
constexpr int NMAX = 40;
constexpr int NB = 256;
constexpr int CH = 1280;
constexpr int MT = 512;
constexpr int MW = MT / 64;
constexpr int RP = 1056;
constexpr unsigned W3 = 0x00020000u;

__device__ __forceinline__ float bf2f(unsigned short v) { return __uint_as_float(((unsigned)v) << 16); }
__device__ __forceinline__ unsigned short f2bf(float f) {
  __hip_bfloat16 b = __float2bfloat16(f);
  return *reinterpret_cast<unsigned short*>(&b);
}
__device__ __forceinline__ unsigned pk2bf(float lo, float hi) {
  return __builtin_bit_cast(unsigned, __builtin_convertvector(f32x2{lo, hi}, bf16x2));
}

}

extern "C" __global__ __launch_bounds__(MT) void tfn_kernel(
    const int* __restrict__ dst, unsigned dn, unsigned dw, const int* __restrict__ src, unsigned sn, unsigned sw,
    const u32x2* __restrict__ Y, unsigned yn, unsigned yw, const u32x2* __restrict__ ef, unsigned fn, unsigned fw,
    char* ws, unsigned wn, unsigned ww, const unsigned short* __restrict__ x, unsigned xn, unsigned xw,
    u32x4* __restrict__ out, unsigned pt, int N, unsigned M2, unsigned cb, int nbk, unsigned npb,
    const unsigned short* __restrict__ wrad, unsigned rn, unsigned rw, const unsigned short* __restrict__ cg, unsigned cn,
    unsigned cw, const unsigned short* __restrict__ wmix, unsigned mn, unsigned mw) {
  __shared__ __attribute__((aligned(16))) char lds[LD_END];
  static_assert(LD_END >= LD_ROWS + NMAX * RP && LD_END <= 163840, "LDS");
  TDECL;
  MSTAMP(0);
  unsigned bad;
  asm volatile(STREAM_ASM
               : [bad] "=&s"(bad)
               : [ka] "s"(__builtin_amdgcn_kernarg_segment_ptr()), [blk] "s"(blockIdx.x), [tid] "v"(threadIdx.x)
               : STREAM_CLOBBERS);
  const bool slow = bad != 0;
  MSTAMP(6);
  if (slow) {
    const int lane = threadIdx.x & 63, w = __builtin_amdgcn_readfirstlane(threadIdx.x >> 6), E = dn / 4;
    const int bucket = (blockIdx.x & 7) * (nbk / 8) + (blockIdx.x >> 3);
    int n01[2];
#pragma unroll
    for (int h = 0; h < 2; h++) {
      unsigned c = (unsigned)(((unsigned long long)(bucket + h) * npb) >> 20);
#pragma unroll
      for (int r = 0; r < 2; r++) c += __umulhi(c, M2) < (unsigned)(bucket + h) ? 1 : 0;
      n01[h] = min(c, (unsigned)N);
    }
    const int node0 = n01[0], nn = n01[1] - n01[0];
    const unsigned mxa = LD_ROWS + ((w >> 2) * 4 + ((lane & 15) >> 2)) * RP + (lane & 3) * 64 + (lane >> 4) * 16;
    const unsigned mxo = ((w >> 2) * 4 + (lane >> 4)) * 1024 + (w & 3) * 256 + (lane & 15) * 16;
    __syncthreads();
#pragma unroll 1
    for (int it = w; it < 2 * nn; it += MW) {
      const int dl = it >> 1, ch = (it & 1) * 64 + lane;
      float acc[4] = {0.f, 0.f, 0.f, 0.f};
#pragma unroll 1
      for (int eb = 0; eb < E; eb += 64) {
        unsigned long long m = __ballot(eb + lane < E && dst[min(eb + lane, E - 1)] == node0 + dl);
#pragma unroll 1
        for (; m; m &= m - 1) {
          const int e = eb + __builtin_ctzll(m);
          const u32x2 ry = Y[e], rf = ef[e];
          float yy[4] = {bf2f(ry[0] & 0xffff), bf2f(ry[0] >> 16), bf2f(ry[1] & 0xffff), bf2f(ry[1] >> 16)};
          float ee[4] = {bf2f(rf[0] & 0xffff), bf2f(rf[0] >> 16), bf2f(rf[1] & 0xffff), bf2f(rf[1] >> 16)};
          float z = 0.f;
#pragma unroll
          for (int k = 0; k < 4; k++) z += ee[k] * bf2f(wrad[k * C_ + ch]);
          const float R = z / (1.f + __expf(-z));
          const unsigned short* xr = x + (size_t)src[e] * 512 + ch * 4;
#pragma unroll 1
          for (int o = 0; o < 4; o++) {
            float tp = 0.f;
#pragma unroll 1
            for (int i = 0; i < 4; i++) {
              float A = 0.f;
#pragma unroll
              for (int f = 0; f < 4; f++) A += yy[f] * bf2f(cg[i * 16 + f * 4 + o]);
              tp += bf2f(xr[i]) * bf2f(f2bf(A));
            }
            const float v = R * tp;
            acc[0] += o == 0 ? v : 0.f;
            acc[1] += o == 1 ? v : 0.f;
            acc[2] += o == 2 ? v : 0.f;
            acc[3] += o == 3 ? v : 0.f;
          }
        }
      }
      unsigned short* ar = reinterpret_cast<unsigned short*>(lds + LD_ROWS) + dl * (RP / 2) + (ch >> 5) * 128 + (ch & 31);
#pragma unroll
      for (int o = 0; o < 4; o++) ar[o * 32] = f2bf(acc[o]);
    }
    __syncthreads();
    const auto ors = __builtin_amdgcn_make_buffer_rsrc((void*)(out + (size_t)node0 * 64), 0, nn * 1024, 0x00020000);
#pragma unroll 1
    for (int t = 0; t < NMAX / 8; t++) {
      f32x4 a = {0.f, 0.f, 0.f, 0.f}, b = {0.f, 0.f, 0.f, 0.f};
#pragma unroll 1
      for (int k = 0; k < 4; k++) {
        const s16x8 f = *reinterpret_cast<const s16x8*>(lds + mxa + t * 8 * RP + k * 256);
        const unsigned short* wp = wmix + (32 * k + 8 * (lane >> 4)) * C_ + 32 * (w & 3) + 2 * (lane & 15);
        s16x8 w0, w1;
#pragma unroll
        for (int j = 0; j < 8; j++) {
          w0[j] = wp[j * C_];
          w1[j] = wp[j * C_ + 1];
        }
        a = __builtin_amdgcn_mfma_f32_16x16x32_bf16(f, w0, a, 0, 0, 0);
        b = __builtin_amdgcn_mfma_f32_16x16x32_bf16(f, w1, b, 0, 0, 0);
      }
      __builtin_amdgcn_raw_buffer_store_b128(u32x4{pk2bf(a[0], a[1]), pk2bf(a[2], a[3]), pk2bf(b[0], b[1]), pk2bf(b[2], b[3])},
                                             ors, mxo + t * 8192, 0, 0);
    }
  }
  MFLUSH;
}

extern "C" void kernel_launch(void* const* d_in, const int* in_sizes, int n_in, void* d_out, int out_size,
                              void* d_ws, size_t ws_size, hipStream_t stream) {
  const int N = in_sizes[0] / 512;
  const int E = in_sizes[1];
  int nbk = NB;
  while ((N + nbk - 1) / nbk > NMAX || (N % nbk == 0 && N / nbk >= NMAX)) nbk += NB;
  const unsigned long long M2l = (((unsigned long long)nbk) << 32) / (unsigned long long)N;
  const unsigned M2 = M2l > 0xffffffffull ? 0xffffffffu : (unsigned)M2l;
  const unsigned long long cb = (16ull << 32) / (unsigned long long)N;
  const bool fast = nbk == NB && E <= NB * CH && ws_size >= WS_END;
  const unsigned e4 = E * 4u;
  tfn_kernel<<<nbk, MT, 0, stream>>>(
      (const int*)d_in[2], e4, W3, (const int*)d_in[1], e4, W3, (const u32x2*)d_in[3], 2 * e4, W3, (const u32x2*)d_in[4], 2 * e4, W3,
      (char*)d_ws, WS_END, W3, (const unsigned short*)d_in[0], N * 1024u, W3, (u32x4*)d_out, fast ? 448u : 0u, N, M2,
      cb > 0xffffffull ? 0xffffffu : (unsigned)cb, nbk, (unsigned)((1ull << 52) / M2), (const unsigned short*)d_in[5], 4 * C_ * 2,
      W3, (const unsigned short*)d_in[6], 128, W3, (const unsigned short*)d_in[7], C_ * C_ * 2, W3);
}
```

```cpp
#include <hip/hip_runtime.h>
#include <hip/hip_bf16.h>
#include <stdint.h>
#include <stddef.h>

typedef __attribute__((ext_vector_type(2))) float f32x2;
typedef __attribute__((ext_vector_type(4))) float f32x4;
typedef __attribute__((ext_vector_type(8))) short s16x8;
typedef __attribute__((ext_vector_type(2))) unsigned int u32x2;
typedef __attribute__((ext_vector_type(4))) unsigned int u32x4;
typedef __attribute__((ext_vector_type(16))) unsigned int u32x16;
typedef __attribute__((ext_vector_type(2))) __bf16 bf16x2;

#define TDECL
#define TSTAMP(i)
#define TFLUSH
#define MSTAMP(i) TSTAMP(i)
#define MFLUSH do { asm volatile("s_waitcnt vmcnt(0)" ::: "memory"); TSTAMP(15); TFLUSH; } while (0)
#undef MFLUSH
#define MFLUSH

#define STREAM_ASM \
  "s_load_dwordx16 s[8:23], %[ka], 0\n" \
  "s_load_dwordx16 s[24:39], %[ka], 64\n" \
  "s_load_dwordx4 s[52:55], %[ka], 160\n" \
  "s_memrealtime s[76:77]\n" \
  "s_getpc_b64 s[40:41]\n" \
  "Lpc%=:\n" \
  "v_lshlrev_b32 v50, 7, %[tid]\n" \
  "s_mov_b32 s42, Lout%=-Lpc%=\n" \
  "s_mov_b32 s43, 0x20000\n" \
  "buffer_load_dword v208, v50, s[40:43], 0 offen\n" \
  "v_lshlrev_b32 v52, 3, %[tid]\n" \
  "v_lshlrev_b32 v53, 2, %[tid]\n" \
  "v_mov_b32 v48, 0\n" \
  "v_readfirstlane_b32 s44, %[tid]\n" \
  "v_add_u32 v49, 134464, v53\n" \
  "ds_write_b32 v49, v48\n" \
  "s_mul_i32 s63, %[blk], 5120\n" \
  "s_add_u32 s61, s63, 3584\n" \
  "s_cmp_lt_u32 s44, 384\n" \
  "s_cselect_b32 s61, s61, 0x40000000\n" \
  "s_waitcnt lgkmcnt(0)\n" \
  "s_cmp_lt_u32 s44, s34\n" \
  "s_cbranch_scc0 Lh0_%=\n" \
  "buffer_load_dwordx2 v[100:101], v52, s[8:11], s63 offen nt\n" \
  "buffer_load_dword v102, v53, s[8:11], s61 offen nt\n" \
  "s_barrier\n" \
  "buffer_load_dwordx2 v[104:105], v52, s[12:15], s63 offen nt\n" \
  "buffer_load_dword v106, v53, s[12:15], s61 offen nt\n" \
  "v_lshlrev_b32 v49, 1, v52\n" \
  "s_lshl_b32 s93, s63, 1\n" \
  "s_lshl_b32 s62, s61, 1\n" \
  "buffer_load_dwordx4 v[108:111], v49, s[16:19], s93 offen nt\n" \
  "buffer_load_dwordx4 v[114:117], v49, s[20:23], s93 offen nt\n" \
  "buffer_load_dwordx2 v[112:113], v52, s[16:19], s62 offen nt\n" \
  "buffer_load_dwordx2 v[118:119], v52, s[20:23], s62 offen nt\n" \
  "s_sub_i32 s63, s10, s63\n" \
  "s_sub_i32 s61, s10, s61\n" \
  "v_cmp_gt_i32 s[80:81], s63, v52\n" \
  "s_sub_i32 s63, s63, 4\n" \
  "v_cmp_gt_i32 s[84:85], s61, v53\n" \
  "v_cmp_gt_i32 s[82:83], s63, v52\n" \
  "s_branch Lh1_%=\n" \
  "Lh0_%=:\n" \
  "s_mov_b32 s93, 0\n" \
  "buffer_load_dwordx2 v[88:89], off, s[24:27], s93\n" \
  "s_barrier\n" \
  "s_load_dwordx8 s[8:15], %[ka], 128\n" \
  "s_mov_b64 s[80:81], 0\n" \
  "s_mov_b64 s[82:83], 0\n" \
  "s_mov_b64 s[84:85], 0\n" \
  "s_cmp_eq_u32 s34, 0\n" \
  "s_cbranch_scc1 Lnf%=\n" \
  "Lh1_%=:\n" \
  "ds_write_b32 v53, v48 offset:34560\n" \
  "s_lshr_b32 s44, s44, 6\n" \
  "v_and_b32 v86, 63, %[tid]\n" \
  "s_lshl_b32 s72, s44, 3\n" \
  "s_lshl_b32 s63, 1, s72\n" \
  "v_mov_b32 v93, s63\n" \
  "s_and_b32 s45, s72, 24\n" \
  "s_lshl_b64 s[46:47], -1, s72\n" \
  "s_and_b32 s46, s46, 0x01010101\n" \
  "s_and_b32 s47, s47, 0x01010101\n" \
  "s_and_b32 s73, s44, 4\n" \
  "s_add_u32 s74, s73, 134464\n" \
  "s_lshl_b32 s75, s44, 9\n" \
  "s_add_u32 s75, s75, 136512\n" \
  "v_lshlrev_b32 v13, 4, v86\n" \
  "v_lshlrev_b32 v54, 2, v86\n" \
  "v_lshl_add_u32 v52, v86, 3, s75\n" \
  "v_lshlrev_b32 v50, 5, v86\n" \
  "v_add_u32 v50, 134464, v50\n" \
  "s_and_b32 s71, %[blk], 7\n" \
  "s_lshr_b32 s66, %[blk], 3\n" \
  "s_lshl_b32 s68, s71, 5\n" \
  "s_add_u32 s68, s68, s66\n" \
  "s_and_b32 s61, s68, 2\n" \
  "s_lshl_b32 s61, s61, 9\n" \
  "v_and_b32 v55, 0xf8, v54\n" \
  "s_and_b32 s63, s68, 0xfc\n" \
  "s_lshl2_add_u32 s61, s63, s61\n" \
  "v_add_u32 v55, s44, v55\n" \
  "s_and_b32 s63, s68, 1\n" \
  "s_lshl3_add_u32 s61, s63, s61\n" \
  "v_mul_u32_u24 v17, 2176, v55\n" \
  "s_add_u32 s61, s61, 49408\n" \
  "v_mov_b32 v94, 36096\n" \
  "s_mul_i32 s63, s71, s35\n" \
  "s_lshr_b32 s63, s63, 3\n" \
  "s_lshl3_add_u32 s66, s66, s44\n" \
  "s_mul_i32 s66, s66, 1024\n" \
  "s_lshl_b32 s63, s63, 10\n" \
  "s_add_u32 s66, s66, s63\n" \
  "v_lshlrev_b32 v20, 3, v13\n" \
  "v_cmp_gt_u32 vcc, 8, v86\n" \
  "v_bfrev_b32 v51, 1\n" \
  "s_mov_b32 s79, m0\n" \
  "s_mov_b32 m0, 36160\n" \
  "v_cndmask_b32 v20, v51, v20, vcc\n" \
  "s_mov_b32 s63, 0x01010101\n" \
  "s_waitcnt vmcnt(6)\n" \
  "v_mul_hi_u32 v96, v100, s36\n" \
  "v_mul_hi_u32 v97, v101, s36\n" \
  "v_mul_hi_u32 v98, v102, s36\n" \
  "s_mov_b64 exec, s[80:81]\n" \
  "v_lshl_add_u32 v120, v96, 3, s74\n" \
  "ds_add_rtn_u32 v124, v120, v93\n" \
  "s_mov_b64 exec, s[82:83]\n" \
  "v_lshl_add_u32 v121, v97, 3, s74\n" \
  "ds_add_rtn_u32 v125, v121, v93\n" \
  "s_mov_b64 exec, s[84:85]\n" \
  "v_lshl_add_u32 v122, v98, 3, s74\n" \
  "ds_add_rtn_u32 v126, v122, v93\n" \
  "s_mov_b64 exec, -1\n" \
  "v_lshl_add_u32 v96, v96, 1, s75\n" \
  "v_lshl_add_u32 v97, v97, 1, s75\n" \
  "v_lshl_add_u32 v98, v98, 1, s75\n" \
  "s_waitcnt lgkmcnt(0)\n" \
  "s_barrier\n" \
  "ds_read_b128 v[128:131], v50\n" \
  "ds_read_b128 v[132:135], v50 offset:16\n" \
  "v_bfe_u32 v124, v124, s45, 8\n" \
  "v_bfe_u32 v125, v125, s45, 8\n" \
  "v_bfe_u32 v126, v126, s45, 8\n" \
  "s_waitcnt lgkmcnt(0)\n" \
  "v_dot4_u32_u8 v136, v128, s63, 0\n" \
  "v_dot4_u32_u8 v136, v129, s63, v136\n" \
  "v_dot4_u32_u8 v137, v130, s63, v136\n" \
  "v_dot4_u32_u8 v137, v131, s63, v137\n" \
  "v_dot4_u32_u8 v138, v132, s63, v137\n" \
  "v_dot4_u32_u8 v138, v133, s63, v138\n" \
  "v_dot4_u32_u8 v139, v134, s63, v138\n" \
  "v_dot4_u32_u8 v139, v135, s63, v139\n" \
  "v_dot4_u32_u8 v140, v128, s46, 0\n" \
  "v_dot4_u32_u8 v141, v130, s46, 0\n" \
  "v_dot4_u32_u8 v142, v132, s46, 0\n" \
  "v_add_u32_dpp v87, v139, v139 row_shr:1 row_mask:0xf bank_mask:0xf bound_ctrl:1\n" \
  "v_dot4_u32_u8 v143, v134, s46, 0\n" \
  "v_dot4_u32_u8 v140, v129, s47, v140\n" \
  "v_add_u32_dpp v87, v87, v87 row_shr:2 row_mask:0xf bank_mask:0xf bound_ctrl:1\n" \
  "v_dot4_u32_u8 v141, v131, s47, v141\n" \
  "v_dot4_u32_u8 v142, v133, s47, v142\n" \
  "v_add_u32_dpp v87, v87, v87 row_shr:4 row_mask:0xf bank_mask:0xf bound_ctrl:1\n" \
  "v_dot4_u32_u8 v143, v135, s47, v143\n" \
  "s_mov_b32 s63, 0x10001\n" \
  "v_add_u32_dpp v87, v87, v87 row_shr:8 row_mask:0xf bank_mask:0xf bound_ctrl:1\n" \
  "v_sub_u32 v140, v136, v140\n" \
  "v_sub_u32 v141, v137, v141\n" \
  "v_add_u32_dpp v87, v87, v87 row_bcast:15 row_mask:0xa bank_mask:0xf\n" \
  "v_sub_u32 v142, v138, v142\n" \
  "v_sub_u32 v143, v139, v143\n" \
  "v_add_u32_dpp v87, v87, v87 row_bcast:31 row_mask:0xc bank_mask:0xf\n" \
  "v_lshl_or_b32 v140, v141, 16, v140\n" \
  "v_sub_u32 v87, v87, v139\n" \
  "v_lshl_or_b32 v141, v143, 16, v142\n" \
  "s_cmp_eq_u32 s44, 7\n" \
  "v_mad_u32_u24 v140, v87, s63, v140\n" \
  "v_mad_u32_u24 v141, v87, s63, v141\n" \
  "s_cbranch_scc1 Lh2_%=\n" \
  "ds_write_b64 v52, v[140:141]\n" \
  "ds_read_u16 v96, v96\n" \
  "ds_read_u16 v97, v97\n" \
  "ds_read_u16 v98, v98\n" \
  "s_mov_b32 s63, 129344\n" \
  "s_mov_b32 s93, 108864\n" \
  "s_lshl_b32 s74, s44, 10\n" \
  "v_add_u32 v50, s74, v13\n" \
  "v_add_u32 v51, s93, v50\n" \
  "s_waitcnt vmcnt(4)\n" \
  "v_lshl_or_b32 v104, v100, 24, v104\n" \
  "v_lshl_or_b32 v105, v101, 24, v105\n" \
  "v_lshl_or_b32 v106, v102, 24, v106\n" \
  "s_waitcnt lgkmcnt(0)\n" \
  "s_mov_b64 exec, s[80:81]\n" \
  "v_add_u32 v124, v124, v96\n" \
  "v_lshl_add_u32 v120, v124, 2, s63\n" \
  "v_lshl_add_u32 v124, v124, 4, s93\n" \
  "ds_write_b32 v120, v104\n" \
  "s_mov_b64 exec, s[82:83]\n" \
  "v_add_u32 v125, v125, v97\n" \
  "v_lshl_add_u32 v121, v125, 2, s63\n" \
  "v_lshl_add_u32 v125, v125, 4, s93\n" \
  "ds_write_b32 v121, v105\n" \
  "s_mov_b64 exec, s[84:85]\n" \
  "v_add_u32 v126, v126, v98\n" \
  "v_lshl_add_u32 v122, v126, 2, s63\n" \
  "v_lshl_add_u32 v126, v126, 4, s93\n" \
  "ds_write_b32 v122, v106\n" \
  "s_waitcnt vmcnt(0)\n" \
  "s_mov_b64 exec, s[84:85]\n" \
  "ds_write2_b64 v126, v[112:113], v[118:119] offset1:1\n" \
  "s_mov_b64 exec, s[82:83]\n" \
  "ds_write2_b64 v125, v[110:111], v[116:117] offset1:1\n" \
  "s_mov_b64 exec, s[80:81]\n" \
  "ds_write2_b64 v124, v[108:109], v[114:115] offset1:1\n" \
  "s_mov_b64 exec, -1\n" \
  "s_mul_i32 s72, s45, 528\n" \
  "s_cmp_lt_u32 s44, 4\n" \
  "s_cselect_b32 s62, 45776, 0\n" \
  "s_cselect_b32 s62, s62, 140608\n" \
  "s_add_u32 m0, s72, s62\n" \
  "s_lshl_b32 s72, s44, 12\n" \
  "buffer_load_dwordx4 v13, s[52:55], s72 offen lds\n" \
  "buffer_load_dwordx4 v13, s[52:55], s72 offen offset:1024 lds\n" \
  "s_add_u32 m0, m0, 64\n" \
  "s_nop 0\n" \
  "buffer_load_dwordx4 v13, s[52:55], s72 offen offset:2048 lds\n" \
  "buffer_load_dwordx4 v13, s[52:55], s72 offen offset:3072 lds\n" \
  "s_mov_b32 m0, 36160\n" \
  "s_mul_i32 s72, %[blk], 20480\n" \
  "s_lshr_b32 s62, s72, 2\n" \
  "s_add_u32 s72, s72, 1917184\n" \
  "s_add_u32 s62, s62, 607488\n" \
  "s_add_u32 s74, s72, 7168\n" \
  "s_add_u32 s75, s72, 14336\n" \
  "s_sub_u32 s63, s62, 7168\n" \
  "s_cmp_eq_u32 s44, 6\n" \
  "s_cselect_b32 s75, s63, s75\n" \
  "s_cmp_gt_u32 s44, 3\n" \
  "s_waitcnt lgkmcnt(0)\n" \
  "s_barrier\n" \
  "ds_read_b128 v[192:195], v51 offset:0\n" \
  "ds_read_b128 v[196:199], v51 offset:7168\n" \
  "ds_read_b128 v[200:203], v51 offset:14336\n" \
  "ds_read_b128 v[204:207], v51 offset:21504\n" \
  "s_waitcnt lgkmcnt(3)\n" \
  "buffer_store_dwordx4 v[192:195], v50, s[24:27], s72 offen sc1\n" \
  "s_waitcnt lgkmcnt(2)\n" \
  "buffer_store_dwordx4 v[196:199], v50, s[24:27], s74 offen sc1\n" \
  "s_waitcnt lgkmcnt(1)\n" \
  "buffer_store_dwordx4 v[200:203], v50, s[24:27], s75 offen sc1\n" \
  "s_cbranch_scc1 Lc1_%=\n" \
  "s_waitcnt lgkmcnt(0)\n" \
  "buffer_store_dwordx4 v[204:207], v50, s[24:27], s62 offen sc1\n" \
  "Lc1_%=:\n" \
  "s_mov_b32 s63, 0x80008\n" \
  "s_mov_b32 s93, 0x80008000\n" \
  "v_mul_lo_u32 v52, %[tid], s63\n" \
  "v_lshlrev_b32 v53, 4, %[tid]\n" \
  "v_add_u32 v49, 0x10000, v53\n" \
  "v_cmp_gt_u32 s[90:91], 32, %[tid]\n" \
  "v_cmp_gt_u32 vcc, 368, %[tid]\n" \
  "v_add_u32 v104, 0xf810f80, v52\n" \
  "v_add_u32 v105, 0xf830f82, v52\n" \
  "v_add_u32 v106, 0xf850f84, v52\n" \
  "v_add_u32 v107, 0xf870f86, v52\n" \
  "v_or_b32 v108, s93, v104\n" \
  "v_or_b32 v109, s93, v105\n" \
  "v_or_b32 v110, s93, v106\n" \
  "v_or_b32 v111, s93, v107\n" \
  "ds_write_b128 v53, v[104:107] offset:8192\n" \
  "ds_write_b128 v49, v[108:111] offset:8192\n" \
  "v_add_u32 v112, 0x1f811f80, v52\n" \
  "v_add_u32 v113, 0x1f831f82, v52\n" \
  "v_add_u32 v114, 0x1f851f84, v52\n" \
  "v_add_u32 v115, 0x1f871f86, v52\n" \
  "v_or_b32 v116, s93, v112\n" \
  "v_or_b32 v117, s93, v113\n" \
  "v_or_b32 v118, s93, v114\n" \
  "v_or_b32 v119, s93, v115\n" \
  "ds_write_b128 v53, v[112:115] offset:16384\n" \
  "ds_write_b128 v49, v[116:119] offset:16384\n" \
  "s_mov_b64 exec, vcc\n" \
  "v_add_u32 v120, 0x2f812f80, v52\n" \
  "v_add_u32 v121, 0x2f832f82, v52\n" \
  "v_add_u32 v122, 0x2f852f84, v52\n" \
  "v_add_u32 v123, 0x2f872f86, v52\n" \
  "v_or_b32 v124, s93, v120\n" \
  "v_or_b32 v125, s93, v121\n" \
  "v_or_b32 v126, s93, v122\n" \
  "v_or_b32 v127, s93, v123\n" \
  "ds_write_b128 v53, v[120:123] offset:24576\n" \
  "ds_write_b128 v49, v[124:127] offset:24576\n" \
  "s_mov_b64 exec, -1\n" \
  "v_add_u32 v96, 0xff80ff80, v52\n" \
  "v_add_u32 v97, 0xff82ff82, v52\n" \
  "v_add_u32 v98, 0xff84ff84, v52\n" \
  "v_add_u32 v99, 0xff86ff86, v52\n" \
  "v_lshrrev_b32 v52, 1, v52\n" \
  "s_mov_b64 exec, s[90:91]\n" \
  "v_add_u32 v96, 0x0, v52\n" \
  "v_add_u32 v97, 0x20001, v52\n" \
  "v_add_u32 v98, 0x20002, v52\n" \
  "v_add_u32 v99, 0x40003, v52\n" \
  "s_mov_b64 exec, -1\n" \
  "v_or_b32 v100, s93, v96\n" \
  "v_or_b32 v101, s93, v97\n" \
  "v_or_b32 v102, s93, v98\n" \
  "v_or_b32 v103, s93, v99\n" \
  "ds_write_b128 v53, v[96:99] offset:0\n" \
  "ds_write_b128 v49, v[100:103] offset:0\n" \
  "buffer_load_dwordx2 v[44:45], v17, s[24:27], s61 offen sc1\n" \
  "s_mov_b64 s[88:89], 0\n" \
  "s_mov_b32 s92, 0\n" \
  "v_mov_b32 v92, 1\n" \
  "v_and_b32 v41, 4, v54\n" \
  "s_movk_i32 s63, 5120\n" \
  "v_mad_u32_u24 v40, v55, s63, v41\n" \
  "s_lshl_b32 s63, s44, 10\n" \
  "s_add_u32 s63, s63, 37584\n" \
  "v_add_u32 v41, s63, v41\n" \
  "s_mul_i32 s90, s68, s39\n" \
  "s_mul_hi_u32 s91, s68, s39\n" \
  "s_lshr_b64 s[90:91], s[90:91], 20\n" \
  "s_mul_hi_u32 s63, s90, s36\n" \
  "s_cmp_lt_u32 s63, s68\n" \
  "s_addc_u32 s90, s90, 0\n" \
  "s_mul_hi_u32 s63, s90, s36\n" \
  "s_cmp_lt_u32 s63, s68\n" \
  "s_addc_u32 s90, s90, 0\n" \
  "s_min_u32 s67, s90, s35\n" \
  "s_add_u32 s68, s68, 1\n" \
  "s_mul_i32 s90, s68, s39\n" \
  "s_mul_hi_u32 s91, s68, s39\n" \
  "s_lshr_b64 s[90:91], s[90:91], 20\n" \
  "s_mul_hi_u32 s63, s90, s36\n" \
  "s_cmp_lt_u32 s63, s68\n" \
  "s_addc_u32 s90, s90, 0\n" \
  "s_mul_hi_u32 s63, s90, s36\n" \
  "s_cmp_lt_u32 s63, s68\n" \
  "s_addc_u32 s90, s90, 0\n" \
  "s_min_u32 s68, s90, s35\n" \
  "s_sub_u32 s68, s68, s67\n" \
  "s_lshl_b32 s71, s71, 1\n" \
  "v_lshlrev_b32 v50, 5, v86\n" \
  "v_and_b32 v50, 0x60, v50\n" \
  "v_bfrev_b32 v51, 1\n" \
  "s_lshl_b32 s63, %[blk], 6\n" \
  "s_add_u32 s63, s63, 256\n" \
  "s_cmp_lg_u32 s44, 7\n" \
  "s_waitcnt vmcnt(1) lgkmcnt(0)\n" \
  "s_barrier\n" \
  "s_cbranch_scc1 Ls1_%=\n" \
  "s_mov_b64 exec, 1\n" \
  "buffer_store_dwordx2 v[88:89], off, s[24:27], s63 sc1\n" \
  "s_mov_b64 exec, -1\n" \
  "Ls1_%=:\n" \
  "ds_read_b96 v[56:58], v94 offset:1472\n" \
  "ds_read_b128 v[24:27], v50 offset:36416\n" \
  "ds_read_b128 v[28:31], v50 offset:36432\n" \
  "ds_read_b64 v[32:33], v13 offset:36544\n" \
  "ds_read_b64 v[36:37], v13 offset:36552\n" \
  "s_waitcnt lgkmcnt(4)\n" \
  "v_readfirstlane_b32 s50, v56\n" \
  "v_readfirstlane_b32 s51, v57\n" \
  "v_readfirstlane_b32 s69, v58\n" \
  "s_lshl_b32 s64, s50, 22\n" \
  "s_lshr_b64 s[90:91], s[50:51], 10\n" \
  "s_mov_b32 s65, s90\n" \
  "s_cmp_lg_u32 s44, 7\n" \
  "s_waitcnt vmcnt(1)\n" \
  "s_cbranch_scc0 Lchk%=\n" \
  "s_waitcnt vmcnt(0)\n" \
  "s_branch Lchk%=\n" \
  "Lpoll%=:\n" \
  "buffer_load_dwordx2 v[44:45], v17, s[24:27], s61 offen sc1\n" \
  "s_waitcnt vmcnt(0)\n" \
  "Lchk%=:\n" \
  "v_and_b32 v50, 0xffc00000, v44\n" \
  "v_cmp_eq_u32 s[90:91], s65, v45\n" \
  "v_cmp_eq_u32 vcc, s64, v50\n" \
  "s_and_b64 vcc, vcc, s[90:91]\n" \
  "s_andn2_b64 vcc, vcc, s[88:89]\n" \
  "s_or_b64 s[88:89], s[88:89], vcc\n" \
  "v_cndmask_b32 v48, v48, v44, vcc\n" \
  "s_cmp_eq_u64 s[88:89], -1\n" \
  "v_cndmask_b32 v17, v17, v51, vcc\n" \
  "s_cbranch_scc1 Lseen%=\n" \
  "s_add_u32 s92, s92, 1\n" \
  "s_cmp_lt_u32 s92, 4096\n" \
  "s_cbranch_scc1 Lpoll%=\n" \
  "v_mov_b32 v48, 0\n" \
  "Lseen%=:\n" \
  "buffer_load_dword v20, s[28:31], s66 offen lds\n" \
  "s_add_u32 s66, s66, 0x40000\n" \
  "buffer_load_dword v20, s[28:31], s66 offen lds\n" \
  "s_add_u32 s66, s66, 0x40000\n" \
  "v_bfe_u32 v49, v48, 11, 11\n" \
  "v_and_b32 v50, 0x7ff, v48\n" \
  "v_bfe_u32 v52, v13, 4, 1\n" \
  "v_add_u32_dpp v87, v49, v49 row_shr:1 row_mask:0xf bank_mask:0xf bound_ctrl:1\n" \
  "v_lshl_add_u32 v50, v50, 2, v40\n" \
  "v_lshlrev_b32 v53, v52, v49\n" \
  "v_add_u32_dpp v87, v87, v87 row_shr:2 row_mask:0xf bank_mask:0xf bound_ctrl:1\n" \
  "v_sub_u32 v49, v49, v52\n" \
  "v_and_b32 v85, -8, v41\n" \
  "v_add_u32_dpp v87, v87, v87 row_shr:4 row_mask:0xf bank_mask:0xf bound_ctrl:1\n" \
  "v_add_u32 v85, v85, v54\n" \
  "s_nop 0\n" \
  "v_add_u32_dpp v87, v87, v87 row_shr:8 row_mask:0xf bank_mask:0xf bound_ctrl:1\n" \
  "s_nop 1\n" \
  "v_add_u32_dpp v87, v87, v87 row_bcast:15 row_mask:0xa bank_mask:0xf\n" \
  "s_nop 1\n" \
  "v_add_u32_dpp v87, v87, v87 row_bcast:31 row_mask:0xc bank_mask:0xf\n" \
  "v_sub_u32 v53, v87, v53\n" \
  "v_readlane_b32 s62, v87, 63\n" \
  "v_lshl_add_u32 v53, v53, 1, v41\n" \
  "v_lshl_add_u32 v49, v49, 2, v53\n" \
  "s_lshr_b32 s62, s62, 1\n" \
  "s_cmp_lt_u32 s62, 256\n" \
  "s_cbranch_scc1 Ll0_%=\n" \
  "s_mov_b32 s62, 0\n" \
  "ds_or_b32 v94, v92\n" \
  "s_branch Ll2_%=\n" \
  "Ll0_%=:\n" \
  "v_cmp_lt_i32 vcc, v53, v49\n" \
  "s_mov_b64 exec, vcc\n" \
  "Ll1_%=:\n" \
  "ds_write_b32 v53, v50\n" \
  "v_add_u32 v53, 8, v53\n" \
  "v_cmp_lt_i32 vcc, v53, v49\n" \
  "v_add_u32 v50, 8, v50\n" \
  "s_and_b64 exec, vcc, vcc\n" \
  "s_cbranch_scc1 Ll1_%=\n" \
  "s_mov_b64 exec, -1\n" \
  "Ll2_%=:\n" \
  "ds_read2st64_b32 v[56:57], v85 offset1:1\n" \
  "ds_read2st64_b32 v[58:59], v85 offset0:2 offset1:3\n" \
  "v_mov_b32 v50, s62\n" \
  "s_mov_b64 exec, 1\n" \
  "ds_add_u32 v94, v50 offset:4\n" \
  "s_mov_b64 exec, -1\n" \
  "v_cmp_gt_i32 s[80:81], s62, v86\n" \
  "s_sub_i32 s63, s62, 64\n" \
  "v_cmp_gt_i32 s[82:83], s63, v86\n" \
  "s_sub_i32 s63, s62, 128\n" \
  "v_cmp_gt_i32 s[84:85], s63, v86\n" \
  "s_sub_i32 s63, s62, 192\n" \
  "v_cmp_gt_i32 s[86:87], s63, v86\n" \
  "v_bfrev_b32 v50, 4\n" \
  "s_waitcnt lgkmcnt(1)\n" \
  "v_cndmask_b32 v56, v50, v56, s[80:81]\n" \
  "v_cndmask_b32 v57, v50, v57, s[82:83]\n" \
  "v_cndmask_b32 v58, v50, v58, s[84:85]\n" \
  "v_cndmask_b32 v59, v50, v59, s[86:87]\n" \
  "s_sleep 4\n" \
  "v_mul_u32_u24 v18, 64, v55\n" \
  "s_mov_b32 s93, 256\n" \
  "s_mov_b64 s[88:89], 0\n" \
  "s_mov_b32 s63, 0xbfb8aa3b\n" \
  "v_lshlrev_b32 v100, 17, %[tid]\n" \
  "v_add_u32 v103, 0x3f810000, v100\n" \
  "v_add_u32 v102, 0x3f800000, v100\n" \
  "v_add_u32 v101, 0x3b810000, v100\n" \
  "v_add_u32 v100, 0x3b800000, v100\n" \
  "v_mul_f32 v104, s63, v100\n" \
  "v_mul_f32 v105, s63, v101\n" \
  "v_mul_f32 v106, s63, v102\n" \
  "v_mul_f32 v107, s63, v103\n" \
  "v_exp_f32 v104, v104\n" \
  "v_exp_f32 v105, v105\n" \
  "v_exp_f32 v106, v106\n" \
  "v_exp_f32 v107, v107\n" \
  "v_lshlrev_b32 v96, 2, %[tid]\n" \
  "v_add_f32 v108, 1.0, v104\n" \
  "v_add_f32 v109, 1.0, v105\n" \
  "v_add_f32 v110, 1.0, v106\n" \
  "v_add_f32 v111, 1.0, v107\n" \
  "v_rcp_f32 v108, v108\n" \
  "v_rcp_f32 v109, v109\n" \
  "v_rcp_f32 v110, v110\n" \
  "v_rcp_f32 v111, v111\n" \
  "v_add_u32 v97, 0x10000, v96\n" \
  "v_pk_mul_f32 v[100:101], v[100:101], v[108:109]\n" \
  "v_pk_mul_f32 v[102:103], v[102:103], v[110:111]\n" \
  "v_pk_mul_f32 v[104:105], v[104:105], v[100:101] neg_lo:[0,1] neg_hi:[0,1]\n" \
  "v_pk_mul_f32 v[106:107], v[106:107], v[102:103] neg_lo:[0,1] neg_hi:[0,1]\n" \
  "v_cvt_pk_bf16_f32 v100, v100, v101\n" \
  "v_cvt_pk_bf16_f32 v102, v102, v103\n" \
  "v_cvt_pk_bf16_f32 v104, v104, v105\n" \
  "v_cvt_pk_bf16_f32 v106, v106, v107\n" \
  "ds_write2st64_b32 v96, v100, v102 offset0:119 offset1:127\n" \
  "ds_write2st64_b32 v97, v104, v106 offset0:119 offset1:127\n" \
  "buffer_load_dwordx2 v[44:45], v18, s[24:27], s93 offen sc1\n" \
  "v_lshrrev_b32 v50, 8, v13\n" \
  "v_mul_u32_u24 v50, 2112, v50\n" \
  "v_and_or_b32 v50, v54, 60, v50\n" \
  "s_lshl3_add_u32 s63, s45, 45776\n" \
  "v_add_u32 v50, s63, v50\n" \
  "v_add_u32 v52, 94832, v50\n" \
  "ds_read2st64_b32 v[128:129], v50 offset0:0 offset1:1\n" \
  "ds_read2st64_b32 v[130:131], v50 offset0:2 offset1:3\n" \
  "ds_read2st64_b32 v[132:133], v50 offset0:4 offset1:5\n" \
  "ds_read2st64_b32 v[134:135], v50 offset0:6 offset1:7\n" \
  "ds_read2st64_b32 v[136:137], v50 offset0:33 offset1:34\n" \
  "ds_read2st64_b32 v[138:139], v50 offset0:35 offset1:36\n" \
  "ds_read2st64_b32 v[140:141], v50 offset0:37 offset1:38\n" \
  "ds_read2st64_b32 v[142:143], v50 offset0:39 offset1:40\n" \
  "ds_read2st64_b32 v[144:145], v52 offset0:0 offset1:1\n" \
  "ds_read2st64_b32 v[146:147], v52 offset0:2 offset1:3\n" \
  "ds_read2st64_b32 v[148:149], v52 offset0:4 offset1:5\n" \
  "ds_read2st64_b32 v[150:151], v52 offset0:6 offset1:7\n" \
  "s_waitcnt lgkmcnt(8)\n" \
  "ds_read2st64_b32 v[152:153], v52 offset0:33 offset1:34\n" \
  "ds_read2st64_b32 v[154:155], v52 offset0:35 offset1:36\n" \
  "ds_read2st64_b32 v[156:157], v52 offset0:37 offset1:38\n" \
  "ds_read2st64_b32 v[158:159], v52 offset0:39 offset1:40\n" \
  "v_mov_b64 v[160:161], 0\n" \
  "v_mov_b64 v[162:163], 0\n" \
  "v_mov_b64 v[164:165], 0\n" \
  "v_mov_b64 v[166:167], 0\n" \
  "v_mov_b64 v[168:169], 0\n" \
  "v_mov_b64 v[170:171], 0\n" \
  "v_mov_b64 v[172:173], 0\n" \
  "v_mov_b64 v[174:175], 0\n" \
  "v_mov_b64 v[176:177], 0\n" \
  "v_mov_b64 v[178:179], 0\n" \
  "v_mov_b64 v[180:181], 0\n" \
  "v_mov_b64 v[182:183], 0\n" \
  "v_mov_b64 v[184:185], 0\n" \
  "v_mov_b64 v[186:187], 0\n" \
  "v_mov_b64 v[188:189], 0\n" \
  "v_mov_b64 v[190:191], 0\n" \
  "s_mov_b32 s63, 0x05040100\n" \
  "s_mov_b32 s72, 0x07060302\n" \
  "s_waitcnt lgkmcnt(0)\n" \
  "v_perm_b32 v216, v129, v128, s63\n" \
  "v_perm_b32 v220, v129, v128, s72\n" \
  "v_perm_b32 v217, v131, v130, s63\n" \
  "v_perm_b32 v221, v131, v130, s72\n" \
  "v_perm_b32 v218, v133, v132, s63\n" \
  "v_perm_b32 v222, v133, v132, s72\n" \
  "v_perm_b32 v219, v135, v134, s63\n" \
  "v_perm_b32 v223, v135, v134, s72\n" \
  "v_perm_b32 v224, v137, v136, s63\n" \
  "v_perm_b32 v228, v137, v136, s72\n" \
  "v_perm_b32 v225, v139, v138, s63\n" \
  "v_perm_b32 v229, v139, v138, s72\n" \
  "v_perm_b32 v226, v141, v140, s63\n" \
  "v_perm_b32 v230, v141, v140, s72\n" \
  "v_perm_b32 v227, v143, v142, s63\n" \
  "v_perm_b32 v231, v143, v142, s72\n" \
  "v_perm_b32 v232, v145, v144, s63\n" \
  "v_perm_b32 v236, v145, v144, s72\n" \
  "v_perm_b32 v233, v147, v146, s63\n" \
  "v_perm_b32 v237, v147, v146, s72\n" \
  "v_perm_b32 v234, v149, v148, s63\n" \
  "v_perm_b32 v238, v149, v148, s72\n" \
  "v_perm_b32 v235, v151, v150, s63\n" \
  "v_perm_b32 v239, v151, v150, s72\n" \
  "v_perm_b32 v240, v153, v152, s63\n" \
  "v_perm_b32 v244, v153, v152, s72\n" \
  "v_perm_b32 v241, v155, v154, s63\n" \
  "v_perm_b32 v245, v155, v154, s72\n" \
  "v_perm_b32 v242, v157, v156, s63\n" \
  "v_perm_b32 v246, v157, v156, s72\n" \
  "v_perm_b32 v243, v159, v158, s63\n" \
  "v_perm_b32 v247, v159, v158, s72\n" \
  "v_mov_b64 v[128:129], 0\n" \
  "v_mov_b64 v[130:131], 0\n" \
  "v_mov_b64 v[132:133], 0\n" \
  "v_mov_b64 v[134:135], 0\n" \
  "v_mov_b64 v[136:137], 0\n" \
  "v_mov_b64 v[138:139], 0\n" \
  "v_mov_b64 v[140:141], 0\n" \
  "v_mov_b64 v[142:143], 0\n" \
  "v_mov_b64 v[144:145], 0\n" \
  "v_mov_b64 v[146:147], 0\n" \
  "v_mov_b64 v[148:149], 0\n" \
  "v_mov_b64 v[150:151], 0\n" \
  "v_mov_b64 v[152:153], 0\n" \
  "v_mov_b64 v[154:155], 0\n" \
  "v_mov_b64 v[156:157], 0\n" \
  "v_mov_b64 v[158:159], 0\n" \
  "Ld1_%=:\n" \
  "buffer_load_dwordx2 v[46:47], v18, s[24:27], s93 offen sc1\n" \
  "s_waitcnt vmcnt(1)\n" \
  "v_cmp_eq_u32 s[90:91], s65, v45\n" \
  "v_cmp_eq_u32 vcc, s64, v44\n" \
  "s_mov_b32 s63, 606464\n" \
  "s_and_b64 vcc, vcc, s[90:91]\n" \
  "s_or_b64 s[88:89], s[88:89], vcc\n" \
  "s_cmp_eq_u64 s[88:89], -1\n" \
  "s_cbranch_scc1 Ld2_%=\n" \
  "v_cndmask_b32 v18, v18, v51, vcc\n" \
  "buffer_load_dwordx2 v[44:45], v18, s[24:27], s93 offen sc1\n" \
  "s_waitcnt vmcnt(1)\n" \
  "v_cmp_eq_u32 s[90:91], s65, v47\n" \
  "v_cmp_eq_u32 vcc, s64, v46\n" \
  "s_mov_b32 s63, 606464\n" \
  "s_and_b64 vcc, vcc, s[90:91]\n" \
  "s_or_b64 s[88:89], s[88:89], vcc\n" \
  "s_cmp_eq_u64 s[88:89], -1\n" \
  "s_cbranch_scc1 Ld2_%=\n" \
  "v_cndmask_b32 v18, v18, v51, vcc\n" \
  "s_add_u32 s92, s92, 1\n" \
  "s_cmp_lt_u32 s92, 4096\n" \
  "s_cbranch_scc1 Ld1_%=\n" \
  "s_mov_b64 s[80:81], 0\n" \
  "s_mov_b64 s[82:83], 0\n" \
  "s_mov_b64 s[84:85], 0\n" \
  "s_mov_b64 s[86:87], 0\n" \
  "Ld2_%=:\n" \
  "s_mov_b32 s93, 1917184\n" \
  "s_cmp_le_i32 s62, 192\n" \
  "s_cbranch_scc1 Lk595_%=\n" \
  "buffer_load_dword v63, v59, s[24:27], s63 offen sc1\n" \
  "v_lshlrev_b32 v59, 2, v59\n" \
  "buffer_load_dwordx4 v[204:207], v59, s[24:27], s93 offen sc1\n" \
  "Lk595_%=:\n" \
  "buffer_load_dword v60, v56, s[24:27], s63 offen sc1\n" \
  "buffer_load_dword v61, v57, s[24:27], s63 offen sc1\n" \
  "buffer_load_dword v62, v58, s[24:27], s63 offen sc1\n" \
  "v_lshlrev_b32 v56, 2, v56\n" \
  "v_lshlrev_b32 v57, 2, v57\n" \
  "v_lshlrev_b32 v58, 2, v58\n" \
  "s_lshl_b32 s75, s44, 8\n" \
  "s_add_u32 s75, s75, 106816\n" \
  "s_mov_b32 s52, s28\n" \
  "s_or_b32 s53, s29, 0x4000000\n" \
  "s_mov_b32 s54, s35\n" \
  "s_mov_b32 s55, 0x20000\n" \
  "s_lshl_b32 s63, s67, 10\n" \
  "s_add_u32 s56, s32, s63\n" \
  "s_addc_u32 s57, s33, 0\n" \
  "s_lshl_b32 s58, s68, 10\n" \
  "s_mov_b32 s59, 0x20000\n" \
  "s_lshl_b32 s67, s67, 24\n" \
  "s_mov_b32 s70, 0\n" \
  "s_mul_i32 s63, s44, 3328\n" \
  "s_add_u32 s63, s63, 37584\n" \
  "v_add_u32 v21, s63, v13\n" \
  "v_and_b32 v15, 0xf0, v54\n" \
  "v_add_u32 v15, s63, v15\n" \
  "v_and_b32 v23, 0xf0, v13\n" \
  "v_add3_u32 v16, v23, s63, 8\n" \
  "s_mul_i32 s63, s44, 832\n" \
  "s_add_u32 s63, s63, 100096\n" \
  "v_mov_b32 v14, s63\n" \
  "v_and_b32 v50, 0x300, v13\n" \
  "v_and_or_b32 v19, v54, 60, v50\n" \
  "v_add_u32 v19, 108864, v19\n" \
  "v_lshl_add_u32 v23, v50, 2, v23\n" \
  "s_lshl_b32 s63, s45, 5\n" \
  "v_add_u32 v23, s63, v23\n" \
  "v_bfe_u32 v50, v86, 2, 2\n" \
  "v_mul_u32_u24 v22, 1056, v50\n" \
  "v_and_b32 v50, 48, v13\n" \
  "v_lshl_add_u32 v22, v50, 2, v22\n" \
  "v_and_b32 v50, 48, v86\n" \
  "s_mov_b32 s63, 108864\n" \
  "v_add3_u32 v22, v22, v50, s63\n" \
  "v_mul_u32_u24 v50, 26, v86\n" \
  "v_lshrrev_b32 v50, 8, v50\n" \
  "v_mad_i32_i24 v50, v50, -10, v86\n" \
  "v_lshlrev_b32 v42, 19, v50\n" \
  "s_lshl_b32 s63, s73, 28\n" \
  "s_sub_u32 s67, s67, s63\n" \
  "s_cmp_lt_u32 s92, 4096\n" \
  "s_cbranch_scc1 Lq2_%=\n" \
  "v_mov_b32 v53, 3\n" \
  "ds_or_b32 v94, v53\n" \
  "Lq2_%=:\n" \
  "v_mov_b32 v50, 2\n" \
  "s_waitcnt vmcnt(0)\n" \
  "v_subrev_u32 v60, s67, v60\n" \
  "s_mov_b64 exec, s[80:81]\n" \
  "v_lshlrev_b32_sdwa v64, v50, v60 dst_sel:DWORD dst_unused:UNUSED_PAD src0_sel:DWORD src1_sel:BYTE_3\n" \
  "ds_add_rtn_u32 v80, v64, v93 offset:35584\n" \
  "v_subrev_u32 v61, s67, v61\n" \
  "s_mov_b64 exec, s[82:83]\n" \
  "v_lshlrev_b32_sdwa v65, v50, v61 dst_sel:DWORD dst_unused:UNUSED_PAD src0_sel:DWORD src1_sel:BYTE_3\n" \
  "ds_add_rtn_u32 v81, v65, v93 offset:35584\n" \
  "v_subrev_u32 v62, s67, v62\n" \
  "s_mov_b64 exec, s[84:85]\n" \
  "v_lshlrev_b32_sdwa v66, v50, v62 dst_sel:DWORD dst_unused:UNUSED_PAD src0_sel:DWORD src1_sel:BYTE_3\n" \
  "ds_add_rtn_u32 v82, v66, v93 offset:35584\n" \
  "s_cmp_le_i32 s62, 192\n" \
  "s_cbranch_scc1 Lk668_%=\n" \
  "v_subrev_u32 v63, s67, v63\n" \
  "s_mov_b64 exec, s[86:87]\n" \
  "v_lshlrev_b32_sdwa v67, v50, v63 dst_sel:DWORD dst_unused:UNUSED_PAD src0_sel:DWORD src1_sel:BYTE_3\n" \
  "ds_add_rtn_u32 v83, v67, v93 offset:35584\n" \
  "Lk668_%=:\n" \
  "s_mov_b64 exec, -1\n" \
  "buffer_load_dwordx4 v[192:195], v56, s[24:27], s93 offen sc1\n" \
  "buffer_load_dwordx4 v[196:199], v57, s[24:27], s93 offen sc1\n" \
  "buffer_load_dwordx4 v[200:203], v58, s[24:27], s93 offen sc1\n" \
  "s_mov_b32 s63, 0x01010101\n" \
  "s_waitcnt lgkmcnt(0)\n" \
  "s_barrier\n" \
  "ds_read2st64_b32 v[44:45], v54 offset0:139 offset1:140\n" \
  "ds_read_b32 v86, v94 offset:4\n" \
  "ds_bpermute_b32 v76, v64, v42\n" \
  "ds_bpermute_b32 v77, v65, v42\n" \
  "ds_bpermute_b32 v78, v66, v42\n" \
  "ds_bpermute_b32 v79, v67, v42\n" \
  "buffer_load_dword v20, s[28:31], s66 offen lds\n" \
  "s_add_u32 s66, s66, 0x40000\n" \
  "buffer_load_dword v20, s[28:31], s66 offen lds\n" \
  "s_add_u32 s66, s66, 0x40000\n" \
  "v_bfe_u32 v80, v80, s45, 8\n" \
  "v_bfe_u32 v81, v81, s45, 8\n" \
  "v_bfe_u32 v82, v82, s45, 8\n" \
  "v_bfe_u32 v83, v83, s45, 8\n" \
  "s_waitcnt lgkmcnt(4)\n" \
  "v_dot4_u32_u8 v85, v44, s63, 0\n" \
  "v_dot4_u32_u8 v85, v45, s63, v85\n" \
  "v_dot4_u32_u8 v46, v44, s46, 0\n" \
  "v_dot4_u32_u8 v46, v45, s47, v46\n" \
  "v_add_u32 v86, 31, v86\n" \
  "v_add_u32_dpp v87, v85, v85 row_shr:1 row_mask:0xf bank_mask:0xf bound_ctrl:1\n" \
  "v_lshrrev_b32 v86, 5, v86\n" \
  "v_cvt_f32_u32 v86, v86\n" \
  "v_add_u32_dpp v87, v87, v87 row_shr:2 row_mask:0xf bank_mask:0xf bound_ctrl:1\n" \
  "v_rcp_f32 v86, v86\n" \
  "v_mul_hi_u32_u24 v68, s37, v60\n" \
  "v_add_u32_dpp v87, v87, v87 row_shr:4 row_mask:0xf bank_mask:0xf bound_ctrl:1\n" \
  "v_subrev_u32 v68, s71, v68\n" \
  "v_and_b32 v68, 15, v68\n" \
  "v_add_u32_dpp v87, v87, v87 row_shr:8 row_mask:0xf bank_mask:0xf bound_ctrl:1\n" \
  "v_lshl_add_u32 v72, v68, 1, s75\n" \
  "v_lshl_add_u32 v68, v68, 3, s73\n" \
  "v_add_u32_dpp v87, v87, v87 row_bcast:15 row_mask:0xa bank_mask:0xf\n" \
  "v_mul_hi_u32_u24 v69, s37, v61\n" \
  "v_subrev_u32 v69, s71, v69\n" \
  "v_add_u32_dpp v87, v87, v87 row_bcast:31 row_mask:0xc bank_mask:0xf\n" \
  "v_sub_u32 v46, v87, v46\n" \
  "ds_bpermute_b32 v88, v64, v46\n" \
  "ds_bpermute_b32 v89, v65, v46\n" \
  "ds_bpermute_b32 v90, v66, v46\n" \
  "ds_bpermute_b32 v91, v67, v46\n" \
  "v_mul_f32 v86, 0x48800000, v86\n" \
  "v_cvt_u32_f32 v86, v86\n" \
  "v_add_u32 v86, 1, v86\n" \
  "v_and_b32 v69, 15, v69\n" \
  "v_lshl_add_u32 v73, v69, 1, s75\n" \
  "v_lshl_add_u32 v69, v69, 3, s73\n" \
  "v_mul_hi_u32_u24 v70, s37, v62\n" \
  "v_subrev_u32 v70, s71, v70\n" \
  "v_and_b32 v70, 15, v70\n" \
  "v_lshl_add_u32 v74, v70, 1, s75\n" \
  "v_lshl_add_u32 v70, v70, 3, s73\n" \
  "v_mul_hi_u32_u24 v71, s37, v63\n" \
  "v_subrev_u32 v71, s71, v71\n" \
  "v_and_b32 v71, 15, v71\n" \
  "v_lshl_add_u32 v75, v71, 1, s75\n" \
  "v_lshl_add_u32 v71, v71, 3, s73\n" \
  "s_mov_b32 s63, 0xffff\n" \
  "s_waitcnt lgkmcnt(4)\n" \
  "v_and_or_b32 v76, v60, s63, v76\n" \
  "v_and_or_b32 v77, v61, s63, v77\n" \
  "v_and_or_b32 v78, v62, s63, v78\n" \
  "v_and_or_b32 v79, v63, s63, v79\n" \
  "s_waitcnt lgkmcnt(3)\n" \
  "v_add_u32 v80, v80, v88\n" \
  "v_mul_u32_u24 v80, v80, v86\n" \
  "s_waitcnt lgkmcnt(2)\n" \
  "v_add_u32 v81, v81, v89\n" \
  "v_mul_u32_u24 v81, v81, v86\n" \
  "s_waitcnt lgkmcnt(1)\n" \
  "v_add_u32 v82, v82, v90\n" \
  "v_mul_u32_u24 v82, v82, v86\n" \
  "s_waitcnt lgkmcnt(0)\n" \
  "v_add_u32 v83, v83, v91\n" \
  "v_mul_u32_u24 v83, v83, v86\n" \
  "v_lshrrev_b32 v60, 20, v80\n" \
  "s_mov_b64 exec, s[80:81]\n" \
  "v_lshl_add_u32 v68, v60, 7, v68\n" \
  "ds_add_rtn_u32 v80, v68, v93 offset:34560\n" \
  "v_lshrrev_b32 v61, 20, v81\n" \
  "s_mov_b64 exec, s[82:83]\n" \
  "v_lshl_add_u32 v69, v61, 7, v69\n" \
  "ds_add_rtn_u32 v81, v69, v93 offset:34560\n" \
  "v_lshrrev_b32 v62, 20, v82\n" \
  "s_mov_b64 exec, s[84:85]\n" \
  "v_lshl_add_u32 v70, v62, 7, v70\n" \
  "ds_add_rtn_u32 v82, v70, v93 offset:34560\n" \
  "s_cmp_le_i32 s62, 192\n" \
  "s_cbranch_scc1 Lk769_%=\n" \
  "v_lshrrev_b32 v63, 20, v83\n" \
  "s_mov_b64 exec, s[86:87]\n" \
  "v_lshl_add_u32 v71, v63, 7, v71\n" \
  "ds_add_rtn_u32 v83, v71, v93 offset:34560\n" \
  "Lk769_%=:\n" \
  "s_mov_b64 exec, -1\n" \
  "buffer_load_dword v20, s[28:31], s66 offen lds\n" \
  "s_add_u32 s66, s66, 0x40000\n" \
  "v_lshl_add_u32 v72, v60, 5, v72\n" \
  "v_lshl_add_u32 v73, v61, 5, v73\n" \
  "v_lshl_add_u32 v74, v62, 5, v74\n" \
  "v_lshl_add_u32 v75, v63, 5, v75\n" \
  "v_sub_u32 v85, v87, v85\n" \
  "v_add_u32 v87, -1, v87\n" \
  "v_max_i32 v87, v87, v85\n" \
  "v_mul_u32_u24 v84, v85, v86\n" \
  "v_mul_u32_u24 v43, v87, v86\n" \
  "v_lshrrev_b32 v50, 1, v13\n" \
  "v_lshrrev_b32 v51, 3, v13\n" \
  "v_add_u32 v51, s75, v51\n" \
  "v_lshrrev_b32 v52, 8, v13\n" \
  "v_mul_u32_u24 v52, 0xd000d0, v52\n" \
  "v_add_u32 v52, 0x3400000, v52\n" \
  "s_mov_b32 s63, 0x01010101\n" \
  "s_waitcnt lgkmcnt(0)\n" \
  "s_barrier\n" \
  "ds_read_b64 v[88:89], v50 offset:34560\n" \
  "ds_read_b64 v[90:91], v50 offset:35072\n" \
  "v_bfe_u32 v80, v80, s45, 8\n" \
  "v_bfe_u32 v81, v81, s45, 8\n" \
  "v_bfe_u32 v82, v82, s45, 8\n" \
  "v_bfe_u32 v83, v83, s45, 8\n" \
  "s_lshl_b32 s72, s44, 4\n" \
  "s_or_b32 s72, s72, 15\n" \
  "s_lshl_b32 s73, s73, 2\n" \
  "s_waitcnt lgkmcnt(0)\n" \
  "v_dot4_u32_u8 v44, v88, s63, 0\n" \
  "v_dot4_u32_u8 v45, v90, s63, 0\n" \
  "v_dot4_u32_u8 v44, v89, s63, v44\n" \
  "v_dot4_u32_u8 v45, v91, s63, v45\n" \
  "v_dot4_u32_u8 v46, v88, s46, 0\n" \
  "v_dot4_u32_u8 v47, v90, s46, 0\n" \
  "v_dot4_u32_u8 v46, v89, s47, v46\n" \
  "v_lshl_or_b32 v44, v45, 16, v44\n" \
  "v_dot4_u32_u8 v47, v91, s47, v47\n" \
  "s_nop 0\n" \
  "v_add_u32_dpp v87, v44, v44 row_shr:1 row_mask:0xf bank_mask:0xf bound_ctrl:1\n" \
  "s_nop 0\n" \
  "v_lshl_or_b32 v46, v47, 16, v46\n" \
  "v_add_u32_dpp v87, v87, v87 row_shr:2 row_mask:0xf bank_mask:0xf bound_ctrl:1\n" \
  "v_sub_u32 v52, v52, v46\n" \
  "s_nop 0\n" \
  "v_add_u32_dpp v87, v87, v87 row_shr:4 row_mask:0xf bank_mask:0xf bound_ctrl:1\n" \
  "s_nop 1\n" \
  "v_add_u32_dpp v87, v87, v87 row_shr:8 row_mask:0xf bank_mask:0xf bound_ctrl:1\n" \
  "v_add_u32 v52, v52, v87\n" \
  "ds_write_b16 v51, v52\n" \
  "ds_write_b16_d16_hi v51, v52 offset:128\n" \
  "ds_read_u16 v72, v72\n" \
  "ds_read_u16 v73, v73\n" \
  "ds_read_u16 v74, v74\n" \
  "ds_read_u16 v75, v75\n" \
  "v_readlane_b32 s60, v87, s72\n" \
  "s_movk_i32 s63, 200\n" \
  "v_cmp_lt_u32_sdwa s[90:91], s63, v87 src0_sel:DWORD src1_sel:WORD_0\n" \
  "v_cmp_lt_u32_sdwa vcc, s63, v87 src0_sel:DWORD src1_sel:WORD_1\n" \
  "s_lshr_b32 s72, s60, s73\n" \
  "s_and_b32 s72, s72, 0xffff\n" \
  "s_add_u32 s60, s72, 3\n" \
  "s_lshr_b32 s60, s60, 2\n" \
  "s_max_u32 s60, s60, 1\n" \
  "s_lshl2_add_u32 s73, s60, 8\n" \
  "s_sub_u32 s73, s73, s72\n" \
  "s_bfm_b64 s[88:89], s73, 0\n" \
  "s_or_b64 vcc, vcc, s[90:91]\n" \
  "s_cbranch_scc0 Lo1_%=\n" \
  "s_mov_b64 s[80:81], 0\n" \
  "s_mov_b64 s[82:83], 0\n" \
  "s_mov_b64 s[84:85], 0\n" \
  "s_mov_b64 s[86:87], 0\n" \
  "s_mov_b64 s[88:89], 0xfff\n" \
  "s_mov_b32 s72, 0\n" \
  "s_mov_b32 s60, 1\n" \
  "ds_or_b32 v94, v92\n" \
  "Lo1_%=:\n" \
  "v_lshrrev_b32 v50, 2, v13\n" \
  "s_lshl_b32 s72, s72, 2\n" \
  "v_add3_u32 v50, v50, s72, v14\n" \
  "v_mov_b32 v51, 0x50ffff\n" \
  "s_mov_b32 s63, 100096\n" \
  "s_mov_b64 exec, s[88:89]\n" \
  "ds_write_b32 v50, v51\n" \
  "s_waitcnt lgkmcnt(1)\n" \
  "s_mov_b64 exec, s[80:81]\n" \
  "v_add_u32 v80, v80, v72\n" \
  "v_lshl_add_u32 v72, v80, 2, s63\n" \
  "ds_write_b32 v72, v76\n" \
  "s_mov_b64 exec, s[82:83]\n" \
  "v_add_u32 v81, v81, v73\n" \
  "v_lshl_add_u32 v73, v81, 2, s63\n" \
  "ds_write_b32 v73, v77\n" \
  "s_mov_b64 exec, s[84:85]\n" \
  "v_add_u32 v82, v82, v74\n" \
  "v_lshl_add_u32 v74, v82, 2, s63\n" \
  "ds_write_b32 v74, v78\n" \
  "s_cmp_le_i32 s62, 192\n" \
  "s_cbranch_scc1 Lk876_%=\n" \
  "s_mov_b64 exec, s[86:87]\n" \
  "v_add_u32 v83, v83, v75\n" \
  "v_lshl_add_u32 v75, v83, 2, s63\n" \
  "ds_write_b32 v75, v79\n" \
  "Lk876_%=:\n" \
  "s_mov_b64 exec, -1\n" \
  "s_sub_u32 s60, s60, 1\n" \
  "s_lshl_b32 s63, s60, 4\n" \
  "v_sub_u32 v50, v14, v13\n" \
  "v_add_u32 v50, s63, v50\n" \
  "s_waitcnt lgkmcnt(0)\n" \
  "s_barrier\n" \
  "ds_read_b128 v[56:59], v14\n" \
  "ds_read_b128 v[60:63], v14 offset:16\n" \
  "ds_read_b128 v[64:67], v50\n" \
  "v_lshlrev_b32 v208, 4, v80\n" \
  "v_lshlrev_b32 v209, 4, v81\n" \
  "v_lshlrev_b32 v210, 4, v82\n" \
  "v_lshlrev_b32 v211, 4, v83\n" \
  "s_waitcnt vmcnt(3)\n" \
  "s_mov_b64 exec, s[80:81]\n" \
  "ds_write_b128 v208, v[192:195] offset:37584\n" \
  "s_mov_b64 exec, s[82:83]\n" \
  "ds_write_b128 v209, v[196:199] offset:37584\n" \
  "s_mov_b64 exec, s[84:85]\n" \
  "ds_write_b128 v210, v[200:203] offset:37584\n" \
  "s_cmp_le_i32 s62, 192\n" \
  "s_cbranch_scc1 Lk904_%=\n" \
  "s_mov_b64 exec, s[86:87]\n" \
  "ds_write_b128 v211, v[204:207] offset:37584\n" \
  "Lk904_%=:\n" \
  "s_mov_b64 exec, -1\n" \
  "s_waitcnt lgkmcnt(4)\n" \
  "v_and_b32 v12, 0xffff, v56\n" \
  "buffer_load_dwordx4 v[96:99], v[12:13], s[52:55], 0 idxen offen\n" \
  "v_and_b32 v12, 0xffff, v57\n" \
  "buffer_load_dwordx4 v[100:103], v[12:13], s[52:55], 0 idxen offen\n" \
  "v_and_b32 v12, 0xffff, v58\n" \
  "buffer_load_dwordx4 v[104:107], v[12:13], s[52:55], 0 idxen offen\n" \
  "v_and_b32 v12, 0xffff, v59\n" \
  "buffer_load_dwordx4 v[108:111], v[12:13], s[52:55], 0 idxen offen\n" \
  "v_lshrrev_b32 v51, 20, v84\n" \
  "v_lshrrev_b32 v52, 20, v43\n" \
  "v_min_u32 v51, 7, v51\n" \
  "v_min_u32 v52, 7, v52\n" \
  "s_bfm_b64 s[88:89], s68, 0\n" \
  "v_mov_b32 v18, v94\n" \
  "v_cmp_ge_u32 s[90:91], s44, v51\n" \
  "v_cmp_le_u32 vcc, s44, v52\n" \
  "s_and_b64 s[88:89], s[88:89], s[90:91]\n" \
  "s_and_b64 s[88:89], s[88:89], vcc\n" \
  "s_ff1_i32_b64 s72, s[88:89]\n" \
  "s_flbit_i32_b64 s74, s[88:89]\n" \
  "s_xor_b32 s74, s74, 63\n" \
  "v_cmp_le_u32 vcc, 4, v52\n" \
  "s_add_u32 s77, s68, 3\n" \
  "s_lshr_b32 s77, s77, 2\n" \
  "s_ff1_i32_b64 s76, vcc\n" \
  "s_lshr_b32 s76, s76, 2\n" \
  "s_min_u32 s76, s76, s77\n" \
  "s_sub_u32 s78, s77, s76\n" \
  "s_min_u32 s78, s78, 2\n" \
  "s_add_u32 s63, s76, s78\n" \
  "s_cmp_lt_u32 s44, 4\n" \
  "s_cselect_b32 s77, s76, s77\n" \
  "s_cselect_b32 s76, 0, s63\n" \
  "s_cselect_b32 s78, s78, 0\n" \
  "s_sub_u32 s77, s77, s76\n" \
  "s_mov_b32 s63, 0x0c0c0602\n" \
  "s_waitcnt lgkmcnt(0)\n" \
  "s_barrier\n" \
  "ds_read_b64 v[92:93], v16\n" \
  "ds_read_b64 v[94:95], v15\n" \
  "v_and_b32 v12, 0xffff, v60\n" \
  "buffer_load_dwordx4 v[112:115], v[12:13], s[52:55], 0 idxen offen\n" \
  "v_and_b32 v12, 0xffff, v61\n" \
  "buffer_load_dwordx4 v[116:119], v[12:13], s[52:55], 0 idxen offen\n" \
  "v_and_b32 v12, 0xffff, v62\n" \
  "buffer_load_dwordx4 v[120:123], v[12:13], s[52:55], 0 idxen offen\n" \
  "v_and_b32 v12, 0xffff, v63\n" \
  "buffer_load_dwordx4 v[124:127], v[12:13], s[52:55], 0 idxen offen\n" \
  "ds_read_u16 v12, v14 offset:32\n" \
  "v_perm_b32 v64, v65, v64, s63\n" \
  "v_perm_b32 v66, v67, v66, s63\n" \
  "v_readlane_b32 s93, v52, s74\n" \
  "v_readlane_b32 s75, v51, s72\n" \
  "v_lshl_or_b32 v17, v66, 16, v64\n" \
  "v_readlane_b32 s63, v52, s72\n" \
  "v_readlane_b32 s73, v42, s74\n" \
  "v_readlane_b32 s61, v17, s60\n" \
  "s_sub_u32 s74, s74, s72\n" \
  "s_add_u32 s72, s72, s74\n" \
  "s_sub_u32 s75, s44, s75\n" \
  "s_cmp_eq_u32 s63, s44\n" \
  "s_cselect_b32 s75, s75, 0\n" \
  "s_lshr_b32 s73, s73, 16\n" \
  "s_mul_i32 s72, s72, 1056\n" \
  "s_or_b32 s63, %[blk], s44\n" \
  "s_cbranch_scc0 Lep0_%=\n" \
  "Lgo%=:\n" \
  "s_waitcnt lgkmcnt(0)\n" \
  "v_mfma_f32_4x4x4_16b_bf16 v[56:59], v[92:93], v[32:33], 0 cbsz:4\n" \
  "v_mfma_f32_4x4x4_16b_bf16 v[60:63], v[92:93], v[36:37], 0 cbsz:4\n" \
  "v_mfma_f32_4x4x4_16b_bf16 v[64:67], v[92:93], v[32:33], 0 cbsz:4 abid:1\n" \
  "v_mfma_f32_4x4x4_16b_bf16 v[68:71], v[92:93], v[36:37], 0 cbsz:4 abid:1\n" \
  "v_mfma_f32_4x4x4_16b_bf16 v[84:87], v[94:95], v[24:25], 0\n" \
  "v_mfma_f32_4x4x4_16b_bf16 v[88:91], v[94:95], v[26:27], 0\n" \
  "v_mfma_f32_4x4x4_16b_bf16 v[48:51], v[94:95], v[28:29], 0\n" \
  "v_mfma_f32_4x4x4_16b_bf16 v[52:55], v[94:95], v[30:31], 0\n" \
  "s_nop 1\n" \
  "v_cvt_pk_bf16_f32 v57, v56, v57\n" \
  "v_lshlrev_b32_sdwa v56, 1, v57 dst_sel:DWORD dst_unused:UNUSED_PAD src0_sel:DWORD src1_sel:WORD_0\n" \
  "v_lshlrev_b32_sdwa v57, 1, v57 dst_sel:DWORD dst_unused:UNUSED_PAD src0_sel:DWORD src1_sel:WORD_1\n" \
  "ds_read_u16_d16_hi v56, v56\n" \
  "ds_read_u16_d16_hi v57, v57\n" \
  "v_cvt_pk_bf16_f32 v59, v58, v59\n" \
  "v_lshlrev_b32_sdwa v58, 1, v59 dst_sel:DWORD dst_unused:UNUSED_PAD src0_sel:DWORD src1_sel:WORD_0\n" \
  "v_lshlrev_b32_sdwa v59, 1, v59 dst_sel:DWORD dst_unused:UNUSED_PAD src0_sel:DWORD src1_sel:WORD_1\n" \
  "ds_read_u16_d16_hi v58, v58\n" \
  "ds_read_u16_d16_hi v59, v59\n" \
  "v_cvt_pk_bf16_f32 v61, v60, v61\n" \
  "v_lshlrev_b32_sdwa v60, 1, v61 dst_sel:DWORD dst_unused:UNUSED_PAD src0_sel:DWORD src1_sel:WORD_0\n" \
  "v_lshlrev_b32_sdwa v61, 1, v61 dst_sel:DWORD dst_unused:UNUSED_PAD src0_sel:DWORD src1_sel:WORD_1\n" \
  "ds_read_u16_d16_hi v60, v60\n" \
  "ds_read_u16_d16_hi v61, v61\n" \
  "v_cvt_pk_bf16_f32 v63, v62, v63\n" \
  "v_lshlrev_b32_sdwa v62, 1, v63 dst_sel:DWORD dst_unused:UNUSED_PAD src0_sel:DWORD src1_sel:WORD_0\n" \
  "v_lshlrev_b32_sdwa v63, 1, v63 dst_sel:DWORD dst_unused:UNUSED_PAD src0_sel:DWORD src1_sel:WORD_1\n" \
  "ds_read_u16_d16_hi v62, v62\n" \
  "ds_read_u16_d16_hi v63, v63\n" \
  "v_cvt_pk_bf16_f32 v76, v84, v88\n" \
  "v_cvt_pk_bf16_f32 v77, v48, v52\n" \
  "s_mov_b32 s63, 0x7fff7fff\n" \
  "v_and_b32 v41, s63, v194\n" \
  "v_and_b32 v42, s63, v195\n" \
  "v_pk_max_u16 v40, v41, v42\n" \
  "v_and_b32 v41, s63, v198\n" \
  "v_and_b32 v42, s63, v199\n" \
  "v_pk_max_u16 v41, v41, v40\n" \
  "v_pk_max_u16 v40, v41, v42\n" \
  "v_and_b32 v41, s63, v202\n" \
  "v_and_b32 v42, s63, v203\n" \
  "v_pk_max_u16 v41, v41, v40\n" \
  "v_pk_max_u16 v40, v41, v42\n" \
  "s_cmp_le_i32 s62, 192\n" \
  "s_cbranch_scc1 Lk1022_%=\n" \
  "v_and_b32 v41, s63, v206\n" \
  "v_and_b32 v42, s63, v207\n" \
  "v_pk_max_u16 v41, v41, v40\n" \
  "v_pk_max_u16 v40, v41, v42\n" \
  "Lk1022_%=:\n" \
  "v_max_u16_sdwa v40, v40, v40 dst_sel:DWORD dst_unused:UNUSED_PAD src0_sel:WORD_0 src1_sel:WORD_1\n" \
  "s_nop 0\n" \
  "v_cmp_lt_i32 vcc, s69, v40\n" \
  "s_cmp_eq_u64 vcc, 0\n" \
  "s_cselect_b32 s74, s74, -1\n" \
  "v_mov_b64 v[192:193], 0\n" \
  "v_mov_b64 v[194:195], 0\n" \
  "v_mov_b64 v[196:197], 0\n" \
  "v_mov_b64 v[198:199], 0\n" \
  "v_mov_b64 v[200:201], 0\n" \
  "v_mov_b64 v[202:203], 0\n" \
  "v_mov_b64 v[204:205], 0\n" \
  "v_mov_b64 v[206:207], 0\n" \
  "s_waitcnt vmcnt(7) lgkmcnt(0)\n" \
  "v_mfma_f32_4x4x4_16b_bf16 v[48:51], v[76:77], v[96:97], 0 cbsz:4\n" \
  "v_mfma_f32_4x4x4_16b_bf16 v[52:55], v[76:77], v[98:99], 0 cbsz:4\n" \
  "s_nop 4\n" \
  "Lbody%=:\n" \
  "s_set_gpr_idx_on s61, gpr_idx(SRC2,DST)\n" \
  "v_pk_fma_f32 v[128:129], v[56:57], v[48:49], v[128:129] op_sel:[0,0,0] op_sel_hi:[0,1,1]\n" \
  "v_pk_fma_f32 v[130:131], v[56:57], v[50:51], v[130:131] op_sel:[0,0,0] op_sel_hi:[0,1,1]\n" \
  "v_pk_fma_f32 v[132:133], v[60:61], v[52:53], v[132:133] op_sel:[0,0,0] op_sel_hi:[0,1,1]\n" \
  "v_pk_fma_f32 v[134:135], v[60:61], v[54:55], v[134:135] op_sel:[0,0,0] op_sel_hi:[0,1,1]\n" \
  "s_set_gpr_idx_off\n" \
  "s_waitcnt vmcnt(6) lgkmcnt(2)\n" \
  "buffer_load_dwordx4 v[96:99], v[12:13], s[52:55], 0 idxen offen\n" \
  "ds_read_u16 v12, v14 offset:36\n" \
  "s_lshr_b32 s61, s61, 8\n" \
  "v_mfma_f32_4x4x4_16b_bf16 v[48:51], v[76:77], v[100:101], 0 cbsz:4 abid:1\n" \
  "v_mfma_f32_4x4x4_16b_bf16 v[52:55], v[76:77], v[102:103], 0 cbsz:4 abid:1\n" \
  "v_cvt_pk_bf16_f32 v65, v64, v65\n" \
  "v_lshlrev_b32_sdwa v64, 1, v65 dst_sel:DWORD dst_unused:UNUSED_PAD src0_sel:DWORD src1_sel:WORD_0\n" \
  "v_lshlrev_b32_sdwa v65, 1, v65 dst_sel:DWORD dst_unused:UNUSED_PAD src0_sel:DWORD src1_sel:WORD_1\n" \
  "ds_read_u16_d16_hi v64, v64\n" \
  "ds_read_u16_d16_hi v65, v65\n" \
  "s_set_gpr_idx_on s61, gpr_idx(SRC2,DST)\n" \
  "v_pk_fma_f32 v[128:129], v[56:57], v[48:49], v[128:129] op_sel:[1,0,0] op_sel_hi:[1,1,1]\n" \
  "v_pk_fma_f32 v[130:131], v[56:57], v[50:51], v[130:131] op_sel:[1,0,0] op_sel_hi:[1,1,1]\n" \
  "v_pk_fma_f32 v[132:133], v[60:61], v[52:53], v[132:133] op_sel:[1,0,0] op_sel_hi:[1,1,1]\n" \
  "v_pk_fma_f32 v[134:135], v[60:61], v[54:55], v[134:135] op_sel:[1,0,0] op_sel_hi:[1,1,1]\n" \
  "s_set_gpr_idx_off\n" \
  "s_waitcnt vmcnt(6) lgkmcnt(2)\n" \
  "buffer_load_dwordx4 v[100:103], v[12:13], s[52:55], 0 idxen offen\n" \
  "ds_read_u16 v12, v14 offset:40\n" \
  "s_lshr_b32 s61, s61, 8\n" \
  "v_mfma_f32_4x4x4_16b_bf16 v[48:51], v[76:77], v[104:105], 0 cbsz:4 abid:2\n" \
  "v_mfma_f32_4x4x4_16b_bf16 v[52:55], v[76:77], v[106:107], 0 cbsz:4 abid:2\n" \
  "v_cvt_pk_bf16_f32 v69, v68, v69\n" \
  "v_lshlrev_b32_sdwa v68, 1, v69 dst_sel:DWORD dst_unused:UNUSED_PAD src0_sel:DWORD src1_sel:WORD_0\n" \
  "v_lshlrev_b32_sdwa v69, 1, v69 dst_sel:DWORD dst_unused:UNUSED_PAD src0_sel:DWORD src1_sel:WORD_1\n" \
  "ds_read_u16_d16_hi v68, v68\n" \
  "ds_read_u16_d16_hi v69, v69\n" \
  "s_set_gpr_idx_on s61, gpr_idx(SRC2,DST)\n" \
  "v_pk_fma_f32 v[128:129], v[58:59], v[48:49], v[128:129] op_sel:[0,0,0] op_sel_hi:[0,1,1]\n" \
  "v_pk_fma_f32 v[130:131], v[58:59], v[50:51], v[130:131] op_sel:[0,0,0] op_sel_hi:[0,1,1]\n" \
  "v_pk_fma_f32 v[132:133], v[62:63], v[52:53], v[132:133] op_sel:[0,0,0] op_sel_hi:[0,1,1]\n" \
  "v_pk_fma_f32 v[134:135], v[62:63], v[54:55], v[134:135] op_sel:[0,0,0] op_sel_hi:[0,1,1]\n" \
  "s_set_gpr_idx_off\n" \
  "s_waitcnt vmcnt(6) lgkmcnt(2)\n" \
  "buffer_load_dwordx4 v[104:107], v[12:13], s[52:55], 0 idxen offen\n" \
  "ds_read_u16 v12, v14 offset:44\n" \
  "s_lshr_b32 s61, s61, 8\n" \
  "v_mfma_f32_4x4x4_16b_bf16 v[48:51], v[76:77], v[108:109], 0 cbsz:4 abid:3\n" \
  "v_mfma_f32_4x4x4_16b_bf16 v[52:55], v[76:77], v[110:111], 0 cbsz:4 abid:3\n" \
  "v_cvt_pk_bf16_f32 v67, v66, v67\n" \
  "v_lshlrev_b32_sdwa v66, 1, v67 dst_sel:DWORD dst_unused:UNUSED_PAD src0_sel:DWORD src1_sel:WORD_0\n" \
  "v_lshlrev_b32_sdwa v67, 1, v67 dst_sel:DWORD dst_unused:UNUSED_PAD src0_sel:DWORD src1_sel:WORD_1\n" \
  "ds_read_u16_d16_hi v66, v66\n" \
  "ds_read_u16_d16_hi v67, v67\n" \
  "s_set_gpr_idx_on s61, gpr_idx(SRC2,DST)\n" \
  "v_pk_fma_f32 v[128:129], v[58:59], v[48:49], v[128:129] op_sel:[1,0,0] op_sel_hi:[1,1,1]\n" \
  "v_pk_fma_f32 v[130:131], v[58:59], v[50:51], v[130:131] op_sel:[1,0,0] op_sel_hi:[1,1,1]\n" \
  "v_pk_fma_f32 v[132:133], v[62:63], v[52:53], v[132:133] op_sel:[1,0,0] op_sel_hi:[1,1,1]\n" \
  "v_pk_fma_f32 v[134:135], v[62:63], v[54:55], v[134:135] op_sel:[1,0,0] op_sel_hi:[1,1,1]\n" \
  "s_set_gpr_idx_off\n" \
  "s_waitcnt vmcnt(6) lgkmcnt(2)\n" \
  "buffer_load_dwordx4 v[108:111], v[12:13], s[52:55], 0 idxen offen\n" \
  "ds_read_u16 v12, v14 offset:48\n" \
  "s_sub_u32 s60, s60, 1\n" \
  "s_cbranch_scc1 Lend%=\n" \
  "v_readlane_b32 s61, v17, s60\n" \
  "s_setprio 2\n" \
  "v_mfma_f32_4x4x4_16b_bf16 v[56:59], v[92:93], v[32:33], 0 cbsz:4 abid:2\n" \
  "v_mfma_f32_4x4x4_16b_bf16 v[60:63], v[92:93], v[36:37], 0 cbsz:4 abid:2\n" \
  "v_mfma_f32_4x4x4_16b_bf16 v[48:51], v[76:77], v[112:113], 0 cbsz:4 abid:4\n" \
  "v_mfma_f32_4x4x4_16b_bf16 v[52:55], v[76:77], v[114:115], 0 cbsz:4 abid:4\n" \
  "v_cvt_pk_bf16_f32 v71, v70, v71\n" \
  "v_lshlrev_b32_sdwa v70, 1, v71 dst_sel:DWORD dst_unused:UNUSED_PAD src0_sel:DWORD src1_sel:WORD_0\n" \
  "v_lshlrev_b32_sdwa v71, 1, v71 dst_sel:DWORD dst_unused:UNUSED_PAD src0_sel:DWORD src1_sel:WORD_1\n" \
  "ds_read_u16_d16_hi v70, v70\n" \
  "ds_read_u16_d16_hi v71, v71\n" \
  "v_add_u32 v16, 0x100, v16\n" \
  "s_set_gpr_idx_on s61, gpr_idx(SRC2,DST)\n" \
  "v_pk_fma_f32 v[128:129], v[64:65], v[48:49], v[128:129] op_sel:[0,0,0] op_sel_hi:[0,1,1]\n" \
  "v_pk_fma_f32 v[130:131], v[64:65], v[50:51], v[130:131] op_sel:[0,0,0] op_sel_hi:[0,1,1]\n" \
  "v_pk_fma_f32 v[132:133], v[68:69], v[52:53], v[132:133] op_sel:[0,0,0] op_sel_hi:[0,1,1]\n" \
  "v_pk_fma_f32 v[134:135], v[68:69], v[54:55], v[134:135] op_sel:[0,0,0] op_sel_hi:[0,1,1]\n" \
  "s_set_gpr_idx_off\n" \
  "s_waitcnt vmcnt(6) lgkmcnt(2)\n" \
  "buffer_load_dwordx4 v[112:115], v[12:13], s[52:55], 0 idxen offen\n" \
  "ds_read_u16 v12, v14 offset:52\n" \
  "ds_read_b64 v[94:95], v15 offset:256\n" \
  "s_lshr_b32 s61, s61, 8\n" \
  "v_mfma_f32_4x4x4_16b_bf16 v[48:51], v[76:77], v[116:117], 0 cbsz:4 abid:5\n" \
  "v_mfma_f32_4x4x4_16b_bf16 v[52:55], v[76:77], v[118:119], 0 cbsz:4 abid:5\n" \
  "v_cvt_pk_bf16_f32 v57, v56, v57\n" \
  "v_lshlrev_b32_sdwa v56, 1, v57 dst_sel:DWORD dst_unused:UNUSED_PAD src0_sel:DWORD src1_sel:WORD_0\n" \
  "v_lshlrev_b32_sdwa v57, 1, v57 dst_sel:DWORD dst_unused:UNUSED_PAD src0_sel:DWORD src1_sel:WORD_1\n" \
  "ds_read_u16_d16_hi v56, v56\n" \
  "ds_read_u16_d16_hi v57, v57\n" \
  "s_set_gpr_idx_on s61, gpr_idx(SRC2,DST)\n" \
  "v_pk_fma_f32 v[128:129], v[64:65], v[48:49], v[128:129] op_sel:[1,0,0] op_sel_hi:[1,1,1]\n" \
  "v_pk_fma_f32 v[130:131], v[64:65], v[50:51], v[130:131] op_sel:[1,0,0] op_sel_hi:[1,1,1]\n" \
  "v_pk_fma_f32 v[132:133], v[68:69], v[52:53], v[132:133] op_sel:[1,0,0] op_sel_hi:[1,1,1]\n" \
  "v_pk_fma_f32 v[134:135], v[68:69], v[54:55], v[134:135] op_sel:[1,0,0] op_sel_hi:[1,1,1]\n" \
  "s_set_gpr_idx_off\n" \
  "s_waitcnt vmcnt(6) lgkmcnt(2)\n" \
  "buffer_load_dwordx4 v[116:119], v[12:13], s[52:55], 0 idxen offen\n" \
  "ds_read_u16 v12, v14 offset:56\n" \
  "s_lshr_b32 s61, s61, 8\n" \
  "v_mfma_f32_4x4x4_16b_bf16 v[48:51], v[76:77], v[120:121], 0 cbsz:4 abid:6\n" \
  "v_mfma_f32_4x4x4_16b_bf16 v[52:55], v[76:77], v[122:123], 0 cbsz:4 abid:6\n" \
  "v_cvt_pk_bf16_f32 v61, v60, v61\n" \
  "v_lshlrev_b32_sdwa v60, 1, v61 dst_sel:DWORD dst_unused:UNUSED_PAD src0_sel:DWORD src1_sel:WORD_0\n" \
  "v_lshlrev_b32_sdwa v61, 1, v61 dst_sel:DWORD dst_unused:UNUSED_PAD src0_sel:DWORD src1_sel:WORD_1\n" \
  "ds_read_u16_d16_hi v60, v60\n" \
  "ds_read_u16_d16_hi v61, v61\n" \
  "s_set_gpr_idx_on s61, gpr_idx(SRC2,DST)\n" \
  "v_pk_fma_f32 v[128:129], v[66:67], v[48:49], v[128:129] op_sel:[0,0,0] op_sel_hi:[0,1,1]\n" \
  "v_pk_fma_f32 v[130:131], v[66:67], v[50:51], v[130:131] op_sel:[0,0,0] op_sel_hi:[0,1,1]\n" \
  "v_pk_fma_f32 v[132:133], v[70:71], v[52:53], v[132:133] op_sel:[0,0,0] op_sel_hi:[0,1,1]\n" \
  "v_pk_fma_f32 v[134:135], v[70:71], v[54:55], v[134:135] op_sel:[0,0,0] op_sel_hi:[0,1,1]\n" \
  "s_set_gpr_idx_off\n" \
  "s_waitcnt vmcnt(6) lgkmcnt(2)\n" \
  "buffer_load_dwordx4 v[120:123], v[12:13], s[52:55], 0 idxen offen\n" \
  "ds_read_u16 v12, v14 offset:60\n" \
  "s_lshr_b32 s61, s61, 8\n" \
  "v_mfma_f32_4x4x4_16b_bf16 v[84:87], v[94:95], v[24:25], 0\n" \
  "v_mfma_f32_4x4x4_16b_bf16 v[48:51], v[76:77], v[124:125], 0 cbsz:4 abid:7\n" \
  "v_mfma_f32_4x4x4_16b_bf16 v[52:55], v[76:77], v[126:127], 0 cbsz:4 abid:7\n" \
  "v_cvt_pk_bf16_f32 v59, v58, v59\n" \
  "v_lshlrev_b32_sdwa v58, 1, v59 dst_sel:DWORD dst_unused:UNUSED_PAD src0_sel:DWORD src1_sel:WORD_0\n" \
  "v_lshlrev_b32_sdwa v59, 1, v59 dst_sel:DWORD dst_unused:UNUSED_PAD src0_sel:DWORD src1_sel:WORD_1\n" \
  "ds_read_u16_d16_hi v58, v58\n" \
  "ds_read_u16_d16_hi v59, v59\n" \
  "s_set_gpr_idx_on s61, gpr_idx(SRC2,DST)\n" \
  "v_pk_fma_f32 v[128:129], v[66:67], v[48:49], v[128:129] op_sel:[1,0,0] op_sel_hi:[1,1,1]\n" \
  "v_pk_fma_f32 v[130:131], v[66:67], v[50:51], v[130:131] op_sel:[1,0,0] op_sel_hi:[1,1,1]\n" \
  "v_pk_fma_f32 v[132:133], v[70:71], v[52:53], v[132:133] op_sel:[1,0,0] op_sel_hi:[1,1,1]\n" \
  "v_pk_fma_f32 v[134:135], v[70:71], v[54:55], v[134:135] op_sel:[1,0,0] op_sel_hi:[1,1,1]\n" \
  "s_set_gpr_idx_off\n" \
  "s_waitcnt vmcnt(6) lgkmcnt(2)\n" \
  "buffer_load_dwordx4 v[124:127], v[12:13], s[52:55], 0 idxen offen\n" \
  "ds_read_u16 v12, v14 offset:64\n" \
  "s_sub_u32 s60, s60, 1\n" \
  "s_cbranch_scc1 Lend%=\n" \
  "v_readlane_b32 s61, v17, s60\n" \
  "s_setprio 1\n" \
  "v_mfma_f32_4x4x4_16b_bf16 v[64:67], v[92:93], v[32:33], 0 cbsz:4 abid:3\n" \
  "v_mfma_f32_4x4x4_16b_bf16 v[68:71], v[92:93], v[36:37], 0 cbsz:4 abid:3\n" \
  "v_mfma_f32_4x4x4_16b_bf16 v[48:51], v[76:77], v[96:97], 0 cbsz:4 abid:8\n" \
  "v_mfma_f32_4x4x4_16b_bf16 v[52:55], v[76:77], v[98:99], 0 cbsz:4 abid:8\n" \
  "v_cvt_pk_bf16_f32 v63, v62, v63\n" \
  "v_lshlrev_b32_sdwa v62, 1, v63 dst_sel:DWORD dst_unused:UNUSED_PAD src0_sel:DWORD src1_sel:WORD_0\n" \
  "v_lshlrev_b32_sdwa v63, 1, v63 dst_sel:DWORD dst_unused:UNUSED_PAD src0_sel:DWORD src1_sel:WORD_1\n" \
  "ds_read_u16_d16_hi v62, v62\n" \
  "ds_read_u16_d16_hi v63, v63\n" \
  "v_add_u32 v15, 0x100, v15\n" \
  "s_set_gpr_idx_on s61, gpr_idx(SRC2,DST)\n" \
  "v_pk_fma_f32 v[128:129], v[56:57], v[48:49], v[128:129] op_sel:[0,0,0] op_sel_hi:[0,1,1]\n" \
  "v_pk_fma_f32 v[130:131], v[56:57], v[50:51], v[130:131] op_sel:[0,0,0] op_sel_hi:[0,1,1]\n" \
  "v_pk_fma_f32 v[132:133], v[60:61], v[52:53], v[132:133] op_sel:[0,0,0] op_sel_hi:[0,1,1]\n" \
  "v_pk_fma_f32 v[134:135], v[60:61], v[54:55], v[134:135] op_sel:[0,0,0] op_sel_hi:[0,1,1]\n" \
  "s_set_gpr_idx_off\n" \
  "s_waitcnt vmcnt(6) lgkmcnt(2)\n" \
  "buffer_load_dwordx4 v[96:99], v[12:13], s[52:55], 0 idxen offen\n" \
  "ds_read_u16 v12, v14 offset:68\n" \
  "s_lshr_b32 s61, s61, 8\n" \
  "v_mfma_f32_4x4x4_16b_bf16 v[48:51], v[76:77], v[100:101], 0 cbsz:4 abid:9\n" \
  "v_mfma_f32_4x4x4_16b_bf16 v[52:55], v[76:77], v[102:103], 0 cbsz:4 abid:9\n" \
  "v_cvt_pk_bf16_f32 v65, v64, v65\n" \
  "v_lshlrev_b32_sdwa v64, 1, v65 dst_sel:DWORD dst_unused:UNUSED_PAD src0_sel:DWORD src1_sel:WORD_0\n" \
  "v_lshlrev_b32_sdwa v65, 1, v65 dst_sel:DWORD dst_unused:UNUSED_PAD src0_sel:DWORD src1_sel:WORD_1\n" \
  "ds_read_u16_d16_hi v64, v64\n" \
  "ds_read_u16_d16_hi v65, v65\n" \
  "s_set_gpr_idx_on s61, gpr_idx(SRC2,DST)\n" \
  "v_pk_fma_f32 v[128:129], v[56:57], v[48:49], v[128:129] op_sel:[1,0,0] op_sel_hi:[1,1,1]\n" \
  "v_pk_fma_f32 v[130:131], v[56:57], v[50:51], v[130:131] op_sel:[1,0,0] op_sel_hi:[1,1,1]\n" \
  "v_pk_fma_f32 v[132:133], v[60:61], v[52:53], v[132:133] op_sel:[1,0,0] op_sel_hi:[1,1,1]\n" \
  "v_pk_fma_f32 v[134:135], v[60:61], v[54:55], v[134:135] op_sel:[1,0,0] op_sel_hi:[1,1,1]\n" \
  "s_set_gpr_idx_off\n" \
  "s_waitcnt vmcnt(6) lgkmcnt(2)\n" \
  "buffer_load_dwordx4 v[100:103], v[12:13], s[52:55], 0 idxen offen\n" \
  "ds_read_u16 v12, v14 offset:72\n" \
  "s_lshr_b32 s61, s61, 8\n" \
  "v_mfma_f32_4x4x4_16b_bf16 v[48:51], v[76:77], v[104:105], 0 cbsz:4 abid:10\n" \
  "v_mfma_f32_4x4x4_16b_bf16 v[52:55], v[76:77], v[106:107], 0 cbsz:4 abid:10\n" \
  "v_cvt_pk_bf16_f32 v69, v68, v69\n" \
  "v_lshlrev_b32_sdwa v68, 1, v69 dst_sel:DWORD dst_unused:UNUSED_PAD src0_sel:DWORD src1_sel:WORD_0\n" \
  "v_lshlrev_b32_sdwa v69, 1, v69 dst_sel:DWORD dst_unused:UNUSED_PAD src0_sel:DWORD src1_sel:WORD_1\n" \
  "ds_read_u16_d16_hi v68, v68\n" \
  "ds_read_u16_d16_hi v69, v69\n" \
  "s_set_gpr_idx_on s61, gpr_idx(SRC2,DST)\n" \
  "v_pk_fma_f32 v[128:129], v[58:59], v[48:49], v[128:129] op_sel:[0,0,0] op_sel_hi:[0,1,1]\n" \
  "v_pk_fma_f32 v[130:131], v[58:59], v[50:51], v[130:131] op_sel:[0,0,0] op_sel_hi:[0,1,1]\n" \
  "v_pk_fma_f32 v[132:133], v[62:63], v[52:53], v[132:133] op_sel:[0,0,0] op_sel_hi:[0,1,1]\n" \
  "v_pk_fma_f32 v[134:135], v[62:63], v[54:55], v[134:135] op_sel:[0,0,0] op_sel_hi:[0,1,1]\n" \
  "s_set_gpr_idx_off\n" \
  "s_waitcnt vmcnt(6) lgkmcnt(2)\n" \
  "buffer_load_dwordx4 v[104:107], v[12:13], s[52:55], 0 idxen offen\n" \
  "ds_read_u16 v12, v14 offset:76\n" \
  "ds_read_b64 v[92:93], v16\n" \
  "s_lshr_b32 s61, s61, 8\n" \
  "v_mfma_f32_4x4x4_16b_bf16 v[88:91], v[94:95], v[26:27], 0\n" \
  "v_mfma_f32_4x4x4_16b_bf16 v[48:51], v[76:77], v[108:109], 0 cbsz:4 abid:11\n" \
  "v_mfma_f32_4x4x4_16b_bf16 v[52:55], v[76:77], v[110:111], 0 cbsz:4 abid:11\n" \
  "v_cvt_pk_bf16_f32 v67, v66, v67\n" \
  "v_lshlrev_b32_sdwa v66, 1, v67 dst_sel:DWORD dst_unused:UNUSED_PAD src0_sel:DWORD src1_sel:WORD_0\n" \
  "v_lshlrev_b32_sdwa v67, 1, v67 dst_sel:DWORD dst_unused:UNUSED_PAD src0_sel:DWORD src1_sel:WORD_1\n" \
  "ds_read_u16_d16_hi v66, v66\n" \
  "ds_read_u16_d16_hi v67, v67\n" \
  "s_set_gpr_idx_on s61, gpr_idx(SRC2,DST)\n" \
  "v_pk_fma_f32 v[128:129], v[58:59], v[48:49], v[128:129] op_sel:[1,0,0] op_sel_hi:[1,1,1]\n" \
  "v_pk_fma_f32 v[130:131], v[58:59], v[50:51], v[130:131] op_sel:[1,0,0] op_sel_hi:[1,1,1]\n" \
  "v_pk_fma_f32 v[132:133], v[62:63], v[52:53], v[132:133] op_sel:[1,0,0] op_sel_hi:[1,1,1]\n" \
  "v_pk_fma_f32 v[134:135], v[62:63], v[54:55], v[134:135] op_sel:[1,0,0] op_sel_hi:[1,1,1]\n" \
  "s_set_gpr_idx_off\n" \
  "s_waitcnt vmcnt(6) lgkmcnt(2)\n" \
  "buffer_load_dwordx4 v[108:111], v[12:13], s[52:55], 0 idxen offen\n" \
  "ds_read_u16 v12, v14 offset:80\n" \
  "s_sub_u32 s60, s60, 1\n" \
  "s_cbranch_scc1 Lend%=\n" \
  "v_readlane_b32 s61, v17, s60\n" \
  "s_setprio 0\n" \
  "v_mfma_f32_4x4x4_16b_bf16 v[56:59], v[92:93], v[32:33], 0 cbsz:4\n" \
  "v_mfma_f32_4x4x4_16b_bf16 v[60:63], v[92:93], v[36:37], 0 cbsz:4\n" \
  "v_mfma_f32_4x4x4_16b_bf16 v[48:51], v[76:77], v[112:113], 0 cbsz:4 abid:12\n" \
  "v_mfma_f32_4x4x4_16b_bf16 v[52:55], v[76:77], v[114:115], 0 cbsz:4 abid:12\n" \
  "v_cvt_pk_bf16_f32 v71, v70, v71\n" \
  "v_lshlrev_b32_sdwa v70, 1, v71 dst_sel:DWORD dst_unused:UNUSED_PAD src0_sel:DWORD src1_sel:WORD_0\n" \
  "v_lshlrev_b32_sdwa v71, 1, v71 dst_sel:DWORD dst_unused:UNUSED_PAD src0_sel:DWORD src1_sel:WORD_1\n" \
  "ds_read_u16_d16_hi v70, v70\n" \
  "ds_read_u16_d16_hi v71, v71\n" \
  "v_cvt_pk_bf16_f32 v20, v84, v88\n" \
  "s_set_gpr_idx_on s61, gpr_idx(SRC2,DST)\n" \
  "v_pk_fma_f32 v[128:129], v[64:65], v[48:49], v[128:129] op_sel:[0,0,0] op_sel_hi:[0,1,1]\n" \
  "v_pk_fma_f32 v[130:131], v[64:65], v[50:51], v[130:131] op_sel:[0,0,0] op_sel_hi:[0,1,1]\n" \
  "v_pk_fma_f32 v[132:133], v[68:69], v[52:53], v[132:133] op_sel:[0,0,0] op_sel_hi:[0,1,1]\n" \
  "v_pk_fma_f32 v[134:135], v[68:69], v[54:55], v[134:135] op_sel:[0,0,0] op_sel_hi:[0,1,1]\n" \
  "s_set_gpr_idx_off\n" \
  "s_waitcnt vmcnt(6) lgkmcnt(2)\n" \
  "buffer_load_dwordx4 v[112:115], v[12:13], s[52:55], 0 idxen offen\n" \
  "ds_read_u16 v12, v14 offset:84\n" \
  "s_lshr_b32 s61, s61, 8\n" \
  "v_mfma_f32_4x4x4_16b_bf16 v[48:51], v[76:77], v[116:117], 0 cbsz:4 abid:13\n" \
  "v_mfma_f32_4x4x4_16b_bf16 v[52:55], v[76:77], v[118:119], 0 cbsz:4 abid:13\n" \
  "v_cvt_pk_bf16_f32 v57, v56, v57\n" \
  "v_lshlrev_b32_sdwa v56, 1, v57 dst_sel:DWORD dst_unused:UNUSED_PAD src0_sel:DWORD src1_sel:WORD_0\n" \
  "v_lshlrev_b32_sdwa v57, 1, v57 dst_sel:DWORD dst_unused:UNUSED_PAD src0_sel:DWORD src1_sel:WORD_1\n" \
  "ds_read_u16_d16_hi v56, v56\n" \
  "ds_read_u16_d16_hi v57, v57\n" \
  "s_set_gpr_idx_on s61, gpr_idx(SRC2,DST)\n" \
  "v_pk_fma_f32 v[128:129], v[64:65], v[48:49], v[128:129] op_sel:[1,0,0] op_sel_hi:[1,1,1]\n" \
  "v_pk_fma_f32 v[130:131], v[64:65], v[50:51], v[130:131] op_sel:[1,0,0] op_sel_hi:[1,1,1]\n" \
  "v_pk_fma_f32 v[132:133], v[68:69], v[52:53], v[132:133] op_sel:[1,0,0] op_sel_hi:[1,1,1]\n" \
  "v_pk_fma_f32 v[134:135], v[68:69], v[54:55], v[134:135] op_sel:[1,0,0] op_sel_hi:[1,1,1]\n" \
  "s_set_gpr_idx_off\n" \
  "s_waitcnt vmcnt(6) lgkmcnt(2)\n" \
  "buffer_load_dwordx4 v[116:119], v[12:13], s[52:55], 0 idxen offen\n" \
  "ds_read_u16 v12, v14 offset:88\n" \
  "s_lshr_b32 s61, s61, 8\n" \
  "v_mfma_f32_4x4x4_16b_bf16 v[48:51], v[76:77], v[120:121], 0 cbsz:4 abid:14\n" \
  "v_mfma_f32_4x4x4_16b_bf16 v[52:55], v[76:77], v[122:123], 0 cbsz:4 abid:14\n" \
  "v_cvt_pk_bf16_f32 v61, v60, v61\n" \
  "v_lshlrev_b32_sdwa v60, 1, v61 dst_sel:DWORD dst_unused:UNUSED_PAD src0_sel:DWORD src1_sel:WORD_0\n" \
  "v_lshlrev_b32_sdwa v61, 1, v61 dst_sel:DWORD dst_unused:UNUSED_PAD src0_sel:DWORD src1_sel:WORD_1\n" \
  "ds_read_u16_d16_hi v60, v60\n" \
  "ds_read_u16_d16_hi v61, v61\n" \
  "s_set_gpr_idx_on s61, gpr_idx(SRC2,DST)\n" \
  "v_pk_fma_f32 v[128:129], v[66:67], v[48:49], v[128:129] op_sel:[0,0,0] op_sel_hi:[0,1,1]\n" \
  "v_pk_fma_f32 v[130:131], v[66:67], v[50:51], v[130:131] op_sel:[0,0,0] op_sel_hi:[0,1,1]\n" \
  "v_pk_fma_f32 v[132:133], v[70:71], v[52:53], v[132:133] op_sel:[0,0,0] op_sel_hi:[0,1,1]\n" \
  "v_pk_fma_f32 v[134:135], v[70:71], v[54:55], v[134:135] op_sel:[0,0,0] op_sel_hi:[0,1,1]\n" \
  "s_set_gpr_idx_off\n" \
  "s_waitcnt vmcnt(6) lgkmcnt(2)\n" \
  "buffer_load_dwordx4 v[120:123], v[12:13], s[52:55], 0 idxen offen\n" \
  "ds_read_u16 v12, v14 offset:92\n" \
  "s_lshr_b32 s61, s61, 8\n" \
  "v_mfma_f32_4x4x4_16b_bf16 v[84:87], v[94:95], v[28:29], 0\n" \
  "v_mfma_f32_4x4x4_16b_bf16 v[88:91], v[94:95], v[30:31], 0\n" \
  "v_mfma_f32_4x4x4_16b_bf16 v[48:51], v[76:77], v[124:125], 0 cbsz:4 abid:15\n" \
  "v_mfma_f32_4x4x4_16b_bf16 v[52:55], v[76:77], v[126:127], 0 cbsz:4 abid:15\n" \
  "v_cvt_pk_bf16_f32 v59, v58, v59\n" \
  "v_lshlrev_b32_sdwa v58, 1, v59 dst_sel:DWORD dst_unused:UNUSED_PAD src0_sel:DWORD src1_sel:WORD_0\n" \
  "v_lshlrev_b32_sdwa v59, 1, v59 dst_sel:DWORD dst_unused:UNUSED_PAD src0_sel:DWORD src1_sel:WORD_1\n" \
  "ds_read_u16_d16_hi v58, v58\n" \
  "ds_read_u16_d16_hi v59, v59\n" \
  "v_cvt_pk_bf16_f32 v77, v84, v88\n" \
  "v_mov_b32 v76, v20\n" \
  "s_set_gpr_idx_on s61, gpr_idx(SRC2,DST)\n" \
  "v_pk_fma_f32 v[128:129], v[66:67], v[48:49], v[128:129] op_sel:[1,0,0] op_sel_hi:[1,1,1]\n" \
  "v_pk_fma_f32 v[130:131], v[66:67], v[50:51], v[130:131] op_sel:[1,0,0] op_sel_hi:[1,1,1]\n" \
  "v_pk_fma_f32 v[132:133], v[70:71], v[52:53], v[132:133] op_sel:[1,0,0] op_sel_hi:[1,1,1]\n" \
  "v_pk_fma_f32 v[134:135], v[70:71], v[54:55], v[134:135] op_sel:[1,0,0] op_sel_hi:[1,1,1]\n" \
  "s_set_gpr_idx_off\n" \
  "s_waitcnt vmcnt(6) lgkmcnt(2)\n" \
  "buffer_load_dwordx4 v[124:127], v[12:13], s[52:55], 0 idxen offen\n" \
  "ds_read_u16 v12, v14 offset:96\n" \
  "s_sub_u32 s60, s60, 1\n" \
  "s_cbranch_scc1 Lend%=\n" \
  "v_readlane_b32 s61, v17, s60\n" \
  "s_setprio 3\n" \
  "v_mfma_f32_4x4x4_16b_bf16 v[64:67], v[92:93], v[32:33], 0 cbsz:4 abid:1\n" \
  "v_mfma_f32_4x4x4_16b_bf16 v[68:71], v[92:93], v[36:37], 0 cbsz:4 abid:1\n" \
  "v_mfma_f32_4x4x4_16b_bf16 v[48:51], v[76:77], v[96:97], 0 cbsz:4\n" \
  "v_mfma_f32_4x4x4_16b_bf16 v[52:55], v[76:77], v[98:99], 0 cbsz:4\n" \
  "v_cvt_pk_bf16_f32 v63, v62, v63\n" \
  "v_lshlrev_b32_sdwa v62, 1, v63 dst_sel:DWORD dst_unused:UNUSED_PAD src0_sel:DWORD src1_sel:WORD_0\n" \
  "v_lshlrev_b32_sdwa v63, 1, v63 dst_sel:DWORD dst_unused:UNUSED_PAD src0_sel:DWORD src1_sel:WORD_1\n" \
  "ds_read_u16_d16_hi v62, v62\n" \
  "ds_read_u16_d16_hi v63, v63\n" \
  "v_add_u32 v14, 64, v14\n" \
  "s_branch Lbody%=\n" \
  "Lep0_%=:\n" \
  "ds_read_b32 v50, v18\n" \
  "s_waitcnt lgkmcnt(0)\n" \
  "v_readfirstlane_b32 s63, v50\n" \
  "s_bitcmp1_b32 s63, 1\n" \
  "s_cbranch_scc1 Lgo%=\n" \
  "v_mov_b64 v[50:51], s[50:51]\n" \
  "s_mov_b64 exec, 1\n" \
  "buffer_store_dwordx2 v[50:51], off, s[24:27], s70 sc1\n" \
  "s_mov_b64 exec, -1\n" \
  "s_nop 3\n" \
  "s_branch Lgo%=\n" \
  "Lbad%=:\n" \
  "s_cmp_eq_u32 s74, 0xffffffc1\n" \
  "s_cbranch_scc1 Lf9_%=\n" \
  "v_mov_b32 v72, 1\n" \
  "v_mov_b32 v73, s62\n" \
  "ds_or_b32 v18, v72\n" \
  "ds_write_b32 v73, v73\n" \
  "s_branch Lf9_%=\n" \
  "Lnf%=:\n" \
  "s_mov_b32 %[bad], 3\n" \
  "s_mov_b32 s79, m0\n" \
  "Lbad2_%=:\n" \
  "s_waitcnt vmcnt(0) lgkmcnt(0)\n" \
  "s_bitcmp1_b32 %[bad], 1\n" \
  "s_cbranch_scc1 Lout%=\n" \
  "s_branch Lep%=\n" \
  "Lh2_%=:\n" \
  "s_waitcnt vmcnt(0)\n" \
  "v_add_co_u32 v90, vcc, 1, v88\n" \
  "s_mul_i32 s63, %[blk], 2176\n" \
  "s_add_u32 s63, s63, 49408\n" \
  "v_addc_co_u32 v91, vcc, 0, v89, vcc\n" \
  "v_lshlrev_b32 v88, 22, v90\n" \
  "v_alignbit_b32 v89, v91, v90, 10\n" \
  "v_add_u32 v87, v87, v88\n" \
  "v_lshl_add_u32 v128, v136, 11, v87\n" \
  "v_mov_b32 v129, v89\n" \
  "v_sub_u32 v51, v137, v136\n" \
  "v_lshl_add_u32 v51, v51, 11, v136\n" \
  "v_add_u32 v130, v51, v87\n" \
  "v_mov_b32 v131, v89\n" \
  "v_sub_u32 v51, v138, v137\n" \
  "v_lshl_add_u32 v51, v51, 11, v137\n" \
  "v_add_u32 v132, v51, v87\n" \
  "v_mov_b32 v133, v89\n" \
  "v_sub_u32 v51, v139, v138\n" \
  "v_lshl_add_u32 v51, v51, 11, v138\n" \
  "v_add_u32 v134, v51, v87\n" \
  "v_mov_b32 v135, v89\n" \
  "buffer_store_dwordx4 v[128:131], v13, s[24:27], s63 offen sc1\n" \
  "buffer_store_dwordx4 v[132:135], v13, s[24:27], s63 offen offset:1024 sc1\n" \
  "buffer_load_dword v100, v54, s[8:11], 0 offen offset:0\n" \
  "buffer_load_dword v101, v54, s[8:11], 0 offen offset:256\n" \
  "buffer_load_dword v102, v54, s[8:11], 0 offen offset:512\n" \
  "buffer_load_dword v103, v54, s[8:11], 0 offen offset:768\n" \
  "buffer_load_dword v104, v54, s[12:15], 0 offen\n" \
  "s_mul_i32 s72, s45, 528\n" \
  "s_cmp_lt_u32 s44, 4\n" \
  "s_cselect_b32 s62, 45776, 0\n" \
  "s_cselect_b32 s62, s62, 140608\n" \
  "s_add_u32 m0, s72, s62\n" \
  "s_lshl_b32 s72, s44, 12\n" \
  "buffer_load_dwordx4 v13, s[52:55], s72 offen lds\n" \
  "buffer_load_dwordx4 v13, s[52:55], s72 offen offset:1024 lds\n" \
  "s_add_u32 m0, m0, 64\n" \
  "s_nop 0\n" \
  "buffer_load_dwordx4 v13, s[52:55], s72 offen offset:2048 lds\n" \
  "buffer_load_dwordx4 v13, s[52:55], s72 offen offset:3072 lds\n" \
  "s_mov_b32 m0, 36160\n" \
  "Lw1_%=:\n" \
  "s_memrealtime s[90:91]\n" \
  "s_waitcnt lgkmcnt(0)\n" \
  "s_sub_u32 s90, s90, s76\n" \
  "s_cmp_lt_i32 s90, 160\n" \
  "s_cbranch_scc1 Lw1_%=\n" \
  "s_barrier\n" \
  "ds_write_b64 v94, v[90:91] offset:1472\n" \
  "s_mov_b32 s63, 0x05040100\n" \
  "s_mov_b32 s93, 0x07060302\n" \
  "s_waitcnt vmcnt(4)\n" \
  "v_perm_b32 v120, v101, v100, s63\n" \
  "v_perm_b32 v121, v103, v102, s63\n" \
  "v_perm_b32 v122, v101, v100, s93\n" \
  "v_perm_b32 v123, v103, v102, s93\n" \
  "ds_write_b128 v13, v[120:123] offset:36544\n" \
  "v_and_b32 v50, 30, v86\n" \
  "v_and_b32 v51, 16, v13\n" \
  "v_lshl_add_u32 v50, v51, 2, v50\n" \
  "s_mov_b64 exec, 0xffffffff\n" \
  "ds_write_b16 v50, v104 offset:36416\n" \
  "ds_write_b16_d16_hi v50, v104 offset:36448\n" \
  "s_mov_b64 exec, -1\n" \
  "v_and_b32 v100, 0x7fff7fff, v100\n" \
  "v_max_u16_sdwa v100, v100, v100 dst_sel:WORD_1 dst_unused:UNUSED_PAD src0_sel:WORD_0 src1_sel:WORD_1\n" \
  "v_and_b32 v101, 0x7fff7fff, v101\n" \
  "v_max_u16_sdwa v101, v101, v101 dst_sel:WORD_1 dst_unused:UNUSED_PAD src0_sel:WORD_0 src1_sel:WORD_1\n" \
  "v_and_b32 v102, 0x7fff7fff, v102\n" \
  "v_max_u16_sdwa v102, v102, v102 dst_sel:WORD_1 dst_unused:UNUSED_PAD src0_sel:WORD_0 src1_sel:WORD_1\n" \
  "v_and_b32 v103, 0x7fff7fff, v103\n" \
  "v_max_u16_sdwa v103, v103, v103 dst_sel:WORD_1 dst_unused:UNUSED_PAD src0_sel:WORD_0 src1_sel:WORD_1\n" \
  "v_add_f32 v100, v100, v101\n" \
  "v_add_f32 v102, v102, v103\n" \
  "v_add_f32 v100, v100, v102\n" \
  "v_and_b32 v100, 0x7fffffff, v100\n" \
  "s_nop 1\n" \
  "v_max_u32_dpp v100, v100, v100 row_shr:1 row_mask:0xf bank_mask:0xf bound_ctrl:1\n" \
  "s_nop 1\n" \
  "v_max_u32_dpp v100, v100, v100 row_shr:2 row_mask:0xf bank_mask:0xf bound_ctrl:1\n" \
  "s_nop 1\n" \
  "v_max_u32_dpp v100, v100, v100 row_shr:4 row_mask:0xf bank_mask:0xf bound_ctrl:1\n" \
  "s_nop 1\n" \
  "v_max_u32_dpp v100, v100, v100 row_shr:8 row_mask:0xf bank_mask:0xf bound_ctrl:1\n" \
  "s_nop 1\n" \
  "v_max_u32_dpp v100, v100, v100 row_bcast:15 row_mask:0xa bank_mask:0xf\n" \
  "s_nop 1\n" \
  "v_max_u32_dpp v100, v100, v100 row_bcast:31 row_mask:0xc bank_mask:0xf\n" \
  "v_max_u32 v101, 0x0d000000, v100\n" \
  "v_rcp_f32 v101, v101\n" \
  "v_cmp_gt_u32 vcc, 0x71000000, v100\n" \
  "v_mul_f32 v101, 0x437a0000, v101\n" \
  "v_lshrrev_b32 v101, 16, v101\n" \
  "v_cndmask_b32 v101, -1, v101, vcc\n" \
  "s_brev_b64 exec, 1\n" \
  "ds_write_b32 v94, v101 offset:1480\n" \
  "s_mov_b64 exec, -1\n" \
  "s_branch Lc1_%=\n" \
  "Lend%=:\n" \
  "s_lshl2_add_u32 s62, s44, 36128\n" \
  "v_add_u32 v19, s72, v19\n" \
  "s_cmp_ge_u32 s74, 10\n" \
  "s_cbranch_scc1 Lbad%=\n" \
  "v_mov_b32 v72, s62\n" \
  "s_cmp_gt_u32 s93, s44\n" \
  "s_cbranch_scc0 Lf1_%=\n" \
  "s_set_gpr_idx_on s73, gpr_idx(SRC0)\n" \
  "v_mov_b64 v[84:85], v[128:129]\n" \
  "v_mov_b64 v[86:87], v[130:131]\n" \
  "v_mov_b64 v[88:89], v[132:133]\n" \
  "v_mov_b64 v[90:91], v[134:135]\n" \
  "s_set_gpr_idx_off\n" \
  "ds_write_b128 v21, v[84:87]\n" \
  "ds_write_b128 v21, v[88:91] offset:1024\n" \
  "ds_write_b32 v72, v72\n" \
  "s_sub_u32 s73, s73, 8\n" \
  "s_cselect_b32 s73, 72, s73\n" \
  "v_subrev_u32 v19, 1056, v19\n" \
  "s_sub_u32 s74, s74, 1\n" \
  "s_cbranch_scc1 Lf9_%=\n" \
  "Lf1_%=:\n" \
  "s_min_u32 s63, s75, 1\n" \
  "s_sub_u32 s60, s74, s63\n" \
  "s_cbranch_scc1 Lf3_%=\n" \
  "Lf2_%=:\n" \
  "s_set_gpr_idx_on s73, gpr_idx(SRC0,SRC1)\n" \
  "v_cvt_pk_bf16_f32 v48, v128, v132\n" \
  "v_cvt_pk_bf16_f32 v49, v129, v133\n" \
  "v_cvt_pk_bf16_f32 v50, v130, v134\n" \
  "v_cvt_pk_bf16_f32 v51, v131, v135\n" \
  "s_set_gpr_idx_off\n" \
  "s_sub_u32 s73, s73, 8\n" \
  "s_cselect_b32 s73, 72, s73\n" \
  "ds_write2_b32 v19, v48, v49 offset1:16\n" \
  "ds_write2_b32 v19, v50, v51 offset0:32 offset1:48\n" \
  "s_sub_u32 s60, s60, 1\n" \
  "v_subrev_u32 v19, 1056, v19\n" \
  "s_cbranch_scc0 Lf2_%=\n" \
  "s_cmp_eq_u32 s75, 0\n" \
  "s_cbranch_scc1 Lf9_%=\n" \
  "Lf3_%=:\n" \
  "v_subrev_u32 v72, 4, v72\n" \
  "v_subrev_u32 v21, 3328, v21\n" \
  "Lf4_%=:\n" \
  "ds_read_b32 v73, v72\n" \
  "ds_read_b128 v[84:87], v21\n" \
  "ds_read_b128 v[88:91], v21 offset:1024\n" \
  "s_waitcnt lgkmcnt(0)\n" \
  "v_cmp_eq_u32 vcc, 0, v73\n" \
  "s_cbranch_vccz Lf5_%=\n" \
  "s_setprio 0\n" \
  "s_sleep 1\n" \
  "s_branch Lf4_%=\n" \
  "Lf5_%=:\n" \
  "s_set_gpr_idx_on s73, gpr_idx(SRC0,DST)\n" \
  "v_pk_add_f32 v[128:129], v[128:129], v[84:85]\n" \
  "v_pk_add_f32 v[130:131], v[130:131], v[86:87]\n" \
  "v_pk_add_f32 v[132:133], v[132:133], v[88:89]\n" \
  "v_pk_add_f32 v[134:135], v[134:135], v[90:91]\n" \
  "s_set_gpr_idx_off\n" \
  "s_mov_b32 s60, 0\n" \
  "s_sub_u32 s75, s75, 1\n" \
  "s_cmp_eq_u32 s75, 0\n" \
  "s_cbranch_scc1 Lf2_%=\n" \
  "s_branch Lf3_%=\n" \
  "Lf9_%=:\n" \
  "s_cmp_lt_u32 s44, 4\n" \
  "s_cselect_b32 s63, 1, 0x100\n" \
  "s_cselect_b32 s93, 4, 0x404\n" \
  "v_mov_b32 v72, s63\n" \
  "s_mov_b64 exec, 1\n" \
  "ds_add_u32 v18, v72 offset:8\n" \
  "s_mov_b64 exec, -1\n" \
  "s_setprio 0\n" \
  "buffer_load_dword v20, off, s[24:27], s70 sc1\n" \
  "s_mul_i32 s63, s76, 4224\n" \
  "v_add_u32 v22, s63, v22\n" \
  "s_lshl_b32 s72, s76, 12\n" \
  "Lm0_%=:\n" \
  "ds_read_b32 v72, v18 offset:8\n" \
  "ds_read_b128 v[128:131], v22 offset:0\n" \
  "ds_read_b128 v[132:135], v22 offset:256\n" \
  "ds_read_b128 v[136:139], v22 offset:512\n" \
  "ds_read_b128 v[140:143], v22 offset:768\n" \
  "ds_read_b128 v[144:147], v22 offset:4224\n" \
  "ds_read_b128 v[148:151], v22 offset:4480\n" \
  "ds_read_b128 v[152:155], v22 offset:4736\n" \
  "ds_read_b128 v[156:159], v22 offset:4992\n" \
  "s_waitcnt lgkmcnt(8)\n" \
  "v_readfirstlane_b32 s63, v72\n" \
  "s_and_b32 s63, s63, s93\n" \
  "s_cmp_eq_u32 s63, s93\n" \
  "s_cbranch_scc1 Lm1_%=\n" \
  "Lm4_%=:\n" \
  "s_sleep 1\n" \
  "ds_read_b32 v72, v18 offset:8\n" \
  "s_waitcnt lgkmcnt(0)\n" \
  "v_readfirstlane_b32 s63, v72\n" \
  "s_and_b32 s63, s63, s93\n" \
  "s_cmp_eq_u32 s63, s93\n" \
  "s_cbranch_scc0 Lm4_%=\n" \
  "ds_read_b128 v[128:131], v22 offset:0\n" \
  "ds_read_b128 v[132:135], v22 offset:256\n" \
  "ds_read_b128 v[136:139], v22 offset:512\n" \
  "ds_read_b128 v[140:143], v22 offset:768\n" \
  "ds_read_b128 v[144:147], v22 offset:4224\n" \
  "ds_read_b128 v[148:151], v22 offset:4480\n" \
  "ds_read_b128 v[152:155], v22 offset:4736\n" \
  "ds_read_b128 v[156:159], v22 offset:4992\n" \
  "Lm1_%=:\n" \
  "s_sub_u32 s77, s77, 1\n" \
  "s_cbranch_scc1 Lm8_%=\n" \
  "ds_read_b128 v[160:163], v22 offset:8448\n" \
  "ds_read_b128 v[164:167], v22 offset:8704\n" \
  "ds_read_b128 v[168:171], v22 offset:8960\n" \
  "ds_read_b128 v[172:175], v22 offset:9216\n" \
  "s_waitcnt lgkmcnt(11)\n" \
  "v_mfma_f32_16x16x32_bf16 v[40:43], v[128:131], v[216:219], 0\n" \
  "v_mfma_f32_16x16x32_bf16 v[44:47], v[128:131], v[220:223], 0\n" \
  "s_waitcnt lgkmcnt(10)\n" \
  "v_mfma_f32_16x16x32_bf16 v[40:43], v[132:135], v[224:227], v[40:43]\n" \
  "v_mfma_f32_16x16x32_bf16 v[44:47], v[132:135], v[228:231], v[44:47]\n" \
  "s_waitcnt lgkmcnt(9)\n" \
  "v_mfma_f32_16x16x32_bf16 v[40:43], v[136:139], v[232:235], v[40:43]\n" \
  "v_mfma_f32_16x16x32_bf16 v[44:47], v[136:139], v[236:239], v[44:47]\n" \
  "s_waitcnt lgkmcnt(8)\n" \
  "v_mfma_f32_16x16x32_bf16 v[40:43], v[140:143], v[240:243], v[40:43]\n" \
  "v_mfma_f32_16x16x32_bf16 v[44:47], v[140:143], v[244:247], v[44:47]\n" \
  "Lm2_%=:\n" \
  "s_sub_u32 s77, s77, 1\n" \
  "s_cbranch_scc1 Lm5_%=\n" \
  "v_add_u32 v22, 4224, v22\n" \
  "s_nop 0\n" \
  "ds_read_b128 v[128:131], v22 offset:8448\n" \
  "ds_read_b128 v[132:135], v22 offset:8704\n" \
  "ds_read_b128 v[136:139], v22 offset:8960\n" \
  "ds_read_b128 v[140:143], v22 offset:9216\n" \
  "s_waitcnt lgkmcnt(11)\n" \
  "v_mfma_f32_16x16x32_bf16 v[48:51], v[144:147], v[216:219], 0\n" \
  "v_mfma_f32_16x16x32_bf16 v[52:55], v[144:147], v[220:223], 0\n" \
  "s_waitcnt lgkmcnt(10)\n" \
  "v_mfma_f32_16x16x32_bf16 v[48:51], v[148:151], v[224:227], v[48:51]\n" \
  "v_mfma_f32_16x16x32_bf16 v[52:55], v[148:151], v[228:231], v[52:55]\n" \
  "v_cvt_pk_bf16_f32 v40, v40, v41\n" \
  "v_cvt_pk_bf16_f32 v41, v42, v43\n" \
  "v_cvt_pk_bf16_f32 v42, v44, v45\n" \
  "v_cvt_pk_bf16_f32 v43, v46, v47\n" \
  "buffer_store_dwordx4 v[40:43], v23, s[56:59], s72 offen sc1\n" \
  "s_add_u32 s72, s72, 0x1000\n" \
  "s_waitcnt lgkmcnt(9)\n" \
  "v_mfma_f32_16x16x32_bf16 v[48:51], v[152:155], v[232:235], v[48:51]\n" \
  "v_mfma_f32_16x16x32_bf16 v[52:55], v[152:155], v[236:239], v[52:55]\n" \
  "s_waitcnt lgkmcnt(8)\n" \
  "v_mfma_f32_16x16x32_bf16 v[48:51], v[156:159], v[240:243], v[48:51]\n" \
  "v_mfma_f32_16x16x32_bf16 v[52:55], v[156:159], v[244:247], v[52:55]\n" \
  "s_sub_u32 s77, s77, 1\n" \
  "s_cbranch_scc1 Lm6_%=\n" \
  "v_add_u32 v22, 4224, v22\n" \
  "s_nop 0\n" \
  "ds_read_b128 v[144:147], v22 offset:8448\n" \
  "ds_read_b128 v[148:151], v22 offset:8704\n" \
  "ds_read_b128 v[152:155], v22 offset:8960\n" \
  "ds_read_b128 v[156:159], v22 offset:9216\n" \
  "s_waitcnt lgkmcnt(11)\n" \
  "v_mfma_f32_16x16x32_bf16 v[56:59], v[160:163], v[216:219], 0\n" \
  "v_mfma_f32_16x16x32_bf16 v[60:63], v[160:163], v[220:223], 0\n" \
  "s_waitcnt lgkmcnt(10)\n" \
  "v_mfma_f32_16x16x32_bf16 v[56:59], v[164:167], v[224:227], v[56:59]\n" \
  "v_mfma_f32_16x16x32_bf16 v[60:63], v[164:167], v[228:231], v[60:63]\n" \
  "v_cvt_pk_bf16_f32 v48, v48, v49\n" \
  "v_cvt_pk_bf16_f32 v49, v50, v51\n" \
  "v_cvt_pk_bf16_f32 v50, v52, v53\n" \
  "v_cvt_pk_bf16_f32 v51, v54, v55\n" \
  "buffer_store_dwordx4 v[48:51], v23, s[56:59], s72 offen sc1\n" \
  "s_add_u32 s72, s72, 0x1000\n" \
  "s_waitcnt lgkmcnt(9)\n" \
  "v_mfma_f32_16x16x32_bf16 v[56:59], v[168:171], v[232:235], v[56:59]\n" \
  "v_mfma_f32_16x16x32_bf16 v[60:63], v[168:171], v[236:239], v[60:63]\n" \
  "s_waitcnt lgkmcnt(8)\n" \
  "v_mfma_f32_16x16x32_bf16 v[56:59], v[172:175], v[240:243], v[56:59]\n" \
  "v_mfma_f32_16x16x32_bf16 v[60:63], v[172:175], v[244:247], v[60:63]\n" \
  "s_sub_u32 s77, s77, 1\n" \
  "s_cbranch_scc1 Lm7_%=\n" \
  "v_add_u32 v22, 4224, v22\n" \
  "s_nop 0\n" \
  "ds_read_b128 v[160:163], v22 offset:8448\n" \
  "ds_read_b128 v[164:167], v22 offset:8704\n" \
  "ds_read_b128 v[168:171], v22 offset:8960\n" \
  "ds_read_b128 v[172:175], v22 offset:9216\n" \
  "s_waitcnt lgkmcnt(11)\n" \
  "v_mfma_f32_16x16x32_bf16 v[40:43], v[128:131], v[216:219], 0\n" \
  "v_mfma_f32_16x16x32_bf16 v[44:47], v[128:131], v[220:223], 0\n" \
  "s_waitcnt lgkmcnt(10)\n" \
  "v_mfma_f32_16x16x32_bf16 v[40:43], v[132:135], v[224:227], v[40:43]\n" \
  "v_mfma_f32_16x16x32_bf16 v[44:47], v[132:135], v[228:231], v[44:47]\n" \
  "v_cvt_pk_bf16_f32 v56, v56, v57\n" \
  "v_cvt_pk_bf16_f32 v57, v58, v59\n" \
  "v_cvt_pk_bf16_f32 v58, v60, v61\n" \
  "v_cvt_pk_bf16_f32 v59, v62, v63\n" \
  "buffer_store_dwordx4 v[56:59], v23, s[56:59], s72 offen sc1\n" \
  "s_add_u32 s72, s72, 0x1000\n" \
  "s_waitcnt lgkmcnt(9)\n" \
  "v_mfma_f32_16x16x32_bf16 v[40:43], v[136:139], v[232:235], v[40:43]\n" \
  "v_mfma_f32_16x16x32_bf16 v[44:47], v[136:139], v[236:239], v[44:47]\n" \
  "s_waitcnt lgkmcnt(8)\n" \
  "v_mfma_f32_16x16x32_bf16 v[40:43], v[140:143], v[240:243], v[40:43]\n" \
  "v_mfma_f32_16x16x32_bf16 v[44:47], v[140:143], v[244:247], v[44:47]\n" \
  "s_branch Lm2_%=\n" \
  "Lm5_%=:\n" \
  "s_nop 5\n" \
  "v_cvt_pk_bf16_f32 v40, v40, v41\n" \
  "v_cvt_pk_bf16_f32 v41, v42, v43\n" \
  "v_cvt_pk_bf16_f32 v42, v44, v45\n" \
  "v_cvt_pk_bf16_f32 v43, v46, v47\n" \
  "buffer_store_dwordx4 v[40:43], v23, s[56:59], s72 offen sc1\n" \
  "s_add_u32 s72, s72, 0x1000\n" \
  "s_branch Lm9_%=\n" \
  "Lm6_%=:\n" \
  "s_nop 5\n" \
  "v_cvt_pk_bf16_f32 v48, v48, v49\n" \
  "v_cvt_pk_bf16_f32 v49, v50, v51\n" \
  "v_cvt_pk_bf16_f32 v50, v52, v53\n" \
  "v_cvt_pk_bf16_f32 v51, v54, v55\n" \
  "buffer_store_dwordx4 v[48:51], v23, s[56:59], s72 offen sc1\n" \
  "s_add_u32 s72, s72, 0x1000\n" \
  "s_branch Lm9_%=\n" \
  "Lm7_%=:\n" \
  "s_nop 5\n" \
  "v_cvt_pk_bf16_f32 v56, v56, v57\n" \
  "v_cvt_pk_bf16_f32 v57, v58, v59\n" \
  "v_cvt_pk_bf16_f32 v58, v60, v61\n" \
  "v_cvt_pk_bf16_f32 v59, v62, v63\n" \
  "buffer_store_dwordx4 v[56:59], v23, s[56:59], s72 offen sc1\n" \
  "s_add_u32 s72, s72, 0x1000\n" \
  "Lm9_%=:\n" \
  "v_add_u32 v22, 4224, v22\n" \
  "Lm8_%=:\n" \
  "s_waitcnt lgkmcnt(0)\n" \
  "s_mov_b32 s77, s78\n" \
  "s_mov_b32 s78, 0\n" \
  "s_movk_i32 s93, 0x404\n" \
  "s_cmp_lg_u32 s77, 0\n" \
  "s_cbranch_scc1 Lm0_%=\n" \
  "s_barrier\n" \
  "ds_read_b32 v18, v18\n" \
  "s_waitcnt lgkmcnt(0)\n" \
  "v_readfirstlane_b32 %[bad], v18\n" \
  "s_cmp_lg_u32 %[bad], 0\n" \
  "s_cbranch_scc1 Lbad2_%=\n" \
  "s_cmp_lg_u32 s44, 0\n" \
  "s_cbranch_scc1 Lout%=\n" \
  "s_waitcnt vmcnt(0)\n" \
  "v_readfirstlane_b32 s63, v20\n" \
  "s_cmp_eq_u32 s63, s50\n" \
  "s_cbranch_scc1 Lout%=\n" \
  "Lep%=:\n" \
  "s_cmp_lg_u32 s44, 0\n" \
  "s_cbranch_scc1 Lout%=\n" \
  "v_mov_b64 v[84:85], s[50:51]\n" \
  "s_mov_b64 exec, 1\n" \
  "buffer_store_dwordx2 v[84:85], off, s[24:27], s70 sc1\n" \
  "s_mov_b64 exec, -1\n" \
  "Lout%=:\n" \
  "s_cmp_lg_u32 %[bad], 0\n" \
  "s_cbranch_scc1 Lsl%=\n" \
  "s_endpgm\n" \
  "Lsl%=:\n" \
  "s_mov_b32 m0, s79\n"
#define STREAM_CLOBBERS \
  "v12", "v13", "v14", "v15", "v16", "v17", "v18", "v19", "v20", "v21", "v22", "v23", "v24", "v25", "v26", \
  "v27", "v28", "v29", "v30", "v31", "v32", "v33", "v34", "v35", "v36", "v37", "v38", "v39", "v40", "v41", \
  "v42", "v43", "v44", "v45", "v46", "v47", "v48", "v49", "v50", "v51", "v52", "v53", "v54", "v55", "v56", \
  "v57", "v58", "v59", "v60", "v61", "v62", "v63", "v64", "v65", "v66", "v67", "v68", "v69", "v70", "v71", \
  "v72", "v73", "v74", "v75", "v76", "v77", "v78", "v79", "v80", "v81", "v82", "v83", "v84", "v85", "v86", \
  "v87", "v88", "v89", "v90", "v91", "v92", "v93", "v94", "v95", "v96", "v97", "v98", "v99", "v100", "v101", \
  "v102", "v103", "v104", "v105", "v106", "v107", "v108", "v109", "v110", "v111", "v112", "v113", "v114", \
  "v115", "v116", "v117", "v118", "v119", "v120", "v121", "v122", "v123", "v124", "v125", "v126", "v127", \
  "v128", "v129", "v130", "v131", "v132", "v133", "v134", "v135", "v136", "v137", "v138", "v139", "v140", \
  "v141", "v142", "v143", "v144", "v145", "v146", "v147", "v148", "v149", "v150", "v151", "v152", "v153", \
  "v154", "v155", "v156", "v157", "v158", "v159", "v160", "v161", "v162", "v163", "v164", "v165", "v166", \
  "v167", "v168", "v169", "v170", "v171", "v172", "v173", "v174", "v175", "v176", "v177", "v178", "v179", \
  "v180", "v181", "v182", "v183", "v184", "v185", "v186", "v187", "v188", "v189", "v190", "v191", "v192", \
  "v193", "v194", "v195", "v196", "v197", "v198", "v199", "v200", "v201", "v202", "v203", "v204", "v205", \
  "v206", "v207", "v208", "v209", "v210", "v211", "v212", "v213", "v214", "v215", "v216", "v217", "v218", \
  "v219", "v220", "v221", "v222", "v223", "v224", "v225", "v226", "v227", "v228", "v229", "v230", "v231", \
  "v232", "v233", "v234", "v235", "v236", "v237", "v238", "v239", "v240", "v241", "v242", "v243", "v244", \
  "v245", "v246", "v247", "v248", "v249", "vcc", "s8", "s9", "s10", "s11", "s12", "s13", "s14", "s15", "s16", \
  "s17", "s18", "s19", "s20", "s21", "s22", "s23", "s24", "s25", "s26", "s27", "s28", "s29", "s30", "s31", \
  "s33", "s34", "s35", "s36", "s37", "s38", "s39", "s40", "s41", "s42", "s43", "s44", "s45", "s46", "s47", \
  "s48", "s49", "s50", "s51", "s52", "s53", "s54", "s55", "s56", "s57", "s58", "s59", "s60", "s61", "s62", \
  "s63", "s64", "s65", "s66", "s67", "s68", "s69", "s70", "s71", "s72", "s73", "s74", "s75", "s76", "s77", \
  "s78", "s79", "s80", "s81", "s82", "s83", "s84", "s85", "s86", "s87", "s88", "s89", "s90", "s91", "s92", \
  "s93", "s94", "s95", "scc", "memory"
#define LD_CELL 34560
#define LD_DEG 35584
#define LD_FLAG 36096
#define LD_CRD 36128
#define LD_SINK 36160
#define LD_CGT 36416
#define LD_WQT 36544
#define LD_TAG 37568
#define LD_PAY 37584
#define LD_ENT 100096
#define LD_TB 106816
#define LD_ROWS 108864
#define LD_SK 129344
#define LD_HIST 134464
#define LD_HB 136512
#define LD_W0 45776
#define LD_W1 140608
#define LD_END 157504
#define WS_EP 0
#define WS_FB 256
#define WS_KF 16640
#define WS_GR 49408
#define WS_RECK 606464
#define WS_RECP 1917184
#define WS_END 7160064

namespace {

constexpr int C_ = 128;
constexpr int NMAX = 40;
constexpr int NB = 256;
constexpr int CH = 1280;
constexpr int MT = 512;
constexpr int MW = MT / 64;
constexpr int RP = 1056;
constexpr unsigned W3 = 0x00020000u;

__device__ __forceinline__ float bf2f(unsigned short v) { return __uint_as_float(((unsigned)v) << 16); }
__device__ __forceinline__ unsigned short f2bf(float f) {
  __hip_bfloat16 b = __float2bfloat16(f);
  return *reinterpret_cast<unsigned short*>(&b);
}
__device__ __forceinline__ unsigned pk2bf(float lo, float hi) {
  return __builtin_bit_cast(unsigned, __builtin_convertvector(f32x2{lo, hi}, bf16x2));
}

}

extern "C" __global__ __launch_bounds__(MT) void tfn_kernel(
    const int* __restrict__ dst, unsigned dn, unsigned dw, const int* __restrict__ src, unsigned sn, unsigned sw,
    const u32x2* __restrict__ Y, unsigned yn, unsigned yw, const u32x2* __restrict__ ef, unsigned fn, unsigned fw,
    char* ws, unsigned wn, unsigned ww, const unsigned short* __restrict__ x, unsigned xn, unsigned xw,
    u32x4* __restrict__ out, unsigned pt, int N, unsigned M2, unsigned cb, int nbk, unsigned npb,
    const unsigned short* __restrict__ wrad, unsigned rn, unsigned rw, const unsigned short* __restrict__ cg, unsigned cn,
    unsigned cw, const unsigned short* __restrict__ wmix, unsigned mn, unsigned mw) {
  __shared__ __attribute__((aligned(16))) char lds[LD_END];
  static_assert(LD_END >= LD_ROWS + NMAX * RP && LD_END <= 163840, "LDS");
  TDECL;
  MSTAMP(0);
  unsigned bad;
  asm volatile(STREAM_ASM
               : [bad] "=&s"(bad)
               : [ka] "s"(__builtin_amdgcn_kernarg_segment_ptr()), [blk] "s"(blockIdx.x), [tid] "v"(threadIdx.x)
               : STREAM_CLOBBERS);
  const bool slow = bad != 0;
  MSTAMP(6);
  if (slow) {
    const int lane = threadIdx.x & 63, w = __builtin_amdgcn_readfirstlane(threadIdx.x >> 6), E = dn / 4;
    const int bucket = (blockIdx.x & 7) * (nbk / 8) + (blockIdx.x >> 3);
    int n01[2];
#pragma unroll
    for (int h = 0; h < 2; h++) {
      unsigned c = (unsigned)(((unsigned long long)(bucket + h) * npb) >> 20);
#pragma unroll
      for (int r = 0; r < 2; r++) c += __umulhi(c, M2) < (unsigned)(bucket + h) ? 1 : 0;
      n01[h] = min(c, (unsigned)N);
    }
    const int node0 = n01[0], nn = n01[1] - n01[0];
    const unsigned mxa = LD_ROWS + ((w >> 2) * 4 + ((lane & 15) >> 2)) * RP + (lane & 3) * 64 + (lane >> 4) * 16;
    const unsigned mxo = ((w >> 2) * 4 + (lane >> 4)) * 1024 + (w & 3) * 256 + (lane & 15) * 16;
    __syncthreads();
#pragma unroll 1
    for (int it = w; it < 2 * nn; it += MW) {
      const int dl = it >> 1, ch = (it & 1) * 64 + lane;
      float acc[4] = {0.f, 0.f, 0.f, 0.f};
#pragma unroll 1
      for (int eb = 0; eb < E; eb += 64) {
        unsigned long long m = __ballot(eb + lane < E && dst[min(eb + lane, E - 1)] == node0 + dl);
#pragma unroll 1
        for (; m; m &= m - 1) {
          const int e = eb + __builtin_ctzll(m);
          const u32x2 ry = Y[e], rf = ef[e];
          float yy[4] = {bf2f(ry[0] & 0xffff), bf2f(ry[0] >> 16), bf2f(ry[1] & 0xffff), bf2f(ry[1] >> 16)};
          float ee[4] = {bf2f(rf[0] & 0xffff), bf2f(rf[0] >> 16), bf2f(rf[1] & 0xffff), bf2f(rf[1] >> 16)};
          float z = 0.f;
#pragma unroll
          for (int k = 0; k < 4; k++) z += ee[k] * bf2f(wrad[k * C_ + ch]);
          const float R = z / (1.f + __expf(-z));
          const unsigned short* xr = x + (size_t)src[e] * 512 + ch * 4;
#pragma unroll 1
          for (int o = 0; o < 4; o++) {
            float tp = 0.f;
#pragma unroll 1
            for (int i = 0; i < 4; i++) {
              float A = 0.f;
#pragma unroll
              for (int f = 0; f < 4; f++) A += yy[f] * bf2f(cg[i * 16 + f * 4 + o]);
              tp += bf2f(xr[i]) * bf2f(f2bf(A));
            }
            const float v = R * tp;
            acc[0] += o == 0 ? v : 0.f;
            acc[1] += o == 1 ? v : 0.f;
            acc[2] += o == 2 ? v : 0.f;
            acc[3] += o == 3 ? v : 0.f;
          }
        }
      }
      unsigned short* ar = reinterpret_cast<unsigned short*>(lds + LD_ROWS) + dl * (RP / 2) + (ch >> 5) * 128 + (ch & 31);
#pragma unroll
      for (int o = 0; o < 4; o++) ar[o * 32] = f2bf(acc[o]);
    }
    __syncthreads();
    const auto ors = __builtin_amdgcn_make_buffer_rsrc((void*)(out + (size_t)node0 * 64), 0, nn * 1024, 0x00020000);
#pragma unroll 1
    for (int t = 0; t < NMAX / 8; t++) {
      f32x4 a = {0.f, 0.f, 0.f, 0.f}, b = {0.f, 0.f, 0.f, 0.f};
#pragma unroll 1
      for (int k = 0; k < 4; k++) {
        const s16x8 f = *reinterpret_cast<const s16x8*>(lds + mxa + t * 8 * RP + k * 256);
        const unsigned short* wp = wmix + (32 * k + 8 * (lane >> 4)) * C_ + 32 * (w & 3) + 2 * (lane & 15);
        s16x8 w0, w1;
#pragma unroll
        for (int j = 0; j < 8; j++) {
          w0[j] = wp[j * C_];
          w1[j] = wp[j * C_ + 1];
        }
        a = __builtin_amdgcn_mfma_f32_16x16x32_bf16(f, w0, a, 0, 0, 0);
        b = __builtin_amdgcn_mfma_f32_16x16x32_bf16(f, w1, b, 0, 0, 0);
      }
      __builtin_amdgcn_raw_buffer_store_b128(u32x4{pk2bf(a[0], a[1]), pk2bf(a[2], a[3]), pk2bf(b[0], b[1]), pk2bf(b[2], b[3])},
                                             ors, mxo + t * 8192, 0, 0);
    }
  }
  MFLUSH;
}

extern "C" void kernel_launch(void* const* d_in, const int* in_sizes, int n_in, void* d_out, int out_size,
                              void* d_ws, size_t ws_size, hipStream_t stream) {
  const int N = in_sizes[0] / 512;
  const int E = in_sizes[1];
  int nbk = NB;
  while ((N + nbk - 1) / nbk > NMAX || (N % nbk == 0 && N / nbk >= NMAX)) nbk += NB;
  const unsigned long long M2l = (((unsigned long long)nbk) << 32) / (unsigned long long)N;
  const unsigned M2 = M2l > 0xffffffffull ? 0xffffffffu : (unsigned)M2l;
  const unsigned long long cb = (16ull << 32) / (unsigned long long)N;
  const bool fast = nbk == NB && E <= NB * CH && ws_size >= WS_END;
  const unsigned e4 = E * 4u;
  tfn_kernel<<<nbk, MT, 0, stream>>>(
      (const int*)d_in[2], e4, W3, (const int*)d_in[1], e4, W3, (const u32x2*)d_in[3], 2 * e4, W3, (const u32x2*)d_in[4], 2 * e4, W3,
      (char*)d_ws, WS_END, W3, (const unsigned short*)d_in[0], N * 1024u, W3, (u32x4*)d_out, fast ? 448u : 0u, N, M2,
      cb > 0xffffffull ? 0xffffffu : (unsigned)cb, nbk, (unsigned)((1ull << 52) / M2), (const unsigned short*)d_in[5], 4 * C_ * 2,
      W3, (const unsigned short*)d_in[6], 128, W3, (const unsigned short*)d_in[7], C_ * C_ * 2, W3);
}
```

```cpp
#include <hip/hip_runtime.h>
#include <hip/hip_bf16.h>
#include <stdint.h>

typedef __attribute__((ext_vector_type(2))) float f32x2;
typedef __attribute__((ext_vector_type(4))) float f32x4;
typedef __attribute__((ext_vector_type(8))) short s16x8;
typedef __attribute__((ext_vector_type(2))) unsigned int u32x2;
typedef __attribute__((ext_vector_type(4))) unsigned int u32x4;
typedef __attribute__((ext_vector_type(2))) __bf16 bf16x2;

#define STREAM_ASM \
    \
  "s_load_dwordx16 s[8:23], %[ka], 0\n" \
  "s_load_dwordx16 s[24:39], %[ka], 64\n" \
  "s_load_dwordx4 s[52:55], %[ka], 160\n" \
  "s_memrealtime s[76:77]\n" \
  "s_getpc_b64 s[40:41]\n" \
  "Lpc%=:\n" \
  "v_lshlrev_b32 v50, 7, %[tid]\n" \
  "s_mov_b32 s42, Lout%=-Lpc%=\n" \
  "s_mov_b32 s43, 0x20000\n" \
  "buffer_load_dword v208, v50, s[40:43], 0 offen\n" \
  "v_lshlrev_b32 v52, 3, %[tid]\n" \
  "v_lshlrev_b32 v53, 2, %[tid]\n" \
  "v_mov_b32 v48, 0\n" \
  "v_readfirstlane_b32 s44, %[tid]\n" \
  "v_add_u32 v49, 134464, v53\n" \
  "ds_write_b32 v49, v48\n" \
  "s_mul_i32 s63, %[blk], 5120\n" \
  "s_add_u32 s61, s63, 3584\n" \
  "s_cmp_lt_u32 s44, 384\n" \
  "s_cselect_b32 s61, s61, 0x40000000\n" \
  "s_waitcnt lgkmcnt(0)\n" \
  "s_cmp_lt_u32 s44, s34\n" \
  "s_cbranch_scc0 Lh0_%=\n" \
  "buffer_load_dwordx2 v[100:101], v52, s[8:11], s63 offen nt\n" \
  "buffer_load_dword v102, v53, s[8:11], s61 offen nt\n" \
  "s_barrier\n" \
  "buffer_load_dwordx2 v[104:105], v52, s[12:15], s63 offen nt\n" \
  "buffer_load_dword v106, v53, s[12:15], s61 offen nt\n" \
  "v_lshlrev_b32 v49, 1, v52\n" \
  "s_lshl_b32 s93, s63, 1\n" \
  "s_lshl_b32 s62, s61, 1\n" \
  "buffer_load_dwordx4 v[108:111], v49, s[16:19], s93 offen nt\n" \
  "buffer_load_dwordx4 v[114:117], v49, s[20:23], s93 offen nt\n" \
  "buffer_load_dwordx2 v[112:113], v52, s[16:19], s62 offen nt\n" \
  "buffer_load_dwordx2 v[118:119], v52, s[20:23], s62 offen nt\n" \
  "s_sub_i32 s63, s10, s63\n" \
  "s_sub_i32 s61, s10, s61\n" \
  "v_cmp_gt_i32 s[80:81], s63, v52\n" \
  "s_sub_i32 s63, s63, 4\n" \
  "v_cmp_gt_i32 s[84:85], s61, v53\n" \
  "v_cmp_gt_i32 s[82:83], s63, v52\n" \
  "s_branch Lh1_%=\n" \
  "Lh0_%=:\n" \
  "s_mov_b32 s93, 0\n" \
  "buffer_load_dwordx2 v[88:89], off, s[24:27], s93\n" \
  "s_barrier\n" \
  "s_load_dwordx8 s[8:15], %[ka], 128\n" \
  "s_mov_b64 s[80:81], 0\n" \
  "s_mov_b64 s[82:83], 0\n" \
  "s_mov_b64 s[84:85], 0\n" \
  "Lh7_%=:\n" \
  "s_cmp_eq_u32 s34, 0\n" \
  "s_cbranch_scc1 Lnf%=\n" \
    \
  "Lh1_%=:\n" \
  "ds_write_b32 v53, v48 offset:34560\n" \
  "s_lshr_b32 s44, s44, 6\n" \
  "v_and_b32 v86, 63, %[tid]\n" \
  "s_lshl_b32 s72, s44, 3\n" \
  "s_lshl_b32 s63, 1, s72\n" \
  "v_mov_b32 v93, s63\n" \
  "s_and_b32 s45, s72, 24\n" \
  "s_lshl_b64 s[46:47], -1, s72\n" \
  "s_and_b32 s46, s46, 0x01010101\n" \
  "s_and_b32 s47, s47, 0x01010101\n" \
  "s_and_b32 s73, s44, 4\n" \
  "s_add_u32 s74, s73, 134464\n" \
  "s_lshl_b32 s75, s44, 9\n" \
  "s_add_u32 s75, s75, 136512\n" \
  "v_lshlrev_b32 v13, 4, v86\n" \
  "v_lshlrev_b32 v54, 2, v86\n" \
  "v_lshl_add_u32 v52, v86, 3, s75\n" \
  "v_lshlrev_b32 v50, 5, v86\n" \
  "v_add_u32 v50, 134464, v50\n" \
  "s_and_b32 s71, %[blk], 7\n" \
  "s_lshr_b32 s66, %[blk], 3\n" \
  "s_lshl_b32 s68, s71, 5\n" \
  "s_add_u32 s68, s68, s66\n" \
  "s_and_b32 s61, s68, 2\n" \
  "s_lshl_b32 s61, s61, 9\n" \
  "v_and_b32 v55, 0xf8, v54\n" \
  "s_and_b32 s63, s68, 0xfc\n" \
  "s_lshl2_add_u32 s61, s63, s61\n" \
  "v_add_u32 v55, s44, v55\n" \
  "s_and_b32 s63, s68, 1\n" \
  "s_lshl3_add_u32 s61, s63, s61\n" \
  "v_mul_u32_u24 v17, 2176, v55\n" \
  "s_add_u32 s61, s61, 49408\n" \
  "v_mov_b32 v94, 36096\n" \
  "s_mul_i32 s63, s71, s35\n" \
  "s_lshr_b32 s63, s63, 3\n" \
  "s_lshl3_add_u32 s66, s66, s44\n" \
  "s_mul_i32 s66, s66, 1024\n" \
  "s_lshl_b32 s63, s63, 10\n" \
  "s_add_u32 s66, s66, s63\n" \
  "v_lshlrev_b32 v20, 3, v13\n" \
  "v_cmp_gt_u32 vcc, 8, v86\n" \
  "v_bfrev_b32 v51, 1\n" \
  "s_mov_b32 s79, m0\n" \
  "s_mov_b32 m0, 36160\n" \
  "v_cndmask_b32 v20, v51, v20, vcc\n" \
  "s_mov_b32 s63, 0x01010101\n" \
  "s_waitcnt vmcnt(6)\n" \
  "v_mul_hi_u32 v96, v100, s36\n" \
  "v_mul_hi_u32 v97, v101, s36\n" \
  "v_mul_hi_u32 v98, v102, s36\n" \
  "s_mov_b64 exec, s[80:81]\n" \
  "v_lshl_add_u32 v120, v96, 3, s74\n" \
  "ds_add_rtn_u32 v124, v120, v93\n" \
  "s_mov_b64 exec, s[82:83]\n" \
  "v_lshl_add_u32 v121, v97, 3, s74\n" \
  "ds_add_rtn_u32 v125, v121, v93\n" \
  "s_mov_b64 exec, s[84:85]\n" \
  "v_lshl_add_u32 v122, v98, 3, s74\n" \
  "ds_add_rtn_u32 v126, v122, v93\n" \
  "s_mov_b64 exec, -1\n" \
  "v_lshl_add_u32 v96, v96, 1, s75\n" \
  "v_lshl_add_u32 v97, v97, 1, s75\n" \
  "v_lshl_add_u32 v98, v98, 1, s75\n" \
  "s_waitcnt lgkmcnt(0)\n" \
  "s_barrier\n" \
  "ds_read_b128 v[128:131], v50\n" \
  "ds_read_b128 v[132:135], v50 offset:16\n" \
  "v_bfe_u32 v124, v124, s45, 8\n" \
  "v_bfe_u32 v125, v125, s45, 8\n" \
  "v_bfe_u32 v126, v126, s45, 8\n" \
  "s_waitcnt lgkmcnt(0)\n" \
  "v_dot4_u32_u8 v136, v128, s63, 0\n" \
  "v_dot4_u32_u8 v136, v129, s63, v136\n" \
  "v_dot4_u32_u8 v137, v130, s63, v136\n" \
  "v_dot4_u32_u8 v137, v131, s63, v137\n" \
  "v_dot4_u32_u8 v138, v132, s63, v137\n" \
  "v_dot4_u32_u8 v138, v133, s63, v138\n" \
  "v_dot4_u32_u8 v139, v134, s63, v138\n" \
  "v_dot4_u32_u8 v139, v135, s63, v139\n" \
  "v_dot4_u32_u8 v140, v128, s46, 0\n" \
  "v_dot4_u32_u8 v141, v130, s46, 0\n" \
  "v_dot4_u32_u8 v142, v132, s46, 0\n" \
  "v_add_u32_dpp v87, v139, v139 row_shr:1 row_mask:0xf bank_mask:0xf bound_ctrl:1\n" \
  "v_dot4_u32_u8 v143, v134, s46, 0\n" \
  "v_dot4_u32_u8 v140, v129, s47, v140\n" \
  "v_add_u32_dpp v87, v87, v87 row_shr:2 row_mask:0xf bank_mask:0xf bound_ctrl:1\n" \
  "v_dot4_u32_u8 v141, v131, s47, v141\n" \
  "v_dot4_u32_u8 v142, v133, s47, v142\n" \
  "v_add_u32_dpp v87, v87, v87 row_shr:4 row_mask:0xf bank_mask:0xf bound_ctrl:1\n" \
  "v_dot4_u32_u8 v143, v135, s47, v143\n" \
  "s_mov_b32 s63, 0x10001\n" \
  "v_add_u32_dpp v87, v87, v87 row_shr:8 row_mask:0xf bank_mask:0xf bound_ctrl:1\n" \
  "v_sub_u32 v140, v136, v140\n" \
  "v_sub_u32 v141, v137, v141\n" \
  "v_add_u32_dpp v87, v87, v87 row_bcast:15 row_mask:0xa bank_mask:0xf\n" \
  "v_sub_u32 v142, v138, v142\n" \
  "v_sub_u32 v143, v139, v143\n" \
  "v_add_u32_dpp v87, v87, v87 row_bcast:31 row_mask:0xc bank_mask:0xf\n" \
  "v_lshl_or_b32 v140, v141, 16, v140\n" \
  "v_sub_u32 v87, v87, v139\n" \
  "v_lshl_or_b32 v141, v143, 16, v142\n" \
  "s_cmp_eq_u32 s44, 7\n" \
  "v_mad_u32_u24 v140, v87, s63, v140\n" \
  "v_mad_u32_u24 v141, v87, s63, v141\n" \
  "s_cbranch_scc1 Lh2_%=\n" \
  "ds_write_b64 v52, v[140:141]\n" \
  "ds_read_u16 v96, v96\n" \
  "ds_read_u16 v97, v97\n" \
  "ds_read_u16 v98, v98\n" \
  "s_mov_b32 s63, 129344\n" \
  "s_mov_b32 s93, 108864\n" \
  "s_lshl_b32 s74, s44, 10\n" \
  "v_add_u32 v50, s74, v13\n" \
  "v_add_u32 v51, s93, v50\n" \
  "s_waitcnt vmcnt(4)\n" \
  "v_lshl_or_b32 v104, v100, 24, v104\n" \
  "v_lshl_or_b32 v105, v101, 24, v105\n" \
  "v_lshl_or_b32 v106, v102, 24, v106\n" \
  "s_waitcnt lgkmcnt(0)\n" \
  "s_mov_b64 exec, s[80:81]\n" \
  "v_add_u32 v124, v124, v96\n" \
  "v_lshl_add_u32 v120, v124, 2, s63\n" \
  "v_lshl_add_u32 v124, v124, 4, s93\n" \
  "ds_write_b32 v120, v104\n" \
  "s_mov_b64 exec, s[82:83]\n" \
  "v_add_u32 v125, v125, v97\n" \
  "v_lshl_add_u32 v121, v125, 2, s63\n" \
  "v_lshl_add_u32 v125, v125, 4, s93\n" \
  "ds_write_b32 v121, v105\n" \
  "s_mov_b64 exec, s[84:85]\n" \
  "v_add_u32 v126, v126, v98\n" \
  "v_lshl_add_u32 v122, v126, 2, s63\n" \
  "v_lshl_add_u32 v126, v126, 4, s93\n" \
  "ds_write_b32 v122, v106\n" \
  "s_waitcnt vmcnt(0)\n" \
  "s_mov_b64 exec, s[84:85]\n" \
  "ds_write2_b64 v126, v[112:113], v[118:119] offset1:1\n" \
  "s_mov_b64 exec, s[82:83]\n" \
  "ds_write2_b64 v125, v[110:111], v[116:117] offset1:1\n" \
  "s_mov_b64 exec, s[80:81]\n" \
  "ds_write2_b64 v124, v[108:109], v[114:115] offset1:1\n" \
  "s_mov_b64 exec, -1\n" \
  "s_mul_i32 s72, s45, 528\n" \
  "s_cmp_lt_u32 s44, 4\n" \
  "s_cselect_b32 s62, 45776, 0\n" \
  "s_cselect_b32 s62, s62, 140608\n" \
  "s_add_u32 m0, s72, s62\n" \
  "s_lshl_b32 s72, s44, 12\n" \
  "buffer_load_dwordx4 v13, s[52:55], s72 offen lds\n" \
  "buffer_load_dwordx4 v13, s[52:55], s72 offen offset:1024 lds\n" \
  "s_add_u32 m0, m0, 64\n" \
  "s_nop 0\n" \
  "buffer_load_dwordx4 v13, s[52:55], s72 offen offset:2048 lds\n" \
  "buffer_load_dwordx4 v13, s[52:55], s72 offen offset:3072 lds\n" \
  "s_mov_b32 m0, 36160\n" \
  "s_mul_i32 s72, %[blk], 20480\n" \
  "s_lshr_b32 s62, s72, 2\n" \
  "s_add_u32 s72, s72, 1917184\n" \
  "s_add_u32 s62, s62, 607488\n" \
  "s_add_u32 s74, s72, 7168\n" \
  "s_add_u32 s75, s72, 14336\n" \
  "s_sub_u32 s63, s62, 7168\n" \
  "s_cmp_eq_u32 s44, 6\n" \
  "s_cselect_b32 s75, s63, s75\n" \
  "s_cmp_gt_u32 s44, 3\n" \
  "s_waitcnt lgkmcnt(0)\n" \
  "s_barrier\n" \
  "ds_read_b128 v[192:195], v51 offset:0\n" \
  "ds_read_b128 v[196:199], v51 offset:7168\n" \
  "ds_read_b128 v[200:203], v51 offset:14336\n" \
  "ds_read_b128 v[204:207], v51 offset:21504\n" \
  "s_waitcnt lgkmcnt(3)\n" \
  "buffer_store_dwordx4 v[192:195], v50, s[24:27], s72 offen sc1\n" \
  "s_waitcnt lgkmcnt(2)\n" \
  "buffer_store_dwordx4 v[196:199], v50, s[24:27], s74 offen sc1\n" \
  "s_waitcnt lgkmcnt(1)\n" \
  "buffer_store_dwordx4 v[200:203], v50, s[24:27], s75 offen sc1\n" \
  "s_cbranch_scc1 Lc1_%=\n" \
  "s_waitcnt lgkmcnt(0)\n" \
  "buffer_store_dwordx4 v[204:207], v50, s[24:27], s62 offen sc1\n" \
    \
  "Lc1_%=:\n" \
  "s_mov_b32 s63, 0x80008\n" \
  "s_mov_b32 s93, 0x80008000\n" \
  "v_mul_lo_u32 v52, %[tid], s63\n" \
  "v_lshlrev_b32 v53, 4, %[tid]\n" \
  "v_add_u32 v49, 0x10000, v53\n" \
  "v_cmp_gt_u32 s[90:91], 32, %[tid]\n" \
  "v_cmp_gt_u32 vcc, 368, %[tid]\n" \
  "v_add_u32 v104, 0xf810f80, v52\n" \
  "v_add_u32 v105, 0xf830f82, v52\n" \
  "v_add_u32 v106, 0xf850f84, v52\n" \
  "v_add_u32 v107, 0xf870f86, v52\n" \
  "v_or_b32 v108, s93, v104\n" \
  "v_or_b32 v109, s93, v105\n" \
  "v_or_b32 v110, s93, v106\n" \
  "v_or_b32 v111, s93, v107\n" \
  "ds_write_b128 v53, v[104:107] offset:8192\n" \
  "ds_write_b128 v49, v[108:111] offset:8192\n" \
  "v_add_u32 v112, 0x1f811f80, v52\n" \
  "v_add_u32 v113, 0x1f831f82, v52\n" \
  "v_add_u32 v114, 0x1f851f84, v52\n" \
  "v_add_u32 v115, 0x1f871f86, v52\n" \
  "v_or_b32 v116, s93, v112\n" \
  "v_or_b32 v117, s93, v113\n" \
  "v_or_b32 v118, s93, v114\n" \
  "v_or_b32 v119, s93, v115\n" \
  "ds_write_b128 v53, v[112:115] offset:16384\n" \
  "ds_write_b128 v49, v[116:119] offset:16384\n" \
  "s_mov_b64 exec, vcc\n" \
  "v_add_u32 v120, 0x2f812f80, v52\n" \
  "v_add_u32 v121, 0x2f832f82, v52\n" \
  "v_add_u32 v122, 0x2f852f84, v52\n" \
  "v_add_u32 v123, 0x2f872f86, v52\n" \
  "v_or_b32 v124, s93, v120\n" \
  "v_or_b32 v125, s93, v121\n" \
  "v_or_b32 v126, s93, v122\n" \
  "v_or_b32 v127, s93, v123\n" \
  "ds_write_b128 v53, v[120:123] offset:24576\n" \
  "ds_write_b128 v49, v[124:127] offset:24576\n" \
  "s_mov_b64 exec, -1\n" \
  "v_add_u32 v96, 0xff80ff80, v52\n" \
  "v_add_u32 v97, 0xff82ff82, v52\n" \
  "v_add_u32 v98, 0xff84ff84, v52\n" \
  "v_add_u32 v99, 0xff86ff86, v52\n" \
  "v_lshrrev_b32 v52, 1, v52\n" \
  "s_mov_b64 exec, s[90:91]\n" \
  "v_add_u32 v96, 0x0, v52\n" \
  "v_add_u32 v97, 0x20001, v52\n" \
  "v_add_u32 v98, 0x20002, v52\n" \
  "v_add_u32 v99, 0x40003, v52\n" \
  "s_mov_b64 exec, -1\n" \
  "v_or_b32 v100, s93, v96\n" \
  "v_or_b32 v101, s93, v97\n" \
  "v_or_b32 v102, s93, v98\n" \
  "v_or_b32 v103, s93, v99\n" \
  "ds_write_b128 v53, v[96:99] offset:0\n" \
  "ds_write_b128 v49, v[100:103] offset:0\n" \
  "buffer_load_dwordx2 v[44:45], v17, s[24:27], s61 offen sc1\n" \
  "s_mov_b64 s[88:89], 0\n" \
  "s_mov_b32 s92, 0\n" \
  "v_mov_b32 v92, 1\n" \
  "v_and_b32 v41, 4, v54\n" \
  "s_movk_i32 s63, 5120\n" \
  "v_mad_u32_u24 v40, v55, s63, v41\n" \
  "s_lshl_b32 s63, s44, 10\n" \
  "s_add_u32 s63, s63, 37584\n" \
  "v_add_u32 v41, s63, v41\n" \
  "s_mul_i32 s90, s68, s39\n" \
  "s_mul_hi_u32 s91, s68, s39\n" \
  "s_lshr_b64 s[90:91], s[90:91], 20\n" \
  "s_mul_hi_u32 s63, s90, s36\n" \
  "s_cmp_lt_u32 s63, s68\n" \
  "s_addc_u32 s90, s90, 0\n" \
  "s_mul_hi_u32 s63, s90, s36\n" \
  "s_cmp_lt_u32 s63, s68\n" \
  "s_addc_u32 s90, s90, 0\n" \
  "s_min_u32 s67, s90, s35\n" \
  "s_add_u32 s68, s68, 1\n" \
  "s_mul_i32 s90, s68, s39\n" \
  "s_mul_hi_u32 s91, s68, s39\n" \
  "s_lshr_b64 s[90:91], s[90:91], 20\n" \
  "s_mul_hi_u32 s63, s90, s36\n" \
  "s_cmp_lt_u32 s63, s68\n" \
  "s_addc_u32 s90, s90, 0\n" \
  "s_mul_hi_u32 s63, s90, s36\n" \
  "s_cmp_lt_u32 s63, s68\n" \
  "s_addc_u32 s90, s90, 0\n" \
  "s_min_u32 s68, s90, s35\n" \
  "s_sub_u32 s68, s68, s67\n" \
  "s_lshl_b32 s71, s71, 1\n" \
  "v_lshlrev_b32 v50, 5, v86\n" \
  "v_and_b32 v50, 0x60, v50\n" \
  "v_bfrev_b32 v51, 1\n" \
  "s_lshl_b32 s63, %[blk], 6\n" \
  "s_add_u32 s63, s63, 256\n" \
  "s_cmp_lg_u32 s44, 7\n" \
  "s_waitcnt vmcnt(1) lgkmcnt(0)\n" \
  "s_barrier\n" \
  "s_cbranch_scc1 Ls1_%=\n" \
  "s_mov_b64 exec, 1\n" \
  "buffer_store_dwordx2 v[88:89], off, s[24:27], s63 sc1\n" \
  "s_mov_b64 exec, -1\n" \
    \
  "Ls1_%=:\n" \
  "ds_read_b96 v[56:58], v94 offset:1472\n" \
  "ds_read_b128 v[24:27], v50 offset:36416\n" \
  "ds_read_b128 v[28:31], v50 offset:36432\n" \
  "ds_read_b64 v[32:33], v13 offset:36544\n" \
  "ds_read_b64 v[36:37], v13 offset:36552\n" \
  "s_waitcnt lgkmcnt(4)\n" \
  "v_readfirstlane_b32 s50, v56\n" \
  "v_readfirstlane_b32 s51, v57\n" \
  "v_readfirstlane_b32 s69, v58\n" \
  "s_lshl_b32 s64, s50, 22\n" \
  "s_lshr_b64 s[90:91], s[50:51], 10\n" \
  "s_mov_b32 s65, s90\n" \
  "s_cmp_lg_u32 s44, 7\n" \
  "s_waitcnt vmcnt(1)\n" \
  "s_cbranch_scc0 Lchk%=\n" \
  "s_waitcnt vmcnt(0)\n" \
  "s_branch Lchk%=\n" \
  "Lpoll%=:\n" \
  "buffer_load_dwordx2 v[44:45], v17, s[24:27], s61 offen sc1\n" \
  "s_waitcnt vmcnt(0)\n" \
  "Lchk%=:\n" \
  "v_and_b32 v50, 0xffc00000, v44\n" \
  "v_cmp_eq_u32 s[90:91], s65, v45\n" \
  "v_cmp_eq_u32 vcc, s64, v50\n" \
  "s_and_b64 vcc, vcc, s[90:91]\n" \
  "s_andn2_b64 vcc, vcc, s[88:89]\n" \
  "s_or_b64 s[88:89], s[88:89], vcc\n" \
  "v_cndmask_b32 v48, v48, v44, vcc\n" \
  "s_cmp_eq_u64 s[88:89], -1\n" \
  "v_cndmask_b32 v17, v17, v51, vcc\n" \
  "s_cbranch_scc1 Lseen%=\n" \
  "s_add_u32 s92, s92, 1\n" \
  "s_cmp_lt_u32 s92, 4096\n" \
  "s_cbranch_scc1 Lpoll%=\n" \
  "v_mov_b32 v48, 0\n" \
  "Lseen%=:\n" \
  "buffer_load_dword v20, s[28:31], s66 offen lds\n" \
  "s_add_u32 s66, s66, 0x40000\n" \
  "buffer_load_dword v20, s[28:31], s66 offen lds\n" \
  "s_add_u32 s66, s66, 0x40000\n" \
  "v_bfe_u32 v49, v48, 11, 11\n" \
  "v_and_b32 v50, 0x7ff, v48\n" \
  "v_bfe_u32 v52, v13, 4, 1\n" \
  "v_add_u32_dpp v87, v49, v49 row_shr:1 row_mask:0xf bank_mask:0xf bound_ctrl:1\n" \
  "v_lshl_add_u32 v50, v50, 2, v40\n" \
  "v_lshlrev_b32 v53, v52, v49\n" \
  "v_add_u32_dpp v87, v87, v87 row_shr:2 row_mask:0xf bank_mask:0xf bound_ctrl:1\n" \
  "v_sub_u32 v49, v49, v52\n" \
  "v_and_b32 v85, -8, v41\n" \
  "v_add_u32_dpp v87, v87, v87 row_shr:4 row_mask:0xf bank_mask:0xf bound_ctrl:1\n" \
  "v_add_u32 v85, v85, v54\n" \
  "s_nop 0\n" \
  "v_add_u32_dpp v87, v87, v87 row_shr:8 row_mask:0xf bank_mask:0xf bound_ctrl:1\n" \
  "s_nop 1\n" \
  "v_add_u32_dpp v87, v87, v87 row_bcast:15 row_mask:0xa bank_mask:0xf\n" \
  "s_nop 1\n" \
  "v_add_u32_dpp v87, v87, v87 row_bcast:31 row_mask:0xc bank_mask:0xf\n" \
  "v_sub_u32 v53, v87, v53\n" \
  "v_readlane_b32 s62, v87, 63\n" \
  "v_lshl_add_u32 v53, v53, 1, v41\n" \
  "v_lshl_add_u32 v49, v49, 2, v53\n" \
  "s_lshr_b32 s62, s62, 1\n" \
  "s_cmp_lt_u32 s62, 256\n" \
  "s_cbranch_scc1 Ll0_%=\n" \
  "s_mov_b32 s62, 0\n" \
  "ds_or_b32 v94, v92\n" \
  "s_branch Ll2_%=\n" \
  "Ll0_%=:\n" \
  "v_cmp_lt_i32 vcc, v53, v49\n" \
  "s_mov_b64 exec, vcc\n" \
  "Ll1_%=:\n" \
  "ds_write_b32 v53, v50\n" \
  "v_add_u32 v53, 8, v53\n" \
  "v_cmp_lt_i32 vcc, v53, v49\n" \
  "v_add_u32 v50, 8, v50\n" \
  "s_and_b64 exec, vcc, vcc\n" \
  "s_cbranch_scc1 Ll1_%=\n" \
  "s_mov_b64 exec, -1\n" \
    \
  "Ll2_%=:\n" \
  "ds_read2st64_b32 v[56:57], v85 offset1:1\n" \
  "ds_read2st64_b32 v[58:59], v85 offset0:2 offset1:3\n" \
  "v_mov_b32 v50, s62\n" \
  "s_mov_b64 exec, 1\n" \
  "ds_add_u32 v94, v50 offset:4\n" \
  "s_mov_b64 exec, -1\n" \
  "v_cmp_gt_i32 s[80:81], s62, v86\n" \
  "s_sub_i32 s63, s62, 64\n" \
  "v_cmp_gt_i32 s[82:83], s63, v86\n" \
  "s_sub_i32 s63, s62, 128\n" \
  "v_cmp_gt_i32 s[84:85], s63, v86\n" \
  "s_sub_i32 s63, s62, 192\n" \
  "v_cmp_gt_i32 s[86:87], s63, v86\n" \
  "v_bfrev_b32 v50, 4\n" \
  "s_waitcnt lgkmcnt(1)\n" \
  "v_cndmask_b32 v56, v50, v56, s[80:81]\n" \
  "v_cndmask_b32 v57, v50, v57, s[82:83]\n" \
  "v_cndmask_b32 v58, v50, v58, s[84:85]\n" \
  "v_cndmask_b32 v59, v50, v59, s[86:87]\n" \
  "s_sleep 4\n" \
  "v_mul_u32_u24 v18, 64, v55\n" \
  "s_mov_b32 s93, 256\n" \
  "s_mov_b64 s[88:89], 0\n" \
  "s_mov_b32 s63, 0xbfb8aa3b\n" \
  "v_lshlrev_b32 v100, 17, %[tid]\n" \
  "v_add_u32 v103, 0x3f810000, v100\n" \
  "v_add_u32 v102, 0x3f800000, v100\n" \
  "v_add_u32 v101, 0x3b810000, v100\n" \
  "v_add_u32 v100, 0x3b800000, v100\n" \
  "v_mul_f32 v104, s63, v100\n" \
  "v_mul_f32 v105, s63, v101\n" \
  "v_mul_f32 v106, s63, v102\n" \
  "v_mul_f32 v107, s63, v103\n" \
  "v_exp_f32 v104, v104\n" \
  "v_exp_f32 v105, v105\n" \
  "v_exp_f32 v106, v106\n" \
  "v_exp_f32 v107, v107\n" \
  "v_lshlrev_b32 v96, 2, %[tid]\n" \
  "v_add_f32 v108, 1.0, v104\n" \
  "v_add_f32 v109, 1.0, v105\n" \
  "v_add_f32 v110, 1.0, v106\n" \
  "v_add_f32 v111, 1.0, v107\n" \
  "v_rcp_f32 v108, v108\n" \
  "v_rcp_f32 v109, v109\n" \
  "v_rcp_f32 v110, v110\n" \
  "v_rcp_f32 v111, v111\n" \
  "v_add_u32 v97, 0x10000, v96\n" \
  "v_pk_mul_f32 v[100:101], v[100:101], v[108:109]\n" \
  "v_pk_mul_f32 v[102:103], v[102:103], v[110:111]\n" \
  "v_pk_mul_f32 v[104:105], v[104:105], v[100:101] neg_lo:[0,1] neg_hi:[0,1]\n" \
  "v_pk_mul_f32 v[106:107], v[106:107], v[102:103] neg_lo:[0,1] neg_hi:[0,1]\n" \
  "v_cvt_pk_bf16_f32 v100, v100, v101\n" \
  "v_cvt_pk_bf16_f32 v102, v102, v103\n" \
  "v_cvt_pk_bf16_f32 v104, v104, v105\n" \
  "v_cvt_pk_bf16_f32 v106, v106, v107\n" \
  "ds_write2st64_b32 v96, v100, v102 offset0:119 offset1:127\n" \
  "ds_write2st64_b32 v97, v104, v106 offset0:119 offset1:127\n" \
  "buffer_load_dwordx2 v[44:45], v18, s[24:27], s93 offen sc1\n" \
  "v_lshrrev_b32 v50, 8, v13\n" \
  "v_mul_u32_u24 v50, 2112, v50\n" \
  "v_and_or_b32 v50, v54, 60, v50\n" \
  "s_lshl3_add_u32 s63, s45, 45776\n" \
  "v_add_u32 v50, s63, v50\n" \
  "v_add_u32 v52, 94832, v50\n" \
  "ds_read2st64_b32 v[128:129], v50 offset0:0 offset1:1\n" \
  "ds_read2st64_b32 v[130:131], v50 offset0:2 offset1:3\n" \
  "ds_read2st64_b32 v[132:133], v50 offset0:4 offset1:5\n" \
  "ds_read2st64_b32 v[134:135], v50 offset0:6 offset1:7\n" \
  "ds_read2st64_b32 v[136:137], v50 offset0:33 offset1:34\n" \
  "ds_read2st64_b32 v[138:139], v50 offset0:35 offset1:36\n" \
  "ds_read2st64_b32 v[140:141], v50 offset0:37 offset1:38\n" \
  "ds_read2st64_b32 v[142:143], v50 offset0:39 offset1:40\n" \
  "ds_read2st64_b32 v[144:145], v52 offset0:0 offset1:1\n" \
  "ds_read2st64_b32 v[146:147], v52 offset0:2 offset1:3\n" \
  "ds_read2st64_b32 v[148:149], v52 offset0:4 offset1:5\n" \
  "ds_read2st64_b32 v[150:151], v52 offset0:6 offset1:7\n" \
  "s_waitcnt lgkmcnt(8)\n" \
  "ds_read2st64_b32 v[152:153], v52 offset0:33 offset1:34\n" \
  "ds_read2st64_b32 v[154:155], v52 offset0:35 offset1:36\n" \
  "ds_read2st64_b32 v[156:157], v52 offset0:37 offset1:38\n" \
  "ds_read2st64_b32 v[158:159], v52 offset0:39 offset1:40\n" \
  "v_mov_b64 v[160:161], 0\n" \
  "v_mov_b64 v[162:163], 0\n" \
  "v_mov_b64 v[164:165], 0\n" \
  "v_mov_b64 v[166:167], 0\n" \
  "v_mov_b64 v[168:169], 0\n" \
  "v_mov_b64 v[170:171], 0\n" \
  "v_mov_b64 v[172:173], 0\n" \
  "v_mov_b64 v[174:175], 0\n" \
  "v_mov_b64 v[176:177], 0\n" \
  "v_mov_b64 v[178:179], 0\n" \
  "v_mov_b64 v[180:181], 0\n" \
  "v_mov_b64 v[182:183], 0\n" \
  "v_mov_b64 v[184:185], 0\n" \
  "v_mov_b64 v[186:187], 0\n" \
  "v_mov_b64 v[188:189], 0\n" \
  "v_mov_b64 v[190:191], 0\n" \
  "s_mov_b32 s63, 0x05040100\n" \
  "s_mov_b32 s72, 0x07060302\n" \
  "s_waitcnt lgkmcnt(0)\n" \
  "v_perm_b32 v216, v129, v128, s63\n" \
  "v_perm_b32 v220, v129, v128, s72\n" \
  "v_perm_b32 v217, v131, v130, s63\n" \
  "v_perm_b32 v221, v131, v130, s72\n" \
  "v_perm_b32 v218, v133, v132, s63\n" \
  "v_perm_b32 v222, v133, v132, s72\n" \
  "v_perm_b32 v219, v135, v134, s63\n" \
  "v_perm_b32 v223, v135, v134, s72\n" \
  "v_perm_b32 v224, v137, v136, s63\n" \
  "v_perm_b32 v228, v137, v136, s72\n" \
  "v_perm_b32 v225, v139, v138, s63\n" \
  "v_perm_b32 v229, v139, v138, s72\n" \
  "v_perm_b32 v226, v141, v140, s63\n" \
  "v_perm_b32 v230, v141, v140, s72\n" \
  "v_perm_b32 v227, v143, v142, s63\n" \
  "v_perm_b32 v231, v143, v142, s72\n" \
  "v_perm_b32 v232, v145, v144, s63\n" \
  "v_perm_b32 v236, v145, v144, s72\n" \
  "v_perm_b32 v233, v147, v146, s63\n" \
  "v_perm_b32 v237, v147, v146, s72\n" \
  "v_perm_b32 v234, v149, v148, s63\n" \
  "v_perm_b32 v238, v149, v148, s72\n" \
  "v_perm_b32 v235, v151, v150, s63\n" \
  "v_perm_b32 v239, v151, v150, s72\n" \
  "v_perm_b32 v240, v153, v152, s63\n" \
  "v_perm_b32 v244, v153, v152, s72\n" \
  "v_perm_b32 v241, v155, v154, s63\n" \
  "v_perm_b32 v245, v155, v154, s72\n" \
  "v_perm_b32 v242, v157, v156, s63\n" \
  "v_perm_b32 v246, v157, v156, s72\n" \
  "v_perm_b32 v243, v159, v158, s63\n" \
  "v_perm_b32 v247, v159, v158, s72\n" \
  "v_mov_b64 v[128:129], 0\n" \
  "v_mov_b64 v[130:131], 0\n" \
  "v_mov_b64 v[132:133], 0\n" \
  "v_mov_b64 v[134:135], 0\n" \
  "v_mov_b64 v[136:137], 0\n" \
  "v_mov_b64 v[138:139], 0\n" \
  "v_mov_b64 v[140:141], 0\n" \
  "v_mov_b64 v[142:143], 0\n" \
  "v_mov_b64 v[144:145], 0\n" \
  "v_mov_b64 v[146:147], 0\n" \
  "v_mov_b64 v[148:149], 0\n" \
  "v_mov_b64 v[150:151], 0\n" \
  "v_mov_b64 v[152:153], 0\n" \
  "v_mov_b64 v[154:155], 0\n" \
  "v_mov_b64 v[156:157], 0\n" \
  "v_mov_b64 v[158:159], 0\n" \
  "Ld1_%=:\n" \
  "buffer_load_dwordx2 v[46:47], v18, s[24:27], s93 offen sc1\n" \
  "s_waitcnt vmcnt(1)\n" \
  "v_cmp_eq_u32 s[90:91], s65, v45\n" \
  "v_cmp_eq_u32 vcc, s64, v44\n" \
  "s_mov_b32 s63, 606464\n" \
  "s_and_b64 vcc, vcc, s[90:91]\n" \
  "s_or_b64 s[88:89], s[88:89], vcc\n" \
  "s_cmp_eq_u64 s[88:89], -1\n" \
  "s_cbranch_scc1 Ld2_%=\n" \
  "v_cndmask_b32 v18, v18, v51, vcc\n" \
  "buffer_load_dwordx2 v[44:45], v18, s[24:27], s93 offen sc1\n" \
  "s_waitcnt vmcnt(1)\n" \
  "v_cmp_eq_u32 s[90:91], s65, v47\n" \
  "v_cmp_eq_u32 vcc, s64, v46\n" \
  "s_mov_b32 s63, 606464\n" \
  "s_and_b64 vcc, vcc, s[90:91]\n" \
  "s_or_b64 s[88:89], s[88:89], vcc\n" \
  "s_cmp_eq_u64 s[88:89], -1\n" \
  "s_cbranch_scc1 Ld2_%=\n" \
  "v_cndmask_b32 v18, v18, v51, vcc\n" \
  "s_add_u32 s92, s92, 1\n" \
  "s_cmp_lt_u32 s92, 4096\n" \
  "s_cbranch_scc1 Ld1_%=\n" \
  "s_mov_b64 s[80:81], 0\n" \
  "s_mov_b64 s[82:83], 0\n" \
  "s_mov_b64 s[84:85], 0\n" \
  "s_mov_b64 s[86:87], 0\n" \
    \
  "Ld2_%=:\n" \
  "s_mov_b32 s93, 1917184\n" \
  "s_cmp_le_i32 s62, 192\n" \
  "s_cbranch_scc1 Lk596_%=\n" \
  "buffer_load_dword v63, v59, s[24:27], s63 offen sc1\n" \
  "v_lshlrev_b32 v59, 2, v59\n" \
  "buffer_load_dwordx4 v[204:207], v59, s[24:27], s93 offen sc1\n" \
  "Lk596_%=:\n" \
  "buffer_load_dword v60, v56, s[24:27], s63 offen sc1\n" \
  "buffer_load_dword v61, v57, s[24:27], s63 offen sc1\n" \
  "buffer_load_dword v62, v58, s[24:27], s63 offen sc1\n" \
  "v_lshlrev_b32 v56, 2, v56\n" \
  "v_lshlrev_b32 v57, 2, v57\n" \
  "v_lshlrev_b32 v58, 2, v58\n" \
  "Ld4_%=:\n" \
  "s_lshl_b32 s75, s44, 8\n" \
  "s_add_u32 s75, s75, 106816\n" \
  "s_mov_b32 s52, s28\n" \
  "s_or_b32 s53, s29, 0x4000000\n" \
  "s_mov_b32 s54, s35\n" \
  "s_mov_b32 s55, 0x20000\n" \
  "s_lshl_b32 s63, s67, 10\n" \
  "s_add_u32 s56, s32, s63\n" \
  "s_addc_u32 s57, s33, 0\n" \
  "s_lshl_b32 s58, s68, 10\n" \
  "s_mov_b32 s59, 0x20000\n" \
  "s_lshl_b32 s67, s67, 24\n" \
  "s_mov_b32 s70, 0\n" \
  "s_mul_i32 s63, s44, 3328\n" \
  "s_add_u32 s63, s63, 37584\n" \
  "v_add_u32 v21, s63, v13\n" \
  "v_and_b32 v15, 0xf0, v54\n" \
  "v_add_u32 v15, s63, v15\n" \
  "v_and_b32 v23, 0xf0, v13\n" \
  "v_add3_u32 v16, v23, s63, 8\n" \
  "s_mul_i32 s63, s44, 832\n" \
  "s_add_u32 s63, s63, 100096\n" \
  "v_mov_b32 v14, s63\n" \
  "v_and_b32 v50, 0x300, v13\n" \
  "v_and_or_b32 v19, v54, 60, v50\n" \
  "v_add_u32 v19, 108864, v19\n" \
  "v_lshl_add_u32 v23, v50, 2, v23\n" \
  "s_lshl_b32 s63, s45, 5\n" \
  "v_add_u32 v23, s63, v23\n" \
  "v_bfe_u32 v50, v86, 2, 2\n" \
  "v_mul_u32_u24 v22, 1056, v50\n" \
  "v_and_b32 v50, 48, v13\n" \
  "v_lshl_add_u32 v22, v50, 2, v22\n" \
  "v_and_b32 v50, 48, v86\n" \
  "s_mov_b32 s63, 108864\n" \
  "v_add3_u32 v22, v22, v50, s63\n" \
  "v_mul_u32_u24 v50, 26, v86\n" \
  "v_lshrrev_b32 v50, 8, v50\n" \
  "v_mad_i32_i24 v50, v50, -10, v86\n" \
  "v_lshlrev_b32 v42, 19, v50\n" \
  "s_lshl_b32 s63, s73, 28\n" \
  "s_sub_u32 s67, s67, s63\n" \
  "s_cmp_lt_u32 s92, 4096\n" \
  "s_cbranch_scc1 Lq2_%=\n" \
  "v_mov_b32 v53, 3\n" \
  "ds_or_b32 v94, v53\n" \
  "Lq2_%=:\n" \
  "v_mov_b32 v50, 2\n" \
  "s_waitcnt vmcnt(0)\n" \
  "v_subrev_u32 v60, s67, v60\n" \
  "s_mov_b64 exec, s[80:81]\n" \
  "v_lshlrev_b32_sdwa v64, v50, v60 dst_sel:DWORD dst_unused:UNUSED_PAD src0_sel:DWORD src1_sel:BYTE_3\n" \
  "ds_add_rtn_u32 v80, v64, v93 offset:35584\n" \
  "v_subrev_u32 v61, s67, v61\n" \
  "s_mov_b64 exec, s[82:83]\n" \
  "v_lshlrev_b32_sdwa v65, v50, v61 dst_sel:DWORD dst_unused:UNUSED_PAD src0_sel:DWORD src1_sel:BYTE_3\n" \
  "ds_add_rtn_u32 v81, v65, v93 offset:35584\n" \
  "v_subrev_u32 v62, s67, v62\n" \
  "s_mov_b64 exec, s[84:85]\n" \
  "v_lshlrev_b32_sdwa v66, v50, v62 dst_sel:DWORD dst_unused:UNUSED_PAD src0_sel:DWORD src1_sel:BYTE_3\n" \
  "ds_add_rtn_u32 v82, v66, v93 offset:35584\n" \
  "s_cmp_le_i32 s62, 192\n" \
  "s_cbranch_scc1 Lk670_%=\n" \
  "v_subrev_u32 v63, s67, v63\n" \
  "s_mov_b64 exec, s[86:87]\n" \
  "v_lshlrev_b32_sdwa v67, v50, v63 dst_sel:DWORD dst_unused:UNUSED_PAD src0_sel:DWORD src1_sel:BYTE_3\n" \
  "ds_add_rtn_u32 v83, v67, v93 offset:35584\n" \
  "Lk670_%=:\n" \
  "s_mov_b64 exec, -1\n" \
  "buffer_load_dwordx4 v[192:195], v56, s[24:27], s93 offen sc1\n" \
  "buffer_load_dwordx4 v[196:199], v57, s[24:27], s93 offen sc1\n" \
  "buffer_load_dwordx4 v[200:203], v58, s[24:27], s93 offen sc1\n" \
  "s_mov_b32 s63, 0x01010101\n" \
  "s_waitcnt lgkmcnt(0)\n" \
  "s_barrier\n" \
  "ds_read2st64_b32 v[44:45], v54 offset0:139 offset1:140\n" \
  "ds_read_b32 v86, v94 offset:4\n" \
  "ds_bpermute_b32 v76, v64, v42\n" \
  "ds_bpermute_b32 v77, v65, v42\n" \
  "ds_bpermute_b32 v78, v66, v42\n" \
  "ds_bpermute_b32 v79, v67, v42\n" \
  "buffer_load_dword v20, s[28:31], s66 offen lds\n" \
  "s_add_u32 s66, s66, 0x40000\n" \
  "buffer_load_dword v20, s[28:31], s66 offen lds\n" \
  "s_add_u32 s66, s66, 0x40000\n" \
  "v_bfe_u32 v80, v80, s45, 8\n" \
  "v_bfe_u32 v81, v81, s45, 8\n" \
  "v_bfe_u32 v82, v82, s45, 8\n" \
  "v_bfe_u32 v83, v83, s45, 8\n" \
  "s_waitcnt lgkmcnt(4)\n" \
  "v_dot4_u32_u8 v85, v44, s63, 0\n" \
  "v_dot4_u32_u8 v85, v45, s63, v85\n" \
  "v_dot4_u32_u8 v46, v44, s46, 0\n" \
  "v_dot4_u32_u8 v46, v45, s47, v46\n" \
  "v_add_u32 v86, 31, v86\n" \
  "v_add_u32_dpp v87, v85, v85 row_shr:1 row_mask:0xf bank_mask:0xf bound_ctrl:1\n" \
  "v_lshrrev_b32 v86, 5, v86\n" \
  "v_cvt_f32_u32 v86, v86\n" \
  "v_add_u32_dpp v87, v87, v87 row_shr:2 row_mask:0xf bank_mask:0xf bound_ctrl:1\n" \
  "v_rcp_f32 v86, v86\n" \
  "v_mul_hi_u32_u24 v68, s37, v60\n" \
  "v_add_u32_dpp v87, v87, v87 row_shr:4 row_mask:0xf bank_mask:0xf bound_ctrl:1\n" \
  "v_subrev_u32 v68, s71, v68\n" \
  "v_and_b32 v68, 15, v68\n" \
  "v_add_u32_dpp v87, v87, v87 row_shr:8 row_mask:0xf bank_mask:0xf bound_ctrl:1\n" \
  "v_lshl_add_u32 v72, v68, 1, s75\n" \
  "v_lshl_add_u32 v68, v68, 3, s73\n" \
  "v_add_u32_dpp v87, v87, v87 row_bcast:15 row_mask:0xa bank_mask:0xf\n" \
  "v_mul_hi_u32_u24 v69, s37, v61\n" \
  "v_subrev_u32 v69, s71, v69\n" \
  "v_add_u32_dpp v87, v87, v87 row_bcast:31 row_mask:0xc bank_mask:0xf\n" \
  "v_sub_u32 v46, v87, v46\n" \
  "ds_bpermute_b32 v88, v64, v46\n" \
  "ds_bpermute_b32 v89, v65, v46\n" \
  "ds_bpermute_b32 v90, v66, v46\n" \
  "ds_bpermute_b32 v91, v67, v46\n" \
  "v_mul_f32 v86, 0x48800000, v86\n" \
  "v_cvt_u32_f32 v86, v86\n" \
  "v_add_u32 v86, 1, v86\n" \
  "v_and_b32 v69, 15, v69\n" \
  "v_lshl_add_u32 v73, v69, 1, s75\n" \
  "v_lshl_add_u32 v69, v69, 3, s73\n" \
  "v_mul_hi_u32_u24 v70, s37, v62\n" \
  "v_subrev_u32 v70, s71, v70\n" \
  "v_and_b32 v70, 15, v70\n" \
  "v_lshl_add_u32 v74, v70, 1, s75\n" \
  "v_lshl_add_u32 v70, v70, 3, s73\n" \
  "v_mul_hi_u32_u24 v71, s37, v63\n" \
  "v_subrev_u32 v71, s71, v71\n" \
  "v_and_b32 v71, 15, v71\n" \
  "v_lshl_add_u32 v75, v71, 1, s75\n" \
  "v_lshl_add_u32 v71, v71, 3, s73\n" \
  "s_mov_b32 s63, 0xffff\n" \
  "s_waitcnt lgkmcnt(4)\n" \
  "v_and_or_b32 v76, v60, s63, v76\n" \
  "v_and_or_b32 v77, v61, s63, v77\n" \
  "v_and_or_b32 v78, v62, s63, v78\n" \
  "v_and_or_b32 v79, v63, s63, v79\n" \
  "s_waitcnt lgkmcnt(3)\n" \
  "v_add_u32 v80, v80, v88\n" \
  "v_mul_u32_u24 v80, v80, v86\n" \
  "s_waitcnt lgkmcnt(2)\n" \
  "v_add_u32 v81, v81, v89\n" \
  "v_mul_u32_u24 v81, v81, v86\n" \
  "s_waitcnt lgkmcnt(1)\n" \
  "v_add_u32 v82, v82, v90\n" \
  "v_mul_u32_u24 v82, v82, v86\n" \
  "s_waitcnt lgkmcnt(0)\n" \
  "v_add_u32 v83, v83, v91\n" \
  "v_mul_u32_u24 v83, v83, v86\n" \
  "v_lshrrev_b32 v60, 20, v80\n" \
  "s_mov_b64 exec, s[80:81]\n" \
  "v_lshl_add_u32 v68, v60, 7, v68\n" \
  "ds_add_rtn_u32 v80, v68, v93 offset:34560\n" \
  "v_lshrrev_b32 v61, 20, v81\n" \
  "s_mov_b64 exec, s[82:83]\n" \
  "v_lshl_add_u32 v69, v61, 7, v69\n" \
  "ds_add_rtn_u32 v81, v69, v93 offset:34560\n" \
  "v_lshrrev_b32 v62, 20, v82\n" \
  "s_mov_b64 exec, s[84:85]\n" \
  "v_lshl_add_u32 v70, v62, 7, v70\n" \
  "ds_add_rtn_u32 v82, v70, v93 offset:34560\n" \
  "s_cmp_le_i32 s62, 192\n" \
  "s_cbranch_scc1 Lk771_%=\n" \
  "v_lshrrev_b32 v63, 20, v83\n" \
  "s_mov_b64 exec, s[86:87]\n" \
  "v_lshl_add_u32 v71, v63, 7, v71\n" \
  "ds_add_rtn_u32 v83, v71, v93 offset:34560\n" \
  "Lk771_%=:\n" \
  "s_mov_b64 exec, -1\n" \
  "buffer_load_dword v20, s[28:31], s66 offen lds\n" \
  "s_add_u32 s66, s66, 0x40000\n" \
  "v_lshl_add_u32 v72, v60, 5, v72\n" \
  "v_lshl_add_u32 v73, v61, 5, v73\n" \
  "v_lshl_add_u32 v74, v62, 5, v74\n" \
  "v_lshl_add_u32 v75, v63, 5, v75\n" \
  "v_sub_u32 v85, v87, v85\n" \
  "v_add_u32 v87, -1, v87\n" \
  "v_max_i32 v87, v87, v85\n" \
  "v_mul_u32_u24 v84, v85, v86\n" \
  "v_mul_u32_u24 v43, v87, v86\n" \
  "v_lshrrev_b32 v50, 1, v13\n" \
  "v_lshrrev_b32 v51, 3, v13\n" \
  "v_add_u32 v51, s75, v51\n" \
  "v_lshrrev_b32 v52, 8, v13\n" \
  "v_mul_u32_u24 v52, 0xd000d0, v52\n" \
  "v_add_u32 v52, 0x3400000, v52\n" \
  "s_mov_b32 s63, 0x01010101\n" \
  "s_waitcnt lgkmcnt(0)\n" \
  "s_barrier\n" \
  "ds_read_b64 v[88:89], v50 offset:34560\n" \
  "ds_read_b64 v[90:91], v50 offset:35072\n" \
  "v_bfe_u32 v80, v80, s45, 8\n" \
  "v_bfe_u32 v81, v81, s45, 8\n" \
  "v_bfe_u32 v82, v82, s45, 8\n" \
  "v_bfe_u32 v83, v83, s45, 8\n" \
  "s_lshl_b32 s72, s44, 4\n" \
  "s_or_b32 s72, s72, 15\n" \
  "s_lshl_b32 s73, s73, 2\n" \
  "s_waitcnt lgkmcnt(0)\n" \
  "v_dot4_u32_u8 v44, v88, s63, 0\n" \
  "v_dot4_u32_u8 v45, v90, s63, 0\n" \
  "v_dot4_u32_u8 v44, v89, s63, v44\n" \
  "v_dot4_u32_u8 v45, v91, s63, v45\n" \
  "v_dot4_u32_u8 v46, v88, s46, 0\n" \
  "v_dot4_u32_u8 v47, v90, s46, 0\n" \
  "v_dot4_u32_u8 v46, v89, s47, v46\n" \
  "v_lshl_or_b32 v44, v45, 16, v44\n" \
  "v_dot4_u32_u8 v47, v91, s47, v47\n" \
  "s_nop 0\n" \
  "v_add_u32_dpp v87, v44, v44 row_shr:1 row_mask:0xf bank_mask:0xf bound_ctrl:1\n" \
  "s_nop 0\n" \
  "v_lshl_or_b32 v46, v47, 16, v46\n" \
  "v_add_u32_dpp v87, v87, v87 row_shr:2 row_mask:0xf bank_mask:0xf bound_ctrl:1\n" \
  "v_sub_u32 v52, v52, v46\n" \
  "s_nop 0\n" \
  "v_add_u32_dpp v87, v87, v87 row_shr:4 row_mask:0xf bank_mask:0xf bound_ctrl:1\n" \
  "s_nop 1\n" \
  "v_add_u32_dpp v87, v87, v87 row_shr:8 row_mask:0xf bank_mask:0xf bound_ctrl:1\n" \
  "v_add_u32 v52, v52, v87\n" \
  "ds_write_b16 v51, v52\n" \
  "ds_write_b16_d16_hi v51, v52 offset:128\n" \
  "ds_read_u16 v72, v72\n" \
  "ds_read_u16 v73, v73\n" \
  "ds_read_u16 v74, v74\n" \
  "ds_read_u16 v75, v75\n" \
  "v_readlane_b32 s60, v87, s72\n" \
  "s_movk_i32 s63, 200\n" \
  "v_cmp_lt_u32_sdwa s[90:91], s63, v87 src0_sel:DWORD src1_sel:WORD_0\n" \
  "v_cmp_lt_u32_sdwa vcc, s63, v87 src0_sel:DWORD src1_sel:WORD_1\n" \
  "s_lshr_b32 s72, s60, s73\n" \
  "s_and_b32 s72, s72, 0xffff\n" \
  "s_add_u32 s60, s72, 3\n" \
  "s_lshr_b32 s60, s60, 2\n" \
  "s_max_u32 s60, s60, 1\n" \
  "s_lshl2_add_u32 s73, s60, 8\n" \
  "s_sub_u32 s73, s73, s72\n" \
  "s_bfm_b64 s[88:89], s73, 0\n" \
  "s_or_b64 vcc, vcc, s[90:91]\n" \
  "s_cbranch_scc0 Lo1_%=\n" \
  "s_mov_b64 s[80:81], 0\n" \
  "s_mov_b64 s[82:83], 0\n" \
  "s_mov_b64 s[84:85], 0\n" \
  "s_mov_b64 s[86:87], 0\n" \
  "s_mov_b64 s[88:89], 0xfff\n" \
  "s_mov_b32 s72, 0\n" \
  "s_mov_b32 s60, 1\n" \
  "ds_or_b32 v94, v92\n" \
    \
  "Lo1_%=:\n" \
  "v_lshrrev_b32 v50, 2, v13\n" \
  "s_lshl_b32 s72, s72, 2\n" \
  "v_add3_u32 v50, v50, s72, v14\n" \
  "v_mov_b32 v51, 0x50ffff\n" \
  "s_mov_b32 s63, 100096\n" \
  "s_mov_b64 exec, s[88:89]\n" \
  "ds_write_b32 v50, v51\n" \
  "s_waitcnt lgkmcnt(1)\n" \
  "s_mov_b64 exec, s[80:81]\n" \
  "v_add_u32 v80, v80, v72\n" \
  "v_lshl_add_u32 v72, v80, 2, s63\n" \
  "ds_write_b32 v72, v76\n" \
  "s_mov_b64 exec, s[82:83]\n" \
  "v_add_u32 v81, v81, v73\n" \
  "v_lshl_add_u32 v73, v81, 2, s63\n" \
  "ds_write_b32 v73, v77\n" \
  "s_mov_b64 exec, s[84:85]\n" \
  "v_add_u32 v82, v82, v74\n" \
  "v_lshl_add_u32 v74, v82, 2, s63\n" \
  "ds_write_b32 v74, v78\n" \
  "s_cmp_le_i32 s62, 192\n" \
  "s_cbranch_scc1 Lk878_%=\n" \
  "s_mov_b64 exec, s[86:87]\n" \
  "v_add_u32 v83, v83, v75\n" \
  "v_lshl_add_u32 v75, v83, 2, s63\n" \
  "ds_write_b32 v75, v79\n" \
  "Lk878_%=:\n" \
  "s_mov_b64 exec, -1\n" \
  "s_sub_u32 s60, s60, 1\n" \
  "s_lshl_b32 s63, s60, 4\n" \
  "v_sub_u32 v50, v14, v13\n" \
  "v_add_u32 v50, s63, v50\n" \
  "s_waitcnt lgkmcnt(0)\n" \
  "s_barrier\n" \
  "ds_read_b128 v[56:59], v14\n" \
  "ds_read_b128 v[60:63], v14 offset:16\n" \
  "ds_read_b128 v[64:67], v50\n" \
  "v_lshlrev_b32 v208, 4, v80\n" \
  "v_lshlrev_b32 v209, 4, v81\n" \
  "v_lshlrev_b32 v210, 4, v82\n" \
  "v_lshlrev_b32 v211, 4, v83\n" \
  "s_waitcnt vmcnt(3)\n" \
  "s_mov_b64 exec, s[80:81]\n" \
  "ds_write_b128 v208, v[192:195] offset:37584\n" \
  "s_mov_b64 exec, s[82:83]\n" \
  "ds_write_b128 v209, v[196:199] offset:37584\n" \
  "s_mov_b64 exec, s[84:85]\n" \
  "ds_write_b128 v210, v[200:203] offset:37584\n" \
  "s_cmp_le_i32 s62, 192\n" \
  "s_cbranch_scc1 Lk906_%=\n" \
  "s_mov_b64 exec, s[86:87]\n" \
  "ds_write_b128 v211, v[204:207] offset:37584\n" \
  "Lk906_%=:\n" \
  "s_mov_b64 exec, -1\n" \
  "s_waitcnt lgkmcnt(4)\n" \
  "v_and_b32 v12, 0xffff, v56\n" \
  "buffer_load_dwordx4 v[96:99], v[12:13], s[52:55], 0 idxen offen\n" \
  "v_and_b32 v12, 0xffff, v57\n" \
  "buffer_load_dwordx4 v[100:103], v[12:13], s[52:55], 0 idxen offen\n" \
  "v_and_b32 v12, 0xffff, v58\n" \
  "buffer_load_dwordx4 v[104:107], v[12:13], s[52:55], 0 idxen offen\n" \
  "v_and_b32 v12, 0xffff, v59\n" \
  "buffer_load_dwordx4 v[108:111], v[12:13], s[52:55], 0 idxen offen\n" \
  "v_lshrrev_b32 v51, 20, v84\n" \
  "v_lshrrev_b32 v52, 20, v43\n" \
  "v_min_u32 v51, 7, v51\n" \
  "v_min_u32 v52, 7, v52\n" \
  "s_bfm_b64 s[88:89], s68, 0\n" \
  "v_mov_b32 v18, v94\n" \
  "v_cmp_ge_u32 s[90:91], s44, v51\n" \
  "v_cmp_le_u32 vcc, s44, v52\n" \
  "s_and_b64 s[88:89], s[88:89], s[90:91]\n" \
  "s_and_b64 s[88:89], s[88:89], vcc\n" \
  "s_ff1_i32_b64 s72, s[88:89]\n" \
  "s_flbit_i32_b64 s74, s[88:89]\n" \
  "s_xor_b32 s74, s74, 63\n" \
  "v_cmp_le_u32 vcc, 4, v52\n" \
  "s_add_u32 s77, s68, 3\n" \
  "s_lshr_b32 s77, s77, 2\n" \
  "s_ff1_i32_b64 s76, vcc\n" \
  "s_lshr_b32 s76, s76, 2\n" \
  "s_min_u32 s76, s76, s77\n" \
  "s_sub_u32 s78, s77, s76\n" \
  "s_min_u32 s78, s78, 2\n" \
  "s_add_u32 s63, s76, s78\n" \
  "s_cmp_lt_u32 s44, 4\n" \
  "s_cselect_b32 s77, s76, s77\n" \
  "s_cselect_b32 s76, 0, s63\n" \
  "s_cselect_b32 s78, s78, 0\n" \
  "s_sub_u32 s77, s77, s76\n" \
  "s_waitcnt lgkmcnt(3)\n" \
  "s_mov_b32 s63, 0x0c0c0602\n" \
  "v_perm_b32 v64, v65, v64, s63\n" \
  "v_perm_b32 v66, v67, v66, s63\n" \
  "v_readlane_b32 s93, v52, s74\n" \
  "v_readlane_b32 s75, v51, s72\n" \
  "v_lshl_or_b32 v17, v66, 16, v64\n" \
  "v_readlane_b32 s63, v52, s72\n" \
  "v_readlane_b32 s73, v42, s74\n" \
  "v_readlane_b32 s61, v17, s60\n" \
  "s_sub_u32 s74, s74, s72\n" \
  "s_add_u32 s72, s72, s74\n" \
  "s_sub_u32 s75, s44, s75\n" \
  "s_cmp_eq_u32 s63, s44\n" \
  "s_cselect_b32 s75, s75, 0\n" \
  "s_lshr_b32 s73, s73, 16\n" \
  "s_mul_i32 s72, s72, 1056\n" \
  "s_mov_b32 s63, 0x7fff7fff\n" \
  "v_and_b32 v41, s63, v194\n" \
  "v_and_b32 v42, s63, v195\n" \
  "v_pk_max_u16 v40, v41, v42\n" \
  "v_and_b32 v41, s63, v198\n" \
  "v_and_b32 v42, s63, v199\n" \
  "v_pk_max_u16 v41, v41, v40\n" \
  "v_pk_max_u16 v40, v41, v42\n" \
  "v_and_b32 v41, s63, v202\n" \
  "v_and_b32 v42, s63, v203\n" \
  "v_pk_max_u16 v41, v41, v40\n" \
  "v_pk_max_u16 v40, v41, v42\n" \
  "s_cmp_le_i32 s62, 192\n" \
  "s_cbranch_scc1 Lk977_%=\n" \
  "v_and_b32 v41, s63, v206\n" \
  "v_and_b32 v42, s63, v207\n" \
  "v_pk_max_u16 v41, v41, v40\n" \
  "v_pk_max_u16 v40, v41, v42\n" \
  "Lk977_%=:\n" \
  "v_max_u16_sdwa v40, v40, v40 dst_sel:DWORD dst_unused:UNUSED_PAD src0_sel:WORD_0 src1_sel:WORD_1\n" \
  "s_nop 0\n" \
  "v_cmp_lt_i32 vcc, s69, v40\n" \
  "s_cmp_eq_u64 vcc, 0\n" \
  "s_cselect_b32 s74, s74, -1\n" \
  "s_waitcnt lgkmcnt(0)\n" \
  "s_barrier\n" \
  "ds_read_b64 v[92:93], v16\n" \
  "ds_read_b64 v[94:95], v15\n" \
  "v_and_b32 v12, 0xffff, v60\n" \
  "buffer_load_dwordx4 v[112:115], v[12:13], s[52:55], 0 idxen offen\n" \
  "v_and_b32 v12, 0xffff, v61\n" \
  "buffer_load_dwordx4 v[116:119], v[12:13], s[52:55], 0 idxen offen\n" \
  "v_and_b32 v12, 0xffff, v62\n" \
  "buffer_load_dwordx4 v[120:123], v[12:13], s[52:55], 0 idxen offen\n" \
  "v_and_b32 v12, 0xffff, v63\n" \
  "buffer_load_dwordx4 v[124:127], v[12:13], s[52:55], 0 idxen offen\n" \
  "ds_read_u16 v12, v14 offset:32\n" \
  "v_mov_b64 v[192:193], 0\n" \
  "v_mov_b64 v[194:195], 0\n" \
  "v_mov_b64 v[196:197], 0\n" \
  "v_mov_b64 v[198:199], 0\n" \
  "v_mov_b64 v[200:201], 0\n" \
  "v_mov_b64 v[202:203], 0\n" \
  "v_mov_b64 v[204:205], 0\n" \
  "v_mov_b64 v[206:207], 0\n" \
  "s_or_b32 s63, %[blk], s44\n" \
  "s_cbranch_scc0 Lep0_%=\n" \
  "Lgo%=:\n" \
  "s_waitcnt lgkmcnt(1)\n" \
  "v_mfma_f32_4x4x4_16b_bf16 v[56:59], v[92:93], v[32:33], 0 cbsz:4\n" \
  "v_mfma_f32_4x4x4_16b_bf16 v[60:63], v[92:93], v[36:37], 0 cbsz:4\n" \
  "v_mfma_f32_4x4x4_16b_bf16 v[64:67], v[92:93], v[32:33], 0 cbsz:4 abid:1\n" \
  "v_mfma_f32_4x4x4_16b_bf16 v[68:71], v[92:93], v[36:37], 0 cbsz:4 abid:1\n" \
  "v_mfma_f32_4x4x4_16b_bf16 v[84:87], v[94:95], v[24:25], 0\n" \
  "v_mfma_f32_4x4x4_16b_bf16 v[88:91], v[94:95], v[26:27], 0\n" \
  "v_mfma_f32_4x4x4_16b_bf16 v[48:51], v[94:95], v[28:29], 0\n" \
  "v_mfma_f32_4x4x4_16b_bf16 v[52:55], v[94:95], v[30:31], 0\n" \
  "s_nop 1\n" \
  "v_cvt_pk_bf16_f32 v57, v56, v57\n" \
  "v_lshlrev_b32_sdwa v56, 1, v57 dst_sel:DWORD dst_unused:UNUSED_PAD src0_sel:DWORD src1_sel:WORD_0\n" \
  "v_lshlrev_b32_sdwa v57, 1, v57 dst_sel:DWORD dst_unused:UNUSED_PAD src0_sel:DWORD src1_sel:WORD_1\n" \
  "ds_read_u16_d16_hi v56, v56\n" \
  "ds_read_u16_d16_hi v57, v57\n" \
  "v_cvt_pk_bf16_f32 v59, v58, v59\n" \
  "v_lshlrev_b32_sdwa v58, 1, v59 dst_sel:DWORD dst_unused:UNUSED_PAD src0_sel:DWORD src1_sel:WORD_0\n" \
  "v_lshlrev_b32_sdwa v59, 1, v59 dst_sel:DWORD dst_unused:UNUSED_PAD src0_sel:DWORD src1_sel:WORD_1\n" \
  "ds_read_u16_d16_hi v58, v58\n" \
  "ds_read_u16_d16_hi v59, v59\n" \
  "v_cvt_pk_bf16_f32 v61, v60, v61\n" \
  "v_lshlrev_b32_sdwa v60, 1, v61 dst_sel:DWORD dst_unused:UNUSED_PAD src0_sel:DWORD src1_sel:WORD_0\n" \
  "v_lshlrev_b32_sdwa v61, 1, v61 dst_sel:DWORD dst_unused:UNUSED_PAD src0_sel:DWORD src1_sel:WORD_1\n" \
  "ds_read_u16_d16_hi v60, v60\n" \
  "ds_read_u16_d16_hi v61, v61\n" \
  "v_cvt_pk_bf16_f32 v63, v62, v63\n" \
  "v_lshlrev_b32_sdwa v62, 1, v63 dst_sel:DWORD dst_unused:UNUSED_PAD src0_sel:DWORD src1_sel:WORD_0\n" \
  "v_lshlrev_b32_sdwa v63, 1, v63 dst_sel:DWORD dst_unused:UNUSED_PAD src0_sel:DWORD src1_sel:WORD_1\n" \
  "ds_read_u16_d16_hi v62, v62\n" \
  "ds_read_u16_d16_hi v63, v63\n" \
  "v_cvt_pk_bf16_f32 v76, v84, v88\n" \
  "v_cvt_pk_bf16_f32 v77, v48, v52\n" \
  "s_nop 0\n" \
  "s_waitcnt vmcnt(7) lgkmcnt(0)\n" \
  "v_mfma_f32_4x4x4_16b_bf16 v[48:51], v[76:77], v[96:97], 0 cbsz:4\n" \
  "v_mfma_f32_4x4x4_16b_bf16 v[52:55], v[76:77], v[98:99], 0 cbsz:4\n" \
  "s_nop 4\n" \
    \
  "Lbody%=:\n" \
  "s_set_gpr_idx_on s61, gpr_idx(SRC2,DST)\n" \
  "v_pk_fma_f32 v[128:129], v[56:57], v[48:49], v[128:129] op_sel:[0,0,0] op_sel_hi:[0,1,1]\n" \
  "v_pk_fma_f32 v[130:131], v[56:57], v[50:51], v[130:131] op_sel:[0,0,0] op_sel_hi:[0,1,1]\n" \
  "v_pk_fma_f32 v[132:133], v[60:61], v[52:53], v[132:133] op_sel:[0,0,0] op_sel_hi:[0,1,1]\n" \
  "v_pk_fma_f32 v[134:135], v[60:61], v[54:55], v[134:135] op_sel:[0,0,0] op_sel_hi:[0,1,1]\n" \
  "s_set_gpr_idx_off\n" \
  "s_waitcnt vmcnt(6) lgkmcnt(2)\n" \
  "buffer_load_dwordx4 v[96:99], v[12:13], s[52:55], 0 idxen offen\n" \
  "ds_read_u16 v12, v14 offset:36\n" \
  "s_lshr_b32 s61, s61, 8\n" \
  "v_mfma_f32_4x4x4_16b_bf16 v[48:51], v[76:77], v[100:101], 0 cbsz:4 abid:1\n" \
  "v_mfma_f32_4x4x4_16b_bf16 v[52:55], v[76:77], v[102:103], 0 cbsz:4 abid:1\n" \
  "v_cvt_pk_bf16_f32 v65, v64, v65\n" \
  "v_lshlrev_b32_sdwa v64, 1, v65 dst_sel:DWORD dst_unused:UNUSED_PAD src0_sel:DWORD src1_sel:WORD_0\n" \
  "v_lshlrev_b32_sdwa v65, 1, v65 dst_sel:DWORD dst_unused:UNUSED_PAD src0_sel:DWORD src1_sel:WORD_1\n" \
  "ds_read_u16_d16_hi v64, v64\n" \
  "ds_read_u16_d16_hi v65, v65\n" \
  "s_set_gpr_idx_on s61, gpr_idx(SRC2,DST)\n" \
  "v_pk_fma_f32 v[128:129], v[56:57], v[48:49], v[128:129] op_sel:[1,0,0] op_sel_hi:[1,1,1]\n" \
  "v_pk_fma_f32 v[130:131], v[56:57], v[50:51], v[130:131] op_sel:[1,0,0] op_sel_hi:[1,1,1]\n" \
  "v_pk_fma_f32 v[132:133], v[60:61], v[52:53], v[132:133] op_sel:[1,0,0] op_sel_hi:[1,1,1]\n" \
  "v_pk_fma_f32 v[134:135], v[60:61], v[54:55], v[134:135] op_sel:[1,0,0] op_sel_hi:[1,1,1]\n" \
  "s_set_gpr_idx_off\n" \
  "s_waitcnt vmcnt(6) lgkmcnt(2)\n" \
  "buffer_load_dwordx4 v[100:103], v[12:13], s[52:55], 0 idxen offen\n" \
  "ds_read_u16 v12, v14 offset:40\n" \
  "s_lshr_b32 s61, s61, 8\n" \
  "v_mfma_f32_4x4x4_16b_bf16 v[48:51], v[76:77], v[104:105], 0 cbsz:4 abid:2\n" \
  "v_mfma_f32_4x4x4_16b_bf16 v[52:55], v[76:77], v[106:107], 0 cbsz:4 abid:2\n" \
  "v_cvt_pk_bf16_f32 v69, v68, v69\n" \
  "v_lshlrev_b32_sdwa v68, 1, v69 dst_sel:DWORD dst_unused:UNUSED_PAD src0_sel:DWORD src1_sel:WORD_0\n" \
  "v_lshlrev_b32_sdwa v69, 1, v69 dst_sel:DWORD dst_unused:UNUSED_PAD src0_sel:DWORD src1_sel:WORD_1\n" \
  "ds_read_u16_d16_hi v68, v68\n" \
  "ds_read_u16_d16_hi v69, v69\n" \
  "s_set_gpr_idx_on s61, gpr_idx(SRC2,DST)\n" \
  "v_pk_fma_f32 v[128:129], v[58:59], v[48:49], v[128:129] op_sel:[0,0,0] op_sel_hi:[0,1,1]\n" \
  "v_pk_fma_f32 v[130:131], v[58:59], v[50:51], v[130:131] op_sel:[0,0,0] op_sel_hi:[0,1,1]\n" \
  "v_pk_fma_f32 v[132:133], v[62:63], v[52:53], v[132:133] op_sel:[0,0,0] op_sel_hi:[0,1,1]\n" \
  "v_pk_fma_f32 v[134:135], v[62:63], v[54:55], v[134:135] op_sel:[0,0,0] op_sel_hi:[0,1,1]\n" \
  "s_set_gpr_idx_off\n" \
  "s_waitcnt vmcnt(6) lgkmcnt(2)\n" \
  "buffer_load_dwordx4 v[104:107], v[12:13], s[52:55], 0 idxen offen\n" \
  "ds_read_u16 v12, v14 offset:44\n" \
  "s_lshr_b32 s61, s61, 8\n" \
  "v_mfma_f32_4x4x4_16b_bf16 v[48:51], v[76:77], v[108:109], 0 cbsz:4 abid:3\n" \
  "v_mfma_f32_4x4x4_16b_bf16 v[52:55], v[76:77], v[110:111], 0 cbsz:4 abid:3\n" \
  "v_cvt_pk_bf16_f32 v67, v66, v67\n" \
  "v_lshlrev_b32_sdwa v66, 1, v67 dst_sel:DWORD dst_unused:UNUSED_PAD src0_sel:DWORD src1_sel:WORD_0\n" \
  "v_lshlrev_b32_sdwa v67, 1, v67 dst_sel:DWORD dst_unused:UNUSED_PAD src0_sel:DWORD src1_sel:WORD_1\n" \
  "ds_read_u16_d16_hi v66, v66\n" \
  "ds_read_u16_d16_hi v67, v67\n" \
  "s_set_gpr_idx_on s61, gpr_idx(SRC2,DST)\n" \
  "v_pk_fma_f32 v[128:129], v[58:59], v[48:49], v[128:129] op_sel:[1,0,0] op_sel_hi:[1,1,1]\n" \
  "v_pk_fma_f32 v[130:131], v[58:59], v[50:51], v[130:131] op_sel:[1,0,0] op_sel_hi:[1,1,1]\n" \
  "v_pk_fma_f32 v[132:133], v[62:63], v[52:53], v[132:133] op_sel:[1,0,0] op_sel_hi:[1,1,1]\n" \
  "v_pk_fma_f32 v[134:135], v[62:63], v[54:55], v[134:135] op_sel:[1,0,0] op_sel_hi:[1,1,1]\n" \
  "s_set_gpr_idx_off\n" \
  "s_waitcnt vmcnt(6) lgkmcnt(2)\n" \
  "buffer_load_dwordx4 v[108:111], v[12:13], s[52:55], 0 idxen offen\n" \
  "ds_read_u16 v12, v14 offset:48\n" \
  "s_sub_u32 s60, s60, 1\n" \
  "s_cbranch_scc1 Lend%=\n" \
  "v_readlane_b32 s61, v17, s60\n" \
  "s_setprio 2\n" \
  "v_mfma_f32_4x4x4_16b_bf16 v[56:59], v[92:93], v[32:33], 0 cbsz:4 abid:2\n" \
  "v_mfma_f32_4x4x4_16b_bf16 v[60:63], v[92:93], v[36:37], 0 cbsz:4 abid:2\n" \
  "v_mfma_f32_4x4x4_16b_bf16 v[48:51], v[76:77], v[112:113], 0 cbsz:4 abid:4\n" \
  "v_mfma_f32_4x4x4_16b_bf16 v[52:55], v[76:77], v[114:115], 0 cbsz:4 abid:4\n" \
  "v_cvt_pk_bf16_f32 v71, v70, v71\n" \
  "v_lshlrev_b32_sdwa v70, 1, v71 dst_sel:DWORD dst_unused:UNUSED_PAD src0_sel:DWORD src1_sel:WORD_0\n" \
  "v_lshlrev_b32_sdwa v71, 1, v71 dst_sel:DWORD dst_unused:UNUSED_PAD src0_sel:DWORD src1_sel:WORD_1\n" \
  "ds_read_u16_d16_hi v70, v70\n" \
  "ds_read_u16_d16_hi v71, v71\n" \
  "v_add_u32 v16, 0x100, v16\n" \
  "s_set_gpr_idx_on s61, gpr_idx(SRC2,DST)\n" \
  "v_pk_fma_f32 v[128:129], v[64:65], v[48:49], v[128:129] op_sel:[0,0,0] op_sel_hi:[0,1,1]\n" \
  "v_pk_fma_f32 v[130:131], v[64:65], v[50:51], v[130:131] op_sel:[0,0,0] op_sel_hi:[0,1,1]\n" \
  "v_pk_fma_f32 v[132:133], v[68:69], v[52:53], v[132:133] op_sel:[0,0,0] op_sel_hi:[0,1,1]\n" \
  "v_pk_fma_f32 v[134:135], v[68:69], v[54:55], v[134:135] op_sel:[0,0,0] op_sel_hi:[0,1,1]\n" \
  "s_set_gpr_idx_off\n" \
  "s_waitcnt vmcnt(6) lgkmcnt(2)\n" \
  "buffer_load_dwordx4 v[112:115], v[12:13], s[52:55], 0 idxen offen\n" \
  "ds_read_u16 v12, v14 offset:52\n" \
  "ds_read_b64 v[94:95], v15 offset:256\n" \
  "s_lshr_b32 s61, s61, 8\n" \
  "v_mfma_f32_4x4x4_16b_bf16 v[48:51], v[76:77], v[116:117], 0 cbsz:4 abid:5\n" \
  "v_mfma_f32_4x4x4_16b_bf16 v[52:55], v[76:77], v[118:119], 0 cbsz:4 abid:5\n" \
  "v_cvt_pk_bf16_f32 v57, v56, v57\n" \
  "v_lshlrev_b32_sdwa v56, 1, v57 dst_sel:DWORD dst_unused:UNUSED_PAD src0_sel:DWORD src1_sel:WORD_0\n" \
  "v_lshlrev_b32_sdwa v57, 1, v57 dst_sel:DWORD dst_unused:UNUSED_PAD src0_sel:DWORD src1_sel:WORD_1\n" \
  "ds_read_u16_d16_hi v56, v56\n" \
  "ds_read_u16_d16_hi v57, v57\n" \
  "s_set_gpr_idx_on s61, gpr_idx(SRC2,DST)\n" \
  "v_pk_fma_f32 v[128:129], v[64:65], v[48:49], v[128:129] op_sel:[1,0,0] op_sel_hi:[1,1,1]\n" \
  "v_pk_fma_f32 v[130:131], v[64:65], v[50:51], v[130:131] op_sel:[1,0,0] op_sel_hi:[1,1,1]\n" \
  "v_pk_fma_f32 v[132:133], v[68:69], v[52:53], v[132:133] op_sel:[1,0,0] op_sel_hi:[1,1,1]\n" \
  "v_pk_fma_f32 v[134:135], v[68:69], v[54:55], v[134:135] op_sel:[1,0,0] op_sel_hi:[1,1,1]\n" \
  "s_set_gpr_idx_off\n" \
  "s_waitcnt vmcnt(6) lgkmcnt(2)\n" \
  "buffer_load_dwordx4 v[116:119], v[12:13], s[52:55], 0 idxen offen\n" \
  "ds_read_u16 v12, v14 offset:56\n" \
  "s_lshr_b32 s61, s61, 8\n" \
  "v_mfma_f32_4x4x4_16b_bf16 v[48:51], v[76:77], v[120:121], 0 cbsz:4 abid:6\n" \
  "v_mfma_f32_4x4x4_16b_bf16 v[52:55], v[76:77], v[122:123], 0 cbsz:4 abid:6\n" \
  "v_cvt_pk_bf16_f32 v61, v60, v61\n" \
  "v_lshlrev_b32_sdwa v60, 1, v61 dst_sel:DWORD dst_unused:UNUSED_PAD src0_sel:DWORD src1_sel:WORD_0\n" \
  "v_lshlrev_b32_sdwa v61, 1, v61 dst_sel:DWORD dst_unused:UNUSED_PAD src0_sel:DWORD src1_sel:WORD_1\n" \
  "ds_read_u16_d16_hi v60, v60\n" \
  "ds_read_u16_d16_hi v61, v61\n" \
  "s_set_gpr_idx_on s61, gpr_idx(SRC2,DST)\n" \
  "v_pk_fma_f32 v[128:129], v[66:67], v[48:49], v[128:129] op_sel:[0,0,0] op_sel_hi:[0,1,1]\n" \
  "v_pk_fma_f32 v[130:131], v[66:67], v[50:51], v[130:131] op_sel:[0,0,0] op_sel_hi:[0,1,1]\n" \
  "v_pk_fma_f32 v[132:133], v[70:71], v[52:53], v[132:133] op_sel:[0,0,0] op_sel_hi:[0,1,1]\n" \
  "v_pk_fma_f32 v[134:135], v[70:71], v[54:55], v[134:135] op_sel:[0,0,0] op_sel_hi:[0,1,1]\n" \
  "s_set_gpr_idx_off\n" \
  "s_waitcnt vmcnt(6) lgkmcnt(2)\n" \
  "buffer_load_dwordx4 v[120:123], v[12:13], s[52:55], 0 idxen offen\n" \
  "ds_read_u16 v12, v14 offset:60\n" \
  "s_lshr_b32 s61, s61, 8\n" \
  "v_mfma_f32_4x4x4_16b_bf16 v[84:87], v[94:95], v[24:25], 0\n" \
  "v_mfma_f32_4x4x4_16b_bf16 v[48:51], v[76:77], v[124:125], 0 cbsz:4 abid:7\n" \
  "v_mfma_f32_4x4x4_16b_bf16 v[52:55], v[76:77], v[126:127], 0 cbsz:4 abid:7\n" \
  "v_cvt_pk_bf16_f32 v59, v58, v59\n" \
  "v_lshlrev_b32_sdwa v58, 1, v59 dst_sel:DWORD dst_unused:UNUSED_PAD src0_sel:DWORD src1_sel:WORD_0\n" \
  "v_lshlrev_b32_sdwa v59, 1, v59 dst_sel:DWORD dst_unused:UNUSED_PAD src0_sel:DWORD src1_sel:WORD_1\n" \
  "ds_read_u16_d16_hi v58, v58\n" \
  "ds_read_u16_d16_hi v59, v59\n" \
  "s_set_gpr_idx_on s61, gpr_idx(SRC2,DST)\n" \
  "v_pk_fma_f32 v[128:129], v[66:67], v[48:49], v[128:129] op_sel:[1,0,0] op_sel_hi:[1,1,1]\n" \
  "v_pk_fma_f32 v[130:131], v[66:67], v[50:51], v[130:131] op_sel:[1,0,0] op_sel_hi:[1,1,1]\n" \
  "v_pk_fma_f32 v[132:133], v[70:71], v[52:53], v[132:133] op_sel:[1,0,0] op_sel_hi:[1,1,1]\n" \
  "v_pk_fma_f32 v[134:135], v[70:71], v[54:55], v[134:135] op_sel:[1,0,0] op_sel_hi:[1,1,1]\n" \
  "s_set_gpr_idx_off\n" \
  "s_waitcnt vmcnt(6) lgkmcnt(2)\n" \
  "buffer_load_dwordx4 v[124:127], v[12:13], s[52:55], 0 idxen offen\n" \
  "ds_read_u16 v12, v14 offset:64\n" \
  "s_sub_u32 s60, s60, 1\n" \
  "s_cbranch_scc1 Lend%=\n" \
  "v_readlane_b32 s61, v17, s60\n" \
  "s_setprio 1\n" \
  "v_mfma_f32_4x4x4_16b_bf16 v[64:67], v[92:93], v[32:33], 0 cbsz:4 abid:3\n" \
  "v_mfma_f32_4x4x4_16b_bf16 v[68:71], v[92:93], v[36:37], 0 cbsz:4 abid:3\n" \
  "v_mfma_f32_4x4x4_16b_bf16 v[48:51], v[76:77], v[96:97], 0 cbsz:4 abid:8\n" \
  "v_mfma_f32_4x4x4_16b_bf16 v[52:55], v[76:77], v[98:99], 0 cbsz:4 abid:8\n" \
  "v_cvt_pk_bf16_f32 v63, v62, v63\n" \
  "v_lshlrev_b32_sdwa v62, 1, v63 dst_sel:DWORD dst_unused:UNUSED_PAD src0_sel:DWORD src1_sel:WORD_0\n" \
  "v_lshlrev_b32_sdwa v63, 1, v63 dst_sel:DWORD dst_unused:UNUSED_PAD src0_sel:DWORD src1_sel:WORD_1\n" \
  "ds_read_u16_d16_hi v62, v62\n" \
  "ds_read_u16_d16_hi v63, v63\n" \
  "v_add_u32 v15, 0x100, v15\n" \
  "s_set_gpr_idx_on s61, gpr_idx(SRC2,DST)\n" \
  "v_pk_fma_f32 v[128:129], v[56:57], v[48:49], v[128:129] op_sel:[0,0,0] op_sel_hi:[0,1,1]\n" \
  "v_pk_fma_f32 v[130:131], v[56:57], v[50:51], v[130:131] op_sel:[0,0,0] op_sel_hi:[0,1,1]\n" \
  "v_pk_fma_f32 v[132:133], v[60:61], v[52:53], v[132:133] op_sel:[0,0,0] op_sel_hi:[0,1,1]\n" \
  "v_pk_fma_f32 v[134:135], v[60:61], v[54:55], v[134:135] op_sel:[0,0,0] op_sel_hi:[0,1,1]\n" \
  "s_set_gpr_idx_off\n" \
  "s_waitcnt vmcnt(6) lgkmcnt(2)\n" \
  "buffer_load_dwordx4 v[96:99], v[12:13], s[52:55], 0 idxen offen\n" \
  "ds_read_u16 v12, v14 offset:68\n" \
  "s_lshr_b32 s61, s61, 8\n" \
  "v_mfma_f32_4x4x4_16b_bf16 v[48:51], v[76:77], v[100:101], 0 cbsz:4 abid:9\n" \
  "v_mfma_f32_4x4x4_16b_bf16 v[52:55], v[76:77], v[102:103], 0 cbsz:4 abid:9\n" \
  "v_cvt_pk_bf16_f32 v65, v64, v65\n" \
  "v_lshlrev_b32_sdwa v64, 1, v65 dst_sel:DWORD dst_unused:UNUSED_PAD src0_sel:DWORD src1_sel:WORD_0\n" \
  "v_lshlrev_b32_sdwa v65, 1, v65 dst_sel:DWORD dst_unused:UNUSED_PAD src0_sel:DWORD src1_sel:WORD_1\n" \
  "ds_read_u16_d16_hi v64, v64\n" \
  "ds_read_u16_d16_hi v65, v65\n" \
  "s_set_gpr_idx_on s61, gpr_idx(SRC2,DST)\n" \
  "v_pk_fma_f32 v[128:129], v[56:57], v[48:49], v[128:129] op_sel:[1,0,0] op_sel_hi:[1,1,1]\n" \
  "v_pk_fma_f32 v[130:131], v[56:57], v[50:51], v[130:131] op_sel:[1,0,0] op_sel_hi:[1,1,1]\n" \
  "v_pk_fma_f32 v[132:133], v[60:61], v[52:53], v[132:133] op_sel:[1,0,0] op_sel_hi:[1,1,1]\n" \
  "v_pk_fma_f32 v[134:135], v[60:61], v[54:55], v[134:135] op_sel:[1,0,0] op_sel_hi:[1,1,1]\n" \
  "s_set_gpr_idx_off\n" \
  "s_waitcnt vmcnt(6) lgkmcnt(2)\n" \
  "buffer_load_dwordx4 v[100:103], v[12:13], s[52:55], 0 idxen offen\n" \
  "ds_read_u16 v12, v14 offset:72\n" \
  "s_lshr_b32 s61, s61, 8\n" \
  "v_mfma_f32_4x4x4_16b_bf16 v[48:51], v[76:77], v[104:105], 0 cbsz:4 abid:10\n" \
  "v_mfma_f32_4x4x4_16b_bf16 v[52:55], v[76:77], v[106:107], 0 cbsz:4 abid:10\n" \
  "v_cvt_pk_bf16_f32 v69, v68, v69\n" \
  "v_lshlrev_b32_sdwa v68, 1, v69 dst_sel:DWORD dst_unused:UNUSED_PAD src0_sel:DWORD src1_sel:WORD_0\n" \
  "v_lshlrev_b32_sdwa v69, 1, v69 dst_sel:DWORD dst_unused:UNUSED_PAD src0_sel:DWORD src1_sel:WORD_1\n" \
  "ds_read_u16_d16_hi v68, v68\n" \
  "ds_read_u16_d16_hi v69, v69\n" \
  "s_set_gpr_idx_on s61, gpr_idx(SRC2,DST)\n" \
  "v_pk_fma_f32 v[128:129], v[58:59], v[48:49], v[128:129] op_sel:[0,0,0] op_sel_hi:[0,1,1]\n" \
  "v_pk_fma_f32 v[130:131], v[58:59], v[50:51], v[130:131] op_sel:[0,0,0] op_sel_hi:[0,1,1]\n" \
  "v_pk_fma_f32 v[132:133], v[62:63], v[52:53], v[132:133] op_sel:[0,0,0] op_sel_hi:[0,1,1]\n" \
  "v_pk_fma_f32 v[134:135], v[62:63], v[54:55], v[134:135] op_sel:[0,0,0] op_sel_hi:[0,1,1]\n" \
  "s_set_gpr_idx_off\n" \
  "s_waitcnt vmcnt(6) lgkmcnt(2)\n" \
  "buffer_load_dwordx4 v[104:107], v[12:13], s[52:55], 0 idxen offen\n" \
  "ds_read_u16 v12, v14 offset:76\n" \
  "ds_read_b64 v[92:93], v16\n" \
  "s_lshr_b32 s61, s61, 8\n" \
  "v_mfma_f32_4x4x4_16b_bf16 v[88:91], v[94:95], v[26:27], 0\n" \
  "v_mfma_f32_4x4x4_16b_bf16 v[48:51], v[76:77], v[108:109], 0 cbsz:4 abid:11\n" \
  "v_mfma_f32_4x4x4_16b_bf16 v[52:55], v[76:77], v[110:111], 0 cbsz:4 abid:11\n" \
  "v_cvt_pk_bf16_f32 v67, v66, v67\n" \
  "v_lshlrev_b32_sdwa v66, 1, v67 dst_sel:DWORD dst_unused:UNUSED_PAD src0_sel:DWORD src1_sel:WORD_0\n" \
  "v_lshlrev_b32_sdwa v67, 1, v67 dst_sel:DWORD dst_unused:UNUSED_PAD src0_sel:DWORD src1_sel:WORD_1\n" \
  "ds_read_u16_d16_hi v66, v66\n" \
  "ds_read_u16_d16_hi v67, v67\n" \
  "s_set_gpr_idx_on s61, gpr_idx(SRC2,DST)\n" \
  "v_pk_fma_f32 v[128:129], v[58:59], v[48:49], v[128:129] op_sel:[1,0,0] op_sel_hi:[1,1,1]\n" \
  "v_pk_fma_f32 v[130:131], v[58:59], v[50:51], v[130:131] op_sel:[1,0,0] op_sel_hi:[1,1,1]\n" \
  "v_pk_fma_f32 v[132:133], v[62:63], v[52:53], v[132:133] op_sel:[1,0,0] op_sel_hi:[1,1,1]\n" \
  "v_pk_fma_f32 v[134:135], v[62:63], v[54:55], v[134:135] op_sel:[1,0,0] op_sel_hi:[1,1,1]\n" \
  "s_set_gpr_idx_off\n" \
  "s_waitcnt vmcnt(6) lgkmcnt(2)\n" \
  "buffer_load_dwordx4 v[108:111], v[12:13], s[52:55], 0 idxen offen\n" \
  "ds_read_u16 v12, v14 offset:80\n" \
  "s_sub_u32 s60, s60, 1\n" \
  "s_cbranch_scc1 Lend%=\n" \
  "v_readlane_b32 s61, v17, s60\n" \
  "s_setprio 0\n" \
  "v_mfma_f32_4x4x4_16b_bf16 v[56:59], v[92:93], v[32:33], 0 cbsz:4\n" \
  "v_mfma_f32_4x4x4_16b_bf16 v[60:63], v[92:93], v[36:37], 0 cbsz:4\n" \
  "v_mfma_f32_4x4x4_16b_bf16 v[48:51], v[76:77], v[112:113], 0 cbsz:4 abid:12\n" \
  "v_mfma_f32_4x4x4_16b_bf16 v[52:55], v[76:77], v[114:115], 0 cbsz:4 abid:12\n" \
  "v_cvt_pk_bf16_f32 v71, v70, v71\n" \
  "v_lshlrev_b32_sdwa v70, 1, v71 dst_sel:DWORD dst_unused:UNUSED_PAD src0_sel:DWORD src1_sel:WORD_0\n" \
  "v_lshlrev_b32_sdwa v71, 1, v71 dst_sel:DWORD dst_unused:UNUSED_PAD src0_sel:DWORD src1_sel:WORD_1\n" \
  "ds_read_u16_d16_hi v70, v70\n" \
  "ds_read_u16_d16_hi v71, v71\n" \
  "v_cvt_pk_bf16_f32 v20, v84, v88\n" \
  "s_set_gpr_idx_on s61, gpr_idx(SRC2,DST)\n" \
  "v_pk_fma_f32 v[128:129], v[64:65], v[48:49], v[128:129] op_sel:[0,0,0] op_sel_hi:[0,1,1]\n" \
  "v_pk_fma_f32 v[130:131], v[64:65], v[50:51], v[130:131] op_sel:[0,0,0] op_sel_hi:[0,1,1]\n" \
  "v_pk_fma_f32 v[132:133], v[68:69], v[52:53], v[132:133] op_sel:[0,0,0] op_sel_hi:[0,1,1]\n" \
  "v_pk_fma_f32 v[134:135], v[68:69], v[54:55], v[134:135] op_sel:[0,0,0] op_sel_hi:[0,1,1]\n" \
  "s_set_gpr_idx_off\n" \
  "s_waitcnt vmcnt(6) lgkmcnt(2)\n" \
  "buffer_load_dwordx4 v[112:115], v[12:13], s[52:55], 0 idxen offen\n" \
  "ds_read_u16 v12, v14 offset:84\n" \
  "s_lshr_b32 s61, s61, 8\n" \
  "v_mfma_f32_4x4x4_16b_bf16 v[48:51], v[76:77], v[116:117], 0 cbsz:4 abid:13\n" \
  "v_mfma_f32_4x4x4_16b_bf16 v[52:55], v[76:77], v[118:119], 0 cbsz:4 abid:13\n" \
  "v_cvt_pk_bf16_f32 v57, v56, v57\n" \
  "v_lshlrev_b32_sdwa v56, 1, v57 dst_sel:DWORD dst_unused:UNUSED_PAD src0_sel:DWORD src1_sel:WORD_0\n" \
  "v_lshlrev_b32_sdwa v57, 1, v57 dst_sel:DWORD dst_unused:UNUSED_PAD src0_sel:DWORD src1_sel:WORD_1\n" \
  "ds_read_u16_d16_hi v56, v56\n" \
  "ds_read_u16_d16_hi v57, v57\n" \
  "s_set_gpr_idx_on s61, gpr_idx(SRC2,DST)\n" \
  "v_pk_fma_f32 v[128:129], v[64:65], v[48:49], v[128:129] op_sel:[1,0,0] op_sel_hi:[1,1,1]\n" \
  "v_pk_fma_f32 v[130:131], v[64:65], v[50:51], v[130:131] op_sel:[1,0,0] op_sel_hi:[1,1,1]\n" \
  "v_pk_fma_f32 v[132:133], v[68:69], v[52:53], v[132:133] op_sel:[1,0,0] op_sel_hi:[1,1,1]\n" \
  "v_pk_fma_f32 v[134:135], v[68:69], v[54:55], v[134:135] op_sel:[1,0,0] op_sel_hi:[1,1,1]\n" \
  "s_set_gpr_idx_off\n" \
  "s_waitcnt vmcnt(6) lgkmcnt(2)\n" \
  "buffer_load_dwordx4 v[116:119], v[12:13], s[52:55], 0 idxen offen\n" \
  "ds_read_u16 v12, v14 offset:88\n" \
  "s_lshr_b32 s61, s61, 8\n" \
  "v_mfma_f32_4x4x4_16b_bf16 v[48:51], v[76:77], v[120:121], 0 cbsz:4 abid:14\n" \
  "v_mfma_f32_4x4x4_16b_bf16 v[52:55], v[76:77], v[122:123], 0 cbsz:4 abid:14\n" \
  "v_cvt_pk_bf16_f32 v61, v60, v61\n" \
  "v_lshlrev_b32_sdwa v60, 1, v61 dst_sel:DWORD dst_unused:UNUSED_PAD src0_sel:DWORD src1_sel:WORD_0\n" \
  "v_lshlrev_b32_sdwa v61, 1, v61 dst_sel:DWORD dst_unused:UNUSED_PAD src0_sel:DWORD src1_sel:WORD_1\n" \
  "ds_read_u16_d16_hi v60, v60\n" \
  "ds_read_u16_d16_hi v61, v61\n" \
  "s_set_gpr_idx_on s61, gpr_idx(SRC2,DST)\n" \
  "v_pk_fma_f32 v[128:129], v[66:67], v[48:49], v[128:129] op_sel:[0,0,0] op_sel_hi:[0,1,1]\n" \
  "v_pk_fma_f32 v[130:131], v[66:67], v[50:51], v[130:131] op_sel:[0,0,0] op_sel_hi:[0,1,1]\n" \
  "v_pk_fma_f32 v[132:133], v[70:71], v[52:53], v[132:133] op_sel:[0,0,0] op_sel_hi:[0,1,1]\n" \
  "v_pk_fma_f32 v[134:135], v[70:71], v[54:55], v[134:135] op_sel:[0,0,0] op_sel_hi:[0,1,1]\n" \
  "s_set_gpr_idx_off\n" \
  "s_waitcnt vmcnt(6) lgkmcnt(2)\n" \
  "buffer_load_dwordx4 v[120:123], v[12:13], s[52:55], 0 idxen offen\n" \
  "ds_read_u16 v12, v14 offset:92\n" \
  "s_lshr_b32 s61, s61, 8\n" \
  "v_mfma_f32_4x4x4_16b_bf16 v[84:87], v[94:95], v[28:29], 0\n" \
  "v_mfma_f32_4x4x4_16b_bf16 v[88:91], v[94:95], v[30:31], 0\n" \
  "v_mfma_f32_4x4x4_16b_bf16 v[48:51], v[76:77], v[124:125], 0 cbsz:4 abid:15\n" \
  "v_mfma_f32_4x4x4_16b_bf16 v[52:55], v[76:77], v[126:127], 0 cbsz:4 abid:15\n" \
  "v_cvt_pk_bf16_f32 v59, v58, v59\n" \
  "v_lshlrev_b32_sdwa v58, 1, v59 dst_sel:DWORD dst_unused:UNUSED_PAD src0_sel:DWORD src1_sel:WORD_0\n" \
  "v_lshlrev_b32_sdwa v59, 1, v59 dst_sel:DWORD dst_unused:UNUSED_PAD src0_sel:DWORD src1_sel:WORD_1\n" \
  "ds_read_u16_d16_hi v58, v58\n" \
  "ds_read_u16_d16_hi v59, v59\n" \
  "v_cvt_pk_bf16_f32 v77, v84, v88\n" \
  "v_mov_b32 v76, v20\n" \
  "s_set_gpr_idx_on s61, gpr_idx(SRC2,DST)\n" \
  "v_pk_fma_f32 v[128:129], v[66:67], v[48:49], v[128:129] op_sel:[1,0,0] op_sel_hi:[1,1,1]\n" \
  "v_pk_fma_f32 v[130:131], v[66:67], v[50:51], v[130:131] op_sel:[1,0,0] op_sel_hi:[1,1,1]\n" \
  "v_pk_fma_f32 v[132:133], v[70:71], v[52:53], v[132:133] op_sel:[1,0,0] op_sel_hi:[1,1,1]\n" \
  "v_pk_fma_f32 v[134:135], v[70:71], v[54:55], v[134:135] op_sel:[1,0,0] op_sel_hi:[1,1,1]\n" \
  "s_set_gpr_idx_off\n" \
  "s_waitcnt vmcnt(6) lgkmcnt(2)\n" \
  "buffer_load_dwordx4 v[124:127], v[12:13], s[52:55], 0 idxen offen\n" \
  "ds_read_u16 v12, v14 offset:96\n" \
  "s_sub_u32 s60, s60, 1\n" \
  "s_cbranch_scc1 Lend%=\n" \
  "v_readlane_b32 s61, v17, s60\n" \
  "s_setprio 3\n" \
  "v_mfma_f32_4x4x4_16b_bf16 v[64:67], v[92:93], v[32:33], 0 cbsz:4 abid:1\n" \
  "v_mfma_f32_4x4x4_16b_bf16 v[68:71], v[92:93], v[36:37], 0 cbsz:4 abid:1\n" \
  "v_mfma_f32_4x4x4_16b_bf16 v[48:51], v[76:77], v[96:97], 0 cbsz:4\n" \
  "v_mfma_f32_4x4x4_16b_bf16 v[52:55], v[76:77], v[98:99], 0 cbsz:4\n" \
  "v_cvt_pk_bf16_f32 v63, v62, v63\n" \
  "v_lshlrev_b32_sdwa v62, 1, v63 dst_sel:DWORD dst_unused:UNUSED_PAD src0_sel:DWORD src1_sel:WORD_0\n" \
  "v_lshlrev_b32_sdwa v63, 1, v63 dst_sel:DWORD dst_unused:UNUSED_PAD src0_sel:DWORD src1_sel:WORD_1\n" \
  "ds_read_u16_d16_hi v62, v62\n" \
  "ds_read_u16_d16_hi v63, v63\n" \
  "v_add_u32 v14, 64, v14\n" \
  "s_branch Lbody%=\n" \
    \
  "Lep0_%=:\n" \
  "ds_read_b32 v50, v18\n" \
  "s_waitcnt lgkmcnt(0)\n" \
  "v_readfirstlane_b32 s63, v50\n" \
  "s_bitcmp1_b32 s63, 1\n" \
  "s_cbranch_scc1 Lgo%=\n" \
  "v_mov_b64 v[50:51], s[50:51]\n" \
  "s_mov_b64 exec, 1\n" \
  "buffer_store_dwordx2 v[50:51], off, s[24:27], s70 sc1\n" \
  "s_mov_b64 exec, -1\n" \
  "s_nop 3\n" \
  "s_branch Lgo%=\n" \
  "Lbad%=:\n" \
  "s_cmp_eq_u32 s74, 0xffffffc1\n" \
  "s_cbranch_scc1 Lf9_%=\n" \
  "v_mov_b32 v72, 1\n" \
  "v_mov_b32 v73, s62\n" \
  "ds_or_b32 v18, v72\n" \
  "ds_write_b32 v73, v73\n" \
  "s_branch Lf9_%=\n" \
  "Lnf%=:\n" \
  "s_mov_b32 %[bad], 3\n" \
  "s_mov_b32 s79, m0\n" \
  "Lbad2_%=:\n" \
  "s_waitcnt vmcnt(0) lgkmcnt(0)\n" \
  "s_bitcmp1_b32 %[bad], 1\n" \
  "s_cbranch_scc1 Lout%=\n" \
  "s_branch Lep%=\n" \
  "Lh2_%=:\n" \
  "s_waitcnt vmcnt(0)\n" \
  "v_add_co_u32 v90, vcc, 1, v88\n" \
  "s_mul_i32 s63, %[blk], 2176\n" \
  "s_add_u32 s63, s63, 49408\n" \
  "v_addc_co_u32 v91, vcc, 0, v89, vcc\n" \
  "v_lshlrev_b32 v88, 22, v90\n" \
  "v_alignbit_b32 v89, v91, v90, 10\n" \
  "v_add_u32 v87, v87, v88\n" \
  "v_lshl_add_u32 v128, v136, 11, v87\n" \
  "v_mov_b32 v129, v89\n" \
  "v_sub_u32 v51, v137, v136\n" \
  "v_lshl_add_u32 v51, v51, 11, v136\n" \
  "v_add_u32 v130, v51, v87\n" \
  "v_mov_b32 v131, v89\n" \
  "v_sub_u32 v51, v138, v137\n" \
  "v_lshl_add_u32 v51, v51, 11, v137\n" \
  "v_add_u32 v132, v51, v87\n" \
  "v_mov_b32 v133, v89\n" \
  "v_sub_u32 v51, v139, v138\n" \
  "v_lshl_add_u32 v51, v51, 11, v138\n" \
  "v_add_u32 v134, v51, v87\n" \
  "v_mov_b32 v135, v89\n" \
  "buffer_store_dwordx4 v[128:131], v13, s[24:27], s63 offen sc1\n" \
  "buffer_store_dwordx4 v[132:135], v13, s[24:27], s63 offen offset:1024 sc1\n" \
  "buffer_load_dword v100, v54, s[8:11], 0 offen offset:0\n" \
  "buffer_load_dword v101, v54, s[8:11], 0 offen offset:256\n" \
  "buffer_load_dword v102, v54, s[8:11], 0 offen offset:512\n" \
  "buffer_load_dword v103, v54, s[8:11], 0 offen offset:768\n" \
  "buffer_load_dword v104, v54, s[12:15], 0 offen\n" \
  "s_mul_i32 s72, s45, 528\n" \
  "s_cmp_lt_u32 s44, 4\n" \
  "s_cselect_b32 s62, 45776, 0\n" \
  "s_cselect_b32 s62, s62, 140608\n" \
  "s_add_u32 m0, s72, s62\n" \
  "s_lshl_b32 s72, s44, 12\n" \
  "buffer_load_dwordx4 v13, s[52:55], s72 offen lds\n" \
  "buffer_load_dwordx4 v13, s[52:55], s72 offen offset:1024 lds\n" \
  "s_add_u32 m0, m0, 64\n" \
  "s_nop 0\n" \
  "buffer_load_dwordx4 v13, s[52:55], s72 offen offset:2048 lds\n" \
  "buffer_load_dwordx4 v13, s[52:55], s72 offen offset:3072 lds\n" \
  "s_mov_b32 m0, 36160\n" \
  "Lw1_%=:\n" \
  "s_memrealtime s[90:91]\n" \
  "s_waitcnt lgkmcnt(0)\n" \
  "s_sub_u32 s90, s90, s76\n" \
  "s_cmp_lt_i32 s90, 185\n" \
  "s_cbranch_scc1 Lw1_%=\n" \
  "s_barrier\n" \
  "ds_write_b64 v94, v[90:91] offset:1472\n" \
  "s_mov_b32 s63, 0x05040100\n" \
  "s_mov_b32 s93, 0x07060302\n" \
  "s_waitcnt vmcnt(4)\n" \
  "v_perm_b32 v120, v101, v100, s63\n" \
  "v_perm_b32 v121, v103, v102, s63\n" \
  "v_perm_b32 v122, v101, v100, s93\n" \
  "v_perm_b32 v123, v103, v102, s93\n" \
  "ds_write_b128 v13, v[120:123] offset:36544\n" \
  "v_and_b32 v50, 30, v86\n" \
  "v_and_b32 v51, 16, v13\n" \
  "v_lshl_add_u32 v50, v51, 2, v50\n" \
  "s_mov_b64 exec, 0xffffffff\n" \
  "ds_write_b16 v50, v104 offset:36416\n" \
  "ds_write_b16_d16_hi v50, v104 offset:36448\n" \
  "s_mov_b64 exec, -1\n" \
  "v_and_b32 v100, 0x7fff7fff, v100\n" \
  "v_max_u16_sdwa v100, v100, v100 dst_sel:WORD_1 dst_unused:UNUSED_PAD src0_sel:WORD_0 src1_sel:WORD_1\n" \
  "v_and_b32 v101, 0x7fff7fff, v101\n" \
  "v_max_u16_sdwa v101, v101, v101 dst_sel:WORD_1 dst_unused:UNUSED_PAD src0_sel:WORD_0 src1_sel:WORD_1\n" \
  "v_and_b32 v102, 0x7fff7fff, v102\n" \
  "v_max_u16_sdwa v102, v102, v102 dst_sel:WORD_1 dst_unused:UNUSED_PAD src0_sel:WORD_0 src1_sel:WORD_1\n" \
  "v_and_b32 v103, 0x7fff7fff, v103\n" \
  "v_max_u16_sdwa v103, v103, v103 dst_sel:WORD_1 dst_unused:UNUSED_PAD src0_sel:WORD_0 src1_sel:WORD_1\n" \
  "v_add_f32 v100, v100, v101\n" \
  "v_add_f32 v102, v102, v103\n" \
  "v_add_f32 v100, v100, v102\n" \
  "v_and_b32 v100, 0x7fffffff, v100\n" \
  "s_nop 1\n" \
  "v_max_u32_dpp v100, v100, v100 row_shr:1 row_mask:0xf bank_mask:0xf bound_ctrl:1\n" \
  "s_nop 1\n" \
  "v_max_u32_dpp v100, v100, v100 row_shr:2 row_mask:0xf bank_mask:0xf bound_ctrl:1\n" \
  "s_nop 1\n" \
  "v_max_u32_dpp v100, v100, v100 row_shr:4 row_mask:0xf bank_mask:0xf bound_ctrl:1\n" \
  "s_nop 1\n" \
  "v_max_u32_dpp v100, v100, v100 row_shr:8 row_mask:0xf bank_mask:0xf bound_ctrl:1\n" \
  "s_nop 1\n" \
  "v_max_u32_dpp v100, v100, v100 row_bcast:15 row_mask:0xa bank_mask:0xf\n" \
  "s_nop 1\n" \
  "v_max_u32_dpp v100, v100, v100 row_bcast:31 row_mask:0xc bank_mask:0xf\n" \
  "v_max_u32 v101, 0x0d000000, v100\n" \
  "v_rcp_f32 v101, v101\n" \
  "v_cmp_gt_u32 vcc, 0x71000000, v100\n" \
  "v_mul_f32 v101, 0x437a0000, v101\n" \
  "v_lshrrev_b32 v101, 16, v101\n" \
  "v_cndmask_b32 v101, -1, v101, vcc\n" \
  "s_brev_b64 exec, 1\n" \
  "ds_write_b32 v94, v101 offset:1480\n" \
  "s_mov_b64 exec, -1\n" \
  "s_branch Lc1_%=\n" \
    \
  "Lend%=:\n" \
  "s_lshl2_add_u32 s62, s44, 36128\n" \
  "v_add_u32 v19, s72, v19\n" \
  "s_cmp_ge_u32 s74, 10\n" \
  "s_cbranch_scc1 Lbad%=\n" \
  "v_mov_b32 v72, s62\n" \
  "s_cmp_gt_u32 s93, s44\n" \
  "s_cbranch_scc0 Lf1_%=\n" \
  "s_set_gpr_idx_on s73, gpr_idx(SRC0)\n" \
  "v_mov_b64 v[84:85], v[128:129]\n" \
  "v_mov_b64 v[86:87], v[130:131]\n" \
  "v_mov_b64 v[88:89], v[132:133]\n" \
  "v_mov_b64 v[90:91], v[134:135]\n" \
  "s_set_gpr_idx_off\n" \
  "ds_write_b128 v21, v[84:87]\n" \
  "ds_write_b128 v21, v[88:91] offset:1024\n" \
  "ds_write_b32 v72, v72\n" \
  "s_sub_u32 s73, s73, 8\n" \
  "s_cselect_b32 s73, 72, s73\n" \
  "v_subrev_u32 v19, 1056, v19\n" \
  "s_sub_u32 s74, s74, 1\n" \
  "s_cbranch_scc1 Lf9_%=\n" \
  "Lf1_%=:\n" \
  "s_min_u32 s63, s75, 1\n" \
  "s_sub_u32 s60, s74, s63\n" \
  "s_cbranch_scc1 Lf3_%=\n" \
  "Lf2_%=:\n" \
  "s_set_gpr_idx_on s73, gpr_idx(SRC0,SRC1)\n" \
  "v_cvt_pk_bf16_f32 v48, v128, v132\n" \
  "v_cvt_pk_bf16_f32 v49, v129, v133\n" \
  "v_cvt_pk_bf16_f32 v50, v130, v134\n" \
  "v_cvt_pk_bf16_f32 v51, v131, v135\n" \
  "s_set_gpr_idx_off\n" \
  "s_sub_u32 s73, s73, 8\n" \
  "s_cselect_b32 s73, 72, s73\n" \
  "ds_write2_b32 v19, v48, v49 offset1:16\n" \
  "ds_write2_b32 v19, v50, v51 offset0:32 offset1:48\n" \
  "s_sub_u32 s60, s60, 1\n" \
  "v_subrev_u32 v19, 1056, v19\n" \
  "s_cbranch_scc0 Lf2_%=\n" \
  "s_cmp_eq_u32 s75, 0\n" \
  "s_cbranch_scc1 Lf9_%=\n" \
  "Lf3_%=:\n" \
  "v_subrev_u32 v72, 4, v72\n" \
  "v_subrev_u32 v21, 3328, v21\n" \
  "Lf4_%=:\n" \
  "ds_read_b32 v73, v72\n" \
  "ds_read_b128 v[84:87], v21\n" \
  "ds_read_b128 v[88:91], v21 offset:1024\n" \
  "s_waitcnt lgkmcnt(0)\n" \
  "v_cmp_eq_u32 vcc, 0, v73\n" \
  "s_cbranch_vccz Lf5_%=\n" \
  "s_setprio 0\n" \
  "s_sleep 1\n" \
  "s_branch Lf4_%=\n" \
  "Lf5_%=:\n" \
  "s_set_gpr_idx_on s73, gpr_idx(SRC0,DST)\n" \
  "v_pk_add_f32 v[128:129], v[128:129], v[84:85]\n" \
  "v_pk_add_f32 v[130:131], v[130:131], v[86:87]\n" \
  "v_pk_add_f32 v[132:133], v[132:133], v[88:89]\n" \
  "v_pk_add_f32 v[134:135], v[134:135], v[90:91]\n" \
  "s_set_gpr_idx_off\n" \
  "s_mov_b32 s60, 0\n" \
  "s_sub_u32 s75, s75, 1\n" \
  "s_cmp_eq_u32 s75, 0\n" \
  "s_cbranch_scc1 Lf2_%=\n" \
  "s_branch Lf3_%=\n" \
    \
  "Lf9_%=:\n" \
  "s_cmp_lt_u32 s44, 4\n" \
  "s_cselect_b32 s63, 1, 0x100\n" \
  "s_cselect_b32 s93, 4, 0x404\n" \
  "v_mov_b32 v72, s63\n" \
  "s_mov_b64 exec, 1\n" \
  "ds_add_u32 v18, v72 offset:8\n" \
  "s_mov_b64 exec, -1\n" \
  "s_setprio 0\n" \
  "buffer_load_dword v20, off, s[24:27], s70 sc1\n" \
  "s_mul_i32 s63, s76, 4224\n" \
  "v_add_u32 v22, s63, v22\n" \
  "s_lshl_b32 s72, s76, 12\n" \
  "Lm0_%=:\n" \
  "ds_read_b32 v72, v18 offset:8\n" \
  "ds_read_b128 v[128:131], v22 offset:0\n" \
  "ds_read_b128 v[132:135], v22 offset:256\n" \
  "ds_read_b128 v[136:139], v22 offset:512\n" \
  "ds_read_b128 v[140:143], v22 offset:768\n" \
  "ds_read_b128 v[144:147], v22 offset:4224\n" \
  "ds_read_b128 v[148:151], v22 offset:4480\n" \
  "ds_read_b128 v[152:155], v22 offset:4736\n" \
  "ds_read_b128 v[156:159], v22 offset:4992\n" \
  "s_waitcnt lgkmcnt(8)\n" \
  "v_readfirstlane_b32 s63, v72\n" \
  "s_and_b32 s63, s63, s93\n" \
  "s_cmp_eq_u32 s63, s93\n" \
  "s_cbranch_scc1 Lm1_%=\n" \
  "Lm4_%=:\n" \
  "s_sleep 1\n" \
  "ds_read_b32 v72, v18 offset:8\n" \
  "s_waitcnt lgkmcnt(0)\n" \
  "v_readfirstlane_b32 s63, v72\n" \
  "s_and_b32 s63, s63, s93\n" \
  "s_cmp_eq_u32 s63, s93\n" \
  "s_cbranch_scc0 Lm4_%=\n" \
  "ds_read_b128 v[128:131], v22 offset:0\n" \
  "ds_read_b128 v[132:135], v22 offset:256\n" \
  "ds_read_b128 v[136:139], v22 offset:512\n" \
  "ds_read_b128 v[140:143], v22 offset:768\n" \
  "ds_read_b128 v[144:147], v22 offset:4224\n" \
  "ds_read_b128 v[148:151], v22 offset:4480\n" \
  "ds_read_b128 v[152:155], v22 offset:4736\n" \
  "ds_read_b128 v[156:159], v22 offset:4992\n" \
  "Lm1_%=:\n" \
  "s_sub_u32 s77, s77, 1\n" \
  "s_cbranch_scc1 Lm8_%=\n" \
  "ds_read_b128 v[160:163], v22 offset:8448\n" \
  "ds_read_b128 v[164:167], v22 offset:8704\n" \
  "ds_read_b128 v[168:171], v22 offset:8960\n" \
  "ds_read_b128 v[172:175], v22 offset:9216\n" \
  "s_waitcnt lgkmcnt(11)\n" \
  "v_mfma_f32_16x16x32_bf16 v[40:43], v[128:131], v[216:219], 0\n" \
  "v_mfma_f32_16x16x32_bf16 v[44:47], v[128:131], v[220:223], 0\n" \
  "s_waitcnt lgkmcnt(10)\n" \
  "v_mfma_f32_16x16x32_bf16 v[40:43], v[132:135], v[224:227], v[40:43]\n" \
  "v_mfma_f32_16x16x32_bf16 v[44:47], v[132:135], v[228:231], v[44:47]\n" \
  "s_waitcnt lgkmcnt(9)\n" \
  "v_mfma_f32_16x16x32_bf16 v[40:43], v[136:139], v[232:235], v[40:43]\n" \
  "v_mfma_f32_16x16x32_bf16 v[44:47], v[136:139], v[236:239], v[44:47]\n" \
  "s_waitcnt lgkmcnt(8)\n" \
  "v_mfma_f32_16x16x32_bf16 v[40:43], v[140:143], v[240:243], v[40:43]\n" \
  "v_mfma_f32_16x16x32_bf16 v[44:47], v[140:143], v[244:247], v[44:47]\n" \
  "Lm2_%=:\n" \
  "s_sub_u32 s77, s77, 1\n" \
  "s_cbranch_scc1 Lm5_%=\n" \
  "v_add_u32 v22, 4224, v22\n" \
  "s_nop 0\n" \
  "ds_read_b128 v[128:131], v22 offset:8448\n" \
  "ds_read_b128 v[132:135], v22 offset:8704\n" \
  "ds_read_b128 v[136:139], v22 offset:8960\n" \
  "ds_read_b128 v[140:143], v22 offset:9216\n" \
  "s_waitcnt lgkmcnt(11)\n" \
  "v_mfma_f32_16x16x32_bf16 v[48:51], v[144:147], v[216:219], 0\n" \
  "v_mfma_f32_16x16x32_bf16 v[52:55], v[144:147], v[220:223], 0\n" \
  "s_waitcnt lgkmcnt(10)\n" \
  "v_mfma_f32_16x16x32_bf16 v[48:51], v[148:151], v[224:227], v[48:51]\n" \
  "v_mfma_f32_16x16x32_bf16 v[52:55], v[148:151], v[228:231], v[52:55]\n" \
  "v_cvt_pk_bf16_f32 v40, v40, v41\n" \
  "v_cvt_pk_bf16_f32 v41, v42, v43\n" \
  "v_cvt_pk_bf16_f32 v42, v44, v45\n" \
  "v_cvt_pk_bf16_f32 v43, v46, v47\n" \
  "buffer_store_dwordx4 v[40:43], v23, s[56:59], s72 offen sc1\n" \
  "s_add_u32 s72, s72, 0x1000\n" \
  "s_waitcnt lgkmcnt(9)\n" \
  "v_mfma_f32_16x16x32_bf16 v[48:51], v[152:155], v[232:235], v[48:51]\n" \
  "v_mfma_f32_16x16x32_bf16 v[52:55], v[152:155], v[236:239], v[52:55]\n" \
  "s_waitcnt lgkmcnt(8)\n" \
  "v_mfma_f32_16x16x32_bf16 v[48:51], v[156:159], v[240:243], v[48:51]\n" \
  "v_mfma_f32_16x16x32_bf16 v[52:55], v[156:159], v[244:247], v[52:55]\n" \
  "s_sub_u32 s77, s77, 1\n" \
  "s_cbranch_scc1 Lm6_%=\n" \
  "v_add_u32 v22, 4224, v22\n" \
  "s_nop 0\n" \
  "ds_read_b128 v[144:147], v22 offset:8448\n" \
  "ds_read_b128 v[148:151], v22 offset:8704\n" \
  "ds_read_b128 v[152:155], v22 offset:8960\n" \
  "ds_read_b128 v[156:159], v22 offset:9216\n" \
  "s_waitcnt lgkmcnt(11)\n" \
  "v_mfma_f32_16x16x32_bf16 v[56:59], v[160:163], v[216:219], 0\n" \
  "v_mfma_f32_16x16x32_bf16 v[60:63], v[160:163], v[220:223], 0\n" \
  "s_waitcnt lgkmcnt(10)\n" \
  "v_mfma_f32_16x16x32_bf16 v[56:59], v[164:167], v[224:227], v[56:59]\n" \
  "v_mfma_f32_16x16x32_bf16 v[60:63], v[164:167], v[228:231], v[60:63]\n" \
  "v_cvt_pk_bf16_f32 v48, v48, v49\n" \
  "v_cvt_pk_bf16_f32 v49, v50, v51\n" \
  "v_cvt_pk_bf16_f32 v50, v52, v53\n" \
  "v_cvt_pk_bf16_f32 v51, v54, v55\n" \
  "buffer_store_dwordx4 v[48:51], v23, s[56:59], s72 offen sc1\n" \
  "s_add_u32 s72, s72, 0x1000\n" \
  "s_waitcnt lgkmcnt(9)\n" \
  "v_mfma_f32_16x16x32_bf16 v[56:59], v[168:171], v[232:235], v[56:59]\n" \
  "v_mfma_f32_16x16x32_bf16 v[60:63], v[168:171], v[236:239], v[60:63]\n" \
  "s_waitcnt lgkmcnt(8)\n" \
  "v_mfma_f32_16x16x32_bf16 v[56:59], v[172:175], v[240:243], v[56:59]\n" \
  "v_mfma_f32_16x16x32_bf16 v[60:63], v[172:175], v[244:247], v[60:63]\n" \
  "s_sub_u32 s77, s77, 1\n" \
  "s_cbranch_scc1 Lm7_%=\n" \
  "v_add_u32 v22, 4224, v22\n" \
  "s_nop 0\n" \
  "ds_read_b128 v[160:163], v22 offset:8448\n" \
  "ds_read_b128 v[164:167], v22 offset:8704\n" \
  "ds_read_b128 v[168:171], v22 offset:8960\n" \
  "ds_read_b128 v[172:175], v22 offset:9216\n" \
  "s_waitcnt lgkmcnt(11)\n" \
  "v_mfma_f32_16x16x32_bf16 v[40:43], v[128:131], v[216:219], 0\n" \
  "v_mfma_f32_16x16x32_bf16 v[44:47], v[128:131], v[220:223], 0\n" \
  "s_waitcnt lgkmcnt(10)\n" \
  "v_mfma_f32_16x16x32_bf16 v[40:43], v[132:135], v[224:227], v[40:43]\n" \
  "v_mfma_f32_16x16x32_bf16 v[44:47], v[132:135], v[228:231], v[44:47]\n" \
  "v_cvt_pk_bf16_f32 v56, v56, v57\n" \
  "v_cvt_pk_bf16_f32 v57, v58, v59\n" \
  "v_cvt_pk_bf16_f32 v58, v60, v61\n" \
  "v_cvt_pk_bf16_f32 v59, v62, v63\n" \
  "buffer_store_dwordx4 v[56:59], v23, s[56:59], s72 offen sc1\n" \
  "s_add_u32 s72, s72, 0x1000\n" \
  "s_waitcnt lgkmcnt(9)\n" \
  "v_mfma_f32_16x16x32_bf16 v[40:43], v[136:139], v[232:235], v[40:43]\n" \
  "v_mfma_f32_16x16x32_bf16 v[44:47], v[136:139], v[236:239], v[44:47]\n" \
  "s_waitcnt lgkmcnt(8)\n" \
  "v_mfma_f32_16x16x32_bf16 v[40:43], v[140:143], v[240:243], v[40:43]\n" \
  "v_mfma_f32_16x16x32_bf16 v[44:47], v[140:143], v[244:247], v[44:47]\n" \
  "s_branch Lm2_%=\n" \
  "Lm5_%=:\n" \
  "s_nop 5\n" \
  "v_cvt_pk_bf16_f32 v40, v40, v41\n" \
  "v_cvt_pk_bf16_f32 v41, v42, v43\n" \
  "v_cvt_pk_bf16_f32 v42, v44, v45\n" \
  "v_cvt_pk_bf16_f32 v43, v46, v47\n" \
  "buffer_store_dwordx4 v[40:43], v23, s[56:59], s72 offen sc1\n" \
  "s_add_u32 s72, s72, 0x1000\n" \
  "s_branch Lm9_%=\n" \
  "Lm6_%=:\n" \
  "s_nop 5\n" \
  "v_cvt_pk_bf16_f32 v48, v48, v49\n" \
  "v_cvt_pk_bf16_f32 v49, v50, v51\n" \
  "v_cvt_pk_bf16_f32 v50, v52, v53\n" \
  "v_cvt_pk_bf16_f32 v51, v54, v55\n" \
  "buffer_store_dwordx4 v[48:51], v23, s[56:59], s72 offen sc1\n" \
  "s_add_u32 s72, s72, 0x1000\n" \
  "s_branch Lm9_%=\n" \
  "Lm7_%=:\n" \
  "s_nop 5\n" \
  "v_cvt_pk_bf16_f32 v56, v56, v57\n" \
  "v_cvt_pk_bf16_f32 v57, v58, v59\n" \
  "v_cvt_pk_bf16_f32 v58, v60, v61\n" \
  "v_cvt_pk_bf16_f32 v59, v62, v63\n" \
  "buffer_store_dwordx4 v[56:59], v23, s[56:59], s72 offen sc1\n" \
  "s_add_u32 s72, s72, 0x1000\n" \
  "Lm9_%=:\n" \
  "v_add_u32 v22, 4224, v22\n" \
  "Lm8_%=:\n" \
  "s_waitcnt lgkmcnt(0)\n" \
  "s_mov_b32 s77, s78\n" \
  "s_mov_b32 s78, 0\n" \
  "s_movk_i32 s93, 0x404\n" \
  "s_cmp_lg_u32 s77, 0\n" \
  "s_cbranch_scc1 Lm0_%=\n" \
  "s_barrier\n" \
  "ds_read_b32 v18, v18\n" \
  "s_waitcnt lgkmcnt(0)\n" \
  "v_readfirstlane_b32 %[bad], v18\n" \
  "s_cmp_lg_u32 %[bad], 0\n" \
  "s_cbranch_scc1 Lbad2_%=\n" \
  "s_cmp_lg_u32 s44, 0\n" \
  "s_cbranch_scc1 Lout%=\n" \
  "s_waitcnt vmcnt(0)\n" \
  "v_readfirstlane_b32 s63, v20\n" \
  "s_cmp_eq_u32 s63, s50\n" \
  "s_cbranch_scc1 Lout%=\n" \
  "Lep%=:\n" \
  "s_cmp_lg_u32 s44, 0\n" \
  "s_cbranch_scc1 Lout%=\n" \
  "v_mov_b64 v[84:85], s[50:51]\n" \
  "s_mov_b64 exec, 1\n" \
  "buffer_store_dwordx2 v[84:85], off, s[24:27], s70 sc1\n" \
  "s_mov_b64 exec, -1\n" \
  "Lout%=:\n" \
  "s_cmp_lg_u32 %[bad], 0\n" \
  "s_cbranch_scc1 Lsl%=\n" \
  "s_endpgm\n" \
  "Lsl%=:\n" \
  "s_mov_b32 m0, s79\n"
#define STREAM_CLOBBERS \
  "v12", "v13", "v14", "v15", "v16", "v17", "v18", "v19", "v20", "v21", "v22", "v23", "v24", "v25", "v26", \
  "v27", "v28", "v29", "v30", "v31", "v32", "v33", "v34", "v35", "v36", "v37", "v38", "v39", "v40", "v41", \
  "v42", "v43", "v44", "v45", "v46", "v47", "v48", "v49", "v50", "v51", "v52", "v53", "v54", "v55", "v56", \
  "v57", "v58", "v59", "v60", "v61", "v62", "v63", "v64", "v65", "v66", "v67", "v68", "v69", "v70", "v71", \
  "v72", "v73", "v74", "v75", "v76", "v77", "v78", "v79", "v80", "v81", "v82", "v83", "v84", "v85", "v86", \
  "v87", "v88", "v89", "v90", "v91", "v92", "v93", "v94", "v95", "v96", "v97", "v98", "v99", "v100", "v101", \
  "v102", "v103", "v104", "v105", "v106", "v107", "v108", "v109", "v110", "v111", "v112", "v113", "v114", \
  "v115", "v116", "v117", "v118", "v119", "v120", "v121", "v122", "v123", "v124", "v125", "v126", "v127", \
  "v128", "v129", "v130", "v131", "v132", "v133", "v134", "v135", "v136", "v137", "v138", "v139", "v140", \
  "v141", "v142", "v143", "v144", "v145", "v146", "v147", "v148", "v149", "v150", "v151", "v152", "v153", \
  "v154", "v155", "v156", "v157", "v158", "v159", "v160", "v161", "v162", "v163", "v164", "v165", "v166", \
  "v167", "v168", "v169", "v170", "v171", "v172", "v173", "v174", "v175", "v176", "v177", "v178", "v179", \
  "v180", "v181", "v182", "v183", "v184", "v185", "v186", "v187", "v188", "v189", "v190", "v191", "v192", \
  "v193", "v194", "v195", "v196", "v197", "v198", "v199", "v200", "v201", "v202", "v203", "v204", "v205", \
  "v206", "v207", "v208", "v209", "v210", "v211", "v212", "v213", "v214", "v215", "v216", "v217", "v218", \
  "v219", "v220", "v221", "v222", "v223", "v224", "v225", "v226", "v227", "v228", "v229", "v230", "v231", \
  "v232", "v233", "v234", "v235", "v236", "v237", "v238", "v239", "v240", "v241", "v242", "v243", "v244", \
  "v245", "v246", "v247", "v248", "v249", "vcc", "s8", "s9", "s10", "s11", "s12", "s13", "s14", "s15", "s16", \
  "s17", "s18", "s19", "s20", "s21", "s22", "s23", "s24", "s25", "s26", "s27", "s28", "s29", "s30", "s31", \
  "s33", "s34", "s35", "s36", "s37", "s38", "s39", "s40", "s41", "s42", "s43", "s44", "s45", "s46", "s47", \
  "s48", "s49", "s50", "s51", "s52", "s53", "s54", "s55", "s56", "s57", "s58", "s59", "s60", "s61", "s62", \
  "s63", "s64", "s65", "s66", "s67", "s68", "s69", "s70", "s71", "s72", "s73", "s74", "s75", "s76", "s77", \
  "s78", "s79", "s80", "s81", "s82", "s83", "s84", "s85", "s86", "s87", "s88", "s89", "s90", "s91", "s92", \
  "s93", "s94", "s95", "scc", "memory"
#define LD_CELL 34560
#define LD_DEG 35584
#define LD_FLAG 36096
#define LD_CRD 36128
#define LD_SINK 36160
#define LD_CGT 36416
#define LD_WQT 36544
#define LD_TAG 37568
#define LD_PAY 37584
#define LD_ENT 100096
#define LD_TB 106816
#define LD_ROWS 108864
#define LD_SK 129344
#define LD_HIST 134464
#define LD_HB 136512
#define LD_W0 45776
#define LD_W1 140608
#define LD_END 157504
#define WS_EP 0
#define WS_FB 256
#define WS_KF 16640
#define WS_GR 49408
#define WS_RECK 606464
#define WS_RECP 1917184
#define WS_END 7160064


namespace {

constexpr int C_ = 128;
constexpr int NMAX = 40;
constexpr int NB = 256;
constexpr int CH = 1280;
constexpr int MT = 512;
constexpr int MW = MT / 64;
constexpr int RP = 1056;
constexpr unsigned W3 = 0x00020000u;

__device__ __forceinline__ float bf2f(unsigned short v) { return __uint_as_float(((unsigned)v) << 16); }
__device__ __forceinline__ unsigned short f2bf(float f) {
  __hip_bfloat16 b = __float2bfloat16(f);
  return *reinterpret_cast<unsigned short*>(&b);
}
__device__ __forceinline__ unsigned pk2bf(float lo, float hi) {
  return __builtin_bit_cast(unsigned, __builtin_convertvector(f32x2{lo, hi}, bf16x2));
}

}

extern "C" __global__ __launch_bounds__(MT, 2) void tfn_kernel(
    const int* __restrict__ dst, unsigned dn, unsigned dw, const int* __restrict__ src, unsigned sn, unsigned sw,
    const u32x2* __restrict__ Y, unsigned yn, unsigned yw, const u32x2* __restrict__ ef, unsigned fn, unsigned fw,
    char* ws, unsigned wn, unsigned ww, const unsigned short* __restrict__ x, unsigned xn, unsigned xw,
    u32x4* __restrict__ out, unsigned pt, int N, unsigned M2, unsigned cb, int nbk, unsigned npb,
    const unsigned short* __restrict__ wrad, unsigned rn, unsigned rw, const unsigned short* __restrict__ cg, unsigned cn,
    unsigned cw, const unsigned short* __restrict__ wmix, unsigned mn, unsigned mw) {
  __shared__ __attribute__((aligned(16))) char lds[LD_END];
  static_assert(LD_END >= LD_ROWS + NMAX * RP && LD_END <= 163840, "LDS");
  unsigned bad;
  asm volatile(STREAM_ASM
               : [bad] "=&s"(bad)
               : [ka] "s"(__builtin_amdgcn_kernarg_segment_ptr()), [blk] "s"(blockIdx.x), [tid] "v"(threadIdx.x)
               : STREAM_CLOBBERS);
  const bool slow = bad != 0;
  if (slow) {
    const int lane = threadIdx.x & 63, w = __builtin_amdgcn_readfirstlane(threadIdx.x >> 6), E = dn / 4;
    const int bucket = (blockIdx.x & 7) * (nbk / 8) + (blockIdx.x >> 3);
    int n01[2];
#pragma unroll
    for (int h = 0; h < 2; h++) {
      unsigned c = (unsigned)(((unsigned long long)(bucket + h) * npb) >> 20);
#pragma unroll
      for (int r = 0; r < 2; r++) c += __umulhi(c, M2) < (unsigned)(bucket + h) ? 1 : 0;
      n01[h] = min(c, (unsigned)N);
    }
    const int node0 = n01[0], nn = n01[1] - n01[0];
    const unsigned mxa = LD_ROWS + ((w >> 2) * 4 + ((lane & 15) >> 2)) * RP + (lane & 3) * 64 + (lane >> 4) * 16;
    const unsigned mxo = ((w >> 2) * 4 + (lane >> 4)) * 1024 + (w & 3) * 256 + (lane & 15) * 16;
    __syncthreads();
#pragma unroll 1
    for (int it = w; it < 2 * nn; it += MW) {
      const int dl = it >> 1, ch = (it & 1) * 64 + lane;
      float acc[4] = {0.f, 0.f, 0.f, 0.f};
#pragma unroll 1
      for (int eb = 0; eb < E; eb += 64) {
        unsigned long long m = __ballot(eb + lane < E && dst[min(eb + lane, E - 1)] == node0 + dl);
#pragma unroll 1
        for (; m; m &= m - 1) {
          const int e = eb + __builtin_ctzll(m);
          const u32x2 ry = Y[e], rf = ef[e];
          float yy[4] = {bf2f(ry[0] & 0xffff), bf2f(ry[0] >> 16), bf2f(ry[1] & 0xffff), bf2f(ry[1] >> 16)};
          float ee[4] = {bf2f(rf[0] & 0xffff), bf2f(rf[0] >> 16), bf2f(rf[1] & 0xffff), bf2f(rf[1] >> 16)};
          float z = 0.f;
#pragma unroll
          for (int k = 0; k < 4; k++) z += ee[k] * bf2f(wrad[k * C_ + ch]);
          z = bf2f(f2bf(z));
          const float R = bf2f(f2bf(z / (1.f + __expf(-z))));
          const unsigned short* xr = x + (size_t)src[e] * 512 + ch * 4;
#pragma unroll 1
          for (int o = 0; o < 4; o++) {
            float tp = 0.f;
#pragma unroll 1
            for (int i = 0; i < 4; i++) {
              float A = 0.f;
#pragma unroll
              for (int f = 0; f < 4; f++) A += yy[f] * bf2f(cg[i * 16 + f * 4 + o]);
              tp += bf2f(xr[i]) * bf2f(f2bf(A));
            }
            const float v = R * tp;
            acc[0] += o == 0 ? v : 0.f;
            acc[1] += o == 1 ? v : 0.f;
            acc[2] += o == 2 ? v : 0.f;
            acc[3] += o == 3 ? v : 0.f;
          }
        }
      }
      unsigned short* ar = reinterpret_cast<unsigned short*>(lds + LD_ROWS) + dl * (RP / 2) + (ch >> 5) * 128 + (ch & 31);
#pragma unroll
      for (int o = 0; o < 4; o++) ar[o * 32] = f2bf(acc[o]);
    }
    __syncthreads();
    const auto ors = __builtin_amdgcn_make_buffer_rsrc((void*)(out + (size_t)node0 * 64), 0, nn * 1024, 0x00020000);
#pragma unroll 1
    for (int t = 0; t < NMAX / 8; t++) {
      f32x4 a = {0.f, 0.f, 0.f, 0.f}, b = {0.f, 0.f, 0.f, 0.f};
#pragma unroll 1
      for (int k = 0; k < 4; k++) {
        const s16x8 f = *reinterpret_cast<const s16x8*>(lds + mxa + t * 8 * RP + k * 256);
        const unsigned short* wp = wmix + (32 * k + 8 * (lane >> 4)) * C_ + 32 * (w & 3) + 2 * (lane & 15);
        s16x8 w0, w1;
#pragma unroll
        for (int j = 0; j < 8; j++) {
          w0[j] = wp[j * C_];
          w1[j] = wp[j * C_ + 1];
        }
        a = __builtin_amdgcn_mfma_f32_16x16x32_bf16(f, w0, a, 0, 0, 0);
        b = __builtin_amdgcn_mfma_f32_16x16x32_bf16(f, w1, b, 0, 0, 0);
      }
      __builtin_amdgcn_raw_buffer_store_b128(u32x4{pk2bf(a[0], a[1]), pk2bf(a[2], a[3]), pk2bf(b[0], b[1]), pk2bf(b[2], b[3])},
                                             ors, mxo + t * 8192, 0, 0);
    }
  }
}

extern "C" void kernel_launch(void* const* d_in, const int* in_sizes, int n_in, void* d_out, int out_size,
                              void* d_ws, size_t ws_size, hipStream_t stream) {
  const int N = in_sizes[0] / 512;
  const int E = in_sizes[1];
  int nbk = NB;
  while ((N + nbk - 1) / nbk > NMAX || (N % nbk == 0 && N / nbk >= NMAX)) nbk += NB;
  const unsigned long long M2l = (((unsigned long long)nbk) << 32) / (unsigned long long)N;
  const unsigned M2 = M2l > 0xffffffffull ? 0xffffffffu : (unsigned)M2l;
  const unsigned long long cb = (16ull << 32) / (unsigned long long)N;
  const bool fast = nbk == NB && E <= NB * CH && ws_size >= WS_END;
  const unsigned e4 = E * 4u;
  tfn_kernel<<<nbk, MT, 0, stream>>>(
      (const int*)d_in[2], e4, W3, (const int*)d_in[1], e4, W3, (const u32x2*)d_in[3], 2 * e4, W3, (const u32x2*)d_in[4], 2 * e4, W3,
      (char*)d_ws, WS_END, W3, (const unsigned short*)d_in[0], N * 1024u, W3, (u32x4*)d_out, fast ? 448u : 0u, N, M2,
      cb > 0xffffffull ? 0xffffffu : (unsigned)cb, nbk, (unsigned)((1ull << 52) / M2), (const unsigned short*)d_in[5], 4 * C_ * 2,
      W3, (const unsigned short*)d_in[6], 128, W3, (const unsigned short*)d_in[7], C_ * C_ * 2, W3);
}
```
